```python
import torch

def _softmax(x, axis=-1):
    return torch.softmax(x, dim=axis)

def _gelu(x):
    return torch.nn.functional.gelu(x, approximate='tanh')

def _silu(x):
    return torch.nn.functional.silu(x)

def _sigmoid(x):
    return torch.sigmoid(x)

def _layernorm(x, axis=-1, eps=1e-05):
    m = x.mean(dim=axis, keepdim=True)
    v = x.var(dim=axis, keepdim=True, unbiased=False)
    return (x - m) / torch.sqrt(v + eps)

def _scatter_add(tgt, idx, src, axis=0):
    return tgt.clone().index_add_(axis, idx, src)

def _segment_sum(src, seg, n):
    out = torch.zeros((n,) + src.shape[1:], dtype=src.dtype)
    return out.index_add_(0, seg, src)

def _pad(x, pw):
    flat = tuple((v for pair in reversed(pw) for v in pair))
    return torch.nn.functional.pad(x, flat)
kernel_name = 'adaLN_cond__ssm_conv_s4__dense_activation_relu__prompt'

def setup_inputs():

    _w = lambda a: (lambda t: t.to(torch.bfloat16) if t.is_floating_point() else t)(torch.as_tensor(a))

    def _A_setup():
        B, L, D, N = (1, 2048, 640, 128)
        A_re = -(lambda _x: torch.exp(torch.as_tensor(_x)))((lambda *s: torch.randn(*s, dtype=torch.bfloat16) * 3.2)(D, N))
        A_im = torch.pi * (lambda *s: torch.randn(*s, dtype=torch.bfloat16) * 0.1)(D, N)
        B_re = (lambda *s: torch.randn(*s, dtype=torch.bfloat16) * 0.2)(D, N)
        B_im = (lambda *s: torch.randn(*s, dtype=torch.bfloat16) * 0.2)(D, N)
        C_re = (lambda *s: torch.randn(*s, dtype=torch.bfloat16) * 0.2)(D, N)
        C_im = (lambda *s: torch.randn(*s, dtype=torch.bfloat16) * 0.2)(D, N)
        log_dt = (lambda *s: torch.randn(*s, dtype=torch.bfloat16) * 0.1)(D) * 0.5 - 4.0
        Dskip = (lambda *s: torch.randn(*s, dtype=torch.bfloat16) * 0.2)(D)
        return {'u': _w((lambda *s: torch.randn(*s, dtype=torch.bfloat16) * 0.05)(B, L, D)), 'A_re': _w(A_re), 'A_im': _w(A_im), 'B_re': _w(B_re), 'B_im': _w(B_im), 'C_re': _w(C_re), 'C_im': _w(C_im), 'log_dt': _w(log_dt), 'Dskip': _w(Dskip)}
    _A_in = _A_setup()

    def _B_setup():
        B, S, D, Dff = (1, 2048, 640, 1728)
        return {'x': _w((lambda *s: torch.randn(*s, dtype=torch.bfloat16) * 0.05)(B, S, D)), 'w1': _w((lambda *s: torch.randn(*s, dtype=torch.bfloat16) * 0.05)(D, Dff)), 'w2': _w((lambda *s: torch.randn(*s, dtype=torch.bfloat16) * 0.05)(Dff, D))}
    _B_in = _B_setup()
    return {'A_u': _A_in['u'], 'A_A_re': _A_in['A_re'], 'A_A_im': _A_in['A_im'], 'A_B_re': _A_in['B_re'], 'A_B_im': _A_in['B_im'], 'A_C_re': _A_in['C_re'], 'A_C_im': _A_in['C_im'], 'A_log_dt': _A_in['log_dt'], 'A_Dskip': _A_in['Dskip'], 'B_x': _B_in['x'], 'B_w1': _B_in['w1'], 'B_w2': _B_in['w2'], 'c': _w((lambda *s: torch.randn(*s, dtype=torch.bfloat16) * 0.1)(1, 640) * 8.0), 'w_mod': _w((lambda *s: torch.randn(*s, dtype=torch.bfloat16) * 0.1)(640, 6 * 640) * 640 ** (-0.5) * 8.0)}

def reference(A_u, A_A_re, A_A_im, A_B_re, A_B_im, A_C_re, A_C_im, A_log_dt, A_Dskip, B_x, B_w1, B_w2, c, w_mod):

    def _A_ref(u, A_re, A_im, B_re, B_im, C_re, C_im, log_dt, Dskip):
        Bsz, L, D = u.shape
        dt = torch.exp(log_dt)[:, None]
        A = A_re + 1j * A_im
        Bc = B_re + 1j * B_im
        Cc = C_re + 1j * C_im
        logdA = dt * A
        dB = dt * Bc
        l = (lambda x, dt: x.to(dt))(torch.arange(L), A_re.dtype)
        P = torch.exp(logdA[:, :, None] * l[None, None, :])
        K = torch.einsum('dn,dnl,dn->dl', Cc, P, dB).real
        L2 = 2 * L
        Kp = _pad(K, ((0, 0), (0, L)))
        up = (lambda x, *d: x.permute(*d))(u, 0, 2, 1)
        up = _pad(up, ((0, 0), (0, 0), (0, L)))
        Kf = (lambda x, axes=(-2, -1): torch.fft.rfft2(x.float(), dim=axes))(Kp[:, None, :], axes=(-2, -1))
        Uf = (lambda x, axes=(-2, -1): torch.fft.rfft2(x.float(), dim=axes))(up[:, :, None, :], axes=(-2, -1))
        Yf = Uf * Kf[None, :, :, :]
        y = (lambda x, s, axes=(-2, -1): torch.fft.irfft2(x, s=s, dim=axes).to(torch.bfloat16))(Yf, (1, L2), axes=(-2, -1))[:, :, 0, :L]
        y = (lambda x, *d: x.permute(*d))(y, 0, 2, 1) + u * Dskip[None, None, :]
        return y

    def _B_ref(x, w1, w2):
        h = torch.einsum('bsd,df->bsf', x, w1)
        zero = torch.zeros_like(h)
        h = torch.maximum(h, zero)
        return torch.einsum('bsf,fd->bsd', h, w2)
    x = A_u
    mod = (_silu(c) @ w_mod).reshape(c.shape[0], 6, 1, -1)
    sa, ca, ga, sb, cb, gb = (mod[:, 0].clone(), mod[:, 1].clone(), mod[:, 2].clone(), mod[:, 3].clone(), mod[:, 4].clone(), mod[:, 5].clone())
    h = _layernorm(x, axis=-1) * (1 + ca) + sa
    x = x + ga * _A_ref(h, A_A_re, A_A_im, A_B_re, A_B_im, A_C_re, A_C_im, A_log_dt, A_Dskip)
    h = _layernorm(x, axis=-1) * (1 + cb) + sb
    x = x + gb * _B_ref(h, B_w1, B_w2)
    return x
```

```cpp
#include <hip/hip_runtime.h>
#include <hip/hip_bf16.h>
#include <stdint.h>

typedef __attribute__((ext_vector_type(8))) __bf16 bf16x8;
typedef __attribute__((ext_vector_type(2))) __bf16 bf16x2;
typedef __attribute__((ext_vector_type(4))) float f32x4;
typedef __attribute__((ext_vector_type(2))) float f32x2;
typedef __attribute__((ext_vector_type(4))) unsigned u32x4;
typedef __attribute__((ext_vector_type(2))) unsigned u32x2;

#define DEV __device__ __forceinline__

constexpr int L_ = 2048;
constexpr int D_ = 640;
constexpr int NS = 128;
constexpr int FF = 1728;
constexpr int KREV = 2304;

constexpr size_t WS_MOD  = 0;
constexpr size_t WS_XNT  = 8192;
constexpr size_t WS_XT   = WS_XNT + (size_t)D_ * L_ * 2;
constexpr size_t WS_KREV = WS_XT + (size_t)D_ * L_ * 2;
constexpr size_t WS_W1F  = WS_KREV + (size_t)D_ * 2 * KREV * 2;
constexpr size_t WS_W2F  = WS_W1F + (size_t)D_ * FF * 2;
constexpr size_t WS_X1T  = WS_W2F + (size_t)D_ * FF * 2;
constexpr size_t WS_X1   = WS_X1T + (size_t)D_ * L_ * 2;
constexpr size_t WS_H1F  = WS_X1 + (size_t)D_ * L_ * 2;

DEV float bf_lo(unsigned u) { return __uint_as_float(u << 16); }
DEV float bf_hi(unsigned u) { return __uint_as_float(u & 0xffff0000u); }
DEV unsigned pk_bf(float a, float b) {
  f32x2 v = {a, b};
  bf16x2 r = __builtin_convertvector(v, bf16x2);
  return __builtin_bit_cast(unsigned, r);
}
DEV float rbf(float a) { return bf_lo(pk_bf(a, 0.f)); }
DEV float ldbf(const unsigned short* p) { return __uint_as_float(((unsigned)*p) << 16); }

DEV u32x2 tr_read(unsigned lds_addr) {
  u32x2 r;
  asm volatile("ds_read_b64_tr_b16 %0, %1" : "=v"(r) : "v"(lds_addr) : "memory");
  return r;
}
DEV void lgkm0() { asm volatile("s_waitcnt lgkmcnt(0)" ::: "memory"); __builtin_amdgcn_sched_barrier(0); }

DEV f32x4 mfma_bf16(u32x4 a, u32x4 b, f32x4 c) {
  return __builtin_amdgcn_mfma_f32_16x16x32_bf16(__builtin_bit_cast(bf16x8, a), __builtin_bit_cast(bf16x8, b), c, 0, 0, 0);
}

constexpr int NB_MOD = 60, NB_LN = 128, NB_W = 135, NB_K = 640;

struct K1Args {
  const unsigned short *x, *are, *aim, *bre, *bim, *cre, *cim, *logdt, *w1, *w2, *c, *wmod;
  char* ws;
};

DEV void k1_mod(const K1Args& p, int b, char* smem) {
  const int tid = threadIdx.x;
  const int cc = tid & 7, rg = tid >> 3;
  float* sil = (float*)smem;
  float* red = (float*)(smem + 2560);
  u32x4 w[20];
#pragma unroll
  for (int it = 0; it < 20; ++it)
    w[it] = *(const u32x4*)(p.wmod + (size_t)(rg + 32 * it) * 3840 + 64 * b + 8 * cc);
  for (int i = tid; i < D_; i += 256) {
    float cv = ldbf(p.c + i);
    sil[i] = rbf(cv / (1.f + expf(-cv)));
  }
  __syncthreads();
  float acc[8];
#pragma unroll
  for (int j = 0; j < 8; ++j) acc[j] = 0.f;
#pragma unroll
  for (int it = 0; it < 20; ++it) {
    float s = sil[rg + 32 * it];
#pragma unroll
    for (int q = 0; q < 4; ++q) {
      acc[2 * q] += s * bf_lo(w[it][q]);
      acc[2 * q + 1] += s * bf_hi(w[it][q]);
    }
  }
#pragma unroll
  for (int j = 0; j < 8; ++j) red[rg * 64 + cc * 8 + j] = acc[j];
  __syncthreads();
  if (tid < 64) {
    float s = 0.f;
#pragma unroll
    for (int r = 0; r < 32; ++r) s += red[r * 64 + tid];
    ((unsigned short*)(p.ws + WS_MOD))[64 * b + tid] = (unsigned short)pk_bf(s, 0.f);
  }
}

DEV void k1_ln(const K1Args& p, int b, char* smem) {
  constexpr int STR = 1344;
  const int tid = threadIdx.x;
  const int tok = tid >> 4, sub = tid & 15;
  const int t0 = b * 16;
  u32x4 xv[5];
#pragma unroll
  for (int m = 0; m < 5; ++m)
    xv[m] = *(const u32x4*)(p.x + (size_t)(t0 + tok) * D_ + 8 * (sub + 16 * m));
  float s = 0.f;
#pragma unroll
  for (int m = 0; m < 5; ++m)
#pragma unroll
    for (int q = 0; q < 4; ++q) s += bf_lo(xv[m][q]) + bf_hi(xv[m][q]);
#pragma unroll
  for (int o = 1; o < 16; o <<= 1) s += __shfl_xor(s, o, 64);
  const float mean = s * (1.f / D_);
  float v = 0.f;
#pragma unroll
  for (int m = 0; m < 5; ++m)
#pragma unroll
    for (int q = 0; q < 4; ++q) {
      float a = bf_lo(xv[m][q]) - mean, c = bf_hi(xv[m][q]) - mean;
      v += a * a + c * c;
    }
#pragma unroll
  for (int o = 1; o < 16; o <<= 1) v += __shfl_xor(v, o, 64);
  const float mb = rbf(mean);
  const float sd = rbf(sqrtf(rbf(rbf(v * (1.f / D_)) + 1e-5f)));
  u32x4 xn[5];
#pragma unroll
  for (int m = 0; m < 5; ++m)
#pragma unroll
    for (int q = 0; q < 4; ++q) {
      unsigned d2 = pk_bf(bf_lo(xv[m][q]) - mb, bf_hi(xv[m][q]) - mb);
      xn[m][q] = pk_bf(bf_lo(d2) / sd, bf_hi(d2) / sd);
    }
  const int lane = tid & 63, w = tid >> 6;
  const int g = lane >> 4, i = lane & 15;
  const unsigned sbase = (unsigned)(size_t)smem;
#pragma unroll
  for (int pass = 0; pass < 2; ++pass) {
    if (pass) __syncthreads();
#pragma unroll
    for (int m = 0; m < 5; ++m)
      *(u32x4*)(smem + tok * STR + (sub + 16 * m) * 16) = pass ? xv[m] : xn[m];
    __syncthreads();
    unsigned short* dst = (unsigned short*)(p.ws + (pass ? WS_XT : WS_XNT));
    u32x2 ra[5], rb[5];
#pragma unroll
    for (int it = 0; it < 5; ++it) {
      const int cb = 2 * (w * 5 + it) + (g & 1);
      const int row = 8 * (g >> 1) + (i >> 2);
      const unsigned a = sbase + row * STR + cb * 32 + (i & 3) * 8;
      ra[it] = tr_read(a);
      rb[it] = tr_read(a + 4 * STR);
    }
    lgkm0();
#pragma unroll
    for (int it = 0; it < 5; ++it) {
      const int cb = 2 * (w * 5 + it) + (g & 1);
      u32x4 o = {ra[it][0], ra[it][1], rb[it][0], rb[it][1]};
      *(u32x4*)(dst + (size_t)(cb * 16 + i) * L_ + t0 + 8 * (g >> 1)) = o;
    }
  }
}

DEV void k1_retile(const unsigned short* W, unsigned short* out, int K, int N, int id) {
  const int nocts = N / 8;
  const int no = id % nocts, ko = id / nocts;
  u32x4 r[8];
#pragma unroll
  for (int j = 0; j < 8; ++j) r[j] = *(const u32x4*)(W + (size_t)(8 * ko + j) * N + 8 * no);
  const int ct = no >> 1, kk = ko >> 2, g = ko & 3;
  unsigned short* o = out + ((size_t)(ct * (K / 32) + kk) * 64 + 16 * g + 8 * (no & 1)) * 8;
#pragma unroll
  for (int q = 0; q < 4; ++q) {
    u32x4 e, f;
#pragma unroll
    for (int pp = 0; pp < 4; ++pp) {
      e[pp] = __builtin_amdgcn_perm(r[2 * pp + 1][q], r[2 * pp][q], 0x05040100u);
      f[pp] = __builtin_amdgcn_perm(r[2 * pp + 1][q], r[2 * pp][q], 0x07060302u);
    }
    *(u32x4*)(o + (2 * q) * 8) = e;
    *(u32x4*)(o + (2 * q + 1) * 8) = f;
  }
}

DEV void cexp_(float a2, float br, float m, float& re, float& im) {
  float mag = __builtin_amdgcn_exp2f(a2 * m);
  float ph = __builtin_amdgcn_fractf(br * m);
  re = mag * __builtin_amdgcn_cosf(ph);
  im = mag * __builtin_amdgcn_sinf(ph);
}

DEV void k1_kgen(const K1Args& p, int d, char* smem) {
  const int tid = threadIdx.x;
  const int lane = tid & 63, w = tid >> 6;
  const int g = lane >> 4, x = lane & 15;
  const size_t po = (size_t)d * NS + 32 * w + 8 * g;
  const u32x4 var = *(const u32x4*)(p.are + po), vai = *(const u32x4*)(p.aim + po);
  const u32x4 vbr = *(const u32x4*)(p.bre + po), vbi = *(const u32x4*)(p.bim + po);
  const u32x4 vcr = *(const u32x4*)(p.cre + po), vci = *(const u32x4*)(p.cim + po);
  const float dt = rbf(expf(ldbf(p.logdt + d)));
  f32x4 acc[4];
#pragma unroll
  for (int s = 0; s < 4; ++s) acc[s] = (f32x4){0.f, 0.f, 0.f, 0.f};
  const float fx = (float)x;
#pragma unroll
  for (int tau = 0; tau < 8; ++tau) {
    const int q = tau >> 1;
    const bool hi = tau & 1;
    const float ar = hi ? bf_hi(var[q]) : bf_lo(var[q]), ai = hi ? bf_hi(vai[q]) : bf_lo(vai[q]);
    const float br_ = hi ? bf_hi(vbr[q]) : bf_lo(vbr[q]), bi_ = hi ? bf_hi(vbi[q]) : bf_lo(vbi[q]);
    const float cr = hi ? bf_hi(vcr[q]) : bf_lo(vcr[q]), ci = hi ? bf_hi(vci[q]) : bf_lo(vci[q]);
    const float a2 = (dt * ar) * 1.44269504088896341f;
    const float bv = (dt * ai) * 0.15915494309189534f;
    const float dbr = dt * br_, dbi = dt * bi_;
    const float wr = cr * dbr - ci * dbi, wi = cr * dbi + ci * dbr;
    float vr, vi, gr, gi, hr, hi_;
    cexp_(a2, bv, fx, vr, vi);
    cexp_(a2, bv, 16.f * fx, gr, gi);
    cexp_(a2, bv, 16.f * (fx + 8.f), hr, hi_);
    {
      float ur = wr * gr - wi * gi, ui = wr * gi + wi * gr;
      acc[0] = __builtin_amdgcn_mfma_f32_16x16x4f32(ur, vr, acc[0], 0, 0, 0);
      acc[0] = __builtin_amdgcn_mfma_f32_16x16x4f32(-ui, vi, acc[0], 0, 0, 0);
    }
#pragma unroll
    for (int s = 1; s < 4; ++s) {
      float t1 = vr * vr - vi * vi, t2 = 2.f * vr * vi;
      vr = t1; vi = t2;
      float t3 = hr * hr - hi_ * hi_, t4 = 2.f * hr * hi_;
      hr = t3; hi_ = t4;
      float ur = wr * hr - wi * hi_, ui = wr * hi_ + wi * hr;
      acc[s] = __builtin_amdgcn_mfma_f32_16x16x4f32(ur, vr, acc[s], 0, 0, 0);
      acc[s] = __builtin_amdgcn_mfma_f32_16x16x4f32(-ui, vi, acc[s], 0, 0, 0);
    }
  }
  float* kred = (float*)smem;
  float* kc = (float*)(smem + 16384);
#pragma unroll
  for (int s = 0; s < 4; ++s)
#pragma unroll
    for (int r = 0; r < 4; ++r) kred[(w * 4 + s) * 256 + (4 * g + r) * 16 + x] = acc[s][r];
  __syncthreads();
  {
    float s0 = kred[tid] + kred[1024 + tid] + kred[2048 + tid] + kred[3072 + tid];
    kc[tid] = s0;
#pragma unroll
    for (int it = 0; it < 2; ++it) {
      int e = tid + 256 * it;
      if (e < 385) {
        int s = e < 384 ? 1 + (e >> 7) : 3;
        int m = e < 384 ? (e & 127) : 128;
        int o = s * 256 + m;
        kc[256 + e] = kred[o] + kred[1024 + o] + kred[2048 + o] + kred[3072 + o];
      }
    }
  }
  __syncthreads();
  unsigned short* kout = (unsigned short*)(p.ws + WS_KREV) + (size_t)d * 2 * KREV;
  {
    u32x4 hi, lo;
#pragma unroll
    for (int q = 0; q < 4; ++q) {
      float v[2];
#pragma unroll
      for (int e = 0; e < 2; ++e) {
        int l = 2047 - (8 * tid + 2 * q + e);
        int idx = l < 128 ? l : (int)(pk_bf((float)l, 0.f) & 0xffffu) - 17024;
        v[e] = kc[idx];
      }
      unsigned h = pk_bf(v[0], v[1]);
      hi[q] = h;
      lo[q] = pk_bf(v[0] - bf_lo(h), v[1] - bf_hi(h));
    }
    *(u32x4*)(kout + 8 * tid) = hi;
    *(u32x4*)(kout + KREV + 8 * tid) = lo;
    if (tid < 64) {
      u32x4 z = {0u, 0u, 0u, 0u};
      *(u32x4*)(kout + (tid >> 5) * KREV + 2048 + 8 * (tid & 31)) = z;
    }
  }
}

__global__ __launch_bounds__(256) void k1(K1Args p) {
  __shared__ __attribute__((aligned(16))) char smem[21504];
  int b = blockIdx.x;
  if (b < NB_MOD) { k1_mod(p, b, smem); return; }
  b -= NB_MOD;
  if (b < NB_LN) { k1_ln(p, b, smem); return; }
  b -= NB_LN;
  if (b < NB_W) {
    int id = b * 256 + threadIdx.x;
    if (id < 17280) k1_retile(p.w1, (unsigned short*)(p.ws + WS_W1F), D_, FF, id);
    else k1_retile(p.w2, (unsigned short*)(p.ws + WS_W2F), FF, D_, id - 17280);
    return;
  }
  b -= NB_W;
  k1_kgen(p, b, smem);
}

__global__ __launch_bounds__(256) void k2(const unsigned short* __restrict__ dskip, char* __restrict__ ws) {
  constexpr int CS = 4160;
  constexpr int KOFF = 4 * CS;
  __shared__ __attribute__((aligned(16))) char smem[32768];
  const int d = blockIdx.x, tid = threadIdx.x;
  const int lane = tid & 63, w = tid >> 6;
  const unsigned short* mod = (const unsigned short*)(ws + WS_MOD);
  const unsigned short* xnt = (const unsigned short*)(ws + WS_XNT) + (size_t)d * L_;
  const unsigned short* xt = (const unsigned short*)(ws + WS_XT) + (size_t)d * L_;
  const unsigned short* krev = (const unsigned short*)(ws + WS_KREV) + (size_t)d * 2 * KREV;

  u32x4 xa = *(const u32x4*)(xnt + 8 * tid);
  u32x2 xb = {0u, 0u};
  if (tid < 255) xb = *(const u32x2*)(xnt + 8 * tid + 8);
  u32x4 kv[3];
#pragma unroll
  for (int it = 0; it < 3; ++it) {
    int c = tid + 256 * it;
    if (c < 576) kv[it] = *(const u32x4*)(krev + 8 * c);
  }
  const u32x4 xres = *(const u32x4*)(xt + 8 * tid);
  const float sa = ldbf(mod + d), ca1 = rbf(1.f + ldbf(mod + D_ + d)), ga = ldbf(mod + 2 * D_ + d);
  const float dsk = ldbf(dskip + d);

  unsigned hw[6];
#pragma unroll
  for (int q = 0; q < 6; ++q) {
    unsigned u = q < 4 ? xa[q] : xb[q - 4];
    unsigned m = pk_bf(bf_lo(u) * ca1, bf_hi(u) * ca1);
    hw[q] = pk_bf(bf_lo(m) + sa, bf_hi(m) + sa);
  }
  if (tid == 255) { hw[4] = 0u; hw[5] = 0u; }
  {
    char* dst = smem + (tid + 2) * 16;
    u32x4 c0 = {hw[0], hw[1], hw[2], hw[3]};
    u32x4 c2 = {hw[1], hw[2], hw[3], hw[4]};
    unsigned s1[5];
#pragma unroll
    for (int q = 0; q < 5; ++q) s1[q] = __builtin_amdgcn_alignbit(hw[q + 1], hw[q], 16);
    u32x4 c1 = {s1[0], s1[1], s1[2], s1[3]};
    u32x4 c3 = {s1[1], s1[2], s1[3], s1[4]};
    *(u32x4*)(dst) = c0;
    *(u32x4*)(dst + CS) = c1;
    *(u32x4*)(dst + 2 * CS) = c2;
    *(u32x4*)(dst + 3 * CS) = c3;
    if (tid == 0) {
      u32x4 z = {0u, 0u, 0u, 0u};
      *(u32x4*)(smem) = z; *(u32x4*)(smem + CS) = z; *(u32x4*)(smem + 2 * CS) = z; *(u32x4*)(smem + 3 * CS) = z;
      *(u32x4*)(smem + 16) = z;
      u32x4 e1 = {0u, 0u, 0u, hw[0] << 16};
      u32x4 e2 = {0u, 0u, 0u, hw[0]};
      u32x4 e3 = {0u, 0u, hw[0] << 16, s1[0]};
      *(u32x4*)(smem + CS + 16) = e1;
      *(u32x4*)(smem + 2 * CS + 16) = e2;
      *(u32x4*)(smem + 3 * CS + 16) = e3;
    }
  }
#pragma unroll
  for (int it = 0; it < 3; ++it) {
    int c = tid + 256 * it;
    if (c < 576) *(u32x4*)(smem + KOFF + 16 * c) = kv[it];
  }
  __syncthreads();

  const int r = lane & 15, g = lane >> 4;
  f32x4 acc[8];
#pragma unroll
  for (int q = 0; q < 8; ++q) acc[q] = (f32x4){0.f, 0.f, 0.f, 0.f};
  {
    const int s = (r + 1) & 3;
#pragma unroll
    for (int cc = 0; cc < 2; ++cc) {
      const int c0 = w + 4 * cc;
      const char* ap = smem + s * CS + 2 * (32 * c0 + 8 * g + (r + 1) - s);
      const char* bp = smem + KOFF + 2 * (2032 + 32 * c0 - 16 * r + 8 * g);
      u32x4 A[8], Bh[8], Bl[8];
#pragma unroll
      for (int u = 0; u < 8; ++u) {
        u32x2 a0 = *(const u32x2*)(ap + 512 * u), a1 = *(const u32x2*)(ap + 512 * u + 8);
        A[u] = (u32x4){a0[0], a0[1], a1[0], a1[1]};
        Bh[u] = *(const u32x4*)(bp - 512 * u);
        Bl[u] = *(const u32x4*)(bp - 512 * u + 2 * KREV);
      }
#pragma unroll
      for (int q = 0; q < 8; ++q)
#pragma unroll
        for (int sp = 0; sp <= q; ++sp) {
          acc[q] = mfma_bf16(A[sp], Bh[q - sp], acc[q]);
          acc[q] = mfma_bf16(A[sp], Bl[q - sp], acc[q]);
        }
    }
  }
  __syncthreads();
  float* red = (float*)smem;
#pragma unroll
  for (int q = 0; q < 8; ++q) *(f32x4*)(red + w * 2048 + 256 * q + 16 * r + 4 * g) = acc[q];
  __syncthreads();
  {
    f32x4 y0 = *(const f32x4*)(red + 8 * tid), y1 = *(const f32x4*)(red + 8 * tid + 4);
#pragma unroll
    for (int ww = 1; ww < 4; ++ww) {
      y0 += *(const f32x4*)(red + ww * 2048 + 8 * tid);
      y1 += *(const f32x4*)(red + ww * 2048 + 8 * tid + 4);
    }
    float y[8] = {y0[0], y0[1], y0[2], y0[3], y1[0], y1[1], y1[2], y1[3]};
    u32x4 o;
#pragma unroll
    for (int q = 0; q < 4; ++q) {
      unsigned yb = pk_bf(y[2 * q], y[2 * q + 1]);
      unsigned hd = pk_bf(bf_lo(hw[q]) * dsk, bf_hi(hw[q]) * dsk);
      unsigned y2 = pk_bf(bf_lo(yb) + bf_lo(hd), bf_hi(yb) + bf_hi(hd));
      unsigned gy = pk_bf(bf_lo(y2) * ga, bf_hi(y2) * ga);
      o[q] = pk_bf(bf_lo(xres[q]) + bf_lo(gy), bf_hi(xres[q]) + bf_hi(gy));
    }
    unsigned short* x1t = (unsigned short*)(ws + WS_X1T);
    *(u32x4*)(x1t + ((size_t)(tid >> 2) * D_ + d) * 32 + (tid & 3) * 8) = o;
  }
}

__global__ __launch_bounds__(512) void k3(char* __restrict__ ws) {
  __shared__ __attribute__((aligned(16))) char smem[40960 + 40960 + 2048];
  char* xs = smem;
  char* hf = smem + 40960;
  float* ps = (float*)(smem + 81920);
  const int tid = threadIdx.x, lane = tid & 63, w = tid >> 6;
  const int bx = blockIdx.x;
  const int xcd = bx & 7;
  const int ng = xcd >> 1, mt = (xcd & 1) * 32 + (bx >> 3);
  const int g = lane >> 4, i = lane & 15;

  const int nct = w < 3 ? 4 : 3;
  const int ct0 = ng * 27 + (w < 3 ? 4 * w : 12 + 3 * (w - 3));
  const u32x4* w1f = (const u32x4*)(ws + WS_W1F) + (size_t)ct0 * 20 * 64 + lane;

  {
    const u32x4* src = (const u32x4*)(ws + WS_X1T + (size_t)mt * 40960);
    u32x4 t[5];
#pragma unroll
    for (int it = 0; it < 5; ++it) t[it] = src[tid + 512 * it];
#pragma unroll
    for (int it = 0; it < 5; ++it) *(u32x4*)(xs + (tid + 512 * it) * 16) = t[it];
  }
  constexpr int PF = 4;
  u32x4 wf[PF][4];
#pragma unroll
  for (int k = 0; k < PF; ++k)
#pragma unroll
    for (int c = 0; c < 4; ++c)
      if (c < nct) wf[k][c] = w1f[(c * 20 + k) * 64];

  const unsigned short* mod = (const unsigned short*)(ws + WS_MOD);
  const int tt = g & 1;
  u32x4 cbv[5], sbv[5];
#pragma unroll
  for (int it = 0; it < 5; ++it) {
    const int d0 = 80 * w + 16 * it + 8 * (g >> 1);
    sbv[it] = *(const u32x4*)(mod + 3 * D_ + d0);
    cbv[it] = *(const u32x4*)(mod + 4 * D_ + d0);
  }
  __syncthreads();
  u32x4 xv[5];
  {
    const unsigned sb = (unsigned)(size_t)xs;
    u32x2 ra[5], rb[5];
#pragma unroll
    for (int it = 0; it < 5; ++it) {
      const int d0 = 80 * w + 16 * it + 8 * (g >> 1);
      const unsigned a = sb + (d0 + (i >> 2)) * 64 + tt * 32 + (i & 3) * 8;
      ra[it] = tr_read(a);
      rb[it] = tr_read(a + 256);
    }
    lgkm0();
#pragma unroll
    for (int it = 0; it < 5; ++it) xv[it] = (u32x4){ra[it][0], ra[it][1], rb[it][0], rb[it][1]};
  }
  float s = 0.f;
#pragma unroll
  for (int it = 0; it < 5; ++it)
#pragma unroll
    for (int q = 0; q < 4; ++q) s += bf_lo(xv[it][q]) + bf_hi(xv[it][q]);
  s += __shfl_xor(s, 32, 64);
  if (lane < 32) ps[w * 32 + lane] = s;
  __syncthreads();
  const int tl = lane & 31;
  float mean = 0.f;
#pragma unroll
  for (int ww = 0; ww < 8; ++ww) mean += ps[ww * 32 + tl];
  mean *= (1.f / D_);
  float v = 0.f;
#pragma unroll
  for (int it = 0; it < 5; ++it)
#pragma unroll
    for (int q = 0; q < 4; ++q) {
      float a = bf_lo(xv[it][q]) - mean, c = bf_hi(xv[it][q]) - mean;
      v += a * a + c * c;
    }
  v += __shfl_xor(v, 32, 64);
  if (lane < 32) ps[256 + w * 32 + lane] = v;
  __syncthreads();
  float var = 0.f;
#pragma unroll
  for (int ww = 0; ww < 8; ++ww) var += ps[256 + ww * 32 + tl];
  const float mb = rbf(mean);
  const float sd = rbf(sqrtf(rbf(rbf(var * (1.f / D_)) + 1e-5f)));
  const float rs = 1.f / sd;
  unsigned short* x1 = (unsigned short*)(ws + WS_X1);
#pragma unroll
  for (int it = 0; it < 5; ++it) {
    const int d0 = 80 * w + 16 * it + 8 * (g >> 1);
    u32x4 o;
#pragma unroll
    for (int q = 0; q < 4; ++q) {
      unsigned d2 = pk_bf(bf_lo(xv[it][q]) - mb, bf_hi(xv[it][q]) - mb);
      unsigned xn = pk_bf(bf_lo(d2) * rs, bf_hi(d2) * rs);
      unsigned c1 = pk_bf(1.f + bf_lo(cbv[it][q]), 1.f + bf_hi(cbv[it][q]));
      unsigned m = pk_bf(bf_lo(xn) * bf_lo(c1), bf_hi(xn) * bf_hi(c1));
      o[q] = pk_bf(bf_lo(m) + bf_lo(sbv[it][q]), bf_hi(m) + bf_hi(sbv[it][q]));
    }
    *(u32x4*)(hf + ((tt * 20 + (d0 >> 5)) * 64 + i + 16 * ((d0 & 31) >> 3)) * 16) = o;
    if (ng == 0) *(u32x4*)(x1 + (size_t)(mt * 32 + tl) * D_ + d0) = xv[it];
  }
  __syncthreads();

  f32x4 acc[4][2];
#pragma unroll
  for (int c = 0; c < 4; ++c) { acc[c][0] = (f32x4){0.f, 0.f, 0.f, 0.f}; acc[c][1] = acc[c][0]; }
#pragma unroll
  for (int kk = 0; kk < 20; ++kk) {
    u32x4 b0 = *(const u32x4*)(hf + (kk * 64 + lane) * 16);
    u32x4 b1 = *(const u32x4*)(hf + ((20 + kk) * 64 + lane) * 16);
    u32x4 cur[4];
#pragma unroll
    for (int c = 0; c < 4; ++c) cur[c] = wf[kk % PF][c];
    if (kk + PF < 20) {
#pragma unroll
      for (int c = 0; c < 4; ++c)
        if (c < nct) wf[kk % PF][c] = w1f[(c * 20 + kk + PF) * 64];
    }
#pragma unroll
    for (int c = 0; c < 4; ++c)
      if (c < nct) {
        acc[c][0] = mfma_bf16(cur[c], b0, acc[c][0]);
        acc[c][1] = mfma_bf16(cur[c], b1, acc[c][1]);
      }
  }
  unsigned short* h1f = (unsigned short*)(ws + WS_H1F);
#pragma unroll
  for (int c = 0; c < 4; ++c)
    if (c < nct) {
      const int ct = ct0 + c;
#pragma unroll
      for (int t2 = 0; t2 < 2; ++t2) {
        u32x2 o;
        o[0] = pk_bf(fmaxf(acc[c][t2][0], 0.f), fmaxf(acc[c][t2][1], 0.f));
        o[1] = pk_bf(fmaxf(acc[c][t2][2], 0.f), fmaxf(acc[c][t2][3], 0.f));
        const size_t off = (((size_t)(2 * mt + t2) * 54 + (ct >> 1)) * 64 + i + 16 * (2 * (ct & 1) + (g >> 1))) * 8 + 4 * (g & 1);
        *(u32x2*)(h1f + off) = o;
      }
    }
}

__global__ __launch_bounds__(256) void k4(char* __restrict__ ws, unsigned short* __restrict__ out) {
  constexpr int RS = 84;
  __shared__ __attribute__((aligned(16))) float red[4 * 64 * RS];
  const int tid = threadIdx.x, lane = tid & 63, w = tid >> 6;
  const int bx = blockIdx.x;
  const int xcd = bx & 7, idx = bx >> 3;
  const int mt = xcd * 4 + (idx >> 3), nt = idx & 7;
  const int g = lane >> 4, i = lane & 15;
  const u32x4* w2f = (const u32x4*)(ws + WS_W2F) + (size_t)nt * 5 * 54 * 64 + lane;
  const u32x4* h1f = (const u32x4*)(ws + WS_H1F) + (size_t)mt * 4 * 54 * 64 + lane;

  constexpr int PF = 4;
  constexpr int NSTEP = 14;
  u32x4 wa[PF][5], hb[PF][4];
#pragma unroll
  for (int k = 0; k < PF; ++k) {
    const int kk = w + 4 * k;
#pragma unroll
    for (int c = 0; c < 5; ++c) wa[k][c] = w2f[(c * 54 + kk) * 64];
#pragma unroll
    for (int t = 0; t < 4; ++t) hb[k][t] = h1f[(t * 54 + kk) * 64];
  }
  f32x4 acc[5][4];
#pragma unroll
  for (int c = 0; c < 5; ++c)
#pragma unroll
    for (int t = 0; t < 4; ++t) acc[c][t] = (f32x4){0.f, 0.f, 0.f, 0.f};
#pragma unroll
  for (int st = 0; st < NSTEP; ++st) {
    const int kk = w + 4 * st;
    if (st == NSTEP - 1 && kk >= 54) break;
    u32x4 ca[5], cb[4];
#pragma unroll
    for (int c = 0; c < 5; ++c) ca[c] = wa[st % PF][c];
#pragma unroll
    for (int t = 0; t < 4; ++t) cb[t] = hb[st % PF][t];
    if (st + PF < NSTEP) {
      const int kn = kk + 4 * PF;
      if (kn < 54) {
#pragma unroll
        for (int c = 0; c < 5; ++c) wa[st % PF][c] = w2f[(c * 54 + kn) * 64];
#pragma unroll
        for (int t = 0; t < 4; ++t) hb[st % PF][t] = h1f[(t * 54 + kn) * 64];
      }
    }
#pragma unroll
    for (int c = 0; c < 5; ++c)
#pragma unroll
      for (int t = 0; t < 4; ++t) acc[c][t] = mfma_bf16(ca[c], cb[t], acc[c][t]);
  }
#pragma unroll
  for (int c = 0; c < 5; ++c)
#pragma unroll
    for (int t = 0; t < 4; ++t)
      *(f32x4*)(red + (w * 64 + 16 * t + i) * RS + 16 * c + 4 * g) = acc[c][t];
  __syncthreads();
  const unsigned short* mod = (const unsigned short*)(ws + WS_MOD);
  const unsigned short* x1 = (const unsigned short*)(ws + WS_X1);
#pragma unroll
  for (int it = 0; it < 5; ++it) {
    const int e = tid + 256 * it;
    const int t = e / 20, q4 = e % 20;
    f32x4 s = *(const f32x4*)(red + t * RS + 4 * q4);
#pragma unroll
    for (int ww = 1; ww < 4; ++ww) s += *(const f32x4*)(red + (ww * 64 + t) * RS + 4 * q4);
    const int dcol = nt * 80 + 4 * q4;
    const size_t go = (size_t)(mt * 64 + t) * D_ + dcol;
    const u32x2 xr = *(const u32x2*)(x1 + go);
    const u32x2 gb = *(const u32x2*)(mod + 5 * D_ + dcol);
    u32x2 o;
#pragma unroll
    for (int q = 0; q < 2; ++q) {
      unsigned f = pk_bf(s[2 * q], s[2 * q + 1]);
      unsigned gf = pk_bf(bf_lo(f) * bf_lo(gb[q]), bf_hi(f) * bf_hi(gb[q]));
      o[q] = pk_bf(bf_lo(xr[q]) + bf_lo(gf), bf_hi(xr[q]) + bf_hi(gf));
    }
    *(u32x2*)(out + go) = o;
  }
}

extern "C" void kernel_launch(void* const* d_in, const int* in_sizes, int n_in,
                              void* d_out, int out_size, void* d_ws, size_t ws_size,
                              hipStream_t stream) {
  K1Args a;
  a.x = (const unsigned short*)d_in[0];
  a.are = (const unsigned short*)d_in[1];
  a.aim = (const unsigned short*)d_in[2];
  a.bre = (const unsigned short*)d_in[3];
  a.bim = (const unsigned short*)d_in[4];
  a.cre = (const unsigned short*)d_in[5];
  a.cim = (const unsigned short*)d_in[6];
  a.logdt = (const unsigned short*)d_in[7];
  a.w1 = (const unsigned short*)d_in[10];
  a.w2 = (const unsigned short*)d_in[11];
  a.c = (const unsigned short*)d_in[12];
  a.wmod = (const unsigned short*)d_in[13];
  a.ws = (char*)d_ws;
  k1<<<NB_MOD + NB_LN + NB_W + NB_K, 256, 0, stream>>>(a);
  k2<<<D_, 256, 0, stream>>>((const unsigned short*)d_in[8], (char*)d_ws);
  k3<<<256, 512, 0, stream>>>((char*)d_ws);
  k4<<<256, 256, 0, stream>>>((char*)d_ws, (unsigned short*)d_out);
}
```

```cpp
#include <hip/hip_runtime.h>
#include <hip/hip_bf16.h>
#include <stdint.h>
#include <type_traits>

typedef __attribute__((ext_vector_type(8))) __bf16 bf16x8;
typedef __attribute__((ext_vector_type(2))) __bf16 bf16x2;
typedef __attribute__((ext_vector_type(4))) float f32x4;
typedef __attribute__((ext_vector_type(2))) float f32x2;
typedef __attribute__((ext_vector_type(4))) unsigned u32x4;
typedef __attribute__((ext_vector_type(2))) unsigned u32x2;
typedef __attribute__((ext_vector_type(8))) _Float16 h16x8;
typedef __attribute__((ext_vector_type(2))) _Float16 h16x2;
typedef __attribute__((ext_vector_type(4))) short s16x4;
typedef __attribute__((address_space(3))) s16x4 lds_s16x4;

#if defined(__HIP_DEVICE_COMPILE__)
#define NOPK __attribute__((target("no-packed-fp32-ops")))
#else
#define NOPK
#endif
#define DEV __device__ __forceinline__

constexpr int L_ = 2048;
constexpr int D_ = 640;
constexpr int NS = 128;
constexpr int FF = 1728;
constexpr int KREV = 2304;

constexpr size_t WS_MOD  = 0;
constexpr size_t WS_KSC  = 8192;
constexpr size_t WS_CNT  = 16384;
constexpr size_t WS_CSB  = 32768;
constexpr size_t WS_XNT  = 65536;
constexpr size_t WS_XT   = WS_XNT + (size_t)D_ * L_ * 2;
constexpr size_t WS_KREV = WS_XT + (size_t)D_ * L_ * 2;
constexpr size_t WS_W1F  = WS_KREV + (size_t)D_ * 2 * KREV * 2;
constexpr size_t WS_W2F  = WS_W1F + (size_t)D_ * FF * 2;
constexpr size_t WS_X1T  = WS_W2F + (size_t)D_ * FF * 2;
constexpr size_t WS_X1   = WS_X1T + (size_t)D_ * L_ * 2;
constexpr size_t WS_H1F  = WS_X1 + (size_t)D_ * L_ * 2;

#define STAMP(kid, k) do {} while (0)

DEV float bf_lo(unsigned u) { return __uint_as_float(u << 16); }
DEV float bf_hi(unsigned u) { return __uint_as_float(u & 0xffff0000u); }
DEV unsigned pk_bf(float a, float b) {
  f32x2 v = {a, b};
  bf16x2 r = __builtin_convertvector(v, bf16x2);
  return __builtin_bit_cast(unsigned, r);
}
DEV float rbf(float a) { return bf_lo(pk_bf(a, 0.f)); }
DEV float ldbf(const unsigned short* p) { return __uint_as_float(((unsigned)*p) << 16); }

typedef __amdgpu_buffer_rsrc_t rsrc_t;
DEV rsrc_t ws_rsrc(char* ws) { return __builtin_amdgcn_make_buffer_rsrc((void*)ws, 0, 0x7fffffff, 0x00020000); }
#define ST_AUX 16
DEV void st16(rsrc_t r, size_t off, u32x4 v) { __builtin_amdgcn_raw_buffer_store_b128(v, r, (int)off, 0, ST_AUX); }
#define LN_AUX 16
DEV void st16ln(rsrc_t r, size_t off, u32x4 v) { __builtin_amdgcn_raw_buffer_store_b128(v, r, (int)off, 0, LN_AUX); }
DEV void st8(rsrc_t r, size_t off, u32x2 v) { __builtin_amdgcn_raw_buffer_store_b64(v, r, (int)off, 0, 16); }

DEV u32x2 tr_read(unsigned lds_addr) {
  u32x2 r;
  asm volatile("ds_read_b64_tr_b16 %0, %1" : "=v"(r) : "v"(lds_addr) : "memory");
  return r;
}
DEV void lgkm0() { asm volatile("s_waitcnt lgkmcnt(0)" ::: "memory"); __builtin_amdgcn_sched_barrier(0); }

DEV f32x4 mfma_bf16(u32x4 a, u32x4 b, f32x4 c) {
  return __builtin_amdgcn_mfma_f32_16x16x32_bf16(__builtin_bit_cast(bf16x8, a), __builtin_bit_cast(bf16x8, b), c, 0, 0, 0);
}

constexpr int NB_MOD = 240, NB_LN = 64, NB_W = 136 + 54, NB_K = 640;

struct K1Args {
  const unsigned short *x, *are, *aim, *bre, *bim, *cre, *cim, *logdt, *w1, *w2, *c, *wmod;
  char* ws;
};

DEV void k1_mod(const K1Args& p, int b, char* smem) {
  const int tid = threadIdx.x;
  const int hc = tid & 1, rg = tid >> 1;
  const int cg = (b & 7) * 30 + (b >> 3);
  float* sil = (float*)smem;
  float* red = (float*)(smem + 2560);
  u32x4 w[5];
#pragma unroll
  for (int it = 0; it < 5; ++it)
    w[it] = *(const u32x4*)(p.wmod + (size_t)(rg + 128 * it) * 3840 + 16 * cg + 8 * hc);
  for (int i = tid; i < D_; i += 256) {
    float cv = ldbf(p.c + i);
    sil[i] = rbf(cv / (1.f + expf(-cv)));
  }
  __syncthreads();
  float acc[8];
#pragma unroll
  for (int j = 0; j < 8; ++j) acc[j] = 0.f;
#pragma unroll
  for (int it = 0; it < 5; ++it) {
    float s = sil[rg + 128 * it];
#pragma unroll
    for (int q = 0; q < 4; ++q) {
      acc[2 * q] += s * bf_lo(w[it][q]);
      acc[2 * q + 1] += s * bf_hi(w[it][q]);
    }
  }
#pragma unroll
  for (int o = 2; o < 64; o <<= 1)
#pragma unroll
    for (int j = 0; j < 8; ++j) acc[j] += __shfl_xor(acc[j], o, 64);
  if ((tid & 63) < 2) {
#pragma unroll
    for (int j = 0; j < 8; ++j) red[(tid >> 6) * 16 + hc * 8 + j] = acc[j];
  }
  __syncthreads();
  if (tid == 64 && b < 32) ((unsigned*)(p.ws + WS_CNT))[b * 64] = 0u;
  if (tid < 16) {
    float s = red[tid] + red[16 + tid] + red[32 + tid] + red[48 + tid];
    ((unsigned short*)(p.ws + WS_MOD))[16 * cg + tid] = (unsigned short)pk_bf(s, 0.f);
  }
}

DEV void k1_ln(const K1Args& p, int b, char* smem) {
  constexpr int STR = 704;
  const int tid = threadIdx.x;
  const int tok = tid >> 3, sub = tid & 7;
  const int t0 = b * 32;
  u32x4 xv[10];
#pragma unroll
  for (int m = 0; m < 10; ++m)
    xv[m] = *(const u32x4*)(p.x + (size_t)(t0 + tok) * D_ + 8 * (sub + 8 * m));
  float s = 0.f;
#pragma unroll
  for (int m = 0; m < 10; ++m)
#pragma unroll
    for (int q = 0; q < 4; ++q) s += bf_lo(xv[m][q]) + bf_hi(xv[m][q]);
#pragma unroll
  for (int o = 1; o < 8; o <<= 1) s += __shfl_xor(s, o, 64);
  const float mean = s * (1.f / D_);
  float v = 0.f;
#pragma unroll
  for (int m = 0; m < 10; ++m)
#pragma unroll
    for (int q = 0; q < 4; ++q) {
      float a = bf_lo(xv[m][q]) - mean, c = bf_hi(xv[m][q]) - mean;
      v += a * a + c * c;
    }
#pragma unroll
  for (int o = 1; o < 8; o <<= 1) v += __shfl_xor(v, o, 64);
  const float mb = rbf(mean);
  const float sd = rbf(sqrtf(rbf(rbf(v * (1.f / D_)) + 1e-5f)));
  const float rs = 1.f / sd;
  const int lane = tid & 63, w = tid >> 6;
  const int g = lane >> 4, i = lane & 15;
  const rsrc_t rsrc = ws_rsrc(p.ws);
#pragma unroll
  for (int pass = 0; pass < 4; ++pass) {
    const int hf = pass & 1;
    if (pass) __syncthreads();
#pragma unroll
    for (int m = 0; m < 5; ++m) {
      u32x4 o = xv[5 * hf + m];
      if (pass >= 2) {
#pragma unroll
        for (int q = 0; q < 4; ++q) {
          unsigned d2 = pk_bf(bf_lo(o[q]) - mb, bf_hi(o[q]) - mb);
          o[q] = pk_bf(bf_lo(d2) * rs, bf_hi(d2) * rs);
        }
      }
      *(u32x4*)(smem + tok * STR + (sub + 8 * m) * 16) = o;
    }
    __syncthreads();
    const size_t dsto = (pass >= 2 ? WS_XNT : WS_XT) + ((size_t)320 * hf * L_ + t0) * 2;
#pragma unroll
    for (int it = 0; it < 5; ++it) {
      const int cb = w * 5 + it;
      const char* a = smem + (8 * g + (i >> 2)) * STR + cb * 32 + (i & 3) * 8;
      const u32x2 ra = __builtin_bit_cast(u32x2, __builtin_amdgcn_ds_read_tr16_b64_v4i16((lds_s16x4*)a));
      const u32x2 rb = __builtin_bit_cast(u32x2, __builtin_amdgcn_ds_read_tr16_b64_v4i16((lds_s16x4*)(a + 4 * STR)));
      st16ln(rsrc, dsto + (size_t)(cb * 16 + i) * (L_ * 2) + 16 * g, (u32x4){ra[0], ra[1], rb[0], rb[1]});
    }
  }
}

template <bool SCALE>
DEV void k2_retile(const unsigned short* W, char* ws, size_t outo, int K, int N, int blk, char* smem) {
  const int tid = threadIdx.x;
  const int nct = N / 16, nfr = nct * (K / 32);
  const int f = min(blk * 32 + (tid >> 3), nfr - 1);
  const int kk = f / nct, ct = f % nct;
  const int g = (tid >> 1) & 3, h = tid & 1;
  u32x4 r[8];
#pragma unroll
  for (int j = 0; j < 8; ++j) r[j] = *(const u32x4*)(W + (size_t)(32 * kk + 8 * g + j) * N + 16 * ct + 8 * h);
  if (SCALE) {
    const u32x4 cb = *(const u32x4*)((const unsigned short*)(ws + WS_MOD) + 4 * D_ + 32 * kk + 8 * g);
#pragma unroll
    for (int j = 0; j < 8; ++j) {
      const float c1 = rbf(1.f + ((j & 1) ? bf_hi(cb[j >> 1]) : bf_lo(cb[j >> 1])));
#pragma unroll
      for (int q = 0; q < 4; ++q) r[j][q] = pk_bf(bf_lo(r[j][q]) * c1, bf_hi(r[j][q]) * c1);
    }
  }
  char* o = smem + (tid >> 3) * 1024 + (16 * g + 8 * h) * 16;
#pragma unroll
  for (int q = 0; q < 4; ++q) {
    u32x4 e, f2;
#pragma unroll
    for (int pp = 0; pp < 4; ++pp) {
      e[pp] = __builtin_amdgcn_perm(r[2 * pp + 1][q], r[2 * pp][q], 0x05040100u);
      f2[pp] = __builtin_amdgcn_perm(r[2 * pp + 1][q], r[2 * pp][q], 0x07060302u);
    }
    *(u32x4*)(o + (2 * q) * 16) = e;
    *(u32x4*)(o + (2 * q + 1) * 16) = f2;
  }
  __syncthreads();
  const rsrc_t rs = ws_rsrc(ws);
  const int lane = tid & 63, w = tid >> 6;
#pragma unroll
  for (int it = 0; it < 8; ++it) {
    const int fl = w * 8 + it, fg = blk * 32 + fl;
    if (fg < nfr) {
      const int kk2 = fg / nct, ct2 = fg % nct;
      st16(rs, outo + ((size_t)(ct2 * (K / 32) + kk2) * 64 + lane) * 16, *(const u32x4*)(smem + fl * 1024 + lane * 16));
    }
  }
}

DEV void k2_colsum(const unsigned short* W1, char* ws, int b, char* smem) {
  const int tid = threadIdx.x;
  const int hc = tid & 3, rg = tid >> 2;
  const unsigned short* mod = (const unsigned short*)(ws + WS_MOD);
  float* red = (float*)smem;
  u32x4 w[10];
  unsigned short rc[10], rsb[10];
#pragma unroll
  for (int it = 0; it < 10; ++it) {
    w[it] = *(const u32x4*)(W1 + (size_t)(rg + 64 * it) * FF + 32 * b + 8 * hc);
    rc[it] = mod[4 * D_ + rg + 64 * it];
    rsb[it] = mod[3 * D_ + rg + 64 * it];
  }
  float ac[8], ab[8];
#pragma unroll
  for (int j = 0; j < 8; ++j) { ac[j] = 0.f; ab[j] = 0.f; }
#pragma unroll
  for (int it = 0; it < 10; ++it) {
    const float c1 = rbf(1.f + __uint_as_float((unsigned)rc[it] << 16)), sv = __uint_as_float((unsigned)rsb[it] << 16);
#pragma unroll
    for (int q = 0; q < 4; ++q) {
      const float w0 = bf_lo(w[it][q]), w1 = bf_hi(w[it][q]);
      const unsigned sc = pk_bf(w0 * c1, w1 * c1);
      ac[2 * q] += bf_lo(sc); ac[2 * q + 1] += bf_hi(sc);
      ab[2 * q] += sv * w0; ab[2 * q + 1] += sv * w1;
    }
  }
#pragma unroll
  for (int o = 4; o < 64; o <<= 1)
#pragma unroll
    for (int j = 0; j < 8; ++j) { ac[j] += __shfl_xor(ac[j], o, 64); ab[j] += __shfl_xor(ab[j], o, 64); }
  if ((tid & 63) < 4) {
#pragma unroll
    for (int j = 0; j < 8; ++j) {
      red[(tid >> 6) * 64 + hc * 8 + j] = ac[j];
      red[(tid >> 6) * 64 + 32 + hc * 8 + j] = ab[j];
    }
  }
  __syncthreads();
  if (tid < 64) {
    const float s = red[tid] + red[64 + tid] + red[128 + tid] + red[192 + tid];
    ((float*)(ws + WS_CSB))[(tid >> 5) * FF + 32 * b + (tid & 31)] = s;
  }
}

struct cf { float re, im; };
DEV cf cmul(cf a, cf b) {
  cf r;
  r.re = a.re * b.re - a.im * b.im;
  r.im = a.re * b.im + a.im * b.re;
  return r;
}
DEV cf cexp_(float a2, float br, float m, float m0) {
  float mag = __builtin_amdgcn_exp2f(a2 * (m - m0));
  float ph = __builtin_amdgcn_fractf(br * m);
  cf r; r.re = mag * __builtin_amdgcn_cosf(ph); r.im = mag * __builtin_amdgcn_sinf(ph);
  return r;
}
DEV unsigned pk_h(float a, float b) {
  f32x2 v = {a, b};
  h16x2 r = __builtin_convertvector(v, h16x2);
  return __builtin_bit_cast(unsigned, r);
}
DEV f32x4 mfma_f16(u32x4 a, u32x4 b, f32x4 c) {
  return __builtin_amdgcn_mfma_f32_16x16x32_f16(__builtin_bit_cast(h16x8, a), __builtin_bit_cast(h16x8, b), c, 0, 0, 0);
}
DEV void put2(char* p, cf a, cf b) {
  u32x2 o = {pk_h(a.re, a.im), pk_h(b.re, b.im)};
  *(u32x2*)p = o;
}

constexpr int KG_RS = 544;
DEV void k1_kgen(const K1Args& p, int d, char* smem) {
  constexpr int RS = KG_RS;
  const int tid = threadIdx.x;
  const int lane = tid & 63;
  const int w = __builtin_amdgcn_readfirstlane(tid >> 6);
  const int g = lane >> 4, i = lane & 15;
  char* stamp_ws = p.ws; (void)stamp_ws;
  const size_t po = (size_t)d * NS + 2 * lane;
  const unsigned rar = *(const unsigned*)(p.are + po), rai = *(const unsigned*)(p.aim + po);
  const unsigned rbr = *(const unsigned*)(p.bre + po), rbi = *(const unsigned*)(p.bim + po);
  const unsigned rcr = *(const unsigned*)(p.cre + po), rci = *(const unsigned*)(p.cim + po);
  const float dt = rbf(expf(ldbf(p.logdt + d)));
  STAMP(0, 1);
  float a2[2], bv[2], m0[2];
  a2[0] = (dt * bf_lo(rar)) * 1.44269504088896341f; a2[1] = (dt * bf_hi(rar)) * 1.44269504088896341f;
  bv[0] = (dt * bf_lo(rai)) * 0.15915494309189534f; bv[1] = (dt * bf_hi(rai)) * 0.15915494309189534f;
  m0[0] = a2[0] > 0.f ? 2048.f : 0.f; m0[1] = a2[1] > 0.f ? 2048.f : 0.f;
  char* my = smem + lane * 8;
  if (w == 0) {
    cf v[2], v8[2], e8[2];
    float bound = 0.f;
#pragma unroll
    for (int q = 0; q < 2; ++q) {
      const float dbr = dt * (q ? bf_hi(rbr) : bf_lo(rbr)), dbi = dt * (q ? bf_hi(rbi) : bf_lo(rbi));
      const float cr = q ? bf_hi(rcr) : bf_lo(rcr), ci = q ? bf_hi(rci) : bf_lo(rci);
      const float gsh = __builtin_amdgcn_exp2f(a2[q] * m0[q]);
      v[q].re = (cr * dbr - ci * dbi) * gsh; v[q].im = -(cr * dbi + ci * dbr) * gsh;
      bound += (fabsf(v[q].re) + fabsf(v[q].im)) * __builtin_amdgcn_exp2f(fmaxf(a2[q], 0.f) * 120.f);
      e8[q] = cexp_(a2[q], -bv[q], 8.f, 0.f);
      v8[q] = cexp_(a2[q], -bv[q], 64.f, 0.f);
    }
#pragma unroll
    for (int o = 1; o < 64; o <<= 1) bound += __shfl_xor(bound, o, 64);
    int ex = (int)((__float_as_uint(bound) >> 23) & 0xff);
    ex = ex < 20 ? 20 : (ex > 240 ? 240 : ex);
    const float sv = __uint_as_float((unsigned)(127 + 13 + 127 - ex) << 23);
    if (lane == 0) ((float*)(p.ws + WS_KSC))[d] = __uint_as_float((unsigned)(ex - 13) << 23);
#pragma unroll
    for (int q = 0; q < 2; ++q) { v[q].re *= sv; v[q].im *= sv; v8[q] = cmul(v[q], v8[q]); }
#pragma unroll
    for (int j = 0; j < 8; ++j) {
      put2(my + (41 + j) * RS, v[0], v[1]);
      put2(my + (49 + j) * RS, v8[0], v8[1]);
      if (j < 7) {
#pragma unroll
        for (int q = 0; q < 2; ++q) { v[q] = cmul(v[q], e8[q]); v8[q] = cmul(v8[q], e8[q]); }
      }
    }
  } else if (w == 1) {
    cf e1[2], u[2], u2[2];
#pragma unroll
    for (int q = 0; q < 2; ++q) {
      e1[q] = cexp_(a2[q], bv[q], 1.f, 0.f);
      u[q].re = __builtin_amdgcn_exp2f(-a2[q] * m0[q]); u[q].im = 0.f;
      u2[q] = cexp_(a2[q], bv[q], 128.f, m0[q]);
    }
#pragma unroll
    for (int r = 0; r < 8; ++r) {
      put2(my + r * RS, u[0], u[1]);
      put2(my + (8 + r) * RS, u2[0], u2[1]);
      if (r < 7) {
#pragma unroll
        for (int q = 0; q < 2; ++q) { u[q] = cmul(u[q], e1[q]); u2[q] = cmul(u2[q], e1[q]); }
      }
    }
  } else if (w == 2) {
    cf e2[2], u[2], u2[2];
#pragma unroll
    for (int q = 0; q < 2; ++q) {
      e2[q] = cexp_(a2[q], bv[q], 2.f, 0.f);
      u[q] = cexp_(a2[q], bv[q], 256.f, m0[q]);
      u2[q] = cexp_(a2[q], bv[q], 384.f, m0[q]);
    }
#pragma unroll
    for (int r = 0; r < 4; ++r) {
      put2(my + (16 + r) * RS, u[0], u[1]);
      put2(my + (20 + r) * RS, u2[0], u2[1]);
      if (r < 3) {
#pragma unroll
        for (int q = 0; q < 2; ++q) { u[q] = cmul(u[q], e2[q]); u2[q] = cmul(u2[q], e2[q]); }
      }
    }
    cf e4[2] = {cmul(e2[0], e2[0]), cmul(e2[1], e2[1])};
#pragma unroll
    for (int r = 0; r < 2; ++r) {
      cf c0 = cexp_(a2[0], bv[0], 512.f + 128.f * r, m0[0]), c1 = cexp_(a2[1], bv[1], 512.f + 128.f * r, m0[1]);
      put2(my + (24 + 2 * r) * RS, c0, c1);
      put2(my + (25 + 2 * r) * RS, cmul(c0, e4[0]), cmul(c1, e4[1]));
    }
  } else {
    cf e4[2], e128[2], u[2], u2[2];
#pragma unroll
    for (int q = 0; q < 2; ++q) {
      e4[q] = cexp_(a2[q], bv[q], 4.f, 0.f);
      e128[q] = cexp_(a2[q], bv[q], 128.f, 0.f);
      u[q] = cexp_(a2[q], bv[q], 1024.f, m0[q]);
      u2[q] = cexp_(a2[q], bv[q], 1536.f, m0[q]);
    }
#pragma unroll
    for (int r = 2; r < 4; ++r) {
      cf c0 = cexp_(a2[0], bv[0], 512.f + 128.f * r, m0[0]), c1 = cexp_(a2[1], bv[1], 512.f + 128.f * r, m0[1]);
      put2(my + (24 + 2 * r) * RS, c0, c1);
      put2(my + (25 + 2 * r) * RS, cmul(c0, e4[0]), cmul(c1, e4[1]));
    }
#pragma unroll
    for (int r = 0; r < 4; ++r) {
      put2(my + (32 + r) * RS, u[0], u[1]);
      put2(my + (36 + r) * RS, u2[0], u2[1]);
#pragma unroll
      for (int q = 0; q < 2; ++q) { if (r < 3) u[q] = cmul(u[q], e128[q]); u2[q] = cmul(u2[q], e128[q]); }
    }
    put2(my + 40 * RS, u2[0], u2[1]);
  }
  __syncthreads();
  STAMP(0, 2);
  _Float16* kh = (_Float16*)(smem + 57 * RS);
  if (w < 3) {
    const char* ua = smem + (16 * w + i) * RS + g * 16;
    const char* va = smem + (41 + i) * RS + g * 16;
    f32x4 acc = {0.f, 0.f, 0.f, 0.f};
#pragma unroll
    for (int ks = 0; ks < 8; ++ks)
      acc = mfma_f16(*(const u32x4*)(ua + ks * 64), *(const u32x4*)(va + ks * 64), acc);
    int idx0, st;
    if (w == 0) { idx0 = (g < 2 ? 4 * g : 120 + 4 * g) + 8 * i; st = 1; }
    else if (w == 1) { idx0 = g < 2 ? 256 + 64 * g + 4 * i : 384 + 64 * (g - 2) + 2 * i; st = 1; }
    else { idx0 = 512 + 64 * g + i; st = 16; }
#pragma unroll
    for (int rg = 0; rg < 4; ++rg) {
      int idx = idx0 + st * rg;
      if (w == 1 && g >= 2) idx = idx0 + 32 * (rg >> 1) + (rg & 1);
      if (w == 2 && g >= 2) idx = (g == 2 && rg == 0 && i == 0) ? 640 : 644;
      kh[idx] = (_Float16)acc[rg];
    }
  }
  __syncthreads();
  STAMP(0, 3);
  const unsigned short* ku = (const unsigned short*)kh;
  const size_t kout = WS_KREV + (size_t)d * KREV * 2;
  {
    const int l0 = 2040 - 8 * tid;
    u32x4 o;
    if (w < 2) {
      const int q = l0 >> 3;
      const unsigned a = ku[384 + q], b = ku[385 + q];
      const unsigned t = (q & 1) ? b : a;
      o[0] = b | (b << 16); o[1] = b | (t << 16); o[2] = a | (a << 16); o[3] = o[2];
    } else if (w == 2) {
      const int q = l0 >> 2;
      const unsigned a = ku[256 + q], b = ku[257 + q], c = ku[258 + q];
      o[0] = c | (c << 16); o[1] = b | (b << 16); o[2] = b | (a << 16); o[3] = a | (a << 16);
    } else {
      unsigned v[8];
#pragma unroll
      for (int e = 0; e < 8; ++e) {
        const int l = l0 + 7 - e;
        const int idx = l < 256 ? l : (int)(pk_bf((float)l, 0.f) & 0xffffu) - 17024;
        v[e] = ku[idx];
      }
#pragma unroll
      for (int q = 0; q < 4; ++q) o[q] = v[2 * q] | (v[2 * q + 1] << 16);
    }
    st16(ws_rsrc(p.ws), kout + 16 * tid, o);
  }
}

__global__ NOPK __launch_bounds__(256) void k1(K1Args p) {
  __shared__ __attribute__((aligned(16))) char smem[57 * KG_RS + 1312];
  int b = blockIdx.x;
  char* stamp_ws = p.ws; (void)stamp_ws;
  STAMP(0, 0);
  if (b < NB_K) { k1_kgen(p, b, smem); STAMP(0, 7); return; }
  b -= NB_K;
  __builtin_amdgcn_s_setprio(3);
  if (b < NB_LN) { k1_ln(p, b, smem); STAMP(0, 7); return; }
  b -= NB_LN;
  k1_mod(p, b, smem);
  STAMP(0, 7);
}

constexpr int K2_CS = 4160;
constexpr int K2_KOFF = 4 * K2_CS;
constexpr int K2_XOFF = K2_CS;
constexpr int K2_CH = K2_KOFF + 4608;
DEV void k2_body(const unsigned short* __restrict__ dskip, char* __restrict__ ws,
                 const unsigned short* __restrict__ w1, const unsigned short* __restrict__ w2, char* smem_all) {
  constexpr int CS = K2_CS, KOFF = K2_KOFF;
  const int tid = threadIdx.x;
  const int lane = tid & 63;
  const int w = __builtin_amdgcn_readfirstlane(tid >> 6);
  char* stamp_ws = ws; (void)stamp_ws;
  STAMP(1, 0);
  if (blockIdx.x >= D_ / 2) {
    int blk = blockIdx.x - D_ / 2;
    __builtin_amdgcn_s_sleep(24);
    if (blk < 68) k2_retile<true>(w1, ws, WS_W1F, D_, FF, blk, smem_all);
    else if (blk < 136) k2_retile<false>(w2, ws, WS_W2F, FF, D_, blk - 68, smem_all);
    else k2_colsum(w1, ws, blk - 136, smem_all);
    STAMP(1, 7);
    return;
  }
  const int bx = blockIdx.x;
  const int d = ((bx & 7) * 40 + (bx >> 3)) * 2 + (w >> 1);
  const int hw = w & 1, th = tid & 127;
  char* smem = smem_all + (w >> 1) * K2_CH;
  const unsigned short* mod = (const unsigned short*)(ws + WS_MOD);
  const unsigned short* krev = (const unsigned short*)(ws + WS_KREV) + (size_t)d * KREV;
  const int r = lane & 15, g = lane >> 4;

  const size_t xo = ((size_t)d * L_ + 16 * th) * 2;
  const size_t xo2 = xo + (th < 127 ? 32 : 0);
  const u32x4 xa0 = *(const u32x4*)(ws + WS_XNT + xo), xa1 = *(const u32x4*)(ws + WS_XNT + xo + 16);
  const u32x2 xb = *(const u32x2*)(ws + WS_XNT + xo2);
  const u32x4 kv0 = *(const u32x4*)(krev + 8 * th), kv1 = *(const u32x4*)(krev + 1024 + 8 * th);
  const unsigned short rsa = mod[d], rca = mod[D_ + d], rga = mod[2 * D_ + d], rds = dskip[d];
  const float kinv = ((const float*)(ws + WS_KSC))[d];
  u32x2 xres[4];
#pragma unroll
  for (int q = 0; q < 4; ++q)
    xres[q] = *(const u32x2*)(ws + WS_XT + ((size_t)d * L_ + 256 * (4 * hw + q) + 16 * r + 4 * g) * 2);
  const float sa = __uint_as_float((unsigned)rsa << 16), ca1 = rbf(1.f + __uint_as_float((unsigned)rca << 16));
  const float ga = __uint_as_float((unsigned)rga << 16), dsk = __uint_as_float((unsigned)rds << 16);
  const float hb = 26.f * fabsf(ca1) + fabsf(sa);
  int ex = (int)((__float_as_uint(hb) >> 23) & 0xff);
  ex = ex < 20 ? 20 : (ex > 240 ? 240 : ex);
  const float hs = __uint_as_float((unsigned)(127 + 14 + 127 - ex) << 23);
  const float hinv = __uint_as_float((unsigned)(ex - 14) << 23);
  const float yinv = kinv * hinv;

  unsigned hh[10];
#pragma unroll
  for (int q = 0; q < 10; ++q) {
    unsigned u = q < 4 ? xa0[q] : (q < 8 ? xa1[q - 4] : xb[q - 8]);
    unsigned m = pk_bf(bf_lo(u) * ca1, bf_hi(u) * ca1);
    unsigned h = pk_bf(bf_lo(m) + sa, bf_hi(m) + sa);
    hh[q] = pk_h(bf_lo(h) * hs, bf_hi(h) * hs);
  }
  STAMP(1, 1);
  {
    char* dst = smem + (2 * th + 2) * 16;
    unsigned s1[9];
#pragma unroll
    for (int q = 0; q < 9; ++q) s1[q] = __builtin_amdgcn_alignbit(hh[q + 1], hh[q], 16);
#pragma unroll
    for (int c = 0; c < 2; ++c) {
      *(u32x4*)(dst + 16 * c) = (u32x4){hh[4 * c], hh[4 * c + 1], hh[4 * c + 2], hh[4 * c + 3]};
      *(u32x4*)(dst + 16 * c + CS) = (u32x4){s1[4 * c], s1[4 * c + 1], s1[4 * c + 2], s1[4 * c + 3]};
      *(u32x4*)(dst + 16 * c + 2 * CS) = (u32x4){hh[4 * c + 1], hh[4 * c + 2], hh[4 * c + 3], hh[4 * c + 4]};
      *(u32x4*)(dst + 16 * c + 3 * CS) = (u32x4){s1[4 * c + 1], s1[4 * c + 2], s1[4 * c + 3], s1[4 * c + 4]};
    }
    if (th == 0) {
      u32x4 z = {0u, 0u, 0u, 0u};
      *(u32x4*)(smem) = z; *(u32x4*)(smem + CS) = z; *(u32x4*)(smem + 2 * CS) = z; *(u32x4*)(smem + 3 * CS) = z;
      *(u32x4*)(smem + 16) = z;
      *(u32x4*)(smem + CS + 16) = (u32x4){0u, 0u, 0u, hh[0] << 16};
      *(u32x4*)(smem + 2 * CS + 16) = (u32x4){0u, 0u, 0u, hh[0]};
      *(u32x4*)(smem + 3 * CS + 16) = (u32x4){0u, 0u, hh[0] << 16, s1[0]};
    }
  }
  *(u32x4*)(smem + KOFF + 16 * th) = kv0;
  *(u32x4*)(smem + KOFF + 2048 + 16 * th) = kv1;
  if (th < 32) *(u32x4*)(smem + KOFF + 4096 + 16 * th) = (u32x4){0u, 0u, 0u, 0u};
  __syncthreads();
  STAMP(1, 2);

  f32x4 acc[8];
#pragma unroll
  for (int q = 0; q < 8; ++q) acc[q] = (f32x4){0.f, 0.f, 0.f, 0.f};
  {
    const int s = (r + 1) & 3;
    const unsigned ap0 = (unsigned)(size_t)(smem + s * CS + 2 * (128 * hw + 8 * g + (r + 1) - s));
    const unsigned bp0 = (unsigned)(size_t)(smem + KOFF + 2 * (2032 + 128 * hw - 16 * r + 8 * g) - 3584);
    u32x2 Al[2][8], Ah[2][8];
    u32x4 Bf[2][8];
#define K2_LOAD(buf, cc, u)                                                                                     \
    asm volatile("ds_read_b64 %0, %3 offset:%5\n\tds_read_b64 %1, %3 offset:%6\n\tds_read_b128 %2, %4 offset:%7" \
                 : "=&v"(Al[buf][u]), "=&v"(Ah[buf][u]), "=&v"(Bf[buf][u])                                        \
                 : "v"(ap0), "v"(bp0), "i"(64 * (cc) + 512 * (u)), "i"(64 * (cc) + 512 * (u) + 8),               \
                   "i"(64 * (cc) + 3584 - 512 * (u)))
#pragma unroll
    for (int u = 0; u < 8; ++u) K2_LOAD(0, 0, u);
#pragma unroll
    for (int cc = 0; cc < 4; ++cc) {
      lgkm0();
#pragma unroll
      for (int q = 0; q < 8; ++q) {
        if (cc < 3) K2_LOAD((cc + 1) & 1, cc + 1, q);
#pragma unroll
        for (int sp = 0; sp <= q; ++sp) {
          const u32x4 A = {Al[cc & 1][sp][0], Al[cc & 1][sp][1], Ah[cc & 1][sp][0], Ah[cc & 1][sp][1]};
          acc[q] = mfma_f16(A, Bf[cc & 1][q - sp], acc[q]);
        }
        __builtin_amdgcn_sched_barrier(0);
      }
    }
#undef K2_LOAD
  }
  STAMP(1, 3);
  __syncthreads();
  f32x4 keep[4];
  {
    f32x4* xw = (f32x4*)(smem + K2_XOFF + hw * 4096) + lane;
    if (hw) {
#pragma unroll
      for (int q = 0; q < 4; ++q) { xw[64 * q] = acc[q]; keep[q] = acc[4 + q]; }
    } else {
#pragma unroll
      for (int q = 0; q < 4; ++q) { xw[64 * q] = acc[4 + q]; keep[q] = acc[q]; }
    }
  }
  __syncthreads();
  {
    const f32x4* xrd = (const f32x4*)(smem + K2_XOFF + (hw ^ 1) * 4096) + lane;
    const rsrc_t rs = ws_rsrc(ws);
#pragma unroll
    for (int q = 0; q < 4; ++q) {
      const f32x4 y = keep[q] + xrd[64 * q];
      const int t = 256 * (4 * hw + q) + 16 * r + 4 * g;
      const u32x2 hv = *(const u32x2*)(smem + 2 * (t + 16));
      u32x2 o;
#pragma unroll
      for (int e = 0; e < 2; ++e) {
        const unsigned hu = hv[e];
        const h16x2 h2 = __builtin_bit_cast(h16x2, hu);
        const float h0 = (float)h2[0] * hinv, h1 = (float)h2[1] * hinv;
        unsigned yb = pk_bf(y[2 * e] * yinv, y[2 * e + 1] * yinv);
        unsigned hd = pk_bf(h0 * dsk, h1 * dsk);
        unsigned y2 = pk_bf(bf_lo(yb) + bf_lo(hd), bf_hi(yb) + bf_hi(hd));
        unsigned gy = pk_bf(bf_lo(y2) * ga, bf_hi(y2) * ga);
        o[e] = pk_bf(bf_lo(xres[q][e]) + bf_lo(gy), bf_hi(xres[q][e]) + bf_hi(gy));
      }
      st8(rs, WS_X1T + (((size_t)(t >> 5) * D_ + d) * 32 + (t & 31)) * 2, o);
    }
  }
  STAMP(1, 7);
}

__global__ NOPK __launch_bounds__(256) void k2(const unsigned short* __restrict__ dskip, char* __restrict__ ws,
                                               const unsigned short* __restrict__ w1, const unsigned short* __restrict__ w2) {
  __shared__ __attribute__((aligned(16))) char smem[2 * K2_CH];
  k2_body(dskip, ws, w1, w2, smem);
}

#define SB() __builtin_amdgcn_sched_barrier(0)
DEV u32x4 ld16_sc1(rsrc_t r, size_t off) { return __builtin_amdgcn_raw_buffer_load_b128(r, (int)off, 0, 16); }
DEV u32x4 tr_frag(const char* p) {
  const u32x2 a = __builtin_bit_cast(u32x2, __builtin_amdgcn_ds_read_tr16_b64_v4i16((lds_s16x4*)p));
  const u32x2 b = __builtin_bit_cast(u32x2, __builtin_amdgcn_ds_read_tr16_b64_v4i16((lds_s16x4*)(p + 256)));
  return (u32x4){a[0], a[1], b[0], b[1]};
}

constexpr int KF_BIG = 90112;
constexpr int KF_TAB = KF_BIG;
constexpr int KF_PS = KF_TAB + 320;
constexpr int KF_XR = KF_PS + 512;
constexpr int KF_LDS = KF_XR + 64 * 176;

DEV void kf_body(char* __restrict__ ws, unsigned short* __restrict__ out, char* smem) {
  char* xs = smem;
  float* tab = (float*)(smem + KF_TAB);
  float* ps = (float*)(smem + KF_PS);
  char* xr = smem + KF_XR;
  const int tid = threadIdx.x, lane = tid & 63;
  const int w = __builtin_amdgcn_readfirstlane(tid >> 6);
  const int bx = blockIdx.x;
  const int ng = (bx >> 3) & 7, mt = (bx & 7) * 4 + (bx >> 6);
  const int g = lane >> 4, i = lane & 15;
  char* stamp_ws = ws; (void)stamp_ws;
  STAMP(2, 0);
  const rsrc_t rs_ = ws_rsrc(ws);

  const int ntl = ng < 4 ? 14 : 13;
  const int ctb = ng < 4 ? 14 * ng : 56 + 13 * (ng - 4);
  const bool two = w < ntl - 8;
  const int ct0 = ctb + (two ? 2 * w : (ntl - 8) + w);
  const u32x4* w1f = (const u32x4*)(ws + WS_W1F) + (size_t)ct0 * 20 * 64 + lane;
  const u32x4* w1g = w1f + (two ? 20 * 64 : 0);
  const u32x4* w2f = (const u32x4*)(ws + WS_W2F) + ((size_t)ng * 5 * 54 + w) * 64 + lane;

  u32x4 t[10];
  {
    const u32x4* src = (const u32x4*)(ws + WS_X1T + (size_t)mt * 81920);
#pragma unroll
    for (int it = 0; it < 10; ++it) t[it] = src[tid + 512 * it];
  }
  const unsigned short mg = ((const unsigned short*)(ws + WS_MOD))[5 * D_ + 80 * ng + min(tid, 79)];
  SB();
  __builtin_amdgcn_s_barrier();
  SB();
  u32x4 ring[32];
#pragma unroll
  for (int k = 0; k < 6; ++k) { ring[2 * k] = w1f[k * 64]; ring[2 * k + 1] = w1g[k * 64]; }
  SB();
#pragma unroll
  for (int it = 0; it < 10; ++it) {
    const int c = tid + 512 * it;
    const int row = c >> 2;
    *(u32x4*)(xs + row * 64 + (((c & 3) ^ ((row >> 2) & 2)) << 4)) = t[it];
  }
  if (tid < 80) tab[tid] = __uint_as_float((unsigned)mg << 16);
  SB();
  f32x4 csv[2], b1v[2];
#pragma unroll
  for (int c = 0; c < 2; ++c) {
    const int f0 = 16 * (ct0 + ((c && two) ? 1 : 0)) + 4 * g;
    csv[c] = *(const f32x4*)((const float*)(ws + WS_CSB) + f0);
    b1v[c] = *(const f32x4*)((const float*)(ws + WS_CSB) + FF + f0);
  }
  SB();
  __syncthreads();
  STAMP(2, 1);

  constexpr int NW2R = 24;
  f32x4 acc[2][4];
#pragma unroll
  for (int c = 0; c < 2; ++c)
#pragma unroll
    for (int q = 0; q < 4; ++q) acc[c][q] = (f32x4){0.f, 0.f, 0.f, 0.f};
  const char* bb0 = xs + (8 * g + (i >> 2)) * 64 + ((g & 1) << 5) + (i & 3) * 8;
  const char* bb1 = xs + (8 * g + (i >> 2)) * 64 + (((g & 1) ^ 1) << 5) + (i & 3) * 8;
  const u32x4 ones = {0x3f803f80u, 0x3f803f80u, 0x3f803f80u, 0x3f803f80u};
  auto gemm1 = [&](auto role_tag) __attribute__((always_inline)) {
    constexpr int ROLE = decltype(role_tag)::value;
    u32x4 bn[4];
#pragma unroll
    for (int q = 0; q < 4; ++q) bn[q] = tr_frag(((q & 1) ? bb1 : bb0) + (q >> 1) * 40960);
#pragma unroll
    for (int kk = 0; kk < 20; ++kk) {
      u32x4 b[4];
#pragma unroll
      for (int q = 0; q < 4; ++q) b[q] = bn[q];
      if (kk < 19) {
#pragma unroll
        for (int q = 0; q < 4; ++q) bn[q] = tr_frag(((q & 1) ? bb1 : bb0) + (q >> 1) * 40960 + (kk + 1) * 2048);
      }
      SB();
#pragma unroll
      for (int q = 0; q < 4; ++q) acc[0][q] = mfma_bf16(ring[(2 * kk) % 32], b[q], acc[0][q]);
      if (ROLE == 0) {
#pragma unroll
        for (int q = 0; q < 4; ++q) acc[1][q] = mfma_bf16(ring[(2 * kk + 1) % 32], b[q], acc[1][q]);
      } else if (ROLE >= 2) {
        constexpr int q0 = ROLE >= 2 ? (ROLE - 2) * 2 : 0;
        acc[1][0] = mfma_bf16(ones, b[q0], acc[1][0]); acc[1][1] = mfma_bf16(b[q0], b[q0], acc[1][1]);
        acc[1][2] = mfma_bf16(ones, b[q0 + 1], acc[1][2]); acc[1][3] = mfma_bf16(b[q0 + 1], b[q0 + 1], acc[1][3]);
        if (32 * kk + 32 > 80 * ng && 32 * kk < 80 * ng + 80) {
          const unsigned dl = (unsigned)(32 * kk + 8 * g - 80 * ng);
          if (dl < 80u) {
            *(u32x4*)(xr + (q0 * 16 + i) * 176 + dl * 2) = b[q0];
            *(u32x4*)(xr + ((q0 + 1) * 16 + i) * 176 + dl * 2) = b[q0 + 1];
          }
        }
      }
      SB();
      if (kk < 7) {
#pragma unroll
        for (int u = 0; u < 2; ++u) {
          const int kn = 6 + 2 * kk + u;
          ring[(2 * kn) % 32] = w1f[kn * 64];
          if (ROLE == 0) ring[(2 * kn + 1) % 32] = w1g[kn * 64];
        }
      }
      if (kk >= 7 && kk < 19) {
#pragma unroll
        for (int u = 0; u < 2; ++u) {
          const int e = 2 * (kk - 7) + u;
          ring[8 + e] = w2f[((e % 5) * 54 + 8 * (e / 5)) * 64];
        }
      }
      SB();
    }
  };
  if (two) gemm1(std::integral_constant<int, 0>{});
  else if (w == 6) gemm1(std::integral_constant<int, 2>{});
  else if (w == 7) gemm1(std::integral_constant<int, 3>{});
  else gemm1(std::integral_constant<int, 1>{});
  if (w >= 6) {
#pragma unroll
    for (int h = 0; h < 2; ++h) {
      const f32x4 aq = acc[1][2 * h + 1];
      const float dq = (i & 2) ? ((i & 1) ? aq[3] : aq[2]) : ((i & 1) ? aq[1] : aq[0]);
      const int tk = ((w - 6) * 2 + h) * 16 + i;
      if (g == (i >> 2)) {
        const float mean = acc[1][2 * h][0] * (1.f / D_);
        const float var = fmaxf(dq * (1.f / D_) - mean * mean, 0.f);
        const float sd = rbf(sqrtf(rbf(rbf(var) + 1e-5f)));
        const float rs = 1.f / sd;
        ps[tk] = rs;
        ps[64 + tk] = -rbf(mean) * rs;
      }
    }
  }
  __syncthreads();
  STAMP(2, 2);
#pragma unroll
  for (int q = 0; q < 4; ++q) {
    const float rs = ps[q * 16 + i], nm = ps[64 + q * 16 + i];
#pragma unroll
    for (int c = 0; c < 2; ++c)
      if (c == 0 || two) {
        const int ct = ct0 + c;
        float v[4];
#pragma unroll
        for (int r = 0; r < 4; ++r) v[r] = fmaxf(fmaf(acc[c][q][r], rs, fmaf(nm, csv[c][r], b1v[c][r])), 0.f);
        u32x2 o = {pk_bf(v[0], v[1]), pk_bf(v[2], v[3])};
        const size_t off = (((size_t)(4 * mt + q) * 54 + (ct >> 1)) * 64 + i + 16 * (2 * (ct & 1) + (g >> 1))) * 8 + 4 * (g & 1);
        st8(rs_, WS_H1F + off * 2, o);
      }
  }
  const bool full = w < 6;
  asm volatile("s_waitcnt vmcnt(0)" ::: "memory");
  __syncthreads();
  STAMP(2, 3);
  if (tid == 0) __hip_atomic_fetch_add((unsigned*)(ws + WS_CNT) + mt * 64, 1u, __ATOMIC_RELAXED, __HIP_MEMORY_SCOPE_AGENT);
#pragma unroll
  for (int e = NW2R; e < 35; ++e) {
    const int sn = (e / 5 == 6 && !full) ? 5 : e / 5;
    __builtin_amdgcn_global_load_lds((const __attribute__((address_space(1))) void*)(w2f + ((e % 5) * 54 + 8 * sn) * 64),
                                     (__attribute__((address_space(3))) void*)(xs + (w * 11 + (e - NW2R)) * 1024), 16, 0, 0);
  }
  if (tid == 0) {
    unsigned* cnt = (unsigned*)(ws + WS_CNT) + mt * 64;
    for (unsigned spin = 0; spin < (1u << 22); ++spin) {
      if (__hip_atomic_load(cnt, __ATOMIC_RELAXED, __HIP_MEMORY_SCOPE_AGENT) >= 8u) break;
      __builtin_amdgcn_s_sleep(1);
    }
  }
  asm volatile("s_waitcnt vmcnt(0)" ::: "memory");
  __syncthreads();
  STAMP(2, 4);

  constexpr int PH = 3;
  const size_t h1o = WS_H1F + (((size_t)mt * 4 * 54 + w) * 64 + lane) * 16;
  u32x4 hb[PH][4];
#pragma unroll
  for (int s = 0; s < PH; ++s)
#pragma unroll
    for (int q = 0; q < 4; ++q) hb[s][q] = ld16_sc1(rs_, h1o + (size_t)(q * 54 + 8 * s) * 1024);
  SB();
  f32x4 ac2[5][4];
#pragma unroll
  for (int c = 0; c < 5; ++c)
#pragma unroll
    for (int q = 0; q < 4; ++q) ac2[c][q] = (f32x4){0.f, 0.f, 0.f, 0.f};
#pragma unroll
  for (int s = 0; s < 7; ++s) {
    if (s < 6 || full) {
#pragma unroll
      for (int c = 0; c < 5; ++c) {
        const int e = 5 * s + c;
        const u32x4 a = e < NW2R ? ring[8 + (e < NW2R ? e : 0)] : *(const u32x4*)(xs + (w * 11 + (e - NW2R)) * 1024 + lane * 16);
#pragma unroll
        for (int q = 0; q < 4; ++q) ac2[c][q] = mfma_bf16(a, hb[s % PH][q], ac2[c][q]);
      }
    }
    SB();
    if (s + PH < 7) {
      const int sn = (s + PH == 6 && !full) ? 5 : s + PH;
#pragma unroll
      for (int q = 0; q < 4; ++q) hb[s % PH][q] = ld16_sc1(rs_, h1o + (size_t)(q * 54 + 8 * sn) * 1024);
    }
    SB();
  }
  STAMP(2, 5);
  constexpr int RS = 84;
  float* red = (float*)smem;
  float* rb_ = red + ((w & 3) * 64 + i) * RS + 4 * g;
  __syncthreads();
  if (w >= 4) {
#pragma unroll
    for (int c = 0; c < 5; ++c)
#pragma unroll
      for (int q = 0; q < 4; ++q) *(f32x4*)(rb_ + 16 * q * RS + 16 * c) = ac2[c][q];
  }
  __syncthreads();
  if (w < 4) {
#pragma unroll
    for (int c = 0; c < 5; ++c)
#pragma unroll
      for (int q = 0; q < 4; ++q) {
        f32x4 v = *(const f32x4*)(rb_ + 16 * q * RS + 16 * c);
        *(f32x4*)(rb_ + 16 * q * RS + 16 * c) = v + ac2[c][q];
      }
  }
  __syncthreads();
  STAMP(2, 6);
#pragma unroll
  for (int it = 0; it < 3; ++it) {
    const int e = tid + 512 * it;
    if (e < 1280) {
      const int tk = e / 20, q4 = e % 20;
      f32x4 s = *(const f32x4*)(red + tk * RS + 4 * q4);
#pragma unroll
      for (int ww = 1; ww < 4; ++ww) s += *(const f32x4*)(red + (ww * 64 + tk) * RS + 4 * q4);
      const u32x2 xv2 = *(const u32x2*)(xr + tk * 176 + q4 * 8);
      const f32x4 gb = *(const f32x4*)(tab + 4 * q4);
      u32x2 o;
#pragma unroll
      for (int q = 0; q < 2; ++q) {
        unsigned f = pk_bf(s[2 * q], s[2 * q + 1]);
        unsigned gf = pk_bf(bf_lo(f) * gb[2 * q], bf_hi(f) * gb[2 * q + 1]);
        o[q] = pk_bf(bf_lo(xv2[q]) + bf_lo(gf), bf_hi(xv2[q]) + bf_hi(gf));
      }
      *(u32x2*)(out + (size_t)(mt * 64 + tk) * D_ + ng * 80 + 4 * q4) = o;
    }
  }
  STAMP(2, 7);
}

__global__ NOPK __launch_bounds__(512, 2) void kf(char* __restrict__ ws, unsigned short* __restrict__ out) {
  __shared__ __attribute__((aligned(16))) char smem[KF_LDS];
  kf_body(ws, out, smem);
}

extern "C" void kernel_launch(void* const* d_in, const int* in_sizes, int n_in,
                              void* d_out, int out_size, void* d_ws, size_t ws_size,
                              hipStream_t stream) {
  K1Args a;
  a.x = (const unsigned short*)d_in[0];
  a.are = (const unsigned short*)d_in[1];
  a.aim = (const unsigned short*)d_in[2];
  a.bre = (const unsigned short*)d_in[3];
  a.bim = (const unsigned short*)d_in[4];
  a.cre = (const unsigned short*)d_in[5];
  a.cim = (const unsigned short*)d_in[6];
  a.logdt = (const unsigned short*)d_in[7];
  a.w1 = (const unsigned short*)d_in[10];
  a.w2 = (const unsigned short*)d_in[11];
  a.c = (const unsigned short*)d_in[12];
  a.wmod = (const unsigned short*)d_in[13];
  a.ws = (char*)d_ws;
  k1<<<NB_MOD + NB_LN + NB_K, 256, 0, stream>>>(a);
  k2<<<D_ / 2 + NB_W, 256, 0, stream>>>((const unsigned short*)d_in[8], (char*)d_ws, a.w1, a.w2);
  kf<<<256, 512, 0, stream>>>((char*)d_ws, (unsigned short*)d_out);
}
```

```cpp
#include <hip/hip_runtime.h>
#include <hip/hip_bf16.h>
#include <stdint.h>
#include <type_traits>

typedef __attribute__((ext_vector_type(8))) __bf16 bf16x8;
typedef __attribute__((ext_vector_type(2))) __bf16 bf16x2;
typedef __attribute__((ext_vector_type(4))) float f32x4;
typedef __attribute__((ext_vector_type(2))) float f32x2;
typedef __attribute__((ext_vector_type(4))) unsigned u32x4;
typedef __attribute__((ext_vector_type(2))) unsigned u32x2;
typedef __attribute__((ext_vector_type(8))) _Float16 h16x8;
typedef __attribute__((ext_vector_type(2))) _Float16 h16x2;
typedef __attribute__((ext_vector_type(4))) short s16x4;
typedef __attribute__((address_space(3))) s16x4 lds_s16x4;

#if defined(__HIP_DEVICE_COMPILE__)
#define NOPK __attribute__((target("no-packed-fp32-ops")))
#else
#define NOPK
#endif
#define DEV __device__ __forceinline__

constexpr int L_ = 2048;
constexpr int D_ = 640;
constexpr int NS = 128;
constexpr int FF = 1728;
constexpr int KREV = 2304;

constexpr size_t WS_KSC  = 8192;
constexpr size_t WS_CNT  = 16384;
constexpr size_t WS_CSB  = 32768;
constexpr size_t WS_MOD  = 65536;
constexpr size_t WS_XNT  = 196608;
constexpr size_t WS_XT   = WS_XNT + (size_t)D_ * L_ * 2;
constexpr size_t WS_KREV = WS_XT + (size_t)D_ * L_ * 2;
constexpr size_t WS_W1F  = WS_KREV + (size_t)D_ * 2 * KREV * 2;
constexpr size_t WS_W2F  = WS_W1F + (size_t)D_ * FF * 2;
constexpr size_t WS_X1T  = WS_W2F + (size_t)D_ * FF * 2;
constexpr size_t WS_X1   = WS_X1T + (size_t)D_ * L_ * 2;
constexpr size_t WS_H1F  = WS_X1 + (size_t)D_ * L_ * 2;

#define STAMP(kid, k) do {} while (0)

DEV float bf_lo(unsigned u) { return __uint_as_float(u << 16); }
DEV float bf_hi(unsigned u) { return __uint_as_float(u & 0xffff0000u); }
DEV unsigned pk_bf(float a, float b) {
  f32x2 v = {a, b};
  bf16x2 r = __builtin_convertvector(v, bf16x2);
  return __builtin_bit_cast(unsigned, r);
}
DEV float rbf(float a) { return bf_lo(pk_bf(a, 0.f)); }
DEV float ldbf(const unsigned short* p) { return __uint_as_float(((unsigned)*p) << 16); }

typedef __amdgpu_buffer_rsrc_t rsrc_t;
DEV rsrc_t ws_rsrc(char* ws) { return __builtin_amdgcn_make_buffer_rsrc((void*)ws, 0, 0x7fffffff, 0x00020000); }
#define ST_AUX 16
DEV void st16(rsrc_t r, size_t off, u32x4 v) { __builtin_amdgcn_raw_buffer_store_b128(v, r, (int)off, 0, ST_AUX); }
#define LN_AUX 0
DEV void st16ln(rsrc_t r, size_t off, u32x4 v) { __builtin_amdgcn_raw_buffer_store_b128(v, r, (int)off, 0, LN_AUX); }
DEV void st8(rsrc_t r, size_t off, u32x2 v) { __builtin_amdgcn_raw_buffer_store_b64(v, r, (int)off, 0, 16); }

DEV u32x2 tr_read(unsigned lds_addr) {
  u32x2 r;
  asm volatile("ds_read_b64_tr_b16 %0, %1" : "=v"(r) : "v"(lds_addr) : "memory");
  return r;
}
DEV void lgkm0() { asm volatile("s_waitcnt lgkmcnt(0)" ::: "memory"); __builtin_amdgcn_sched_barrier(0); }

DEV f32x4 mfma_bf16(u32x4 a, u32x4 b, f32x4 c) {
  return __builtin_amdgcn_mfma_f32_16x16x32_bf16(__builtin_bit_cast(bf16x8, a), __builtin_bit_cast(bf16x8, b), c, 0, 0, 0);
}

constexpr int NB_MOD = 120, NB_LN = 256, NB_W = 136 + 54, NB_K = 640;

struct K1Args {
  const unsigned short *x, *are, *aim, *bre, *bim, *cre, *cim, *logdt, *w1, *w2, *c, *wmod;
  char* ws;
};

DEV void k1_mod(const K1Args& p, int b, char* smem) {
  const int tid = threadIdx.x;
  const int rg = b & 7, cgp = b >> 3;
  const int c32 = tid & 31, r8 = tid >> 5;
  float* sil = (float*)smem;
  float* red = (float*)(smem + 512);
  u32x4 w[10];
#pragma unroll
  for (int it = 0; it < 10; ++it)
    w[it] = *(const u32x4*)(p.wmod + (size_t)(rg * 80 + r8 + 8 * it) * 3840 + 256 * cgp + 8 * c32);
  if (tid < 80) {
    float cv = ldbf(p.c + rg * 80 + tid);
    sil[tid] = rbf(cv / (1.f + expf(-cv)));
  }
  if (tid == 128 && b < 64) ((unsigned*)(p.ws + WS_CNT))[b * 32] = 0u;
  __syncthreads();
  float acc[8];
#pragma unroll
  for (int j = 0; j < 8; ++j) acc[j] = 0.f;
#pragma unroll
  for (int it = 0; it < 10; ++it) {
    float s = sil[r8 + 8 * it];
#pragma unroll
    for (int q = 0; q < 4; ++q) {
      acc[2 * q] += s * bf_lo(w[it][q]);
      acc[2 * q + 1] += s * bf_hi(w[it][q]);
    }
  }
#pragma unroll
  for (int j = 0; j < 8; ++j) acc[j] += __shfl_xor(acc[j], 32, 64);
  if ((tid & 63) < 32) {
    *(f32x4*)(red + (tid >> 6) * 256 + 8 * c32) = (f32x4){acc[0], acc[1], acc[2], acc[3]};
    *(f32x4*)(red + (tid >> 6) * 256 + 8 * c32 + 4) = (f32x4){acc[4], acc[5], acc[6], acc[7]};
  }
  __syncthreads();
  ((float*)(p.ws + WS_MOD))[rg * 3840 + 256 * cgp + tid] = (red[tid] + red[256 + tid]) + (red[512 + tid] + red[768 + tid]);
}
DEV float mod_sum(const char* ws, int idx) {
  const float* mp = (const float*)(ws + WS_MOD) + idx;
  float v[8];
#pragma unroll
  for (int r = 0; r < 8; ++r) v[r] = mp[r * 3840];
  return rbf(((v[0] + v[1]) + (v[2] + v[3])) + ((v[4] + v[5]) + (v[6] + v[7])));
}
DEV f32x4 mod_sum4(const char* ws, int idx) {
  const float* mp = (const float*)(ws + WS_MOD) + idx;
  f32x4 v[8];
#pragma unroll
  for (int r = 0; r < 8; ++r) v[r] = *(const f32x4*)(mp + r * 3840);
  f32x4 t = ((v[0] + v[1]) + (v[2] + v[3])) + ((v[4] + v[5]) + (v[6] + v[7]));
  return (f32x4){rbf(t[0]), rbf(t[1]), rbf(t[2]), rbf(t[3])};
}

DEV void k1_ln(const K1Args& p, int b, char* smem) {
  constexpr int STR = 704;
  const int tid = threadIdx.x;
  const int tok = tid >> 3, sub = tid & 7;
  const int tile = (b & 7) * 8 + ((b >> 3) & 7), pass = b >> 6;
  const int hf = pass & 1;
  const bool norm = pass < 2;
  const int t0 = tile * 32;
  const unsigned short* xr = p.x + (size_t)(t0 + tok) * D_ + 8 * sub;
  u32x4 xv[5], xo[5];
#pragma unroll
  for (int m = 0; m < 5; ++m) xv[m] = *(const u32x4*)(xr + 320 * hf + 64 * m);
  if (norm) {
#pragma unroll
    for (int m = 0; m < 5; ++m) xo[m] = *(const u32x4*)(xr + 320 * (hf ^ 1) + 64 * m);
    float s = 0.f, v = 0.f;
    const bf16x2 one2 = __builtin_bit_cast(bf16x2, 0x3f803f80u);
#pragma unroll
    for (int m = 0; m < 10; ++m)
#pragma unroll
      for (int q = 0; q < 4; ++q) {
        const unsigned u = m < 5 ? xv[m][q] : xo[m - 5][q];
        const bf16x2 x2 = __builtin_bit_cast(bf16x2, u);
        s = __builtin_amdgcn_fdot2_f32_bf16(x2, one2, s, false);
        v = __builtin_amdgcn_fdot2_f32_bf16(x2, x2, v, false);
      }
#pragma unroll
    for (int o = 1; o < 8; o <<= 1) { s += __shfl_xor(s, o, 64); v += __shfl_xor(v, o, 64); }
    const float mean = s * (1.f / D_);
    const float var = fmaxf(v * (1.f / D_) - mean * mean, 0.f);
    const float sd = rbf(sqrtf(rbf(rbf(var) + 1e-5f)));
    const float rs = 1.f / sd;
    const float mb = rbf(mean);
#pragma unroll
    for (int m = 0; m < 5; ++m)
#pragma unroll
      for (int q = 0; q < 4; ++q) {
        unsigned d2 = pk_bf(bf_lo(xv[m][q]) - mb, bf_hi(xv[m][q]) - mb);
        xv[m][q] = pk_bf(bf_lo(d2) * rs, bf_hi(d2) * rs);
      }
  }
  const int lane = tid & 63, w = tid >> 6;
  const int g = lane >> 4, i = lane & 15;
  const rsrc_t rsrc = ws_rsrc(p.ws);
#pragma unroll
  for (int m = 0; m < 5; ++m) *(u32x4*)(smem + tok * STR + (sub + 8 * m) * 16) = xv[m];
  __syncthreads();
  const size_t dsto = (norm ? WS_XNT : WS_XT) + ((size_t)320 * hf * L_ + t0) * 2;
#pragma unroll
  for (int it = 0; it < 5; ++it) {
    const int cb = w * 5 + it;
    const char* a = smem + (8 * g + (i >> 2)) * STR + cb * 32 + (i & 3) * 8;
    const u32x2 ra = __builtin_bit_cast(u32x2, __builtin_amdgcn_ds_read_tr16_b64_v4i16((lds_s16x4*)a));
    const u32x2 rb = __builtin_bit_cast(u32x2, __builtin_amdgcn_ds_read_tr16_b64_v4i16((lds_s16x4*)(a + 4 * STR)));
    st16ln(rsrc, dsto + (size_t)(cb * 16 + i) * (L_ * 2) + 16 * g, (u32x4){ra[0], ra[1], rb[0], rb[1]});
  }
}

template <bool SCALE>
DEV void k2_retile(const unsigned short* W, char* ws, size_t outo, int K, int N, int blk, char* smem) {
  const int tid = threadIdx.x;
  const int nct = N / 16, nfr = nct * (K / 32);
  const int f = min(blk * 32 + (tid >> 3), nfr - 1);
  const int kk = f / nct, ct = f % nct;
  const int g = (tid >> 1) & 3, h = tid & 1;
  u32x4 r[8];
#pragma unroll
  for (int j = 0; j < 8; ++j) r[j] = *(const u32x4*)(W + (size_t)(32 * kk + 8 * g + j) * N + 16 * ct + 8 * h);
  if (SCALE) {
    const f32x4 cb0 = mod_sum4(ws, 4 * D_ + 32 * kk + 8 * g), cb1 = mod_sum4(ws, 4 * D_ + 32 * kk + 8 * g + 4);
#pragma unroll
    for (int j = 0; j < 8; ++j) {
      const float c1 = rbf(1.f + (j < 4 ? cb0[j & 3] : cb1[j & 3]));
#pragma unroll
      for (int q = 0; q < 4; ++q) r[j][q] = pk_bf(bf_lo(r[j][q]) * c1, bf_hi(r[j][q]) * c1);
    }
  }
  char* o = smem + (tid >> 3) * 1024 + (16 * g + 8 * h) * 16;
#pragma unroll
  for (int q = 0; q < 4; ++q) {
    u32x4 e, f2;
#pragma unroll
    for (int pp = 0; pp < 4; ++pp) {
      e[pp] = __builtin_amdgcn_perm(r[2 * pp + 1][q], r[2 * pp][q], 0x05040100u);
      f2[pp] = __builtin_amdgcn_perm(r[2 * pp + 1][q], r[2 * pp][q], 0x07060302u);
    }
    *(u32x4*)(o + (2 * q) * 16) = e;
    *(u32x4*)(o + (2 * q + 1) * 16) = f2;
  }
  __syncthreads();
  const rsrc_t rs = ws_rsrc(ws);
  const int lane = tid & 63, w = tid >> 6;
#pragma unroll
  for (int it = 0; it < 8; ++it) {
    const int fl = w * 8 + it, fg = blk * 32 + fl;
    if (fg < nfr) {
      const int kk2 = fg / nct, ct2 = fg % nct;
      const int fo = SCALE ? fg : ct2 * (K / 32) + kk2;
      st16(rs, outo + ((size_t)fo * 64 + lane) * 16, *(const u32x4*)(smem + fl * 1024 + lane * 16));
    }
  }
}

DEV void k2_colsum(const unsigned short* W1, char* ws, int b, char* smem) {
  const int tid = threadIdx.x;
  const int hc = tid & 3, rg = tid >> 2;
  float* red = (float*)smem;
  float* tab = (float*)(smem + 1024);
  u32x4 w[10];
#pragma unroll
  for (int it = 0; it < 10; ++it) w[it] = *(const u32x4*)(W1 + (size_t)(rg + 64 * it) * FF + 32 * b + 8 * hc);
  *(f32x4*)(tab + 4 * tid) = mod_sum4(ws, 3 * D_ + 4 * tid);
  if (tid < 64) *(f32x4*)(tab + 1024 + 4 * tid) = mod_sum4(ws, 3 * D_ + 1024 + 4 * tid);
  __syncthreads();
  float ac[8], ab[8];
#pragma unroll
  for (int j = 0; j < 8; ++j) { ac[j] = 0.f; ab[j] = 0.f; }
#pragma unroll
  for (int it = 0; it < 10; ++it) {
    const float c1 = rbf(1.f + tab[D_ + rg + 64 * it]), sv = tab[rg + 64 * it];
#pragma unroll
    for (int q = 0; q < 4; ++q) {
      const float w0 = bf_lo(w[it][q]), w1 = bf_hi(w[it][q]);
      const unsigned sc = pk_bf(w0 * c1, w1 * c1);
      ac[2 * q] += bf_lo(sc); ac[2 * q + 1] += bf_hi(sc);
      ab[2 * q] += sv * w0; ab[2 * q + 1] += sv * w1;
    }
  }
#pragma unroll
  for (int o = 4; o < 64; o <<= 1)
#pragma unroll
    for (int j = 0; j < 8; ++j) { ac[j] += __shfl_xor(ac[j], o, 64); ab[j] += __shfl_xor(ab[j], o, 64); }
  if ((tid & 63) < 4) {
#pragma unroll
    for (int j = 0; j < 8; ++j) {
      red[(tid >> 6) * 64 + hc * 8 + j] = ac[j];
      red[(tid >> 6) * 64 + 32 + hc * 8 + j] = ab[j];
    }
  }
  __syncthreads();
  if (tid < 64) {
    const float s = red[tid] + red[64 + tid] + red[128 + tid] + red[192 + tid];
    ((float*)(ws + WS_CSB))[(tid >> 5) * FF + 32 * b + (tid & 31)] = s;
  }
}

struct cf { float re, im; };
DEV cf cmul(cf a, cf b) {
  cf r;
  r.re = a.re * b.re - a.im * b.im;
  r.im = a.re * b.im + a.im * b.re;
  return r;
}
DEV cf cexp_(float a2, float br, float m, float m0) {
  float mag = __builtin_amdgcn_exp2f(a2 * (m - m0));
  float ph = __builtin_amdgcn_fractf(br * m);
  cf r; r.re = mag * __builtin_amdgcn_cosf(ph); r.im = mag * __builtin_amdgcn_sinf(ph);
  return r;
}
DEV unsigned pk_h(float a, float b) {
  f32x2 v = {a, b};
  h16x2 r = __builtin_convertvector(v, h16x2);
  return __builtin_bit_cast(unsigned, r);
}
DEV f32x4 mfma_f16(u32x4 a, u32x4 b, f32x4 c) {
  return __builtin_amdgcn_mfma_f32_16x16x32_f16(__builtin_bit_cast(h16x8, a), __builtin_bit_cast(h16x8, b), c, 0, 0, 0);
}
DEV void put2(char* p, cf a, cf b) {
  u32x2 o = {pk_h(a.re, a.im), pk_h(b.re, b.im)};
  *(u32x2*)p = o;
}

constexpr int KG_RS = 544;
DEV void k1_kgen(const K1Args& p, int d, char* smem) {
  constexpr int RS = KG_RS;
  const int tid = threadIdx.x;
  const int lane = tid & 63;
  const int w = __builtin_amdgcn_readfirstlane(tid >> 6);
  const int g = lane >> 4, i = lane & 15;
  char* stamp_ws = p.ws; (void)stamp_ws;
  const size_t po = (size_t)d * NS + 2 * lane;
  const unsigned rar = *(const unsigned*)(p.are + po), rai = *(const unsigned*)(p.aim + po);
  const unsigned rbr = *(const unsigned*)(p.bre + po), rbi = *(const unsigned*)(p.bim + po);
  const unsigned rcr = *(const unsigned*)(p.cre + po), rci = *(const unsigned*)(p.cim + po);
  const float dt = rbf(expf(ldbf(p.logdt + d)));
  STAMP(0, 1);
  float a2[2], bv[2], m0[2];
  a2[0] = (dt * bf_lo(rar)) * 1.44269504088896341f; a2[1] = (dt * bf_hi(rar)) * 1.44269504088896341f;
  bv[0] = (dt * bf_lo(rai)) * 0.15915494309189534f; bv[1] = (dt * bf_hi(rai)) * 0.15915494309189534f;
  m0[0] = a2[0] > 0.f ? 2048.f : 0.f; m0[1] = a2[1] > 0.f ? 2048.f : 0.f;
  char* my = smem + lane * 8;
  if (w == 0) {
    cf v[2], v8[2], e8[2];
    float bound = 0.f;
#pragma unroll
    for (int q = 0; q < 2; ++q) {
      const float dbr = dt * (q ? bf_hi(rbr) : bf_lo(rbr)), dbi = dt * (q ? bf_hi(rbi) : bf_lo(rbi));
      const float cr = q ? bf_hi(rcr) : bf_lo(rcr), ci = q ? bf_hi(rci) : bf_lo(rci);
      const float gsh = __builtin_amdgcn_exp2f(a2[q] * m0[q]);
      v[q].re = (cr * dbr - ci * dbi) * gsh; v[q].im = -(cr * dbi + ci * dbr) * gsh;
      bound += (fabsf(v[q].re) + fabsf(v[q].im)) * __builtin_amdgcn_exp2f(fmaxf(a2[q], 0.f) * 120.f);
      e8[q] = cexp_(a2[q], -bv[q], 8.f, 0.f);
      v8[q] = cexp_(a2[q], -bv[q], 64.f, 0.f);
    }
#pragma unroll
    for (int o = 1; o < 64; o <<= 1) bound += __shfl_xor(bound, o, 64);
    int ex = (int)((__float_as_uint(bound) >> 23) & 0xff);
    ex = ex < 20 ? 20 : (ex > 240 ? 240 : ex);
    const float sv = __uint_as_float((unsigned)(127 + 13 + 127 - ex) << 23);
    if (lane == 0) ((float*)(p.ws + WS_KSC))[d] = __uint_as_float((unsigned)(ex - 13) << 23);
#pragma unroll
    for (int q = 0; q < 2; ++q) { v[q].re *= sv; v[q].im *= sv; v8[q] = cmul(v[q], v8[q]); }
#pragma unroll
    for (int j = 0; j < 8; ++j) {
      put2(my + (41 + j) * RS, v[0], v[1]);
      put2(my + (49 + j) * RS, v8[0], v8[1]);
      if (j < 7) {
#pragma unroll
        for (int q = 0; q < 2; ++q) { v[q] = cmul(v[q], e8[q]); v8[q] = cmul(v8[q], e8[q]); }
      }
    }
  } else if (w == 1) {
    cf e1[2], u[2], u2[2];
#pragma unroll
    for (int q = 0; q < 2; ++q) {
      e1[q] = cexp_(a2[q], bv[q], 1.f, 0.f);
      u[q].re = __builtin_amdgcn_exp2f(-a2[q] * m0[q]); u[q].im = 0.f;
      u2[q] = cexp_(a2[q], bv[q], 128.f, m0[q]);
    }
#pragma unroll
    for (int r = 0; r < 8; ++r) {
      put2(my + r * RS, u[0], u[1]);
      put2(my + (8 + r) * RS, u2[0], u2[1]);
      if (r < 7) {
#pragma unroll
        for (int q = 0; q < 2; ++q) { u[q] = cmul(u[q], e1[q]); u2[q] = cmul(u2[q], e1[q]); }
      }
    }
  } else if (w == 2) {
    cf e2[2], u[2], u2[2];
#pragma unroll
    for (int q = 0; q < 2; ++q) {
      e2[q] = cexp_(a2[q], bv[q], 2.f, 0.f);
      u[q] = cexp_(a2[q], bv[q], 256.f, m0[q]);
      u2[q] = cexp_(a2[q], bv[q], 384.f, m0[q]);
    }
#pragma unroll
    for (int r = 0; r < 4; ++r) {
      put2(my + (16 + r) * RS, u[0], u[1]);
      put2(my + (20 + r) * RS, u2[0], u2[1]);
      if (r < 3) {
#pragma unroll
        for (int q = 0; q < 2; ++q) { u[q] = cmul(u[q], e2[q]); u2[q] = cmul(u2[q], e2[q]); }
      }
    }
    cf e4[2] = {cmul(e2[0], e2[0]), cmul(e2[1], e2[1])};
#pragma unroll
    for (int r = 0; r < 2; ++r) {
      cf c0 = cexp_(a2[0], bv[0], 512.f + 128.f * r, m0[0]), c1 = cexp_(a2[1], bv[1], 512.f + 128.f * r, m0[1]);
      put2(my + (24 + 2 * r) * RS, c0, c1);
      put2(my + (25 + 2 * r) * RS, cmul(c0, e4[0]), cmul(c1, e4[1]));
    }
  } else {
    cf e4[2], e128[2], u[2], u2[2];
#pragma unroll
    for (int q = 0; q < 2; ++q) {
      e4[q] = cexp_(a2[q], bv[q], 4.f, 0.f);
      e128[q] = cexp_(a2[q], bv[q], 128.f, 0.f);
      u[q] = cexp_(a2[q], bv[q], 1024.f, m0[q]);
      u2[q] = cexp_(a2[q], bv[q], 1536.f, m0[q]);
    }
#pragma unroll
    for (int r = 2; r < 4; ++r) {
      cf c0 = cexp_(a2[0], bv[0], 512.f + 128.f * r, m0[0]), c1 = cexp_(a2[1], bv[1], 512.f + 128.f * r, m0[1]);
      put2(my + (24 + 2 * r) * RS, c0, c1);
      put2(my + (25 + 2 * r) * RS, cmul(c0, e4[0]), cmul(c1, e4[1]));
    }
#pragma unroll
    for (int r = 0; r < 4; ++r) {
      put2(my + (32 + r) * RS, u[0], u[1]);
      put2(my + (36 + r) * RS, u2[0], u2[1]);
#pragma unroll
      for (int q = 0; q < 2; ++q) { if (r < 3) u[q] = cmul(u[q], e128[q]); u2[q] = cmul(u2[q], e128[q]); }
    }
    put2(my + 40 * RS, u2[0], u2[1]);
  }
  __syncthreads();
  STAMP(0, 2);
  _Float16* kh = (_Float16*)(smem + 57 * RS);
  if (w < 3) {
    const char* ua = smem + (16 * w + i) * RS + g * 16;
    const char* va = smem + (41 + i) * RS + g * 16;
    f32x4 acc = {0.f, 0.f, 0.f, 0.f};
#pragma unroll
    for (int ks = 0; ks < 8; ++ks)
      acc = mfma_f16(*(const u32x4*)(ua + ks * 64), *(const u32x4*)(va + ks * 64), acc);
    int idx0, st;
    if (w == 0) { idx0 = (g < 2 ? 4 * g : 120 + 4 * g) + 8 * i; st = 1; }
    else if (w == 1) { idx0 = g < 2 ? 256 + 64 * g + 4 * i : 384 + 64 * (g - 2) + 2 * i; st = 1; }
    else { idx0 = 512 + 64 * g + i; st = 16; }
#pragma unroll
    for (int rg = 0; rg < 4; ++rg) {
      int idx = idx0 + st * rg;
      if (w == 1 && g >= 2) idx = idx0 + 32 * (rg >> 1) + (rg & 1);
      if (w == 2 && g >= 2) idx = (g == 2 && rg == 0 && i == 0) ? 640 : 644;
      kh[idx] = (_Float16)acc[rg];
    }
  }
  __syncthreads();
  STAMP(0, 3);
  const unsigned short* ku = (const unsigned short*)kh;
  const size_t kout = WS_KREV + (size_t)d * KREV * 2;
  {
    const int l0 = 2040 - 8 * tid;
    u32x4 o;
    if (w < 2) {
      const int q = l0 >> 3;
      const unsigned a = ku[384 + q], b = ku[385 + q];
      const unsigned t = (q & 1) ? b : a;
      o[0] = b | (b << 16); o[1] = b | (t << 16); o[2] = a | (a << 16); o[3] = o[2];
    } else if (w == 2) {
      const int q = l0 >> 2;
      const unsigned a = ku[256 + q], b = ku[257 + q], c = ku[258 + q];
      o[0] = c | (c << 16); o[1] = b | (b << 16); o[2] = b | (a << 16); o[3] = a | (a << 16);
    } else {
      unsigned v[8];
#pragma unroll
      for (int e = 0; e < 8; ++e) {
        const int l = l0 + 7 - e;
        const int idx = l < 256 ? l : (int)(pk_bf((float)l, 0.f) & 0xffffu) - 17024;
        v[e] = ku[idx];
      }
#pragma unroll
      for (int q = 0; q < 4; ++q) o[q] = v[2 * q] | (v[2 * q + 1] << 16);
    }
    st16(ws_rsrc(p.ws), kout + 16 * tid, o);
  }
}

__global__ NOPK __launch_bounds__(256) void k1(K1Args p) {
  __shared__ __attribute__((aligned(16))) char smem[57 * KG_RS + 1312];
  int b = blockIdx.x;
  char* stamp_ws = p.ws; (void)stamp_ws;
  STAMP(0, 0);
  if (b < NB_K) { k1_kgen(p, b, smem); STAMP(0, 7); return; }
  b -= NB_K;
  __builtin_amdgcn_s_setprio(3);
  if (b < NB_LN) { k1_ln(p, b, smem); STAMP(0, 7); return; }
  b -= NB_LN;
  k1_mod(p, b, smem);
  STAMP(0, 7);
}

constexpr int K2_CS = 4160;
constexpr int K2_KOFF = 4 * K2_CS;
constexpr int K2_XOFF = K2_CS;
constexpr int K2_CH = K2_KOFF + 4608;
DEV void k2_body(const unsigned short* __restrict__ dskip, char* __restrict__ ws,
                 const unsigned short* __restrict__ w1, const unsigned short* __restrict__ w2, char* smem_all) {
  constexpr int CS = K2_CS, KOFF = K2_KOFF;
  const int tid = threadIdx.x;
  const int lane = tid & 63;
  const int w = __builtin_amdgcn_readfirstlane(tid >> 6);
  char* stamp_ws = ws; (void)stamp_ws;
  STAMP(1, 0);
  if (blockIdx.x >= D_ / 2) {
    int blk = blockIdx.x - D_ / 2;
    __builtin_amdgcn_s_sleep(24);
    if (blk < 68) k2_retile<true>(w1, ws, WS_W1F, D_, FF, blk, smem_all);
    else if (blk < 136) k2_retile<false>(w2, ws, WS_W2F, FF, D_, blk - 68, smem_all);
    else k2_colsum(w1, ws, blk - 136, smem_all);
    STAMP(1, 7);
    return;
  }
  const int bx = blockIdx.x;
  const int d = ((bx & 7) * 40 + (bx >> 3)) * 2 + (w >> 1);
  const int hw = w & 1, th = tid & 127;
  char* smem = smem_all + (w >> 1) * K2_CH;
  const unsigned short* krev = (const unsigned short*)(ws + WS_KREV) + (size_t)d * KREV;
  const int r = lane & 15, g = lane >> 4;

  const size_t xo = ((size_t)d * L_ + 16 * th) * 2;
  const size_t xo2 = xo + (th < 127 ? 32 : 0);
  const u32x4 xa0 = *(const u32x4*)(ws + WS_XNT + xo), xa1 = *(const u32x4*)(ws + WS_XNT + xo + 16);
  const u32x2 xb = *(const u32x2*)(ws + WS_XNT + xo2);
  const u32x4 kv0 = *(const u32x4*)(krev + 8 * th), kv1 = *(const u32x4*)(krev + 1024 + 8 * th);
  const unsigned short rds = dskip[d];
  const float sa = mod_sum(ws, d), ca1 = rbf(1.f + mod_sum(ws, D_ + d)), ga = mod_sum(ws, 2 * D_ + d);
  const float kinv = ((const float*)(ws + WS_KSC))[d];
  u32x2 xres[4];
#pragma unroll
  for (int q = 0; q < 4; ++q)
    xres[q] = *(const u32x2*)(ws + WS_XT + ((size_t)d * L_ + 256 * (4 * hw + q) + 16 * r + 4 * g) * 2);
  const float dsk = __uint_as_float((unsigned)rds << 16);
  const float hb = 26.f * fabsf(ca1) + fabsf(sa);
  int ex = (int)((__float_as_uint(hb) >> 23) & 0xff);
  ex = ex < 20 ? 20 : (ex > 240 ? 240 : ex);
  const float hs = __uint_as_float((unsigned)(127 + 14 + 127 - ex) << 23);
  const float hinv = __uint_as_float((unsigned)(ex - 14) << 23);
  const float yinv = kinv * hinv;

  unsigned hh[10];
#pragma unroll
  for (int q = 0; q < 10; ++q) {
    unsigned u = q < 4 ? xa0[q] : (q < 8 ? xa1[q - 4] : xb[q - 8]);
    unsigned m = pk_bf(bf_lo(u) * ca1, bf_hi(u) * ca1);
    unsigned h = pk_bf(bf_lo(m) + sa, bf_hi(m) + sa);
    hh[q] = pk_h(bf_lo(h) * hs, bf_hi(h) * hs);
  }
  STAMP(1, 1);
  {
    char* dst = smem + (2 * th + 2) * 16;
    unsigned s1[9];
#pragma unroll
    for (int q = 0; q < 9; ++q) s1[q] = __builtin_amdgcn_alignbit(hh[q + 1], hh[q], 16);
#pragma unroll
    for (int c = 0; c < 2; ++c) {
      *(u32x4*)(dst + 16 * c) = (u32x4){hh[4 * c], hh[4 * c + 1], hh[4 * c + 2], hh[4 * c + 3]};
      *(u32x4*)(dst + 16 * c + CS) = (u32x4){s1[4 * c], s1[4 * c + 1], s1[4 * c + 2], s1[4 * c + 3]};
      *(u32x4*)(dst + 16 * c + 2 * CS) = (u32x4){hh[4 * c + 1], hh[4 * c + 2], hh[4 * c + 3], hh[4 * c + 4]};
      *(u32x4*)(dst + 16 * c + 3 * CS) = (u32x4){s1[4 * c + 1], s1[4 * c + 2], s1[4 * c + 3], s1[4 * c + 4]};
    }
    if (th == 0) {
      u32x4 z = {0u, 0u, 0u, 0u};
      *(u32x4*)(smem) = z; *(u32x4*)(smem + CS) = z; *(u32x4*)(smem + 2 * CS) = z; *(u32x4*)(smem + 3 * CS) = z;
      *(u32x4*)(smem + 16) = z;
      *(u32x4*)(smem + CS + 16) = (u32x4){0u, 0u, 0u, hh[0] << 16};
      *(u32x4*)(smem + 2 * CS + 16) = (u32x4){0u, 0u, 0u, hh[0]};
      *(u32x4*)(smem + 3 * CS + 16) = (u32x4){0u, 0u, hh[0] << 16, s1[0]};
    }
  }
  *(u32x4*)(smem + KOFF + 16 * th) = kv0;
  *(u32x4*)(smem + KOFF + 2048 + 16 * th) = kv1;
  if (th < 32) *(u32x4*)(smem + KOFF + 4096 + 16 * th) = (u32x4){0u, 0u, 0u, 0u};
  __syncthreads();
  STAMP(1, 2);

  f32x4 acc[8];
#pragma unroll
  for (int q = 0; q < 8; ++q) acc[q] = (f32x4){0.f, 0.f, 0.f, 0.f};
  {
    const int s = (r + 1) & 3;
    const unsigned ap0 = (unsigned)(size_t)(smem + s * CS + 2 * (128 * hw + 8 * g + (r + 1) - s));
    const unsigned bp0 = (unsigned)(size_t)(smem + KOFF + 2 * (2032 + 128 * hw - 16 * r + 8 * g) - 3584);
    u32x2 Al[2][8], Ah[2][8];
    u32x4 Bf[2][8];
#define K2_LOAD(buf, cc, u)                                                                                     \
    asm volatile("ds_read_b64 %0, %3 offset:%5\n\tds_read_b64 %1, %3 offset:%6\n\tds_read_b128 %2, %4 offset:%7" \
                 : "=&v"(Al[buf][u]), "=&v"(Ah[buf][u]), "=&v"(Bf[buf][u])                                        \
                 : "v"(ap0), "v"(bp0), "i"(64 * (cc) + 512 * (u)), "i"(64 * (cc) + 512 * (u) + 8),               \
                   "i"(64 * (cc) + 3584 - 512 * (u)))
#pragma unroll
    for (int u = 0; u < 8; ++u) K2_LOAD(0, 0, u);
#pragma unroll
    for (int cc = 0; cc < 4; ++cc) {
      lgkm0();
#pragma unroll
      for (int q = 0; q < 8; ++q) {
        if (cc < 3) K2_LOAD((cc + 1) & 1, cc + 1, q);
#pragma unroll
        for (int sp = 0; sp <= q; ++sp) {
          const u32x4 A = {Al[cc & 1][sp][0], Al[cc & 1][sp][1], Ah[cc & 1][sp][0], Ah[cc & 1][sp][1]};
          acc[q] = mfma_f16(A, Bf[cc & 1][q - sp], acc[q]);
        }
        __builtin_amdgcn_sched_barrier(0);
      }
    }
#undef K2_LOAD
  }
  STAMP(1, 3);
  __syncthreads();
  f32x4 keep[4];
  {
    f32x4* xw = (f32x4*)(smem + K2_XOFF + hw * 4096) + lane;
    if (hw) {
#pragma unroll
      for (int q = 0; q < 4; ++q) { xw[64 * q] = acc[q]; keep[q] = acc[4 + q]; }
    } else {
#pragma unroll
      for (int q = 0; q < 4; ++q) { xw[64 * q] = acc[4 + q]; keep[q] = acc[q]; }
    }
  }
  __syncthreads();
  {
    const f32x4* xrd = (const f32x4*)(smem + K2_XOFF + (hw ^ 1) * 4096) + lane;
    const rsrc_t rs = ws_rsrc(ws);
#pragma unroll
    for (int q = 0; q < 4; ++q) {
      const f32x4 y = keep[q] + xrd[64 * q];
      const int t = 256 * (4 * hw + q) + 16 * r + 4 * g;
      const u32x2 hv = *(const u32x2*)(smem + 2 * (t + 16));
      u32x2 o;
#pragma unroll
      for (int e = 0; e < 2; ++e) {
        const unsigned hu = hv[e];
        const h16x2 h2 = __builtin_bit_cast(h16x2, hu);
        const float h0 = (float)h2[0] * hinv, h1 = (float)h2[1] * hinv;
        unsigned yb = pk_bf(y[2 * e] * yinv, y[2 * e + 1] * yinv);
        unsigned hd = pk_bf(h0 * dsk, h1 * dsk);
        unsigned y2 = pk_bf(bf_lo(yb) + bf_lo(hd), bf_hi(yb) + bf_hi(hd));
        unsigned gy = pk_bf(bf_lo(y2) * ga, bf_hi(y2) * ga);
        o[e] = pk_bf(bf_lo(xres[q][e]) + bf_lo(gy), bf_hi(xres[q][e]) + bf_hi(gy));
      }
      st8(rs, WS_X1T + (((size_t)(t >> 5) * D_ + d) * 32 + (t & 31)) * 2, o);
    }
  }
  STAMP(1, 7);
}

__global__ NOPK __launch_bounds__(256) void k2(const unsigned short* __restrict__ dskip, char* __restrict__ ws,
                                               const unsigned short* __restrict__ w1, const unsigned short* __restrict__ w2) {
  __shared__ __attribute__((aligned(16))) char smem[2 * K2_CH];
  k2_body(dskip, ws, w1, w2, smem);
}

#define SB() __builtin_amdgcn_sched_barrier(0)
DEV u32x4 ld16_sc1(rsrc_t r, int voff, int soff) { return __builtin_amdgcn_raw_buffer_load_b128(r, voff, soff, 16); }
DEV u32x4 ld16(rsrc_t r, int voff, int soff) { return __builtin_amdgcn_raw_buffer_load_b128(r, voff, soff, 0); }
DEV u32x4 tr_frag(const char* p) {
  const u32x2 a = __builtin_bit_cast(u32x2, __builtin_amdgcn_ds_read_tr16_b64_v4i16((lds_s16x4*)p));
  const u32x2 b = __builtin_bit_cast(u32x2, __builtin_amdgcn_ds_read_tr16_b64_v4i16((lds_s16x4*)(p + 256)));
  return (u32x4){a[0], a[1], b[0], b[1]};
}

constexpr int KF_BIG = 8 * 32 * 84 * 4;
constexpr int KF_W2L = KF_BIG;
constexpr int KF_TAB = KF_W2L + 57344;
constexpr int KF_PS = KF_TAB + 320;
constexpr int KF_XR = KF_PS + 512;
constexpr int KF_LDS = KF_XR + 64 * 176;
#define W2_START 10

DEV void kf_body(char* __restrict__ ws, unsigned short* __restrict__ out, char* smem) {
  char* xs = smem;
  float* tab = (float*)(smem + KF_TAB);
  float* ps = (float*)(smem + KF_PS);
  char* xr = smem + KF_XR;
  const int tid = threadIdx.x, lane = tid & 63;
  const int w = __builtin_amdgcn_readfirstlane(tid >> 6);
  const int bx = blockIdx.x;
  const int ng = (bx >> 3) & 7, mt = (bx & 7) * 4 + (bx >> 6);
  const int g = lane >> 4, i = lane & 15;
  char* stamp_ws = ws; (void)stamp_ws;
  STAMP(2, 0);
  const rsrc_t rs_ = ws_rsrc(ws);
  unsigned* cnt = (unsigned*)(ws + WS_CNT) + mt * 64;

  const int ntl = ng < 4 ? 14 : 13;
  const int ctb = ng < 4 ? 14 * ng : 56 + 13 * (ng - 4);
  const bool two = w < ntl - 8;
  const bool full = w < 6;
  const int ct0 = ctb + (two ? 2 * w : (ntl - 8) + w);
  const int lo16 = lane * 16;
  const int w1s = (int)WS_W1F + ct0 * 1024;
  const int w2s = (int)WS_W2F + (ng * 5 * 54 + w) * 1024;

  constexpr int NW2R = 28;
  u32x4 ring[32];
  const char* bb0 = xs + (8 * g + (i >> 2)) * 64 + ((g & 1) << 5) + (i & 3) * 8;
  const char* bb1 = xs + (8 * g + (i >> 2)) * 64 + (((g & 1) ^ 1) << 5) + (i & 3) * 8;
  auto w2_off = [&](int e) __attribute__((always_inline)) {
    const int sn = (e / 5 == 6 && !full) ? 5 : e / 5;
    return w2s + ((e % 5) * 54 + 8 * sn) * 1024;
  };

  auto gemm1 = [&](auto two_tag) __attribute__((always_inline)) {
    constexpr bool TWO = decltype(two_tag)::value;
    u32x4 t[10];
#pragma unroll
    for (int it = 0; it < 10; ++it) t[it] = ld16(rs_, tid * 16, (int)WS_X1T + mt * 81920 + 8192 * it);
#define W1_PRE 10
#define W1_EARLY 3
#pragma unroll
    for (int k = 0; k < W1_EARLY; ++k) {
      ring[2 * k] = ld16(rs_, lo16, w1s + k * 108 * 1024);
      if (TWO) ring[2 * k + 1] = ld16(rs_, lo16, w1s + (k * 108 + 1) * 1024);
    }
    SB();
    __builtin_amdgcn_s_barrier();
    SB();
#pragma unroll
    for (int k = W1_EARLY; k < W1_PRE; ++k) {
      ring[2 * k] = ld16(rs_, lo16, w1s + k * 108 * 1024);
      if (TWO) ring[2 * k + 1] = ld16(rs_, lo16, w1s + (k * 108 + 1) * 1024);
    }
    SB();
#pragma unroll
    for (int it = 0; it < 10; ++it) {
      const int c = tid + 512 * it;
      const int row = c >> 2;
      *(u32x4*)(xs + row * 64 + (((c & 3) ^ ((row >> 2) & 2)) << 4)) = t[it];
    }
    SB();
    f32x4 csv[2], b1v[2];
#pragma unroll
    for (int c = 0; c < (TWO ? 2 : 1); ++c) {
      const int f0 = 16 * (ct0 + c) + 4 * g;
      csv[c] = *(const f32x4*)((const float*)(ws + WS_CSB) + f0);
      b1v[c] = *(const f32x4*)((const float*)(ws + WS_CSB) + FF + f0);
    }
    SB();
    __syncthreads();
    STAMP(2, 1);
    {
      const int kb = (80 * ng) >> 5, tq = w & 3;
#pragma unroll
      for (int u = 0; u < 2; ++u) {
        const int kk = min(kb + 2 * (w >> 2) + u, 19);
        const u32x4 f = tr_frag(((tq & 1) ? bb1 : bb0) + (tq >> 1) * 40960 + kk * 2048);
        const unsigned dl = (unsigned)(32 * kk + 8 * g - 80 * ng);
        if (dl < 80u) *(u32x4*)(xr + (tq * 16 + i) * 176 + dl * 2) = f;
      }
    }
    const int hs = TWO ? 0 : (w & 1);
    const char* ba[4] = {bb0 + hs * 40960, bb1 + hs * 40960, bb0 + (hs ^ 1) * 40960, bb1 + (hs ^ 1) * 40960};
    const u32x4 ones = {0x3f803f80u, 0x3f803f80u, 0x3f803f80u, 0x3f803f80u};
    f32x4 acc[2][4];
#pragma unroll
    for (int c = 0; c < 2; ++c)
#pragma unroll
      for (int q = 0; q < 4; ++q) acc[c][q] = (f32x4){0.f, 0.f, 0.f, 0.f};
    u32x4 bn[2][4];
#pragma unroll
    for (int q = 0; q < 4; ++q) bn[0][q] = tr_frag(ba[q]);
#pragma unroll
    for (int q = 0; q < 4; ++q) bn[1][q] = tr_frag(ba[q] + 2048);
#pragma unroll
    for (int kk = 0; kk < 20; ++kk) {
      u32x4 b[4];
#pragma unroll
      for (int q = 0; q < 4; ++q) b[q] = bn[kk & 1][q];
      SB();
#pragma unroll
      for (int q = 0; q < 4; ++q) acc[0][q] = mfma_bf16(ring[(2 * kk) % 32], b[q], acc[0][q]);
      if (TWO) {
#pragma unroll
        for (int q = 0; q < 4; ++q) acc[1][q] = mfma_bf16(ring[(2 * kk + 1) % 32], b[q], acc[1][q]);
      } else {
        acc[1][0] = mfma_bf16(ones, b[0], acc[1][0]); acc[1][1] = mfma_bf16(b[0], b[0], acc[1][1]);
        acc[1][2] = mfma_bf16(ones, b[1], acc[1][2]); acc[1][3] = mfma_bf16(b[1], b[1], acc[1][3]);
      }
      SB();
      if (kk < 18) {
#pragma unroll
        for (int q = 0; q < 4; ++q) bn[kk & 1][q] = tr_frag(ba[q] + (kk + 2) * 2048);
      }
      if (kk + W1_PRE < 20) {
        const int kn = kk + W1_PRE;
        ring[(2 * kn) % 32] = ld16(rs_, lo16, w1s + kn * 108 * 1024);
        if (TWO) ring[(2 * kn + 1) % 32] = ld16(rs_, lo16, w1s + (kn * 108 + 1) * 1024);
      }
      if (kk >= W2_START) {
#pragma unroll
        for (int u = 0; u < 2; ++u) {
          const int e = 2 * (kk - W2_START) + u;
          if (e < 24) ring[8 + e] = ld16(rs_, lo16, w2_off(e));
        }
      }
      SB();
#define LOCK_N 0
#define LOCK_M 1
      if (kk < LOCK_N && (kk % LOCK_M) == LOCK_M - 1) { __builtin_amdgcn_s_barrier(); SB(); }
    }
    if (!TWO) {
#pragma unroll
      for (int h = 0; h < 2; ++h) {
        const f32x4 aq = acc[1][2 * h + 1];
        const float dq = (i & 2) ? ((i & 1) ? aq[3] : aq[2]) : ((i & 1) ? aq[1] : aq[0]);
        const int tk = (2 * hs + h) * 16 + i;
        if (g == (i >> 2)) {
          const float mean = acc[1][2 * h][0] * (1.f / D_);
          const float var = fmaxf(dq * (1.f / D_) - mean * mean, 0.f);
          const float sd = rbf(sqrtf(rbf(rbf(var) + 1e-5f)));
          const float rs = 1.f / sd;
          ps[tk] = rs;
          ps[64 + tk] = -rbf(mean) * rs;
        }
      }
    }
    __syncthreads();
    STAMP(2, 2);
#pragma unroll
    for (int q = 0; q < 4; ++q) {
      const int tq = q ^ (2 * hs);
      const float rs = ps[tq * 16 + i], nm = ps[64 + tq * 16 + i];
#pragma unroll
      for (int c = 0; c < (TWO ? 2 : 1); ++c) {
        const int ct = ct0 + c;
        float v[4];
#pragma unroll
        for (int r = 0; r < 4; ++r) v[r] = fmaxf(fmaf(acc[c][q][r], rs, fmaf(nm, csv[c][r], b1v[c][r])), 0.f);
        u32x2 o = {pk_bf(v[0], v[1]), pk_bf(v[2], v[3])};
        const size_t off = (((size_t)(4 * mt + tq) * 54 + (ct >> 1)) * 64 + i + 16 * (2 * (ct & 1) + (g >> 1))) * 8 + 4 * (g & 1);
        st8(rs_, WS_H1F + off * 2, o);
      }
    }
    SB();
    constexpr int E0 = (20 - W2_START) * 2 < 24 ? (20 - W2_START) * 2 : 24;
#define W2_MID 4
#pragma unroll
    for (int e = E0; e < E0 + W2_MID; ++e) ring[(8 + e) % 32] = ld16(rs_, lo16, w2_off(e));
    SB();
    asm volatile("s_waitcnt vmcnt(%0)" :: "n"(W2_MID) : "memory");
    if (lane == 0) __hip_atomic_fetch_add(cnt, 1u, __ATOMIC_RELAXED, __HIP_MEMORY_SCOPE_AGENT);
    SB();
#pragma unroll
    for (int e = E0 + W2_MID; e < NW2R; ++e) ring[(8 + e) % 32] = ld16(rs_, lo16, w2_off(e));
    SB();
  };
  if (two) gemm1(std::true_type{});
  else gemm1(std::false_type{});
  STAMP(2, 3);
#pragma unroll
  for (int e = NW2R; e < 35; ++e)
    __builtin_amdgcn_global_load_lds((const __attribute__((address_space(1))) void*)(ws + w2_off(e) + lo16),
                                     (__attribute__((address_space(3))) void*)(smem + KF_W2L + (w * 7 + (e - NW2R)) * 1024), 16, 0, 0);
  if (tid >= 64 && tid < 144) tab[tid - 64] = mod_sum(ws, 5 * D_ + 80 * ng + tid - 64);
  if (w == 0) {
    for (unsigned spin = 0; spin < (1u << 22); ++spin) {
      if (__hip_atomic_load(cnt, __ATOMIC_RELAXED, __HIP_MEMORY_SCOPE_AGENT) >= 64u) break;
      __builtin_amdgcn_s_sleep(1);
    }
  }
  asm volatile("s_waitcnt vmcnt(0)" ::: "memory");
  __syncthreads();
  STAMP(2, 4);

  const int h1s = (int)WS_H1F + (mt * 4 * 54 + w) * 1024;
  u32x4 hb[2][7][2];
  auto load_h1 = [&](int H, int s0, int s1) __attribute__((always_inline)) {
#pragma unroll
    for (int s = s0; s < s1; ++s) {
      const int sn = (s == 6 && !full) ? 5 : s;
#pragma unroll
      for (int q = 0; q < 2; ++q) hb[H][s][q] = ld16_sc1(rs_, lo16, h1s + ((2 * H + q) * 54 + 8 * sn) * 1024);
    }
    SB();
  };
  load_h1(0, 0, 7);
  load_h1(1, 0, 3);
  constexpr int RS = 84;
  float* red = (float*)smem;
  float* rb_ = red + (w * 32 + i) * RS + 4 * g;
#pragma unroll
  for (int H = 0; H < 2; ++H) {
    f32x4 ac2[5][2];
#pragma unroll
    for (int c = 0; c < 5; ++c) { ac2[c][0] = (f32x4){0.f, 0.f, 0.f, 0.f}; ac2[c][1] = ac2[c][0]; }
#pragma unroll
    for (int s = 0; s < 7; ++s) {
      if (s < 6 || full) {
#pragma unroll
        for (int c = 0; c < 5; ++c) {
          const int e = 5 * s + c;
          const u32x4 a = e < NW2R ? ring[(8 + (e < NW2R ? e : 0)) % 32]
                                   : *(const u32x4*)(smem + KF_W2L + (w * 7 + (e - NW2R)) * 1024 + lo16);
          ac2[c][0] = mfma_bf16(a, hb[H][s][0], ac2[c][0]);
          ac2[c][1] = mfma_bf16(a, hb[H][s][1], ac2[c][1]);
        }
      }
      if (H == 0 && s == 3) { SB(); load_h1(1, 3, 7); }
    }
    SB();
    if (H == 0) STAMP(2, 5);
    if (H == 1) { STAMP(2, 6); __syncthreads(); }
#pragma unroll
    for (int c = 0; c < 5; ++c) {
      *(f32x4*)(rb_ + 16 * c) = ac2[c][0];
      *(f32x4*)(rb_ + 16 * RS + 16 * c) = ac2[c][1];
    }
    __syncthreads();
#pragma unroll
    for (int it = 0; it < 2; ++it) {
      const int e = tid + 512 * it;
      if (e < 640) {
        const int tk = e / 20, q4 = e % 20;
        f32x4 s = *(const f32x4*)(red + tk * RS + 4 * q4);
#pragma unroll
        for (int ww = 1; ww < 8; ++ww) s += *(const f32x4*)(red + (ww * 32 + tk) * RS + 4 * q4);
        const u32x2 xv2 = *(const u32x2*)(xr + (32 * H + tk) * 176 + q4 * 8);
        const f32x4 gb = *(const f32x4*)(tab + 4 * q4);
        u32x2 o;
#pragma unroll
        for (int q = 0; q < 2; ++q) {
          unsigned f = pk_bf(s[2 * q], s[2 * q + 1]);
          unsigned gf = pk_bf(bf_lo(f) * gb[2 * q], bf_hi(f) * gb[2 * q + 1]);
          o[q] = pk_bf(bf_lo(xv2[q]) + bf_lo(gf), bf_hi(xv2[q]) + bf_hi(gf));
        }
        *(u32x2*)(out + (size_t)(mt * 64 + 32 * H + tk) * D_ + ng * 80 + 4 * q4) = o;
      }
    }
    SB();
  }
  STAMP(2, 7);
}

__global__ NOPK __launch_bounds__(512, 2) void kf(char* __restrict__ ws, unsigned short* __restrict__ out) {
  __shared__ __attribute__((aligned(16))) char smem[KF_LDS];
  kf_body(ws, out, smem);
}

extern "C" void kernel_launch(void* const* d_in, const int* in_sizes, int n_in,
                              void* d_out, int out_size, void* d_ws, size_t ws_size,
                              hipStream_t stream) {
  K1Args a;
  a.x = (const unsigned short*)d_in[0];
  a.are = (const unsigned short*)d_in[1];
  a.aim = (const unsigned short*)d_in[2];
  a.bre = (const unsigned short*)d_in[3];
  a.bim = (const unsigned short*)d_in[4];
  a.cre = (const unsigned short*)d_in[5];
  a.cim = (const unsigned short*)d_in[6];
  a.logdt = (const unsigned short*)d_in[7];
  a.w1 = (const unsigned short*)d_in[10];
  a.w2 = (const unsigned short*)d_in[11];
  a.c = (const unsigned short*)d_in[12];
  a.wmod = (const unsigned short*)d_in[13];
  a.ws = (char*)d_ws;
  k1<<<NB_MOD + NB_LN + NB_K, 256, 0, stream>>>(a);
  k2<<<D_ / 2 + NB_W, 256, 0, stream>>>((const unsigned short*)d_in[8], (char*)d_ws, a.w1, a.w2);
  kf<<<256, 512, 0, stream>>>((char*)d_ws, (unsigned short*)d_out);
}
```

```cpp
#define PF 1
#include <hip/hip_runtime.h>
#include <hip/hip_bf16.h>
#include <stdint.h>
#include <type_traits>

typedef __attribute__((ext_vector_type(8))) __bf16 bf16x8;
typedef __attribute__((ext_vector_type(2))) __bf16 bf16x2;
typedef __attribute__((ext_vector_type(4))) float f32x4;
typedef __attribute__((ext_vector_type(2))) float f32x2;
typedef __attribute__((ext_vector_type(4))) unsigned u32x4;
typedef __attribute__((ext_vector_type(2))) unsigned u32x2;
typedef __attribute__((ext_vector_type(8))) _Float16 h16x8;
typedef __attribute__((ext_vector_type(2))) _Float16 h16x2;
typedef __attribute__((ext_vector_type(4))) short s16x4;
typedef __attribute__((address_space(3))) s16x4 lds_s16x4;

#if defined(__HIP_DEVICE_COMPILE__)
#define NOPK __attribute__((target("no-packed-fp32-ops")))
#else
#define NOPK
#endif
#define DEV __device__ __forceinline__

constexpr int L_ = 2048;
constexpr int D_ = 640;
constexpr int NS = 128;
constexpr int FF = 1728;
constexpr int KREV = 2304;

constexpr size_t WS_KSC  = 8192;
constexpr size_t WS_CNT  = 16384;
constexpr size_t WS_CSB  = 32768;
constexpr size_t WS_MOD  = 65536;
constexpr size_t WS_XNT  = 196608;
constexpr size_t WS_XT   = WS_XNT + (size_t)D_ * L_ * 2;
constexpr size_t WS_KREV = WS_XT + (size_t)D_ * L_ * 2;
constexpr size_t WS_W1F  = WS_KREV + (size_t)D_ * 2 * KREV * 2;
constexpr size_t WS_W2F  = WS_W1F + (size_t)D_ * FF * 2;
constexpr size_t WS_X1T  = WS_W2F + (size_t)D_ * FF * 2;
constexpr size_t WS_X1   = WS_X1T + (size_t)D_ * L_ * 2;
constexpr size_t WS_H1F  = WS_X1 + (size_t)D_ * L_ * 2;

#define STAMP(kid, k) do {} while (0)

DEV float bf_lo(unsigned u) { return __uint_as_float(u << 16); }
DEV float bf_hi(unsigned u) { return __uint_as_float(u & 0xffff0000u); }
DEV unsigned pk_bf(float a, float b) {
  f32x2 v = {a, b};
  bf16x2 r = __builtin_convertvector(v, bf16x2);
  return __builtin_bit_cast(unsigned, r);
}
DEV float rbf(float a) { return bf_lo(pk_bf(a, 0.f)); }
DEV float ldbf(const unsigned short* p) { return __uint_as_float(((unsigned)*p) << 16); }

typedef __amdgpu_buffer_rsrc_t rsrc_t;
DEV rsrc_t ws_rsrc(char* ws) { return __builtin_amdgcn_make_buffer_rsrc((void*)ws, 0, 0x7fffffff, 0x00020000); }
#define ST_AUX 16
DEV void st16(rsrc_t r, size_t off, u32x4 v) { __builtin_amdgcn_raw_buffer_store_b128(v, r, (int)off, 0, ST_AUX); }
#define LN_AUX 0
DEV void st16ln(rsrc_t r, size_t off, u32x4 v) { __builtin_amdgcn_raw_buffer_store_b128(v, r, (int)off, 0, LN_AUX); }
DEV void st8(rsrc_t r, size_t off, u32x2 v) { __builtin_amdgcn_raw_buffer_store_b64(v, r, (int)off, 0, 16); }

DEV u32x2 tr_read(unsigned lds_addr) {
  u32x2 r;
  asm volatile("ds_read_b64_tr_b16 %0, %1" : "=v"(r) : "v"(lds_addr) : "memory");
  return r;
}
DEV void lgkm0() { asm volatile("s_waitcnt lgkmcnt(0)" ::: "memory"); __builtin_amdgcn_sched_barrier(0); }

DEV f32x4 mfma_bf16(u32x4 a, u32x4 b, f32x4 c) {
  return __builtin_amdgcn_mfma_f32_16x16x32_bf16(__builtin_bit_cast(bf16x8, a), __builtin_bit_cast(bf16x8, b), c, 0, 0, 0);
}

constexpr int NB_MOD = 120, NB_LN = 256, NB_W = 136 + 54, NB_K = 640;

struct K1Args {
  const unsigned short *x, *are, *aim, *bre, *bim, *cre, *cim, *logdt, *w1, *w2, *c, *wmod;
  const unsigned short *dskip, *out;
  char* ws;
};

constexpr int NB_PF = 8;
DEV void k1_prefetch(const K1Args& p, int b) {
  const int tid = threadIdx.x;
  unsigned long long pc;
  int o0, o1;
  asm volatile("s_getpc_b64 %0\n.Lpf_here%=:\n\ts_mov_b32 %1, _Z2k16K1Args - .Lpf_here%=\n\ts_mov_b32 %2, _Z5kend_v - .Lpf_here%="
               : "=s"(pc), "=s"(o0), "=s"(o1));
  const char* c0 = (const char*)(pc + (long long)o0);
  long n = o1 - o0;
  n = n < 0 ? 0 : (n > 98304 ? 98304 : n);
  unsigned acc = 0;
  {
    unsigned v[3];
#pragma unroll
    for (int it = 0; it < 3; ++it) {
      const long o = (long)tid * 128 + it * 32768;
      v[it] = o < n ? *(const __attribute__((address_space(1))) unsigned*)(c0 + o) : 0u;
    }
    acc = v[0] ^ v[1] ^ v[2];
  }
  asm volatile("" :: "v"(acc));
}

DEV void k1_mod(const K1Args& p, int b, char* smem) {
  const int tid = threadIdx.x;
  const int rg = b & 7, cgp = b >> 3;
  const int c32 = tid & 31, r8 = tid >> 5;
  float* sil = (float*)smem;
  float* red = (float*)(smem + 512);
  u32x4 w[10];
#pragma unroll
  for (int it = 0; it < 10; ++it)
    w[it] = *(const u32x4*)(p.wmod + (size_t)(rg * 80 + r8 + 8 * it) * 3840 + 256 * cgp + 8 * c32);
  if (tid < 80) {
    float cv = ldbf(p.c + rg * 80 + tid);
    sil[tid] = rbf(cv / (1.f + expf(-cv)));
  }
  if (tid == 128 && b < 64) ((unsigned*)(p.ws + WS_CNT))[b * 32] = 0u;
  __syncthreads();
  float acc[8];
#pragma unroll
  for (int j = 0; j < 8; ++j) acc[j] = 0.f;
#pragma unroll
  for (int it = 0; it < 10; ++it) {
    float s = sil[r8 + 8 * it];
#pragma unroll
    for (int q = 0; q < 4; ++q) {
      acc[2 * q] += s * bf_lo(w[it][q]);
      acc[2 * q + 1] += s * bf_hi(w[it][q]);
    }
  }
#pragma unroll
  for (int j = 0; j < 8; ++j) acc[j] += __shfl_xor(acc[j], 32, 64);
  if ((tid & 63) < 32) {
    *(f32x4*)(red + (tid >> 6) * 256 + 8 * c32) = (f32x4){acc[0], acc[1], acc[2], acc[3]};
    *(f32x4*)(red + (tid >> 6) * 256 + 8 * c32 + 4) = (f32x4){acc[4], acc[5], acc[6], acc[7]};
  }
  __syncthreads();
  ((float*)(p.ws + WS_MOD))[rg * 3840 + 256 * cgp + tid] = (red[tid] + red[256 + tid]) + (red[512 + tid] + red[768 + tid]);
}
DEV float mod_sum(const char* ws, int idx) {
  const float* mp = (const float*)(ws + WS_MOD) + idx;
  float v[8];
#pragma unroll
  for (int r = 0; r < 8; ++r) v[r] = mp[r * 3840];
  return rbf(((v[0] + v[1]) + (v[2] + v[3])) + ((v[4] + v[5]) + (v[6] + v[7])));
}
DEV f32x4 mod_sum4(const char* ws, int idx) {
  const float* mp = (const float*)(ws + WS_MOD) + idx;
  f32x4 v[8];
#pragma unroll
  for (int r = 0; r < 8; ++r) v[r] = *(const f32x4*)(mp + r * 3840);
  f32x4 t = ((v[0] + v[1]) + (v[2] + v[3])) + ((v[4] + v[5]) + (v[6] + v[7]));
  return (f32x4){rbf(t[0]), rbf(t[1]), rbf(t[2]), rbf(t[3])};
}

DEV void k1_ln(const K1Args& p, int b, char* smem) {
  constexpr int STR = 704;
  const int tid = threadIdx.x;
  const int tok = tid >> 3, sub = tid & 7;
  const int tile = (b & 7) * 8 + ((b >> 3) & 7), pass = b >> 6;
  const int hf = pass & 1;
  const bool norm = pass < 2;
  const int t0 = tile * 32;
  const unsigned short* xr = p.x + (size_t)(t0 + tok) * D_ + 8 * sub;
  u32x4 xv[5], xo[5];
#pragma unroll
  for (int m = 0; m < 5; ++m) xv[m] = *(const u32x4*)(xr + 320 * hf + 64 * m);
  if (norm) {
#pragma unroll
    for (int m = 0; m < 5; ++m) xo[m] = *(const u32x4*)(xr + 320 * (hf ^ 1) + 64 * m);
    float s = 0.f, v = 0.f;
    const bf16x2 one2 = __builtin_bit_cast(bf16x2, 0x3f803f80u);
#pragma unroll
    for (int m = 0; m < 10; ++m)
#pragma unroll
      for (int q = 0; q < 4; ++q) {
        const unsigned u = m < 5 ? xv[m][q] : xo[m - 5][q];
        const bf16x2 x2 = __builtin_bit_cast(bf16x2, u);
        s = __builtin_amdgcn_fdot2_f32_bf16(x2, one2, s, false);
        v = __builtin_amdgcn_fdot2_f32_bf16(x2, x2, v, false);
      }
#pragma unroll
    for (int o = 1; o < 8; o <<= 1) { s += __shfl_xor(s, o, 64); v += __shfl_xor(v, o, 64); }
    const float mean = s * (1.f / D_);
    const float var = fmaxf(v * (1.f / D_) - mean * mean, 0.f);
    const float sd = rbf(sqrtf(rbf(rbf(var) + 1e-5f)));
    const float rs = 1.f / sd;
    const float mb = rbf(mean);
#pragma unroll
    for (int m = 0; m < 5; ++m)
#pragma unroll
      for (int q = 0; q < 4; ++q) {
        unsigned d2 = pk_bf(bf_lo(xv[m][q]) - mb, bf_hi(xv[m][q]) - mb);
        xv[m][q] = pk_bf(bf_lo(d2) * rs, bf_hi(d2) * rs);
      }
  }
  const int lane = tid & 63, w = tid >> 6;
  const int g = lane >> 4, i = lane & 15;
  const rsrc_t rsrc = ws_rsrc(p.ws);
#pragma unroll
  for (int m = 0; m < 5; ++m) *(u32x4*)(smem + tok * STR + (sub + 8 * m) * 16) = xv[m];
  __syncthreads();
  const size_t dsto = (norm ? WS_XNT : WS_XT) + ((size_t)320 * hf * L_ + t0) * 2;
#pragma unroll
  for (int it = 0; it < 5; ++it) {
    const int cb = w * 5 + it;
    const char* a = smem + (8 * g + (i >> 2)) * STR + cb * 32 + (i & 3) * 8;
    const u32x2 ra = __builtin_bit_cast(u32x2, __builtin_amdgcn_ds_read_tr16_b64_v4i16((lds_s16x4*)a));
    const u32x2 rb = __builtin_bit_cast(u32x2, __builtin_amdgcn_ds_read_tr16_b64_v4i16((lds_s16x4*)(a + 4 * STR)));
    st16ln(rsrc, dsto + (size_t)(cb * 16 + i) * (L_ * 2) + 16 * g, (u32x4){ra[0], ra[1], rb[0], rb[1]});
  }
}

template <bool SCALE>
DEV void k2_retile(const unsigned short* W, char* ws, size_t outo, int K, int N, int blk, char* smem) {
  const int tid = threadIdx.x;
  const int nct = N / 16, nfr = nct * (K / 32);
  const int f = min(blk * 32 + (tid >> 3), nfr - 1);
  const int kk = f / nct, ct = f % nct;
  const int g = (tid >> 1) & 3, h = tid & 1;
  u32x4 r[8];
#pragma unroll
  for (int j = 0; j < 8; ++j) r[j] = *(const u32x4*)(W + (size_t)(32 * kk + 8 * g + j) * N + 16 * ct + 8 * h);
  if (SCALE) {
    const f32x4 cb0 = mod_sum4(ws, 4 * D_ + 32 * kk + 8 * g), cb1 = mod_sum4(ws, 4 * D_ + 32 * kk + 8 * g + 4);
#pragma unroll
    for (int j = 0; j < 8; ++j) {
      const float c1 = rbf(1.f + (j < 4 ? cb0[j & 3] : cb1[j & 3]));
#pragma unroll
      for (int q = 0; q < 4; ++q) r[j][q] = pk_bf(bf_lo(r[j][q]) * c1, bf_hi(r[j][q]) * c1);
    }
  }
  char* o = smem + (tid >> 3) * 1024 + (16 * g + 8 * h) * 16;
#pragma unroll
  for (int q = 0; q < 4; ++q) {
    u32x4 e, f2;
#pragma unroll
    for (int pp = 0; pp < 4; ++pp) {
      e[pp] = __builtin_amdgcn_perm(r[2 * pp + 1][q], r[2 * pp][q], 0x05040100u);
      f2[pp] = __builtin_amdgcn_perm(r[2 * pp + 1][q], r[2 * pp][q], 0x07060302u);
    }
    *(u32x4*)(o + (2 * q) * 16) = e;
    *(u32x4*)(o + (2 * q + 1) * 16) = f2;
  }
  __syncthreads();
  const rsrc_t rs = ws_rsrc(ws);
  const int lane = tid & 63, w = tid >> 6;
#pragma unroll
  for (int it = 0; it < 8; ++it) {
    const int fl = w * 8 + it, fg = blk * 32 + fl;
    if (fg < nfr) {
      const int kk2 = fg / nct, ct2 = fg % nct;
      const int fo = SCALE ? fg : ct2 * (K / 32) + kk2;
      st16(rs, outo + ((size_t)fo * 64 + lane) * 16, *(const u32x4*)(smem + fl * 1024 + lane * 16));
    }
  }
}

DEV void k2_colsum(const unsigned short* W1, char* ws, int b, char* smem) {
  const int tid = threadIdx.x;
  const int hc = tid & 3, rg = tid >> 2;
  float* red = (float*)smem;
  float* tab = (float*)(smem + 1024);
  u32x4 w[10];
#pragma unroll
  for (int it = 0; it < 10; ++it) w[it] = *(const u32x4*)(W1 + (size_t)(rg + 64 * it) * FF + 32 * b + 8 * hc);
  *(f32x4*)(tab + 4 * tid) = mod_sum4(ws, 3 * D_ + 4 * tid);
  if (tid < 64) *(f32x4*)(tab + 1024 + 4 * tid) = mod_sum4(ws, 3 * D_ + 1024 + 4 * tid);
  __syncthreads();
  float ac[8], ab[8];
#pragma unroll
  for (int j = 0; j < 8; ++j) { ac[j] = 0.f; ab[j] = 0.f; }
#pragma unroll
  for (int it = 0; it < 10; ++it) {
    const float c1 = rbf(1.f + tab[D_ + rg + 64 * it]), sv = tab[rg + 64 * it];
#pragma unroll
    for (int q = 0; q < 4; ++q) {
      const float w0 = bf_lo(w[it][q]), w1 = bf_hi(w[it][q]);
      const unsigned sc = pk_bf(w0 * c1, w1 * c1);
      ac[2 * q] += bf_lo(sc); ac[2 * q + 1] += bf_hi(sc);
      ab[2 * q] += sv * w0; ab[2 * q + 1] += sv * w1;
    }
  }
#pragma unroll
  for (int o = 4; o < 64; o <<= 1)
#pragma unroll
    for (int j = 0; j < 8; ++j) { ac[j] += __shfl_xor(ac[j], o, 64); ab[j] += __shfl_xor(ab[j], o, 64); }
  if ((tid & 63) < 4) {
#pragma unroll
    for (int j = 0; j < 8; ++j) {
      red[(tid >> 6) * 64 + hc * 8 + j] = ac[j];
      red[(tid >> 6) * 64 + 32 + hc * 8 + j] = ab[j];
    }
  }
  __syncthreads();
  if (tid < 64) {
    const float s = red[tid] + red[64 + tid] + red[128 + tid] + red[192 + tid];
    ((float*)(ws + WS_CSB))[(tid >> 5) * FF + 32 * b + (tid & 31)] = s;
  }
}

struct cf { float re, im; };
DEV cf cmul(cf a, cf b) {
  cf r;
  r.re = a.re * b.re - a.im * b.im;
  r.im = a.re * b.im + a.im * b.re;
  return r;
}
DEV cf cexp_(float a2, float br, float m, float m0) {
  float mag = __builtin_amdgcn_exp2f(a2 * (m - m0));
  float ph = __builtin_amdgcn_fractf(br * m);
  cf r; r.re = mag * __builtin_amdgcn_cosf(ph); r.im = mag * __builtin_amdgcn_sinf(ph);
  return r;
}
DEV unsigned pk_h(float a, float b) {
  f32x2 v = {a, b};
  h16x2 r = __builtin_convertvector(v, h16x2);
  return __builtin_bit_cast(unsigned, r);
}
DEV f32x4 mfma_f16(u32x4 a, u32x4 b, f32x4 c) {
  return __builtin_amdgcn_mfma_f32_16x16x32_f16(__builtin_bit_cast(h16x8, a), __builtin_bit_cast(h16x8, b), c, 0, 0, 0);
}
DEV void put2(char* p, cf a, cf b) {
  u32x2 o = {pk_h(a.re, a.im), pk_h(b.re, b.im)};
  *(u32x2*)p = o;
}

constexpr int KG_RS = 544;
DEV void k1_kgen(const K1Args& p, int d, char* smem) {
  constexpr int RS = KG_RS;
  const int tid = threadIdx.x;
  const int lane = tid & 63;
  const int w = __builtin_amdgcn_readfirstlane(tid >> 6);
  const int g = lane >> 4, i = lane & 15;
  char* stamp_ws = p.ws; (void)stamp_ws;
  const size_t po = (size_t)d * NS + 2 * lane;
  const unsigned rar = *(const unsigned*)(p.are + po), rai = *(const unsigned*)(p.aim + po);
  const unsigned rbr = *(const unsigned*)(p.bre + po), rbi = *(const unsigned*)(p.bim + po);
  const unsigned rcr = *(const unsigned*)(p.cre + po), rci = *(const unsigned*)(p.cim + po);
  const float dt = rbf(expf(ldbf(p.logdt + d)));
  STAMP(0, 1);
  float a2[2], bv[2], m0[2];
  a2[0] = (dt * bf_lo(rar)) * 1.44269504088896341f; a2[1] = (dt * bf_hi(rar)) * 1.44269504088896341f;
  bv[0] = (dt * bf_lo(rai)) * 0.15915494309189534f; bv[1] = (dt * bf_hi(rai)) * 0.15915494309189534f;
  m0[0] = a2[0] > 0.f ? 2048.f : 0.f; m0[1] = a2[1] > 0.f ? 2048.f : 0.f;
  char* my = smem + lane * 8;
  if (w == 0) {
    cf v[2], v8[2], e8[2];
    float bound = 0.f;
#pragma unroll
    for (int q = 0; q < 2; ++q) {
      const float dbr = dt * (q ? bf_hi(rbr) : bf_lo(rbr)), dbi = dt * (q ? bf_hi(rbi) : bf_lo(rbi));
      const float cr = q ? bf_hi(rcr) : bf_lo(rcr), ci = q ? bf_hi(rci) : bf_lo(rci);
      const float gsh = __builtin_amdgcn_exp2f(a2[q] * m0[q]);
      v[q].re = (cr * dbr - ci * dbi) * gsh; v[q].im = -(cr * dbi + ci * dbr) * gsh;
      bound += (fabsf(v[q].re) + fabsf(v[q].im)) * __builtin_amdgcn_exp2f(fmaxf(a2[q], 0.f) * 120.f);
      e8[q] = cexp_(a2[q], -bv[q], 8.f, 0.f);
      v8[q] = cexp_(a2[q], -bv[q], 64.f, 0.f);
    }
#pragma unroll
    for (int o = 1; o < 64; o <<= 1) bound += __shfl_xor(bound, o, 64);
    int ex = (int)((__float_as_uint(bound) >> 23) & 0xff);
    ex = ex < 20 ? 20 : (ex > 240 ? 240 : ex);
    const float sv = __uint_as_float((unsigned)(127 + 13 + 127 - ex) << 23);
    if (lane == 0) ((float*)(p.ws + WS_KSC))[d] = __uint_as_float((unsigned)(ex - 13) << 23);
#pragma unroll
    for (int q = 0; q < 2; ++q) { v[q].re *= sv; v[q].im *= sv; v8[q] = cmul(v[q], v8[q]); }
#pragma unroll
    for (int j = 0; j < 8; ++j) {
      put2(my + (41 + j) * RS, v[0], v[1]);
      put2(my + (49 + j) * RS, v8[0], v8[1]);
      if (j < 7) {
#pragma unroll
        for (int q = 0; q < 2; ++q) { v[q] = cmul(v[q], e8[q]); v8[q] = cmul(v8[q], e8[q]); }
      }
    }
  } else if (w == 1) {
    cf e1[2], u[2], u2[2];
#pragma unroll
    for (int q = 0; q < 2; ++q) {
      e1[q] = cexp_(a2[q], bv[q], 1.f, 0.f);
      u[q].re = __builtin_amdgcn_exp2f(-a2[q] * m0[q]); u[q].im = 0.f;
      u2[q] = cexp_(a2[q], bv[q], 128.f, m0[q]);
    }
#pragma unroll
    for (int r = 0; r < 8; ++r) {
      put2(my + r * RS, u[0], u[1]);
      put2(my + (8 + r) * RS, u2[0], u2[1]);
      if (r < 7) {
#pragma unroll
        for (int q = 0; q < 2; ++q) { u[q] = cmul(u[q], e1[q]); u2[q] = cmul(u2[q], e1[q]); }
      }
    }
  } else if (w == 2) {
    cf e2[2], u[2], u2[2];
#pragma unroll
    for (int q = 0; q < 2; ++q) {
      e2[q] = cexp_(a2[q], bv[q], 2.f, 0.f);
      u[q] = cexp_(a2[q], bv[q], 256.f, m0[q]);
      u2[q] = cexp_(a2[q], bv[q], 384.f, m0[q]);
    }
#pragma unroll
    for (int r = 0; r < 4; ++r) {
      put2(my + (16 + r) * RS, u[0], u[1]);
      put2(my + (20 + r) * RS, u2[0], u2[1]);
      if (r < 3) {
#pragma unroll
        for (int q = 0; q < 2; ++q) { u[q] = cmul(u[q], e2[q]); u2[q] = cmul(u2[q], e2[q]); }
      }
    }
    cf e4[2] = {cmul(e2[0], e2[0]), cmul(e2[1], e2[1])};
#pragma unroll
    for (int r = 0; r < 2; ++r) {
      cf c0 = cexp_(a2[0], bv[0], 512.f + 128.f * r, m0[0]), c1 = cexp_(a2[1], bv[1], 512.f + 128.f * r, m0[1]);
      put2(my + (24 + 2 * r) * RS, c0, c1);
      put2(my + (25 + 2 * r) * RS, cmul(c0, e4[0]), cmul(c1, e4[1]));
    }
  } else {
    cf e4[2], e128[2], u[2], u2[2];
#pragma unroll
    for (int q = 0; q < 2; ++q) {
      e4[q] = cexp_(a2[q], bv[q], 4.f, 0.f);
      e128[q] = cexp_(a2[q], bv[q], 128.f, 0.f);
      u[q] = cexp_(a2[q], bv[q], 1024.f, m0[q]);
      u2[q] = cexp_(a2[q], bv[q], 1536.f, m0[q]);
    }
#pragma unroll
    for (int r = 2; r < 4; ++r) {
      cf c0 = cexp_(a2[0], bv[0], 512.f + 128.f * r, m0[0]), c1 = cexp_(a2[1], bv[1], 512.f + 128.f * r, m0[1]);
      put2(my + (24 + 2 * r) * RS, c0, c1);
      put2(my + (25 + 2 * r) * RS, cmul(c0, e4[0]), cmul(c1, e4[1]));
    }
#pragma unroll
    for (int r = 0; r < 4; ++r) {
      put2(my + (32 + r) * RS, u[0], u[1]);
      put2(my + (36 + r) * RS, u2[0], u2[1]);
#pragma unroll
      for (int q = 0; q < 2; ++q) { if (r < 3) u[q] = cmul(u[q], e128[q]); u2[q] = cmul(u2[q], e128[q]); }
    }
    put2(my + 40 * RS, u2[0], u2[1]);
  }
  __syncthreads();
  STAMP(0, 2);
  _Float16* kh = (_Float16*)(smem + 57 * RS);
  if (w < 3) {
    const char* ua = smem + (16 * w + i) * RS + g * 16;
    const char* va = smem + (41 + i) * RS + g * 16;
    f32x4 acc = {0.f, 0.f, 0.f, 0.f};
#pragma unroll
    for (int ks = 0; ks < 8; ++ks)
      acc = mfma_f16(*(const u32x4*)(ua + ks * 64), *(const u32x4*)(va + ks * 64), acc);
    int idx0, st;
    if (w == 0) { idx0 = (g < 2 ? 4 * g : 120 + 4 * g) + 8 * i; st = 1; }
    else if (w == 1) { idx0 = g < 2 ? 256 + 64 * g + 4 * i : 384 + 64 * (g - 2) + 2 * i; st = 1; }
    else { idx0 = 512 + 64 * g + i; st = 16; }
#pragma unroll
    for (int rg = 0; rg < 4; ++rg) {
      int idx = idx0 + st * rg;
      if (w == 1 && g >= 2) idx = idx0 + 32 * (rg >> 1) + (rg & 1);
      if (w == 2 && g >= 2) idx = (g == 2 && rg == 0 && i == 0) ? 640 : 644;
      kh[idx] = (_Float16)acc[rg];
    }
  }
  __syncthreads();
  STAMP(0, 3);
  const unsigned short* ku = (const unsigned short*)kh;
  const size_t kout = WS_KREV + (size_t)d * KREV * 2;
  {
    const int l0 = 2040 - 8 * tid;
    u32x4 o;
    if (w < 2) {
      const int q = l0 >> 3;
      const unsigned a = ku[384 + q], b = ku[385 + q];
      const unsigned t = (q & 1) ? b : a;
      o[0] = b | (b << 16); o[1] = b | (t << 16); o[2] = a | (a << 16); o[3] = o[2];
    } else if (w == 2) {
      const int q = l0 >> 2;
      const unsigned a = ku[256 + q], b = ku[257 + q], c = ku[258 + q];
      o[0] = c | (c << 16); o[1] = b | (b << 16); o[2] = b | (a << 16); o[3] = a | (a << 16);
    } else {
      unsigned v[8];
#pragma unroll
      for (int e = 0; e < 8; ++e) {
        const int l = l0 + 7 - e;
        const int idx = l < 256 ? l : (int)(pk_bf((float)l, 0.f) & 0xffffu) - 17024;
        v[e] = ku[idx];
      }
#pragma unroll
      for (int q = 0; q < 4; ++q) o[q] = v[2 * q] | (v[2 * q + 1] << 16);
    }
    st16(ws_rsrc(p.ws), kout + 16 * tid, o);
  }
}

__global__ NOPK __launch_bounds__(256) void k1(K1Args p) {
  __shared__ __attribute__((aligned(16))) char smem[57 * KG_RS + 1312];
  int b = blockIdx.x;
  char* stamp_ws = p.ws; (void)stamp_ws;
  STAMP(0, 0);
  if (b < NB_PF) { k1_prefetch(p, b); STAMP(0, 7); return; }
  b -= NB_PF;
  if (b < NB_K) { k1_kgen(p, b, smem); STAMP(0, 7); return; }
  b -= NB_K;
  __builtin_amdgcn_s_setprio(3);
  if (b < NB_LN) { k1_ln(p, b, smem); STAMP(0, 7); return; }
  b -= NB_LN;
  k1_mod(p, b, smem);
  STAMP(0, 7);
}

constexpr int K2_CS = 4160;
constexpr int K2_KOFF = 4 * K2_CS;
constexpr int K2_XOFF = K2_CS;
constexpr int K2_CH = K2_KOFF + 4608;
DEV void k2_body(const unsigned short* __restrict__ dskip, char* __restrict__ ws,
                 const unsigned short* __restrict__ w1, const unsigned short* __restrict__ w2, char* smem_all) {
  constexpr int CS = K2_CS, KOFF = K2_KOFF;
  const int tid = threadIdx.x;
  const int lane = tid & 63;
  const int w = __builtin_amdgcn_readfirstlane(tid >> 6);
  char* stamp_ws = ws; (void)stamp_ws;
  STAMP(1, 0);
  if (blockIdx.x >= D_ / 2) {
    int blk = blockIdx.x - D_ / 2;
    __builtin_amdgcn_s_sleep(24);
    if (blk < 68) k2_retile<true>(w1, ws, WS_W1F, D_, FF, blk, smem_all);
    else if (blk < 136) k2_retile<false>(w2, ws, WS_W2F, FF, D_, blk - 68, smem_all);
    else k2_colsum(w1, ws, blk - 136, smem_all);
    STAMP(1, 7);
    return;
  }
  const int bx = blockIdx.x;
  const int d = ((bx & 7) * 40 + (bx >> 3)) * 2 + (w >> 1);
  const int hw = w & 1, th = tid & 127;
  char* smem = smem_all + (w >> 1) * K2_CH;
  const unsigned short* krev = (const unsigned short*)(ws + WS_KREV) + (size_t)d * KREV;
  const int r = lane & 15, g = lane >> 4;

  const size_t xo = ((size_t)d * L_ + 16 * th) * 2;
  const size_t xo2 = xo + (th < 127 ? 32 : 0);
  const u32x4 xa0 = *(const u32x4*)(ws + WS_XNT + xo), xa1 = *(const u32x4*)(ws + WS_XNT + xo + 16);
  const u32x2 xb = *(const u32x2*)(ws + WS_XNT + xo2);
  const u32x4 kv0 = *(const u32x4*)(krev + 8 * th), kv1 = *(const u32x4*)(krev + 1024 + 8 * th);
  const unsigned short rds = dskip[d];
  const float sa = mod_sum(ws, d), ca1 = rbf(1.f + mod_sum(ws, D_ + d)), ga = mod_sum(ws, 2 * D_ + d);
  const float kinv = ((const float*)(ws + WS_KSC))[d];
  u32x2 xres[4];
#pragma unroll
  for (int q = 0; q < 4; ++q)
    xres[q] = *(const u32x2*)(ws + WS_XT + ((size_t)d * L_ + 256 * (4 * hw + q) + 16 * r + 4 * g) * 2);
  const float dsk = __uint_as_float((unsigned)rds << 16);
  const float hb = 26.f * fabsf(ca1) + fabsf(sa);
  int ex = (int)((__float_as_uint(hb) >> 23) & 0xff);
  ex = ex < 20 ? 20 : (ex > 240 ? 240 : ex);
  const float hs = __uint_as_float((unsigned)(127 + 14 + 127 - ex) << 23);
  const float hinv = __uint_as_float((unsigned)(ex - 14) << 23);
  const float yinv = kinv * hinv;

  unsigned hh[10];
#pragma unroll
  for (int q = 0; q < 10; ++q) {
    unsigned u = q < 4 ? xa0[q] : (q < 8 ? xa1[q - 4] : xb[q - 8]);
    unsigned m = pk_bf(bf_lo(u) * ca1, bf_hi(u) * ca1);
    unsigned h = pk_bf(bf_lo(m) + sa, bf_hi(m) + sa);
    hh[q] = pk_h(bf_lo(h) * hs, bf_hi(h) * hs);
  }
  STAMP(1, 1);
  {
    char* dst = smem + (2 * th + 2) * 16;
    unsigned s1[9];
#pragma unroll
    for (int q = 0; q < 9; ++q) s1[q] = __builtin_amdgcn_alignbit(hh[q + 1], hh[q], 16);
#pragma unroll
    for (int c = 0; c < 2; ++c) {
      *(u32x4*)(dst + 16 * c) = (u32x4){hh[4 * c], hh[4 * c + 1], hh[4 * c + 2], hh[4 * c + 3]};
      *(u32x4*)(dst + 16 * c + CS) = (u32x4){s1[4 * c], s1[4 * c + 1], s1[4 * c + 2], s1[4 * c + 3]};
      *(u32x4*)(dst + 16 * c + 2 * CS) = (u32x4){hh[4 * c + 1], hh[4 * c + 2], hh[4 * c + 3], hh[4 * c + 4]};
      *(u32x4*)(dst + 16 * c + 3 * CS) = (u32x4){s1[4 * c + 1], s1[4 * c + 2], s1[4 * c + 3], s1[4 * c + 4]};
    }
    if (th == 0) {
      u32x4 z = {0u, 0u, 0u, 0u};
      *(u32x4*)(smem) = z; *(u32x4*)(smem + CS) = z; *(u32x4*)(smem + 2 * CS) = z; *(u32x4*)(smem + 3 * CS) = z;
      *(u32x4*)(smem + 16) = z;
      *(u32x4*)(smem + CS + 16) = (u32x4){0u, 0u, 0u, hh[0] << 16};
      *(u32x4*)(smem + 2 * CS + 16) = (u32x4){0u, 0u, 0u, hh[0]};
      *(u32x4*)(smem + 3 * CS + 16) = (u32x4){0u, 0u, hh[0] << 16, s1[0]};
    }
  }
  *(u32x4*)(smem + KOFF + 16 * th) = kv0;
  *(u32x4*)(smem + KOFF + 2048 + 16 * th) = kv1;
  if (th < 32) *(u32x4*)(smem + KOFF + 4096 + 16 * th) = (u32x4){0u, 0u, 0u, 0u};
  __syncthreads();
  STAMP(1, 2);

  f32x4 acc[8];
#pragma unroll
  for (int q = 0; q < 8; ++q) acc[q] = (f32x4){0.f, 0.f, 0.f, 0.f};
  {
    const int s = (r + 1) & 3;
    const unsigned ap0 = (unsigned)(size_t)(smem + s * CS + 2 * (128 * hw + 8 * g + (r + 1) - s));
    const unsigned bp0 = (unsigned)(size_t)(smem + KOFF + 2 * (2032 + 128 * hw - 16 * r + 8 * g) - 3584);
    u32x2 Al[2][8], Ah[2][8];
    u32x4 Bf[2][8];
#define K2_LOAD(buf, cc, u)                                                                                     \
    asm volatile("ds_read_b64 %0, %3 offset:%5\n\tds_read_b64 %1, %3 offset:%6\n\tds_read_b128 %2, %4 offset:%7" \
                 : "=&v"(Al[buf][u]), "=&v"(Ah[buf][u]), "=&v"(Bf[buf][u])                                        \
                 : "v"(ap0), "v"(bp0), "i"(64 * (cc) + 512 * (u)), "i"(64 * (cc) + 512 * (u) + 8),               \
                   "i"(64 * (cc) + 3584 - 512 * (u)))
#pragma unroll
    for (int u = 0; u < 8; ++u) K2_LOAD(0, 0, u);
#pragma unroll
    for (int cc = 0; cc < 4; ++cc) {
      lgkm0();
#pragma unroll
      for (int q = 0; q < 8; ++q) {
        if (cc < 3) K2_LOAD((cc + 1) & 1, cc + 1, q);
#pragma unroll
        for (int sp = 0; sp <= q; ++sp) {
          const u32x4 A = {Al[cc & 1][sp][0], Al[cc & 1][sp][1], Ah[cc & 1][sp][0], Ah[cc & 1][sp][1]};
          acc[q] = mfma_f16(A, Bf[cc & 1][q - sp], acc[q]);
        }
        __builtin_amdgcn_sched_barrier(0);
      }
    }
#undef K2_LOAD
  }
  STAMP(1, 3);
  __syncthreads();
  f32x4 keep[4];
  {
    f32x4* xw = (f32x4*)(smem + K2_XOFF + hw * 4096) + lane;
    if (hw) {
#pragma unroll
      for (int q = 0; q < 4; ++q) { xw[64 * q] = acc[q]; keep[q] = acc[4 + q]; }
    } else {
#pragma unroll
      for (int q = 0; q < 4; ++q) { xw[64 * q] = acc[4 + q]; keep[q] = acc[q]; }
    }
  }
  __syncthreads();
  {
    const f32x4* xrd = (const f32x4*)(smem + K2_XOFF + (hw ^ 1) * 4096) + lane;
    const rsrc_t rs = ws_rsrc(ws);
#pragma unroll
    for (int q = 0; q < 4; ++q) {
      const f32x4 y = keep[q] + xrd[64 * q];
      const int t = 256 * (4 * hw + q) + 16 * r + 4 * g;
      const u32x2 hv = *(const u32x2*)(smem + 2 * (t + 16));
      u32x2 o;
#pragma unroll
      for (int e = 0; e < 2; ++e) {
        const unsigned hu = hv[e];
        const h16x2 h2 = __builtin_bit_cast(h16x2, hu);
        const float h0 = (float)h2[0] * hinv, h1 = (float)h2[1] * hinv;
        unsigned yb = pk_bf(y[2 * e] * yinv, y[2 * e + 1] * yinv);
        unsigned hd = pk_bf(h0 * dsk, h1 * dsk);
        unsigned y2 = pk_bf(bf_lo(yb) + bf_lo(hd), bf_hi(yb) + bf_hi(hd));
        unsigned gy = pk_bf(bf_lo(y2) * ga, bf_hi(y2) * ga);
        o[e] = pk_bf(bf_lo(xres[q][e]) + bf_lo(gy), bf_hi(xres[q][e]) + bf_hi(gy));
      }
      st8(rs, WS_X1T + (((size_t)(t >> 5) * D_ + d) * 32 + (t & 31)) * 2, o);
    }
  }
  STAMP(1, 7);
}

__global__ NOPK __launch_bounds__(256) void k2(const unsigned short* __restrict__ dskip, char* __restrict__ ws,
                                               const unsigned short* __restrict__ w1, const unsigned short* __restrict__ w2) {
  __shared__ __attribute__((aligned(16))) char smem[2 * K2_CH];
  k2_body(dskip, ws, w1, w2, smem);
}

#define SB() __builtin_amdgcn_sched_barrier(0)
DEV u32x4 ld16_sc1(rsrc_t r, int voff, int soff) { return __builtin_amdgcn_raw_buffer_load_b128(r, voff, soff, 16); }
DEV u32x4 ld16(rsrc_t r, int voff, int soff) { return __builtin_amdgcn_raw_buffer_load_b128(r, voff, soff, 0); }
DEV u32x4 tr_frag(const char* p) {
  const u32x2 a = __builtin_bit_cast(u32x2, __builtin_amdgcn_ds_read_tr16_b64_v4i16((lds_s16x4*)p));
  const u32x2 b = __builtin_bit_cast(u32x2, __builtin_amdgcn_ds_read_tr16_b64_v4i16((lds_s16x4*)(p + 256)));
  return (u32x4){a[0], a[1], b[0], b[1]};
}

constexpr int KF_BIG = 8 * 32 * 84 * 4;
constexpr int KF_W2L = KF_BIG;
constexpr int KF_TAB = KF_W2L + 57344;
constexpr int KF_PS = KF_TAB + 320;
constexpr int KF_XR = KF_PS + 512;
constexpr int KF_LDS = KF_XR + 64 * 176;
#define W2_START 10

DEV void kf_body(char* __restrict__ ws, unsigned short* __restrict__ out, char* smem) {
  char* xs = smem;
  float* tab = (float*)(smem + KF_TAB);
  float* ps = (float*)(smem + KF_PS);
  char* xr = smem + KF_XR;
  const int tid = threadIdx.x, lane = tid & 63;
  const int w = __builtin_amdgcn_readfirstlane(tid >> 6);
  const int bx = blockIdx.x;
  const int ng = (bx >> 3) & 7, mt = (bx & 7) * 4 + (bx >> 6);
  const int g = lane >> 4, i = lane & 15;
  char* stamp_ws = ws; (void)stamp_ws;
  STAMP(2, 0);
  const rsrc_t rs_ = ws_rsrc(ws);
  unsigned* cnt = (unsigned*)(ws + WS_CNT) + mt * 64;

  const int ntl = ng < 4 ? 14 : 13;
  const int ctb = ng < 4 ? 14 * ng : 56 + 13 * (ng - 4);
  const bool two = w < ntl - 8;
  const bool full = w < 6;
  const int ct0 = ctb + (two ? 2 * w : (ntl - 8) + w);
  const int lo16 = lane * 16;
  const int w1s = (int)WS_W1F + ct0 * 1024;
  const int w2s = (int)WS_W2F + (ng * 5 * 54 + w) * 1024;

  constexpr int NW2R = 28;
  u32x4 ring[32];
  const char* bb0 = xs + (8 * g + (i >> 2)) * 64 + ((g & 1) << 5) + (i & 3) * 8;
  const char* bb1 = xs + (8 * g + (i >> 2)) * 64 + (((g & 1) ^ 1) << 5) + (i & 3) * 8;
  auto w2_off = [&](int e) __attribute__((always_inline)) {
    const int sn = (e / 5 == 6 && !full) ? 5 : e / 5;
    return w2s + ((e % 5) * 54 + 8 * sn) * 1024;
  };

  auto gemm1 = [&](auto two_tag) __attribute__((always_inline)) {
    constexpr bool TWO = decltype(two_tag)::value;
    u32x4 t[10];
#pragma unroll
    for (int it = 0; it < 10; ++it) t[it] = ld16(rs_, tid * 16, (int)WS_X1T + mt * 81920 + 8192 * it);
#define W1_PRE 10
#define W1_EARLY 3
#pragma unroll
    for (int k = 0; k < W1_EARLY; ++k) {
      ring[2 * k] = ld16(rs_, lo16, w1s + k * 108 * 1024);
      if (TWO) ring[2 * k + 1] = ld16(rs_, lo16, w1s + (k * 108 + 1) * 1024);
    }
    SB();
    __builtin_amdgcn_s_barrier();
    SB();
#pragma unroll
    for (int k = W1_EARLY; k < W1_PRE; ++k) {
      ring[2 * k] = ld16(rs_, lo16, w1s + k * 108 * 1024);
      if (TWO) ring[2 * k + 1] = ld16(rs_, lo16, w1s + (k * 108 + 1) * 1024);
    }
    SB();
#pragma unroll
    for (int it = 0; it < 10; ++it) {
      const int c = tid + 512 * it;
      const int row = c >> 2;
      *(u32x4*)(xs + row * 64 + (((c & 3) ^ ((row >> 2) & 2)) << 4)) = t[it];
    }
    SB();
    f32x4 csv[2], b1v[2];
#pragma unroll
    for (int c = 0; c < (TWO ? 2 : 1); ++c) {
      const int f0 = 16 * (ct0 + c) + 4 * g;
      csv[c] = *(const f32x4*)((const float*)(ws + WS_CSB) + f0);
      b1v[c] = *(const f32x4*)((const float*)(ws + WS_CSB) + FF + f0);
    }
    SB();
    __syncthreads();
    STAMP(2, 1);
    {
      const int kb = (80 * ng) >> 5, tq = w & 3;
#pragma unroll
      for (int u = 0; u < 2; ++u) {
        const int kk = min(kb + 2 * (w >> 2) + u, 19);
        const u32x4 f = tr_frag(((tq & 1) ? bb1 : bb0) + (tq >> 1) * 40960 + kk * 2048);
        const unsigned dl = (unsigned)(32 * kk + 8 * g - 80 * ng);
        if (dl < 80u) *(u32x4*)(xr + (tq * 16 + i) * 176 + dl * 2) = f;
      }
    }
    const int hs = TWO ? 0 : (w & 1);
    const char* ba[4] = {bb0 + hs * 40960, bb1 + hs * 40960, bb0 + (hs ^ 1) * 40960, bb1 + (hs ^ 1) * 40960};
    const u32x4 ones = {0x3f803f80u, 0x3f803f80u, 0x3f803f80u, 0x3f803f80u};
    f32x4 acc[2][4];
#pragma unroll
    for (int c = 0; c < 2; ++c)
#pragma unroll
      for (int q = 0; q < 4; ++q) acc[c][q] = (f32x4){0.f, 0.f, 0.f, 0.f};
    u32x4 bn[2][4];
#pragma unroll
    for (int q = 0; q < 4; ++q) bn[0][q] = tr_frag(ba[q]);
#pragma unroll
    for (int q = 0; q < 4; ++q) bn[1][q] = tr_frag(ba[q] + 2048);
#pragma unroll
    for (int kk = 0; kk < 20; ++kk) {
      u32x4 b[4];
#pragma unroll
      for (int q = 0; q < 4; ++q) b[q] = bn[kk & 1][q];
      SB();
#pragma unroll
      for (int q = 0; q < 4; ++q) acc[0][q] = mfma_bf16(ring[(2 * kk) % 32], b[q], acc[0][q]);
      if (TWO) {
#pragma unroll
        for (int q = 0; q < 4; ++q) acc[1][q] = mfma_bf16(ring[(2 * kk + 1) % 32], b[q], acc[1][q]);
      } else {
        acc[1][0] = mfma_bf16(ones, b[0], acc[1][0]); acc[1][1] = mfma_bf16(b[0], b[0], acc[1][1]);
        acc[1][2] = mfma_bf16(ones, b[1], acc[1][2]); acc[1][3] = mfma_bf16(b[1], b[1], acc[1][3]);
      }
      SB();
      if (kk < 18) {
#pragma unroll
        for (int q = 0; q < 4; ++q) bn[kk & 1][q] = tr_frag(ba[q] + (kk + 2) * 2048);
      }
      if (kk + W1_PRE < 20) {
        const int kn = kk + W1_PRE;
        ring[(2 * kn) % 32] = ld16(rs_, lo16, w1s + kn * 108 * 1024);
        if (TWO) ring[(2 * kn + 1) % 32] = ld16(rs_, lo16, w1s + (kn * 108 + 1) * 1024);
      }
      if (kk >= W2_START) {
#pragma unroll
        for (int u = 0; u < 2; ++u) {
          const int e = 2 * (kk - W2_START) + u;
          if (e < 24) ring[8 + e] = ld16(rs_, lo16, w2_off(e));
        }
      }
      SB();
#define LOCK_N 0
#define LOCK_M 1
      if (kk < LOCK_N && (kk % LOCK_M) == LOCK_M - 1) { __builtin_amdgcn_s_barrier(); SB(); }
    }
    if (!TWO) {
#pragma unroll
      for (int h = 0; h < 2; ++h) {
        const f32x4 aq = acc[1][2 * h + 1];
        const float dq = (i & 2) ? ((i & 1) ? aq[3] : aq[2]) : ((i & 1) ? aq[1] : aq[0]);
        const int tk = (2 * hs + h) * 16 + i;
        if (g == (i >> 2)) {
          const float mean = acc[1][2 * h][0] * (1.f / D_);
          const float var = fmaxf(dq * (1.f / D_) - mean * mean, 0.f);
          const float sd = rbf(sqrtf(rbf(rbf(var) + 1e-5f)));
          const float rs = 1.f / sd;
          ps[tk] = rs;
          ps[64 + tk] = -rbf(mean) * rs;
        }
      }
    }
    __syncthreads();
    STAMP(2, 2);
#pragma unroll
    for (int q = 0; q < 4; ++q) {
      const int tq = q ^ (2 * hs);
      const float rs = ps[tq * 16 + i], nm = ps[64 + tq * 16 + i];
#pragma unroll
      for (int c = 0; c < (TWO ? 2 : 1); ++c) {
        const int ct = ct0 + c;
        float v[4];
#pragma unroll
        for (int r = 0; r < 4; ++r) v[r] = fmaxf(fmaf(acc[c][q][r], rs, fmaf(nm, csv[c][r], b1v[c][r])), 0.f);
        u32x2 o = {pk_bf(v[0], v[1]), pk_bf(v[2], v[3])};
        const size_t off = (((size_t)(4 * mt + tq) * 54 + (ct >> 1)) * 64 + i + 16 * (2 * (ct & 1) + (g >> 1))) * 8 + 4 * (g & 1);
        st8(rs_, WS_H1F + off * 2, o);
      }
    }
    SB();
    constexpr int E0 = (20 - W2_START) * 2 < 24 ? (20 - W2_START) * 2 : 24;
#define W2_MID 4
#pragma unroll
    for (int e = E0; e < E0 + W2_MID; ++e) ring[(8 + e) % 32] = ld16(rs_, lo16, w2_off(e));
    SB();
    asm volatile("s_waitcnt vmcnt(%0)" :: "n"(W2_MID) : "memory");
    if (lane == 0) __hip_atomic_fetch_add(cnt, 1u, __ATOMIC_RELAXED, __HIP_MEMORY_SCOPE_AGENT);
    SB();
#pragma unroll
    for (int e = E0 + W2_MID; e < NW2R; ++e) ring[(8 + e) % 32] = ld16(rs_, lo16, w2_off(e));
    SB();
  };
  if (two) gemm1(std::true_type{});
  else gemm1(std::false_type{});
  STAMP(2, 3);
#pragma unroll
  for (int e = NW2R; e < 35; ++e)
    __builtin_amdgcn_global_load_lds((const __attribute__((address_space(1))) void*)(ws + w2_off(e) + lo16),
                                     (__attribute__((address_space(3))) void*)(smem + KF_W2L + (w * 7 + (e - NW2R)) * 1024), 16, 0, 0);
  if (tid >= 64 && tid < 144) tab[tid - 64] = mod_sum(ws, 5 * D_ + 80 * ng + tid - 64);
  if (w == 0) {
    for (unsigned spin = 0; spin < (1u << 22); ++spin) {
      if (__hip_atomic_load(cnt, __ATOMIC_RELAXED, __HIP_MEMORY_SCOPE_AGENT) >= 64u) break;
      __builtin_amdgcn_s_sleep(1);
    }
  }
  asm volatile("s_waitcnt vmcnt(0)" ::: "memory");
  __syncthreads();
  STAMP(2, 4);

  const int h1s = (int)WS_H1F + (mt * 4 * 54 + w) * 1024;
  u32x4 hb[2][7][2];
  auto load_h1 = [&](int H, int s0, int s1) __attribute__((always_inline)) {
#pragma unroll
    for (int s = s0; s < s1; ++s) {
      const int sn = (s == 6 && !full) ? 5 : s;
#pragma unroll
      for (int q = 0; q < 2; ++q) hb[H][s][q] = ld16_sc1(rs_, lo16, h1s + ((2 * H + q) * 54 + 8 * sn) * 1024);
    }
    SB();
  };
  load_h1(0, 0, 7);
  load_h1(1, 0, 3);
  constexpr int RS = 84;
  float* red = (float*)smem;
  float* rb_ = red + (w * 32 + i) * RS + 4 * g;
#pragma unroll
  for (int H = 0; H < 2; ++H) {
    f32x4 ac2[5][2];
#pragma unroll
    for (int c = 0; c < 5; ++c) { ac2[c][0] = (f32x4){0.f, 0.f, 0.f, 0.f}; ac2[c][1] = ac2[c][0]; }
#pragma unroll
    for (int s = 0; s < 7; ++s) {
      if (s < 6 || full) {
#pragma unroll
        for (int c = 0; c < 5; ++c) {
          const int e = 5 * s + c;
          const u32x4 a = e < NW2R ? ring[(8 + (e < NW2R ? e : 0)) % 32]
                                   : *(const u32x4*)(smem + KF_W2L + (w * 7 + (e - NW2R)) * 1024 + lo16);
          ac2[c][0] = mfma_bf16(a, hb[H][s][0], ac2[c][0]);
          ac2[c][1] = mfma_bf16(a, hb[H][s][1], ac2[c][1]);
        }
      }
      if (H == 0 && s == 3) { SB(); load_h1(1, 3, 7); }
    }
    SB();
    if (H == 0) STAMP(2, 5);
    if (H == 1) { STAMP(2, 6); __syncthreads(); }
#pragma unroll
    for (int c = 0; c < 5; ++c) {
      *(f32x4*)(rb_ + 16 * c) = ac2[c][0];
      *(f32x4*)(rb_ + 16 * RS + 16 * c) = ac2[c][1];
    }
    __syncthreads();
#pragma unroll
    for (int it = 0; it < 2; ++it) {
      const int e = tid + 512 * it;
      if (e < 640) {
        const int tk = e / 20, q4 = e % 20;
        f32x4 s = *(const f32x4*)(red + tk * RS + 4 * q4);
#pragma unroll
        for (int ww = 1; ww < 8; ++ww) s += *(const f32x4*)(red + (ww * 32 + tk) * RS + 4 * q4);
        const u32x2 xv2 = *(const u32x2*)(xr + (32 * H + tk) * 176 + q4 * 8);
        const f32x4 gb = *(const f32x4*)(tab + 4 * q4);
        u32x2 o;
#pragma unroll
        for (int q = 0; q < 2; ++q) {
          unsigned f = pk_bf(s[2 * q], s[2 * q + 1]);
          unsigned gf = pk_bf(bf_lo(f) * gb[2 * q], bf_hi(f) * gb[2 * q + 1]);
          o[q] = pk_bf(bf_lo(xv2[q]) + bf_lo(gf), bf_hi(xv2[q]) + bf_hi(gf));
        }
        *(u32x2*)(out + (size_t)(mt * 64 + 32 * H + tk) * D_ + ng * 80 + 4 * q4) = o;
      }
    }
    SB();
  }
  STAMP(2, 7);
}

__global__ NOPK __launch_bounds__(512, 2) void kf(char* __restrict__ ws, unsigned short* __restrict__ out) {
  __shared__ __attribute__((aligned(16))) char smem[KF_LDS];
  kf_body(ws, out, smem);
}

__global__ void kend_() {}

extern "C" void kernel_launch(void* const* d_in, const int* in_sizes, int n_in,
                              void* d_out, int out_size, void* d_ws, size_t ws_size,
                              hipStream_t stream) {
  K1Args a;
  a.x = (const unsigned short*)d_in[0];
  a.are = (const unsigned short*)d_in[1];
  a.aim = (const unsigned short*)d_in[2];
  a.bre = (const unsigned short*)d_in[3];
  a.bim = (const unsigned short*)d_in[4];
  a.cre = (const unsigned short*)d_in[5];
  a.cim = (const unsigned short*)d_in[6];
  a.logdt = (const unsigned short*)d_in[7];
  a.w1 = (const unsigned short*)d_in[10];
  a.w2 = (const unsigned short*)d_in[11];
  a.c = (const unsigned short*)d_in[12];
  a.wmod = (const unsigned short*)d_in[13];
  a.ws = (char*)d_ws;
  a.dskip = (const unsigned short*)d_in[8];
  a.out = (const unsigned short*)d_out;
  k1<<<NB_PF + NB_MOD + NB_LN + NB_K, 256, 0, stream>>>(a);
  k2<<<D_ / 2 + NB_W, 256, 0, stream>>>((const unsigned short*)d_in[8], (char*)d_ws, a.w1, a.w2);
  kf<<<256, 512, 0, stream>>>((char*)d_ws, (unsigned short*)d_out);
}
```

```cpp
#define PF 1
#include <hip/hip_runtime.h>
#include <hip/hip_bf16.h>
#include <stdint.h>
#include <type_traits>

typedef __attribute__((ext_vector_type(8))) __bf16 bf16x8;
typedef __attribute__((ext_vector_type(2))) __bf16 bf16x2;
typedef __attribute__((ext_vector_type(4))) float f32x4;
typedef __attribute__((ext_vector_type(2))) float f32x2;
typedef __attribute__((ext_vector_type(4))) unsigned u32x4;
typedef __attribute__((ext_vector_type(2))) unsigned u32x2;
typedef __attribute__((ext_vector_type(8))) _Float16 h16x8;
typedef __attribute__((ext_vector_type(2))) _Float16 h16x2;
typedef __attribute__((ext_vector_type(4))) short s16x4;
typedef __attribute__((address_space(3))) s16x4 lds_s16x4;

#if defined(__HIP_DEVICE_COMPILE__)
#define NOPK __attribute__((target("no-packed-fp32-ops")))
#else
#define NOPK
#endif
#define DEV __device__ __forceinline__

constexpr int L_ = 2048;
constexpr int D_ = 640;
constexpr int NS = 128;
constexpr int FF = 1728;
constexpr int KREV = 2304;

constexpr size_t WS_KSC  = 8192;
constexpr size_t WS_CNT  = 16384;
constexpr size_t WS_CSB  = 32768;
constexpr size_t WS_MOD  = 65536;
constexpr size_t WS_XNT  = 196608;
constexpr size_t WS_XT   = WS_XNT + (size_t)D_ * L_ * 2;
constexpr size_t WS_KREV = WS_XT + (size_t)D_ * L_ * 2;
constexpr size_t WS_W1F  = WS_KREV + (size_t)D_ * 2 * KREV * 2;
constexpr size_t WS_W2F  = WS_W1F + (size_t)D_ * FF * 2;
constexpr size_t WS_X1T  = WS_W2F + (size_t)D_ * FF * 2;
constexpr size_t WS_X1   = WS_X1T + (size_t)D_ * L_ * 2;
constexpr size_t WS_CSP  = WS_X1;
constexpr size_t WS_H1F  = WS_X1 + (size_t)D_ * L_ * 2;

#define STAMP(kid, k) do {} while (0)

DEV float bf_lo(unsigned u) { return __uint_as_float(u << 16); }
DEV float bf_hi(unsigned u) { return __uint_as_float(u & 0xffff0000u); }
DEV unsigned pk_bf(float a, float b) {
  f32x2 v = {a, b};
  bf16x2 r = __builtin_convertvector(v, bf16x2);
  return __builtin_bit_cast(unsigned, r);
}
DEV float rbf(float a) { return bf_lo(pk_bf(a, 0.f)); }
DEV float ldbf(const unsigned short* p) { return __uint_as_float(((unsigned)*p) << 16); }

typedef __amdgpu_buffer_rsrc_t rsrc_t;
DEV rsrc_t ws_rsrc(char* ws) { return __builtin_amdgcn_make_buffer_rsrc((void*)ws, 0, 0x7fffffff, 0x00020000); }
#define ST_AUX 16
DEV void st16(rsrc_t r, size_t off, u32x4 v) { __builtin_amdgcn_raw_buffer_store_b128(v, r, (int)off, 0, ST_AUX); }
#define LN_AUX 0
DEV void st16ln(rsrc_t r, size_t off, u32x4 v) { __builtin_amdgcn_raw_buffer_store_b128(v, r, (int)off, 0, LN_AUX); }
DEV void st8(rsrc_t r, size_t off, u32x2 v) { __builtin_amdgcn_raw_buffer_store_b64(v, r, (int)off, 0, 16); }

DEV u32x2 tr_read(unsigned lds_addr) {
  u32x2 r;
  asm volatile("ds_read_b64_tr_b16 %0, %1" : "=v"(r) : "v"(lds_addr) : "memory");
  return r;
}
DEV void bar_lds() { asm volatile("s_waitcnt lgkmcnt(0)\n\ts_barrier" ::: "memory"); }
DEV void lgkm0() { asm volatile("s_waitcnt lgkmcnt(0)" ::: "memory"); __builtin_amdgcn_sched_barrier(0); }

DEV f32x4 mfma_bf16(u32x4 a, u32x4 b, f32x4 c) {
  return __builtin_amdgcn_mfma_f32_16x16x32_bf16(__builtin_bit_cast(bf16x8, a), __builtin_bit_cast(bf16x8, b), c, 0, 0, 0);
}

constexpr int NB_MOD = 120, NB_LN = 256, NB_W2 = 108, NB_K = 640;

struct K1Args {
  const unsigned short *x, *are, *aim, *bre, *bim, *cre, *cim, *logdt, *w1, *w2, *c, *wmod;
  const unsigned short *dskip, *out;
  char* ws;
};

constexpr int NB_PF = 8;
DEV void k1_prefetch(const K1Args& p, int b) {
  const int tid = threadIdx.x;
  unsigned long long pc;
  int o0, o1;
  asm volatile("s_getpc_b64 %0\n.Lpf_here%=:\n\ts_mov_b32 %1, _Z2k16K1Args - .Lpf_here%=\n\ts_mov_b32 %2, _Z5kend_v - .Lpf_here%="
               : "=s"(pc), "=s"(o0), "=s"(o1));
  const char* c0 = (const char*)(pc + (long long)o0);
  long n = o1 - o0;
  n = n < 0 ? 0 : (n > 98304 ? 98304 : n);
  unsigned acc = 0;
  {
    unsigned v[3];
#pragma unroll
    for (int it = 0; it < 3; ++it) {
      const long o = (long)tid * 128 + it * 32768;
      v[it] = o < n ? *(const __attribute__((address_space(1))) unsigned*)(c0 + o) : 0u;
    }
    acc = v[0] ^ v[1] ^ v[2];
  }
  asm volatile("" :: "v"(acc));
}

DEV void k1_mod(const K1Args& p, int b, char* smem) {
  const int tid = threadIdx.x;
  const int rg = b & 7, cgp = b >> 3;
  const int c32 = tid & 31, r8 = tid >> 5;
  float* sil = (float*)smem;
  float* red = (float*)(smem + 512);
  u32x4 w[10];
#pragma unroll
  for (int it = 0; it < 10; ++it)
    w[it] = *(const u32x4*)(p.wmod + (size_t)(rg * 80 + r8 + 8 * it) * 3840 + 256 * cgp + 8 * c32);
  if (tid < 80) {
    float cv = ldbf(p.c + rg * 80 + tid);
    sil[tid] = rbf(cv / (1.f + expf(-cv)));
  }
  if (tid >= 128 && tid < 132 && b < 64) ((unsigned*)(p.ws + WS_CNT))[(b * 4 + tid - 128) * 16] = 0u;
  __syncthreads();
  float acc[8];
#pragma unroll
  for (int j = 0; j < 8; ++j) acc[j] = 0.f;
#pragma unroll
  for (int it = 0; it < 10; ++it) {
    float s = sil[r8 + 8 * it];
#pragma unroll
    for (int q = 0; q < 4; ++q) {
      acc[2 * q] += s * bf_lo(w[it][q]);
      acc[2 * q + 1] += s * bf_hi(w[it][q]);
    }
  }
#pragma unroll
  for (int j = 0; j < 8; ++j) acc[j] += __shfl_xor(acc[j], 32, 64);
  if ((tid & 63) < 32) {
    *(f32x4*)(red + (tid >> 6) * 256 + 8 * c32) = (f32x4){acc[0], acc[1], acc[2], acc[3]};
    *(f32x4*)(red + (tid >> 6) * 256 + 8 * c32 + 4) = (f32x4){acc[4], acc[5], acc[6], acc[7]};
  }
  __syncthreads();
  ((float*)(p.ws + WS_MOD))[(256 * cgp + tid) * 8 + rg] = (red[tid] + red[256 + tid]) + (red[512 + tid] + red[768 + tid]);
}
struct ModP { f32x4 a, b; };
DEV ModP mod_ld(const char* ws, int idx) {
  const f32x4* mp = (const f32x4*)(ws + WS_MOD) + 2 * idx;
  ModP m; m.a = mp[0]; m.b = mp[1];
  return m;
}
DEV float mod_fin(const ModP& m) {
  return rbf(((m.a[0] + m.a[1]) + (m.a[2] + m.a[3])) + ((m.b[0] + m.b[1]) + (m.b[2] + m.b[3])));
}
DEV float mod_sum(const char* ws, int idx) { return mod_fin(mod_ld(ws, idx)); }
#define SB() __builtin_amdgcn_sched_barrier(0)

DEV void k1_ln(const K1Args& p, int b, char* smem) {
  constexpr int STR = 704;
  const int tid = threadIdx.x;
  const int tok = tid >> 3, sub = tid & 7;
  const int tile = (b & 7) * 8 + ((b >> 3) & 7), pass = b >> 6;
  const int hf = pass & 1;
  const bool norm = pass < 2;
  const int t0 = tile * 32;
  const unsigned short* xr = p.x + (size_t)(t0 + tok) * D_ + 8 * sub;
  u32x4 xv[5], xo[5];
#pragma unroll
  for (int m = 0; m < 5; ++m) xv[m] = *(const u32x4*)(xr + 320 * hf + 64 * m);
  if (norm) {
#pragma unroll
    for (int m = 0; m < 5; ++m) xo[m] = *(const u32x4*)(xr + 320 * (hf ^ 1) + 64 * m);
    float s = 0.f, v = 0.f;
    const bf16x2 one2 = __builtin_bit_cast(bf16x2, 0x3f803f80u);
#pragma unroll
    for (int m = 0; m < 10; ++m)
#pragma unroll
      for (int q = 0; q < 4; ++q) {
        const unsigned u = m < 5 ? xv[m][q] : xo[m - 5][q];
        const bf16x2 x2 = __builtin_bit_cast(bf16x2, u);
        s = __builtin_amdgcn_fdot2_f32_bf16(x2, one2, s, false);
        v = __builtin_amdgcn_fdot2_f32_bf16(x2, x2, v, false);
      }
#pragma unroll
    for (int o = 1; o < 8; o <<= 1) { s += __shfl_xor(s, o, 64); v += __shfl_xor(v, o, 64); }
    const float mean = s * (1.f / D_);
    const float var = fmaxf(v * (1.f / D_) - mean * mean, 0.f);
    const float sd = rbf(sqrtf(rbf(rbf(var) + 1e-5f)));
    const float rs = 1.f / sd;
    const float mb = rbf(mean);
#pragma unroll
    for (int m = 0; m < 5; ++m)
#pragma unroll
      for (int q = 0; q < 4; ++q) {
        unsigned d2 = pk_bf(bf_lo(xv[m][q]) - mb, bf_hi(xv[m][q]) - mb);
        xv[m][q] = pk_bf(bf_lo(d2) * rs, bf_hi(d2) * rs);
      }
  }
  const int lane = tid & 63, w = tid >> 6;
  const int g = lane >> 4, i = lane & 15;
  const rsrc_t rsrc = ws_rsrc(p.ws);
#pragma unroll
  for (int m = 0; m < 5; ++m) *(u32x4*)(smem + tok * STR + (sub + 8 * m) * 16) = xv[m];
  __syncthreads();
  const size_t dsto = (norm ? WS_XNT : WS_XT) + ((size_t)320 * hf * L_ + t0) * 2;
#pragma unroll
  for (int it = 0; it < 5; ++it) {
    const int cb = w * 5 + it;
    const char* a = smem + (8 * g + (i >> 2)) * STR + cb * 32 + (i & 3) * 8;
    const u32x2 ra = __builtin_bit_cast(u32x2, __builtin_amdgcn_ds_read_tr16_b64_v4i16((lds_s16x4*)a));
    const u32x2 rb = __builtin_bit_cast(u32x2, __builtin_amdgcn_ds_read_tr16_b64_v4i16((lds_s16x4*)(a + 4 * STR)));
    st16ln(rsrc, dsto + (size_t)(cb * 16 + i) * (L_ * 2) + 16 * g, (u32x4){ra[0], ra[1], rb[0], rb[1]});
  }
}

constexpr int RT_STR = 176;
template <int NF> struct RtRaw { u32x4 r[NF]; };
template <int NF>
DEV void rt_load(RtRaw<NF>& t, const unsigned short* W, int N, int kk, int ct0, int lane) {
#pragma unroll
  for (int j = 0; j < NF; ++j) {
    const int c = lane + 64 * j, row = c / (2 * NF), part = c % (2 * NF);
    t.r[j] = *(const u32x4*)(W + (size_t)(32 * kk + row) * N + 16 * ct0 + 8 * part);
  }
}
template <int NF>
DEV void rt_park(const RtRaw<NF>& t, char* scr, int lane) {
#pragma unroll
  for (int j = 0; j < NF; ++j) {
    const int c = lane + 64 * j, row = c / (2 * NF), part = c % (2 * NF);
    *(u32x4*)(scr + row * RT_STR + 16 * part) = t.r[j];
  }
}
DEV u32x4 rt_frag(const char* scr, int j, int lane) {
  const int g = lane >> 4, i = lane & 15;
  const char* p = scr + (8 * g + (i >> 2)) * RT_STR + 32 * j + (i & 3) * 8;
  const u32x2 a = __builtin_bit_cast(u32x2, __builtin_amdgcn_ds_read_tr16_b64_v4i16((lds_s16x4*)p));
  const u32x2 b = __builtin_bit_cast(u32x2, __builtin_amdgcn_ds_read_tr16_b64_v4i16((lds_s16x4*)(p + 4 * RT_STR)));
  return (u32x4){a[0], a[1], b[0], b[1]};
}
struct cf { float re, im; };
DEV cf cmul(cf a, cf b) {
  cf r;
  r.re = a.re * b.re - a.im * b.im;
  r.im = a.re * b.im + a.im * b.re;
  return r;
}
DEV cf cexp_(float a2, float br, float m, float m0) {
  float mag = __builtin_amdgcn_exp2f(a2 * (m - m0));
  float ph = __builtin_amdgcn_fractf(br * m);
  cf r; r.re = mag * __builtin_amdgcn_cosf(ph); r.im = mag * __builtin_amdgcn_sinf(ph);
  return r;
}
DEV unsigned pk_h(float a, float b) {
  f32x2 v = {a, b};
  h16x2 r = __builtin_convertvector(v, h16x2);
  return __builtin_bit_cast(unsigned, r);
}
DEV f32x4 mfma_f16(u32x4 a, u32x4 b, f32x4 c) {
  return __builtin_amdgcn_mfma_f32_16x16x32_f16(__builtin_bit_cast(h16x8, a), __builtin_bit_cast(h16x8, b), c, 0, 0, 0);
}
DEV void put2(char* p, cf a, cf b) {
  u32x2 o = {pk_h(a.re, a.im), pk_h(b.re, b.im)};
  *(u32x2*)p = o;
}

constexpr int KG_RS = 544;
DEV void k1_kgen(const K1Args& p, int d, char* smem) {
  constexpr int RS = KG_RS;
  const int tid = threadIdx.x;
  const int lane = tid & 63;
  const int w = __builtin_amdgcn_readfirstlane(tid >> 6);
  const int g = lane >> 4, i = lane & 15;
  char* stamp_ws = p.ws; (void)stamp_ws;
  const size_t po = (size_t)d * NS + 2 * lane;
  const unsigned rar = *(const unsigned*)(p.are + po), rai = *(const unsigned*)(p.aim + po);
  const unsigned rbr = *(const unsigned*)(p.bre + po), rbi = *(const unsigned*)(p.bim + po);
  const unsigned rcr = *(const unsigned*)(p.cre + po), rci = *(const unsigned*)(p.cim + po);
  const float ldt = ldbf(p.logdt + d);
  asm volatile("" ::: "memory");
  SB();
  const float dt = rbf(expf(ldt));
  STAMP(0, 1);
  float a2[2], bv[2], m0[2];
  a2[0] = (dt * bf_lo(rar)) * 1.44269504088896341f; a2[1] = (dt * bf_hi(rar)) * 1.44269504088896341f;
  bv[0] = (dt * bf_lo(rai)) * 0.15915494309189534f; bv[1] = (dt * bf_hi(rai)) * 0.15915494309189534f;
  m0[0] = a2[0] > 0.f ? 2048.f : 0.f; m0[1] = a2[1] > 0.f ? 2048.f : 0.f;
  char* my = smem + lane * 8;
  if (w == 0) {
    cf v[2], v8[2], e8[2];
    float bound = 0.f;
#pragma unroll
    for (int q = 0; q < 2; ++q) {
      const float dbr = dt * (q ? bf_hi(rbr) : bf_lo(rbr)), dbi = dt * (q ? bf_hi(rbi) : bf_lo(rbi));
      const float cr = q ? bf_hi(rcr) : bf_lo(rcr), ci = q ? bf_hi(rci) : bf_lo(rci);
      const float gsh = __builtin_amdgcn_exp2f(a2[q] * m0[q]);
      v[q].re = (cr * dbr - ci * dbi) * gsh; v[q].im = -(cr * dbi + ci * dbr) * gsh;
      bound += (fabsf(v[q].re) + fabsf(v[q].im)) * __builtin_amdgcn_exp2f(fmaxf(a2[q], 0.f) * 120.f);
      e8[q] = cexp_(a2[q], -bv[q], 8.f, 0.f);
      v8[q] = cexp_(a2[q], -bv[q], 64.f, 0.f);
    }
#pragma unroll
    for (int o = 1; o < 64; o <<= 1) bound += __shfl_xor(bound, o, 64);
    int ex = (int)((__float_as_uint(bound) >> 23) & 0xff);
    ex = ex < 20 ? 20 : (ex > 240 ? 240 : ex);
    const float sv = __uint_as_float((unsigned)(127 + 13 + 127 - ex) << 23);
    if (lane == 0) ((float*)(p.ws + WS_KSC))[d] = __uint_as_float((unsigned)(ex - 13) << 23);
#pragma unroll
    for (int q = 0; q < 2; ++q) { v[q].re *= sv; v[q].im *= sv; v8[q] = cmul(v[q], v8[q]); }
#pragma unroll
    for (int j = 0; j < 8; ++j) {
      put2(my + (41 + j) * RS, v[0], v[1]);
      put2(my + (49 + j) * RS, v8[0], v8[1]);
      if (j < 7) {
#pragma unroll
        for (int q = 0; q < 2; ++q) { v[q] = cmul(v[q], e8[q]); v8[q] = cmul(v8[q], e8[q]); }
      }
    }
  } else if (w == 1) {
    cf e1[2], u[2], u2[2];
#pragma unroll
    for (int q = 0; q < 2; ++q) {
      e1[q] = cexp_(a2[q], bv[q], 1.f, 0.f);
      u[q].re = __builtin_amdgcn_exp2f(-a2[q] * m0[q]); u[q].im = 0.f;
      u2[q] = cexp_(a2[q], bv[q], 128.f, m0[q]);
    }
#pragma unroll
    for (int r = 0; r < 8; ++r) {
      put2(my + r * RS, u[0], u[1]);
      put2(my + (8 + r) * RS, u2[0], u2[1]);
      if (r < 7) {
#pragma unroll
        for (int q = 0; q < 2; ++q) { u[q] = cmul(u[q], e1[q]); u2[q] = cmul(u2[q], e1[q]); }
      }
    }
  } else if (w == 2) {
    cf e2[2], u[2], u2[2];
#pragma unroll
    for (int q = 0; q < 2; ++q) {
      e2[q] = cexp_(a2[q], bv[q], 2.f, 0.f);
      u[q] = cexp_(a2[q], bv[q], 256.f, m0[q]);
      u2[q] = cexp_(a2[q], bv[q], 384.f, m0[q]);
    }
#pragma unroll
    for (int r = 0; r < 4; ++r) {
      put2(my + (16 + r) * RS, u[0], u[1]);
      put2(my + (20 + r) * RS, u2[0], u2[1]);
      if (r < 3) {
#pragma unroll
        for (int q = 0; q < 2; ++q) { u[q] = cmul(u[q], e2[q]); u2[q] = cmul(u2[q], e2[q]); }
      }
    }
    cf e4[2] = {cmul(e2[0], e2[0]), cmul(e2[1], e2[1])};
#pragma unroll
    for (int r = 0; r < 2; ++r) {
      cf c0 = cexp_(a2[0], bv[0], 512.f + 128.f * r, m0[0]), c1 = cexp_(a2[1], bv[1], 512.f + 128.f * r, m0[1]);
      put2(my + (24 + 2 * r) * RS, c0, c1);
      put2(my + (25 + 2 * r) * RS, cmul(c0, e4[0]), cmul(c1, e4[1]));
    }
  } else {
    cf e4[2], e128[2], u[2], u2[2];
#pragma unroll
    for (int q = 0; q < 2; ++q) {
      e4[q] = cexp_(a2[q], bv[q], 4.f, 0.f);
      e128[q] = cexp_(a2[q], bv[q], 128.f, 0.f);
      u[q] = cexp_(a2[q], bv[q], 1024.f, m0[q]);
      u2[q] = cexp_(a2[q], bv[q], 1536.f, m0[q]);
    }
#pragma unroll
    for (int r = 2; r < 4; ++r) {
      cf c0 = cexp_(a2[0], bv[0], 512.f + 128.f * r, m0[0]), c1 = cexp_(a2[1], bv[1], 512.f + 128.f * r, m0[1]);
      put2(my + (24 + 2 * r) * RS, c0, c1);
      put2(my + (25 + 2 * r) * RS, cmul(c0, e4[0]), cmul(c1, e4[1]));
    }
#pragma unroll
    for (int r = 0; r < 4; ++r) {
      put2(my + (32 + r) * RS, u[0], u[1]);
      put2(my + (36 + r) * RS, u2[0], u2[1]);
#pragma unroll
      for (int q = 0; q < 2; ++q) { if (r < 3) u[q] = cmul(u[q], e128[q]); u2[q] = cmul(u2[q], e128[q]); }
    }
    put2(my + 40 * RS, u2[0], u2[1]);
  }
  __syncthreads();
  STAMP(0, 2);
  _Float16* kh = (_Float16*)(smem + 57 * RS);
  if (w < 3) {
    const char* ua = smem + (16 * w + i) * RS + g * 16;
    const char* va = smem + (41 + i) * RS + g * 16;
    f32x4 acc = {0.f, 0.f, 0.f, 0.f};
#pragma unroll
    for (int ks = 0; ks < 8; ++ks)
      acc = mfma_f16(*(const u32x4*)(ua + ks * 64), *(const u32x4*)(va + ks * 64), acc);
    int idx0, st;
    if (w == 0) { idx0 = (g < 2 ? 4 * g : 120 + 4 * g) + 8 * i; st = 1; }
    else if (w == 1) { idx0 = g < 2 ? 256 + 64 * g + 4 * i : 384 + 64 * (g - 2) + 2 * i; st = 1; }
    else { idx0 = 512 + 64 * g + i; st = 16; }
#pragma unroll
    for (int rg = 0; rg < 4; ++rg) {
      int idx = idx0 + st * rg;
      if (w == 1 && g >= 2) idx = idx0 + 32 * (rg >> 1) + (rg & 1);
      if (w == 2 && g >= 2) idx = (g == 2 && rg == 0 && i == 0) ? 640 : 644;
      kh[idx] = (_Float16)acc[rg];
    }
  }
  __syncthreads();
  STAMP(0, 3);
  const unsigned short* ku = (const unsigned short*)kh;
  const size_t kout = WS_KREV + (size_t)d * KREV * 2;
  {
    const int l0 = 2040 - 8 * tid;
    u32x4 o;
    if (w < 2) {
      const int q = l0 >> 3;
      const unsigned a = ku[384 + q], b = ku[385 + q];
      const unsigned t = (q & 1) ? b : a;
      o[0] = b | (b << 16); o[1] = b | (t << 16); o[2] = a | (a << 16); o[3] = o[2];
    } else if (w == 2) {
      const int q = l0 >> 2;
      const unsigned a = ku[256 + q], b = ku[257 + q], c = ku[258 + q];
      o[0] = c | (c << 16); o[1] = b | (b << 16); o[2] = b | (a << 16); o[3] = a | (a << 16);
    } else {
      unsigned v[8];
#pragma unroll
      for (int e = 0; e < 8; ++e) {
        const int l = l0 + 7 - e;
        const int idx = l < 256 ? l : (int)(pk_bf((float)l, 0.f) & 0xffffu) - 17024;
        v[e] = ku[idx];
      }
#pragma unroll
      for (int q = 0; q < 4; ++q) o[q] = v[2 * q] | (v[2 * q + 1] << 16);
    }
    st16(ws_rsrc(p.ws), kout + 16 * tid, o);
  }
}

__global__ NOPK __launch_bounds__(256) void k1(K1Args p) {
  __shared__ __attribute__((aligned(16))) char smem[57 * KG_RS + 1312];
  int b = blockIdx.x;
  char* stamp_ws = p.ws; (void)stamp_ws;
  STAMP(0, 0);
  if (b < NB_PF) { k1_prefetch(p, b); STAMP(0, 7); return; }
  b -= NB_PF;
  if (b < NB_K) { k1_kgen(p, b, smem); STAMP(0, 7); return; }
  b -= NB_K;
  __builtin_amdgcn_s_setprio(3);
  if (b < NB_LN) {
    k1_ln(p, b, smem);
    STAMP(0, 7);
    return;
  }
  b -= NB_LN;
  k1_mod(p, b, smem);
  STAMP(0, 7);
}

constexpr int K2_CS = 4160;
constexpr int K2_KOFF = 4 * K2_CS;
constexpr int K2_XOFF = K2_CS;
constexpr int K2_CH = K2_KOFF + 4608;
constexpr int CV_RED = 3 * K2_CH;
constexpr int CV_RT2 = 86016;
constexpr int CV_RT = 151552;

struct Chan { float sa, ca1, ga, dsk, hs, hinv, yinv; };
struct ChanRaw { ModP msa, mca, mga; float kinv; unsigned short rds; };
DEV ChanRaw chan_ld(const unsigned short* dskip, const char* ws, int d) {
  ChanRaw c;
  c.rds = dskip[d];
  c.msa = mod_ld(ws, d); c.mca = mod_ld(ws, D_ + d); c.mga = mod_ld(ws, 2 * D_ + d);
  c.kinv = ((const float*)(ws + WS_KSC))[d];
  return c;
}
DEV Chan chan_fin(const ChanRaw& r) {
  Chan c;
  c.sa = mod_fin(r.msa); c.ca1 = rbf(1.f + mod_fin(r.mca)); c.ga = mod_fin(r.mga);
  c.dsk = __uint_as_float((unsigned)r.rds << 16);
  const float hb = 26.f * fabsf(c.ca1) + fabsf(c.sa);
  int ex = (int)((__float_as_uint(hb) >> 23) & 0xff);
  ex = ex < 20 ? 20 : (ex > 240 ? 240 : ex);
  c.hs = __uint_as_float((unsigned)(127 + 14 + 127 - ex) << 23);
  c.hinv = __uint_as_float((unsigned)(ex - 14) << 23);
  c.yinv = r.kinv * c.hinv;
  return c;
}
struct StageRaw { u32x4 xa0, xa1, kv0, kv1; u32x2 xb; };
DEV StageRaw stage_ld(const char* ws, int d, int th) {
  StageRaw s;
  const unsigned short* krev = (const unsigned short*)(ws + WS_KREV) + (size_t)d * KREV;
  const size_t xo = ((size_t)d * L_ + 16 * th) * 2;
  s.xa0 = *(const u32x4*)(ws + WS_XNT + xo); s.xa1 = *(const u32x4*)(ws + WS_XNT + xo + 16);
  s.xb = *(const u32x2*)(ws + WS_XNT + xo + (th < 127 ? 32 : 0));
  s.kv0 = *(const u32x4*)(krev + 8 * th); s.kv1 = *(const u32x4*)(krev + 1024 + 8 * th);
  return s;
}
DEV void stage_wr(const StageRaw& s, const Chan& c, char* smem, int th) {
  constexpr int CS = K2_CS, KOFF = K2_KOFF;
  unsigned hh[10];
#pragma unroll
  for (int q = 0; q < 10; ++q) {
    unsigned u = q < 4 ? s.xa0[q] : (q < 8 ? s.xa1[q - 4] : s.xb[q - 8]);
    unsigned m = pk_bf(bf_lo(u) * c.ca1, bf_hi(u) * c.ca1);
    unsigned h = pk_bf(bf_lo(m) + c.sa, bf_hi(m) + c.sa);
    hh[q] = pk_h(bf_lo(h) * c.hs, bf_hi(h) * c.hs);
  }
  char* dst = smem + (2 * th + 2) * 16;
  unsigned s1[9];
#pragma unroll
  for (int q = 0; q < 9; ++q) s1[q] = __builtin_amdgcn_alignbit(hh[q + 1], hh[q], 16);
#pragma unroll
  for (int e = 0; e < 2; ++e) {
    *(u32x4*)(dst + 16 * e) = (u32x4){hh[4 * e], hh[4 * e + 1], hh[4 * e + 2], hh[4 * e + 3]};
    *(u32x4*)(dst + 16 * e + CS) = (u32x4){s1[4 * e], s1[4 * e + 1], s1[4 * e + 2], s1[4 * e + 3]};
    *(u32x4*)(dst + 16 * e + 2 * CS) = (u32x4){hh[4 * e + 1], hh[4 * e + 2], hh[4 * e + 3], hh[4 * e + 4]};
    *(u32x4*)(dst + 16 * e + 3 * CS) = (u32x4){s1[4 * e + 1], s1[4 * e + 2], s1[4 * e + 3], s1[4 * e + 4]};
  }
  if (th == 0) {
    u32x4 z = {0u, 0u, 0u, 0u};
    *(u32x4*)(smem) = z; *(u32x4*)(smem + CS) = z; *(u32x4*)(smem + 2 * CS) = z; *(u32x4*)(smem + 3 * CS) = z;
    *(u32x4*)(smem + 16) = z;
    *(u32x4*)(smem + CS + 16) = (u32x4){0u, 0u, 0u, hh[0] << 16};
    *(u32x4*)(smem + 2 * CS + 16) = (u32x4){0u, 0u, 0u, hh[0]};
    *(u32x4*)(smem + 3 * CS + 16) = (u32x4){0u, 0u, hh[0] << 16, s1[0]};
  }
  *(u32x4*)(smem + KOFF + 16 * th) = s.kv0;
  *(u32x4*)(smem + KOFF + 2048 + 16 * th) = s.kv1;
  if (th < 32) *(u32x4*)(smem + KOFF + 4096 + 16 * th) = (u32x4){0u, 0u, 0u, 0u};
}
DEV u32x2 conv_out(const f32x4& y, const Chan& c, const char* smem, int t, const u32x2& xres) {
  const u32x2 hv = *(const u32x2*)(smem + 2 * (t + 16));
  u32x2 o;
#pragma unroll
  for (int e = 0; e < 2; ++e) {
    const unsigned hu = hv[e];
    const h16x2 h2 = __builtin_bit_cast(h16x2, hu);
    const float h0 = (float)h2[0] * c.hinv, h1 = (float)h2[1] * c.hinv;
    unsigned yb = pk_bf(y[2 * e] * c.yinv, y[2 * e + 1] * c.yinv);
    unsigned hd = pk_bf(h0 * c.dsk, h1 * c.dsk);
    unsigned y2 = pk_bf(bf_lo(yb) + bf_lo(hd), bf_hi(yb) + bf_hi(hd));
    unsigned gy = pk_bf(bf_lo(y2) * c.ga, bf_hi(y2) * c.ga);
    o[e] = pk_bf(bf_lo(xres[e]) + bf_lo(gy), bf_hi(xres[e]) + bf_hi(gy));
  }
  return o;
}
#define K2_LOAD(buf, cc, u)                                                                                     \
  asm volatile("ds_read_b64 %0, %3 offset:%5\n\tds_read_b64 %1, %3 offset:%6\n\tds_read_b128 %2, %4 offset:%7"   \
               : "=&v"(Al[buf][u]), "=&v"(Ah[buf][u]), "=&v"(Bf[buf][u])                                          \
               : "v"(ap0), "v"(bp0), "i"(64 * (cc) + 512 * (u)), "i"(64 * (cc) + 512 * (u) + 8),                 \
                 "i"(64 * (cc) + 3584 - 512 * (u)))
template <int SET> DEV constexpr int half_tile(int j) {
  return SET == 0 ? (j == 0 ? 7 : (j == 1 ? 6 : 2)) : (j < 3 ? 5 - j : 4 - j);
}
template <int SET>
DEV void conv_half(unsigned ap0, unsigned bp0, char* redw) {
  constexpr int NU = SET == 0 ? 8 : 6, NT = SET == 0 ? 3 : 5;
  u32x2 Al[2][NU], Ah[2][NU];
  u32x4 Bf[2][NU];
#pragma unroll
  for (int u = 0; u < NU; ++u) K2_LOAD(0, 0, u);
  f32x4 acc[NT];
#pragma unroll
  for (int j = 0; j < NT; ++j) acc[j] = (f32x4){0.f, 0.f, 0.f, 0.f};
#pragma unroll
  for (int cc = 0; cc < 2; ++cc) {
    lgkm0();
    if (cc == 0) {
#pragma unroll
      for (int u = 0; u < NU; ++u) K2_LOAD(1, 1, u);
    }
#pragma unroll
    for (int sp = 0; sp < NU; ++sp)
#pragma unroll
      for (int j = 0; j < NT; ++j) {
        constexpr int dummy = 0; (void)dummy;
        const int q = half_tile<SET>(j);
        if (sp <= q) {
          const u32x4 A = {Al[cc][sp][0], Al[cc][sp][1], Ah[cc][sp][0], Ah[cc][sp][1]};
          acc[j] = mfma_f16(A, Bf[cc][q - sp], acc[j]);
        }
      }
    SB();
  }
#pragma unroll
  for (int j = 0; j < NT; ++j) *(f32x4*)(redw + j * 4096) = acc[j];
}

template <int NF>
DEV void w1_fin(const RtRaw<NF>& t, const ModP& mp, char* ws, int kk, int ct0, int lane, char* scr) {
  const int g = lane >> 4, i = lane & 15;
  float* tb = (float*)(scr + 32 * RT_STR);
  const float mv = mod_fin(mp);
  tb[lane] = lane < 32 ? rbf(1.f + mv) : mv;
  rt_park<NF>(t, scr, lane);
  lgkm0();
  const f32x4 ca = *(const f32x4*)(tb + 8 * g), cb = *(const f32x4*)(tb + 8 * g + 4);
  const f32x4 sa = *(const f32x4*)(tb + 32 + 8 * g), sb = *(const f32x4*)(tb + 36 + 8 * g);
  const u32x4 sv = {pk_bf(sa[0], sa[1]), pk_bf(sa[2], sa[3]), pk_bf(sb[0], sb[1]), pk_bf(sb[2], sb[3])};
  const u32x4 ones = {0x3f803f80u, 0x3f803f80u, 0x3f803f80u, 0x3f803f80u};
  const f32x4 z = {0.f, 0.f, 0.f, 0.f};
  const rsrc_t rs = ws_rsrc(ws);
#pragma unroll
  for (int j = 0; j < NF; ++j) {
    const u32x4 f = rt_frag(scr, j, lane);
    const u32x4 sc = {pk_bf(bf_lo(f[0]) * ca[0], bf_hi(f[0]) * ca[1]), pk_bf(bf_lo(f[1]) * ca[2], bf_hi(f[1]) * ca[3]),
                      pk_bf(bf_lo(f[2]) * cb[0], bf_hi(f[2]) * cb[1]), pk_bf(bf_lo(f[3]) * cb[2], bf_hi(f[3]) * cb[3])};
    st16(rs, WS_W1F + ((size_t)(kk * 108 + ct0 + j) * 64 + lane) * 16, sc);
    const f32x4 cs = mfma_bf16(sc, ones, z), b1 = mfma_bf16(f, sv, z);
    if (i < 2) st16(rs, WS_CSP + ((size_t)(2 * kk + i) * FF + 16 * (ct0 + j) + 4 * g) * 4, __builtin_bit_cast(u32x4, i ? b1 : cs));
  }
}

DEV void conv_phase(const unsigned short* __restrict__ dskip, char* __restrict__ ws,
                    const unsigned short* __restrict__ w1, const unsigned short* __restrict__ w2, char* smem_all) {
  constexpr int CS = K2_CS, KOFF = K2_KOFF;
  const int tid = threadIdx.x, lane = tid & 63;
  const int w = __builtin_amdgcn_readfirstlane(tid >> 6);
  char* stamp_ws = ws; (void)stamp_ws;
  STAMP(1, 0);
  const int bx = blockIdx.x;
  const int pj = (bx & 7) * 16 + (bx >> 4), par = (bx >> 3) & 1;
  const int r = lane & 15, g = lane >> 4, th = tid & 127;
  const int s4 = (r + 1) & 3;
  const rsrc_t rs = ws_rsrc(ws);
  if (w < 4) {
    const int d = 5 * pj + 2 * par + (w >> 1), hw = w & 1;
    char* smem = smem_all + (w >> 1) * K2_CH;
    const StageRaw sr = stage_ld(ws, d, th);
    const ChanRaw cr = chan_ld(dskip, ws, d);
    u32x2 xres[4];
#pragma unroll
    for (int q = 0; q < 4; ++q)
      xres[q] = *(const u32x2*)(ws + WS_XT + ((size_t)d * L_ + 256 * (4 * hw + q) + 16 * r + 4 * g) * 2);
    SB();
    const Chan c = chan_fin(cr);
    stage_wr(sr, c, smem, th);
    __syncthreads();
    STAMP(1, 2);
    f32x4 acc[8];
#pragma unroll
    for (int q = 0; q < 8; ++q) acc[q] = (f32x4){0.f, 0.f, 0.f, 0.f};
    {
      const unsigned ap0 = (unsigned)(size_t)(smem + s4 * CS + 2 * (128 * hw + 8 * g + (r + 1) - s4));
      const unsigned bp0 = (unsigned)(size_t)(smem + KOFF + 2 * (2032 + 128 * hw - 16 * r + 8 * g) - 3584);
      u32x2 Al[2][8], Ah[2][8];
      u32x4 Bf[2][8];
#pragma unroll
      for (int u = 0; u < 8; ++u) K2_LOAD(0, 0, u);
#pragma unroll
      for (int cc = 0; cc < 4; ++cc) {
        lgkm0();
#pragma unroll
        for (int q = 0; q < 8; ++q) {
          if (cc < 3) K2_LOAD((cc + 1) & 1, cc + 1, q);
#pragma unroll
          for (int sp = 0; sp <= q; ++sp) {
            const u32x4 A = {Al[cc & 1][sp][0], Al[cc & 1][sp][1], Ah[cc & 1][sp][0], Ah[cc & 1][sp][1]};
            acc[q] = mfma_f16(A, Bf[cc & 1][q - sp], acc[q]);
          }
          SB();
        }
      }
    }
    STAMP(1, 3);
    bar_lds();
    f32x4 keep[4];
    {
      f32x4* xw = (f32x4*)(smem + K2_XOFF + hw * 4096) + lane;
      if (hw) {
#pragma unroll
        for (int q = 0; q < 4; ++q) { xw[64 * q] = acc[q]; keep[q] = acc[4 + q]; }
      } else {
#pragma unroll
        for (int q = 0; q < 4; ++q) { xw[64 * q] = acc[4 + q]; keep[q] = acc[q]; }
      }
    }
    bar_lds();
    const f32x4* xrd = (const f32x4*)(smem + K2_XOFF + (hw ^ 1) * 4096) + lane;
#pragma unroll
    for (int qp = 0; qp < 2; ++qp) {
      u32x2 o[2];
#pragma unroll
      for (int h = 0; h < 2; ++h) {
        const int q = 2 * qp + h;
        o[h] = conv_out(keep[q] + xrd[64 * q], c, smem, 256 * (4 * hw + q) + 16 * r + 4 * g, xres[q]);
      }
      const auto s0 = __builtin_amdgcn_permlane16_swap(o[0][0], o[1][0], false, false);
      const auto s1 = __builtin_amdgcn_permlane16_swap(o[0][1], o[1][1], false, false);
      const int t = 256 * (4 * hw + 2 * qp + (g & 1)) + 16 * r + 8 * (g >> 1);
      st16(rs, WS_X1T + (((size_t)(t >> 5) * D_ + d) * 32 + (t & 31)) * 2, (u32x4){s0[0], s1[0], s0[1], s1[1]});
    }
  } else {
    __builtin_amdgcn_s_setprio(2);
    const int d = 5 * pj + 4, wq = w - 4;
    char* smem = smem_all + 2 * K2_CH;
    const ChanRaw cr = chan_ld(dskip, ws, d);
    const int tl0 = par ? (wq < 3 ? 5 - wq : 1) : (wq == 0 ? 7 : (wq == 1 ? 6 : 2)), tl1 = 0;
    const int rtk = min(bx, 239) / 12, rtc = (min(bx, 239) % 12) * 9;
    const unsigned ap0 = (unsigned)(size_t)(smem + s4 * CS + 2 * (64 * wq + 8 * g + (r + 1) - s4));
    const unsigned bp0 = (unsigned)(size_t)(smem + KOFF + 2 * (2032 + 64 * wq - 16 * r + 8 * g) - 3584);
    char* redw = smem_all + CV_RED + wq * 1024 + lane * 16;
    u32x2 xres[2];
    Chan c;
    if (wq < 2) {
      const StageRaw sr = stage_ld(ws, d, th);
      xres[0] = *(const u32x2*)(ws + WS_XT + ((size_t)d * L_ + 256 * tl0 + 16 * r + 4 * g) * 2);
      xres[1] = *(const u32x2*)(ws + WS_XT + ((size_t)d * L_ + 256 * tl1 + 16 * r + 4 * g) * 2);
      SB();
      c = chan_fin(cr);
      stage_wr(sr, c, smem, th);
      const int k2k = min(bx, 215) >> 2, k2c = (min(bx, 215) & 3) * 10 + 5 * wq;
      RtRaw<5> rt;
      rt_load<5>(rt, w2, D_, k2k, k2c, lane);
      SB();
      bar_lds();
      if (par) conv_half<1>(ap0, bp0, redw); else conv_half<0>(ap0, bp0, redw);
      if (bx < 216) {
        char* scr = smem_all + CV_RT2 + wq * (32 * RT_STR);
        rt_park<5>(rt, scr, lane);
        lgkm0();
#pragma unroll
        for (int j = 0; j < 5; ++j) st16(rs, WS_W2F + ((size_t)((k2c + j) * 54 + k2k) * 64 + lane) * 16, rt_frag(scr, j, lane));
      }
    } else {
      xres[0] = *(const u32x2*)(ws + WS_XT + ((size_t)d * L_ + 256 * tl0 + 16 * r + 4 * g) * 2);
      xres[1] = xres[0];
      c = chan_fin(cr);
#define RT_DELAY 16
      __builtin_amdgcn_s_sleep(RT_DELAY);
      const ModP mp = mod_ld(ws, (lane < 32 ? 4 * D_ : 3 * D_ - 32) + 32 * rtk + lane);
      char* scr = smem_all + CV_RT + (wq - 2) * (32 * RT_STR + 256);
      if (wq == 2) {
        RtRaw<5> rt;
        rt_load<5>(rt, w1, FF, rtk, rtc, lane);
        SB();
        __builtin_amdgcn_s_barrier();
        asm volatile("" ::: "memory");
        if (par) conv_half<1>(ap0, bp0, redw); else conv_half<0>(ap0, bp0, redw);
        if (bx < 240) w1_fin<5>(rt, mp, ws, rtk, rtc, lane, scr);
      } else {
        RtRaw<4> rt;
        rt_load<4>(rt, w1, FF, rtk, rtc + 5, lane);
        SB();
        __builtin_amdgcn_s_barrier();
        asm volatile("" ::: "memory");
        if (par) conv_half<1>(ap0, bp0, redw); else conv_half<0>(ap0, bp0, redw);
        if (bx < 240) w1_fin<4>(rt, mp, ws, rtk, rtc + 5, lane, scr);
      }
    }
    bar_lds();
    bar_lds();
    const char* rd = smem_all + CV_RED + lane * 16;
#pragma unroll
    for (int u = 0; u < 2; ++u) {
      const bool on = u == 0 ? (par || wq < 3) : (par && wq == 0);
      if (on) {
        const int sl = u == 0 ? wq : 4, tl = u == 0 ? tl0 : tl1;
        const f32x4 y = (*(const f32x4*)(rd + sl * 4096) + *(const f32x4*)(rd + sl * 4096 + 1024))
                      + (*(const f32x4*)(rd + sl * 4096 + 2048) + *(const f32x4*)(rd + sl * 4096 + 3072));
        const int t = 256 * tl + 16 * r + 4 * g;
        st8(rs, WS_X1T + (((size_t)(t >> 5) * D_ + d) * 32 + (t & 31)) * 2, conv_out(y, c, smem, t, xres[u]));
      }
    }
  }
  STAMP(1, 7);
}

__global__ NOPK __launch_bounds__(512, 2) void k2(const unsigned short* __restrict__ dskip, char* __restrict__ ws,
                                                  const unsigned short* __restrict__ w1, const unsigned short* __restrict__ w2) {
  __shared__ __attribute__((aligned(16))) char smem[CV_RT + 2 * (32 * RT_STR + 256)];
  conv_phase(dskip, ws, w1, w2, smem);
}

DEV u32x4 ld16_sc1(rsrc_t r, int voff, int soff) { return __builtin_amdgcn_raw_buffer_load_b128(r, voff, soff, 16); }
DEV u32x4 ld16(rsrc_t r, int voff, int soff) { return __builtin_amdgcn_raw_buffer_load_b128(r, voff, soff, 0); }
DEV u32x4 tr_frag(const char* p) {
  const u32x2 a = __builtin_bit_cast(u32x2, __builtin_amdgcn_ds_read_tr16_b64_v4i16((lds_s16x4*)p));
  const u32x2 b = __builtin_bit_cast(u32x2, __builtin_amdgcn_ds_read_tr16_b64_v4i16((lds_s16x4*)(p + 256)));
  return (u32x4){a[0], a[1], b[0], b[1]};
}

constexpr int KF_RS = 84;
constexpr int KF_RB = 8 * 16 * KF_RS * 4;
constexpr int KF_BIG = 2 * KF_RB;
#define NW2L_ 8
constexpr int NW2L = NW2L_;
constexpr int NW2R = 35 - NW2L;
constexpr int KF_W2L = KF_BIG;
constexpr int KF_TAB = KF_W2L + 8 * NW2L * 1024;
constexpr int KF_PS = KF_TAB + 320;
constexpr int KF_XR = KF_PS + 512;
constexpr int KF_CNT = KF_XR + 64 * 176;
constexpr int KF_LDS = KF_CNT + 16;
#define W2_START 10
#define W2_RATE 3
#define W2_INL 28
#define SIG_SPLIT 4
#define H1_B0 1
#define H1_BATCH 7
#define W2_MID 4
#define W1_PRE 10
#define W1_EARLY 3
#define STAMP_EXTRA 0
#define H1_MODE 0
#define H1_PRE 17

DEV void kf_body(char* __restrict__ ws, unsigned short* __restrict__ out, char* smem) {
  char* xs = smem;
  float* tab = (float*)(smem + KF_TAB);
  float* ps = (float*)(smem + KF_PS);
  char* xr = smem + KF_XR;
  unsigned* lcnt = (unsigned*)(smem + KF_CNT);
  float* cst = (float*)(smem + 81920);
  const int tid = threadIdx.x, lane = tid & 63;
  const int w = __builtin_amdgcn_readfirstlane(tid >> 6);
  const int bx = blockIdx.x;
  const int ng = (bx >> 3) & 7, mt = (bx & 7) * 4 + (bx >> 6);
  const int g = lane >> 4, i = lane & 15;
  char* stamp_ws = ws; (void)stamp_ws;
  STAMP(2, 0); STAMP(5, 0);
  const rsrc_t rs_ = ws_rsrc(ws);

  unsigned* flags = (unsigned*)(ws + WS_CNT) + mt * 128;
  const int ntl = ng < 6 ? 14 : 12;
  const int ctb = ng < 6 ? 14 * ng : 84 + 12 * (ng - 6);
  const bool two = w < ntl - 8;
  const bool full = w < 6;
  const int ks0 = w < 6 ? 7 * w : 42 + 6 * (w - 6);
  const int ct0 = ctb + (two ? 2 * w : (ntl - 8) + w);
  const int lo16 = lane * 16;
  const int w1s = (int)WS_W1F + ct0 * 1024;
  const int w2s = (int)WS_W2F + (ng * 5 * 54 + ks0) * 1024;

  u32x4 ring[32];
  const char* bb0 = xs + (8 * g + (i >> 2)) * 64 + ((g & 1) << 5) + (i & 3) * 8;
  const char* bb1 = xs + (8 * g + (i >> 2)) * 64 + (((g & 1) ^ 1) << 5) + (i & 3) * 8;
  auto w2_off = [&](int e) __attribute__((always_inline)) {
    const int s = e < 27 ? e / 4 : (e < 34 ? e - 27 : 6), c = e < 27 ? e % 4 : (e < 34 ? 4 : 3);
    const int sn = (s == 6 && !full) ? 5 : s;
    return w2s + (c * 54 + sn) * 1024;
  };

  const unsigned w2lb = (unsigned)(size_t)(smem + KF_W2L) + w * NW2L * 1024;
  auto w2_dma = [&](int n) __attribute__((always_inline)) {
    asm volatile("s_mov_b32 m0, %0\n\ts_nop 0\n\tbuffer_load_dwordx4 %1, %2, %3 offen lds"
                 :: "s"(w2lb + n * 1024), "v"(lo16), "s"(rs_), "s"(w2_off(NW2R + n)) : "memory");
  };

  auto gemm1 = [&](auto two_tag) __attribute__((always_inline)) {
    constexpr bool TWO = decltype(two_tag)::value;
    if (TWO) asm volatile("; two tiles" ::: "memory"); else asm volatile("; one tile" ::: "memory");
    u32x4 t[10];
#pragma unroll
    for (int it = 0; it < 10; ++it) t[it] = ld16(rs_, tid * 16, (int)WS_X1T + mt * 81920 + 8192 * it);
#pragma unroll
    for (int k = 0; k < W1_EARLY; ++k) {
      ring[2 * k] = ld16(rs_, lo16, w1s + k * 108 * 1024);
      if (TWO) ring[2 * k + 1] = ld16(rs_, lo16, w1s + (k * 108 + 1) * 1024);
    }
    SB();
    __builtin_amdgcn_s_barrier();
    SB();
#pragma unroll
    for (int k = W1_EARLY; k < W1_PRE; ++k) {
      ring[2 * k] = ld16(rs_, lo16, w1s + k * 108 * 1024);
      if (TWO) ring[2 * k + 1] = ld16(rs_, lo16, w1s + (k * 108 + 1) * 1024);
    }
    SB();
#pragma unroll
    for (int it = 0; it < 10; ++it) {
      const int c = tid + 512 * it;
      const int row = c >> 2;
      *(u32x4*)(xs + row * 64 + (((c & 3) ^ ((row >> 2) & 2)) << 4)) = t[it];
    }
    SB();
    if (tid == 0) *lcnt = 0u;
    ModP gbp;
    if (!TWO) gbp = mod_ld(ws, 5 * D_ + 80 * ng + min((w & 1) * 64 + lane, 79));
    SB();
    __syncthreads();
    STAMP(2, 1);
    {
      const int kb = (80 * ng) >> 5, tq = w & 3;
#pragma unroll
      for (int u = 0; u < 2; ++u) {
        const int kk = min(kb + 2 * (w >> 2) + u, 19);
        const u32x4 f = tr_frag(((tq & 1) ? bb1 : bb0) + (tq >> 1) * 40960 + kk * 2048);
        const unsigned dl = (unsigned)(32 * kk + 8 * g - 80 * ng);
        if (dl < 80u) *(u32x4*)(xr + (tq * 16 + i) * 176 + dl * 2) = f;
      }
    }
    const int hs = TWO ? 0 : (w & 1);
    const char* ba[4] = {bb0 + hs * 40960, bb1 + hs * 40960, bb0 + (hs ^ 1) * 40960, bb1 + (hs ^ 1) * 40960};
    const u32x4 ones = {0x3f803f80u, 0x3f803f80u, 0x3f803f80u, 0x3f803f80u};
    f32x4 acc[2][4];
#pragma unroll
    for (int c = 0; c < 2; ++c)
#pragma unroll
      for (int q = 0; q < 4; ++q) acc[c][q] = (f32x4){0.f, 0.f, 0.f, 0.f};
    u32x4 csp[20];
    const int cso = ((w & 1) * FF + 16 * ctb + 4 * min(lane, 4 * ntl - 1)) * 4;
    u32x4 bn[2][4];
#pragma unroll
    for (int q = 0; q < 4; ++q) bn[0][q] = tr_frag(ba[q]);
#pragma unroll
    for (int q = 0; q < 4; ++q) bn[1][q] = tr_frag(ba[q] + 2048);
#pragma unroll
    for (int kk = 0; kk < 20; ++kk) {
      u32x4 b[4];
#pragma unroll
      for (int q = 0; q < 4; ++q) b[q] = bn[kk & 1][q];
      SB();
#pragma unroll
      for (int q = 0; q < 4; ++q) acc[0][q] = mfma_bf16(ring[(2 * kk) % 32], b[q], acc[0][q]);
      if (TWO) {
#pragma unroll
        for (int q = 0; q < 4; ++q) acc[1][q] = mfma_bf16(ring[(2 * kk + 1) % 32], b[q], acc[1][q]);
      } else {
        acc[1][0] = mfma_bf16(ones, b[0], acc[1][0]); acc[1][1] = mfma_bf16(b[0], b[0], acc[1][1]);
        acc[1][2] = mfma_bf16(ones, b[1], acc[1][2]); acc[1][3] = mfma_bf16(b[1], b[1], acc[1][3]);
      }
      SB();
      if (kk < 18) {
#pragma unroll
        for (int q = 0; q < 4; ++q) bn[kk & 1][q] = tr_frag(ba[q] + (kk + 2) * 2048);
      }
      if (kk + W1_PRE < 20) {
        const int kn = kk + W1_PRE;
        ring[(2 * kn) % 32] = ld16(rs_, lo16, w1s + kn * 108 * 1024);
        if (TWO) ring[(2 * kn + 1) % 32] = ld16(rs_, lo16, w1s + (kn * 108 + 1) * 1024);
      }
      if (kk >= W2_START) {
#pragma unroll
        for (int u = 0; u < W2_RATE; ++u) {
          const int n = W2_RATE * (kk - W2_START) + u;
          if (n < NW2L) w2_dma(n);
          else if (n < W2_INL) ring[8 + n - NW2L] = ld16(rs_, lo16, w2_off(n - NW2L));
        }
      }
      if (!TWO) {
        if (kk < 10) {
#pragma unroll
          for (int u = 0; u < 2; ++u)
            csp[2 * kk + u] = ld16(rs_, cso, (int)WS_CSP + (2 * kk + u) * 2 * FF * 4);
        }
        if (kk == 14) {
          f32x4 t = __builtin_bit_cast(f32x4, csp[0]);
#pragma unroll
          for (int u = 1; u < 20; ++u) t += __builtin_bit_cast(f32x4, csp[u]);
          if (w >= 6 && lane < 56) *(f32x4*)(cst + (w & 1) * 224 + 4 * lane) = t;
        }
      }
      if (!TWO && kk == 12) { if ((w & 1) * 64 + lane < 80) tab[(w & 1) * 64 + lane] = mod_fin(gbp); }
      SB();
    }
    if (!TWO) {
#pragma unroll
      for (int h = 0; h < 2; ++h) {
        const f32x4 aq = acc[1][2 * h + 1];
        const float dq = (i & 2) ? ((i & 1) ? aq[3] : aq[2]) : ((i & 1) ? aq[1] : aq[0]);
        const int tk = (2 * hs + h) * 16 + i;
        if (g == (i >> 2)) {
          const float mean = acc[1][2 * h][0] * (1.f / D_);
          const float var = fmaxf(dq * (1.f / D_) - mean * mean, 0.f);
          const float sd = rbf(sqrtf(rbf(rbf(var) + 1e-5f)));
          const float rs = 1.f / sd;
          ps[tk] = rs;
          ps[64 + tk] = -rbf(mean) * rs;
        }
      }
    }
    __syncthreads();
    STAMP(2, 2);
    f32x4 csv[2], b1v[2];
#pragma unroll
    for (int c = 0; c < (TWO ? 2 : 1); ++c) {
      csv[c] = *(const f32x4*)(cst + 16 * (ct0 - ctb + c) + 4 * g);
      b1v[c] = *(const f32x4*)(cst + 224 + 16 * (ct0 - ctb + c) + 4 * g);
    }
#pragma unroll
    for (int qp = 0; qp < 2; ++qp) {
      u32x2 o[2][2];
#pragma unroll
      for (int h = 0; h < 2; ++h) {
        const int q = 2 * qp + h, tq = q ^ (2 * hs);
        const float rs = ps[tq * 16 + i], nm = ps[64 + tq * 16 + i];
#pragma unroll
        for (int c = 0; c < (TWO ? 2 : 1); ++c) {
          float v[4];
#pragma unroll
          for (int r = 0; r < 4; ++r) v[r] = fmaxf(fmaf(acc[c][q][r], rs, fmaf(nm, csv[c][r], b1v[c][r])), 0.f);
          o[c][h] = (u32x2){pk_bf(v[0], v[1]), pk_bf(v[2], v[3])};
        }
      }
      const int tl = ((2 * qp) ^ (2 * hs)) + (g & 1);
#pragma unroll
      for (int c = 0; c < (TWO ? 2 : 1); ++c) {
        const int ct = ct0 + c;
        const auto s0 = __builtin_amdgcn_permlane16_swap(o[c][0][0], o[c][1][0], false, false);
        const auto s1 = __builtin_amdgcn_permlane16_swap(o[c][0][1], o[c][1][1], false, false);
        const size_t off = (((size_t)(4 * mt + tl) * 54 + (ct >> 1)) * 64 + i + 16 * (2 * (ct & 1) + (g >> 1))) * 16;
#define H1_ST 16
        __builtin_amdgcn_raw_buffer_store_b128((u32x4){s0[0], s1[0], s0[1], s1[1]}, rs_, (int)(WS_H1F + off), 0, H1_ST);
      }
    }
    SB();
    STAMP(5, 1);
    __builtin_amdgcn_s_barrier();
    SB();
    constexpr int E0 = W2_INL - NW2L;
    auto signal = [&]() __attribute__((always_inline)) {
      if (lane == 0) {
        if (__hip_atomic_fetch_add(lcnt, 1u, __ATOMIC_RELAXED, __HIP_MEMORY_SCOPE_WORKGROUP) == 7u)
          __hip_atomic_store(flags + 16 * ng, 1u, __ATOMIC_RELAXED, __HIP_MEMORY_SCOPE_AGENT);
      }
    };
    if (w >= SIG_SPLIT) { asm volatile("s_waitcnt vmcnt(0)" ::: "memory"); signal(); }
    SB();
#pragma unroll
    for (int e = E0; e < NW2R; ++e) ring[(8 + e) % 32] = ld16(rs_, lo16, w2_off(e));
    SB();
    STAMP(5, 2);
    if (w < SIG_SPLIT) { asm volatile("s_waitcnt vmcnt(%0)" :: "n"(NW2R - E0) : "memory"); signal(); }
    SB();
  };
  if (two) gemm1(std::true_type{});
  else gemm1(std::false_type{});
  STAMP(2, 3);
#pragma nounroll
  for (unsigned spin = 0; spin < (1u << 20); ++spin) {
    if (__hip_atomic_load(flags + 16 * w, __ATOMIC_RELAXED, __HIP_MEMORY_SCOPE_AGENT) != 0u) break;
    __builtin_amdgcn_s_sleep(1);
  }
  asm volatile("" ::: "memory");
  SB();
  STAMP(2, 4);

  const int h1s = (int)WS_H1F + (mt * 4 * 54 + ks0) * 1024;
  u32x4 hb[28];
  auto load_h1 = [&](int n) __attribute__((always_inline)) {
    const int q = n / 7, s = n % 7;
    const int sn = (s == 6 && !full) ? 5 : s;
    hb[n] = ld16_sc1(rs_, lo16, h1s + (q * 54 + sn) * 1024);
  };
#pragma unroll
  for (int n = 0; n < H1_PRE; ++n) {
    if (H1_BATCH && n % H1_BATCH == 0 && n >= H1_B0) { SB(); __builtin_amdgcn_s_barrier(); SB(); }
    load_h1(n);
  }
  SB();
  float* red = (float*)smem;
  float* rb_ = red + (w * 16 + i) * KF_RS + 4 * g;
  const int re = w * 40 + min(lane, 39);
  const int rtk = re / 20, rq4 = re % 20;
  const float* rr_ = red + rtk * KF_RS + 4 * rq4;
  f32x4 ac2[2][5];
  f32x4 rv[4], rsum;
  const char* w2l = smem + KF_W2L + w * NW2L * 1024 + lo16;
  u32x4 wl[2], wl3;
  wl[0] = *(const u32x4*)w2l;
  auto red_wr = [&](int q, int c) __attribute__((always_inline)) {
    *(f32x4*)(rb_ + (q & 1) * (KF_RB / 4) + 16 * c) = ac2[q & 1][c];
  };
  auto red_rd = [&](int q, int h) __attribute__((always_inline)) {
#pragma unroll
    for (int ww = 0; ww < 4; ++ww) rv[ww] = *(const f32x4*)(rr_ + (q & 1) * (KF_RB / 4) + (4 * h + ww) * 16 * KF_RS);
  };
  auto red_out = [&](int q) __attribute__((always_inline)) {
    const f32x4 s = rsum + ((rv[0] + rv[1]) + (rv[2] + rv[3]));
    const u32x2 xv2 = *(const u32x2*)(xr + (16 * q + rtk) * 176 + rq4 * 8);
    const f32x4 gb = *(const f32x4*)(tab + 4 * rq4);
    u32x2 o;
#pragma unroll
    for (int h = 0; h < 2; ++h) {
      unsigned f = pk_bf(s[2 * h], s[2 * h + 1]);
      unsigned gf = pk_bf(bf_lo(f) * gb[2 * h], bf_hi(f) * gb[2 * h + 1]);
      o[h] = pk_bf(bf_lo(xv2[h]) + bf_lo(gf), bf_hi(xv2[h]) + bf_hi(gf));
    }
    if (lane < 40) *(u32x2*)(out + (size_t)(mt * 64 + 16 * q + rtk) * D_ + ng * 80 + 4 * rq4) = o;
  };
#pragma unroll
  for (int q = 0; q < 4; ++q) {
#pragma unroll
    for (int c = 0; c < 5; ++c) ac2[q & 1][c] = (f32x4){0.f, 0.f, 0.f, 0.f};
#pragma unroll
    for (int s = 0; s < 7; ++s) {
      if (q >= 1 && s >= 1 && s <= 5) red_wr(q - 1, s - 1);
      if (q >= 2) {
        if (s == 0) red_rd(q - 2, 0);
        if (s == 2) { rsum = (rv[0] + rv[1]) + (rv[2] + rv[3]); red_rd(q - 2, 1); }
        if (s == 4) red_out(q - 2);
      }
      SB();
      if (q < 3 || s < 6) wl[(7 * q + s + 1) & 1] = *(const u32x4*)(w2l + ((s + 1) % 7) * 1024);
      if (s == 5) wl3 = *(const u32x4*)(w2l + 7 * 1024);
      SB();
      if (s < 6 || full) {
#pragma unroll
        for (int c = 0; c < 5; ++c) {
          const u32x4 a = c == 4 ? wl[(7 * q + s) & 1] : ((s == 6 && c == 3) ? wl3 : ring[(8 + 4 * s + c) % 32]);
          ac2[q & 1][c] = mfma_bf16(a, hb[7 * q + s], ac2[q & 1][c]);
        }
      }
      SB();
      if (7 * q + s + H1_PRE < 28) { load_h1(7 * q + s + H1_PRE); SB(); }
    }
    if (q == 0) STAMP(2, 5);
    if (q >= 1) bar_lds();
  }
  STAMP(2, 6);
  red_rd(2, 0);
#pragma unroll
  for (int c = 0; c < 5; ++c) red_wr(3, c);
  rsum = (rv[0] + rv[1]) + (rv[2] + rv[3]);
  red_rd(2, 1);
  red_out(2);
  bar_lds();
  red_rd(3, 0);
  rsum = (rv[0] + rv[1]) + (rv[2] + rv[3]);
  red_rd(3, 1);
  red_out(3);
  STAMP(2, 7); STAMP(5, 7);
}

__global__ NOPK __launch_bounds__(512, 2) void kf(char* __restrict__ ws, unsigned short* __restrict__ out) {
  __shared__ __attribute__((aligned(16))) char smem[KF_LDS];
  kf_body(ws, out, smem);
}

__global__ void kend_() {}

extern "C" void kernel_launch(void* const* d_in, const int* in_sizes, int n_in,
                              void* d_out, int out_size, void* d_ws, size_t ws_size,
                              hipStream_t stream) {
  K1Args a;
  a.x = (const unsigned short*)d_in[0];
  a.are = (const unsigned short*)d_in[1];
  a.aim = (const unsigned short*)d_in[2];
  a.bre = (const unsigned short*)d_in[3];
  a.bim = (const unsigned short*)d_in[4];
  a.cre = (const unsigned short*)d_in[5];
  a.cim = (const unsigned short*)d_in[6];
  a.logdt = (const unsigned short*)d_in[7];
  a.w1 = (const unsigned short*)d_in[10];
  a.w2 = (const unsigned short*)d_in[11];
  a.c = (const unsigned short*)d_in[12];
  a.wmod = (const unsigned short*)d_in[13];
  a.ws = (char*)d_ws;
  a.dskip = (const unsigned short*)d_in[8];
  a.out = (const unsigned short*)d_out;
  k1<<<NB_PF + NB_MOD + NB_LN + NB_K, 256, 0, stream>>>(a);
  k2<<<256, 512, 0, stream>>>((const unsigned short*)d_in[8], (char*)d_ws, a.w1, a.w2);
  kf<<<256, 512, 0, stream>>>((char*)d_ws, (unsigned short*)d_out);
}
```

```cpp
#define PF 1
#include <hip/hip_runtime.h>
#include <hip/hip_bf16.h>
#include <stdint.h>
#include <type_traits>

typedef __attribute__((ext_vector_type(8))) __bf16 bf16x8;
typedef __attribute__((ext_vector_type(2))) __bf16 bf16x2;
typedef __attribute__((ext_vector_type(4))) float f32x4;
typedef __attribute__((ext_vector_type(2))) float f32x2;
typedef __attribute__((ext_vector_type(4))) unsigned u32x4;
typedef __attribute__((ext_vector_type(2))) unsigned u32x2;
typedef __attribute__((ext_vector_type(8))) _Float16 h16x8;
typedef __attribute__((ext_vector_type(2))) _Float16 h16x2;
typedef __attribute__((ext_vector_type(4))) short s16x4;
typedef __attribute__((address_space(3))) s16x4 lds_s16x4;

#if defined(__HIP_DEVICE_COMPILE__)
#define NOPK __attribute__((target("no-packed-fp32-ops")))
#else
#define NOPK
#endif
#define DEV __device__ __forceinline__

constexpr int L_ = 2048;
constexpr int D_ = 640;
constexpr int NS = 128;
constexpr int FF = 1728;
constexpr int KREV = 2304;

constexpr size_t WS_KSC  = 8192;
constexpr size_t WS_CNT  = 16384;
constexpr size_t WS_CSB  = 32768;
constexpr size_t WS_MOD  = 65536;
constexpr size_t WS_XNT  = 196608;
constexpr size_t WS_XT   = WS_XNT + (size_t)D_ * L_ * 2;
constexpr size_t WS_KREV = WS_XT + (size_t)D_ * L_ * 2;
constexpr size_t WS_W1F  = WS_KREV + (size_t)D_ * 2 * KREV * 2;
constexpr size_t WS_W2F  = WS_W1F + (size_t)D_ * FF * 2;
constexpr size_t WS_X1T  = WS_W2F + (size_t)D_ * FF * 2;
constexpr size_t WS_X1   = WS_X1T + (size_t)D_ * L_ * 2;
constexpr size_t WS_CSP  = WS_X1;
constexpr size_t WS_H1F  = WS_X1 + (size_t)D_ * L_ * 2;

#define STAMP(kid, k) do {} while (0)

DEV float bf_lo(unsigned u) { return __uint_as_float(u << 16); }
DEV float bf_hi(unsigned u) { return __uint_as_float(u & 0xffff0000u); }
DEV unsigned pk_bf(float a, float b) {
  f32x2 v = {a, b};
  bf16x2 r = __builtin_convertvector(v, bf16x2);
  return __builtin_bit_cast(unsigned, r);
}
DEV float rbf(float a) { return bf_lo(pk_bf(a, 0.f)); }
DEV float ldbf(const unsigned short* p) { return __uint_as_float(((unsigned)*p) << 16); }

typedef __amdgpu_buffer_rsrc_t rsrc_t;
DEV rsrc_t ws_rsrc(char* ws) { return __builtin_amdgcn_make_buffer_rsrc((void*)ws, 0, 0x7fffffff, 0x00020000); }
#define ST_AUX 16
DEV void st16(rsrc_t r, size_t off, u32x4 v) { __builtin_amdgcn_raw_buffer_store_b128(v, r, (int)off, 0, ST_AUX); }
#define LN_AUX 16
DEV void st16ln(rsrc_t r, size_t off, u32x4 v) { __builtin_amdgcn_raw_buffer_store_b128(v, r, (int)off, 0, LN_AUX); }
DEV void st8(rsrc_t r, size_t off, u32x2 v) { __builtin_amdgcn_raw_buffer_store_b64(v, r, (int)off, 0, 16); }

DEV u32x2 tr_read(unsigned lds_addr) {
  u32x2 r;
  asm volatile("ds_read_b64_tr_b16 %0, %1" : "=v"(r) : "v"(lds_addr) : "memory");
  return r;
}
DEV void bar_lds() { asm volatile("s_waitcnt lgkmcnt(0)\n\ts_barrier" ::: "memory"); }
DEV void lds_wait(const void* word, unsigned val) {
  unsigned tv, ts, cnt;
  asm volatile("s_mov_b32 %2, 0x100000\n"
               "1:\n\tds_read_b32 %0, %3\n\ts_waitcnt lgkmcnt(0)\n\tv_readfirstlane_b32 %1, %0\n\t"
               "s_cmp_ge_u32 %1, %4\n\ts_cbranch_scc1 2f\n\ts_sleep 1\n\ts_sub_u32 %2, %2, 1\n\t"
               "s_cmp_lg_u32 %2, 0\n\ts_cbranch_scc1 1b\n2:"
               : "=&v"(tv), "=&s"(ts), "=&s"(cnt) : "v"((unsigned)(size_t)word), "s"(val) : "memory", "scc");
}
DEV void lds_post(unsigned* word, int lane) {
  asm volatile("" ::: "memory");
  if (lane == 0) __hip_atomic_fetch_add(word, 1u, __ATOMIC_RELAXED, __HIP_MEMORY_SCOPE_WORKGROUP);
  asm volatile("" ::: "memory");
}
DEV void lgkm0() { asm volatile("s_waitcnt lgkmcnt(0)" ::: "memory"); __builtin_amdgcn_sched_barrier(0); }

DEV f32x4 mfma_bf16(u32x4 a, u32x4 b, f32x4 c) {
  return __builtin_amdgcn_mfma_f32_16x16x32_bf16(__builtin_bit_cast(bf16x8, a), __builtin_bit_cast(bf16x8, b), c, 0, 0, 0);
}

constexpr int NB_MOD = 120, NB_LN = 256, NB_W2 = 108, NB_K = 640;

struct K1Args {
  const unsigned short *x, *are, *aim, *bre, *bim, *cre, *cim, *logdt, *w1, *w2, *c, *wmod;
  const unsigned short *dskip, *out;
  char* ws;
};

constexpr int NB_PF = 8;
DEV void k1_prefetch(const K1Args& p, int b) {
  const int tid = threadIdx.x;
  unsigned long long pc;
  int o0, o1;
  asm volatile("s_getpc_b64 %0\n.Lpf_here%=:\n\ts_mov_b32 %1, _Z2k16K1Args - .Lpf_here%=\n\ts_mov_b32 %2, _Z5kend_v - .Lpf_here%="
               : "=s"(pc), "=s"(o0), "=s"(o1));
  const char* c0 = (const char*)(pc + (long long)o0);
  long n = o1 - o0;
  n = n < 0 ? 0 : (n > 98304 ? 98304 : n);
  unsigned acc = 0;
  {
    unsigned v[3];
#pragma unroll
    for (int it = 0; it < 3; ++it) {
      const long o = (long)tid * 128 + it * 32768;
      v[it] = o < n ? *(const __attribute__((address_space(1))) unsigned*)(c0 + o) : 0u;
    }
    acc = v[0] ^ v[1] ^ v[2];
  }
  asm volatile("" :: "v"(acc));
}

DEV void k1_mod(const K1Args& p, int b, char* smem) {
  const int tid = threadIdx.x;
  const int rg = b & 7, cgp = b >> 3;
  const int c32 = tid & 31, r8 = tid >> 5;
  float* sil = (float*)smem;
  float* red = (float*)(smem + 512);
  u32x4 w[10];
#pragma unroll
  for (int it = 0; it < 10; ++it)
    w[it] = *(const u32x4*)(p.wmod + (size_t)(rg * 80 + r8 + 8 * it) * 3840 + 256 * cgp + 8 * c32);
  if (tid < 80) {
    float cv = ldbf(p.c + rg * 80 + tid);
    sil[tid] = rbf(cv / (1.f + expf(-cv)));
  }
  if (tid >= 128 && tid < 132 && b < 64) ((unsigned*)(p.ws + WS_CNT))[(b * 4 + tid - 128) * 16] = 0u;
  __syncthreads();
  float acc[8];
#pragma unroll
  for (int j = 0; j < 8; ++j) acc[j] = 0.f;
#pragma unroll
  for (int it = 0; it < 10; ++it) {
    float s = sil[r8 + 8 * it];
#pragma unroll
    for (int q = 0; q < 4; ++q) {
      acc[2 * q] += s * bf_lo(w[it][q]);
      acc[2 * q + 1] += s * bf_hi(w[it][q]);
    }
  }
#pragma unroll
  for (int j = 0; j < 8; ++j) acc[j] += __shfl_xor(acc[j], 32, 64);
  if ((tid & 63) < 32) {
    *(f32x4*)(red + (tid >> 6) * 256 + 8 * c32) = (f32x4){acc[0], acc[1], acc[2], acc[3]};
    *(f32x4*)(red + (tid >> 6) * 256 + 8 * c32 + 4) = (f32x4){acc[4], acc[5], acc[6], acc[7]};
  }
  __syncthreads();
  ((float*)(p.ws + WS_MOD))[(256 * cgp + tid) * 8 + rg] = (red[tid] + red[256 + tid]) + (red[512 + tid] + red[768 + tid]);
}
struct ModP { f32x4 a, b; };
DEV ModP mod_ld(const char* ws, int idx) {
  const f32x4* mp = (const f32x4*)(ws + WS_MOD) + 2 * idx;
  ModP m; m.a = mp[0]; m.b = mp[1];
  return m;
}
DEV float mod_fin(const ModP& m) {
  return rbf(((m.a[0] + m.a[1]) + (m.a[2] + m.a[3])) + ((m.b[0] + m.b[1]) + (m.b[2] + m.b[3])));
}
DEV float mod_sum(const char* ws, int idx) { return mod_fin(mod_ld(ws, idx)); }
#define SB() __builtin_amdgcn_sched_barrier(0)

DEV void k1_ln(const K1Args& p, int b, char* smem) {
  constexpr int STR = 704;
  const int tid = threadIdx.x;
  const int tok = tid >> 3, sub = tid & 7;
  const int tile = (b & 7) * 8 + ((b >> 3) & 7), pass = b >> 6;
  const int hf = pass & 1;
  const bool norm = pass < 2;
  const int t0 = tile * 32;
  const unsigned short* xr = p.x + (size_t)(t0 + tok) * D_ + 8 * sub;
  u32x4 xv[5], xo[5];
#pragma unroll
  for (int m = 0; m < 5; ++m) xv[m] = *(const u32x4*)(xr + 320 * hf + 64 * m);
  if (norm) {
#pragma unroll
    for (int m = 0; m < 5; ++m) xo[m] = *(const u32x4*)(xr + 320 * (hf ^ 1) + 64 * m);
    float s = 0.f, v = 0.f;
    const bf16x2 one2 = __builtin_bit_cast(bf16x2, 0x3f803f80u);
#pragma unroll
    for (int m = 0; m < 10; ++m)
#pragma unroll
      for (int q = 0; q < 4; ++q) {
        const unsigned u = m < 5 ? xv[m][q] : xo[m - 5][q];
        const bf16x2 x2 = __builtin_bit_cast(bf16x2, u);
        s = __builtin_amdgcn_fdot2_f32_bf16(x2, one2, s, false);
        v = __builtin_amdgcn_fdot2_f32_bf16(x2, x2, v, false);
      }
#pragma unroll
    for (int o = 1; o < 8; o <<= 1) { s += __shfl_xor(s, o, 64); v += __shfl_xor(v, o, 64); }
    const float mean = s * (1.f / D_);
    const float var = fmaxf(v * (1.f / D_) - mean * mean, 0.f);
    const float sd = rbf(sqrtf(rbf(rbf(var) + 1e-5f)));
    const float rs = 1.f / sd;
    const float mb = rbf(mean);
#pragma unroll
    for (int m = 0; m < 5; ++m)
#pragma unroll
      for (int q = 0; q < 4; ++q) {
        unsigned d2 = pk_bf(bf_lo(xv[m][q]) - mb, bf_hi(xv[m][q]) - mb);
        xv[m][q] = pk_bf(bf_lo(d2) * rs, bf_hi(d2) * rs);
      }
  }
  const int lane = tid & 63, w = tid >> 6;
  const int g = lane >> 4, i = lane & 15;
  const rsrc_t rsrc = ws_rsrc(p.ws);
#pragma unroll
  for (int m = 0; m < 5; ++m) *(u32x4*)(smem + tok * STR + (sub + 8 * m) * 16) = xv[m];
  __syncthreads();
  const size_t dsto = (norm ? WS_XNT : WS_XT) + ((size_t)tile * D_ + 320 * hf) * 64;
#pragma unroll
  for (int it = 0; it < 5; ++it) {
    const int cb = w * 5 + it;
    const char* a = smem + (8 * g + (i >> 2)) * STR + cb * 32 + (i & 3) * 8;
    const u32x2 ra = __builtin_bit_cast(u32x2, __builtin_amdgcn_ds_read_tr16_b64_v4i16((lds_s16x4*)a));
    const u32x2 rb = __builtin_bit_cast(u32x2, __builtin_amdgcn_ds_read_tr16_b64_v4i16((lds_s16x4*)(a + 4 * STR)));
    st16ln(rsrc, dsto + (size_t)(cb * 16 + i) * 64 + 16 * g, (u32x4){ra[0], ra[1], rb[0], rb[1]});
  }
}

constexpr int RT_STR = 176;
template <int NF> struct RtRaw { u32x4 r[NF]; };
template <int NF>
DEV void rt_load(RtRaw<NF>& t, const unsigned short* W, int N, int kk, int ct0, int lane) {
#pragma unroll
  for (int j = 0; j < NF; ++j) {
    const int c = lane + 64 * j, row = c / (2 * NF), part = c % (2 * NF);
    t.r[j] = *(const u32x4*)(W + (size_t)(32 * kk + row) * N + 16 * ct0 + 8 * part);
  }
}
template <int NF>
DEV void rt_park(const RtRaw<NF>& t, char* scr, int lane) {
#pragma unroll
  for (int j = 0; j < NF; ++j) {
    const int c = lane + 64 * j, row = c / (2 * NF), part = c % (2 * NF);
    *(u32x4*)(scr + row * RT_STR + 16 * part) = t.r[j];
  }
}
DEV u32x4 rt_frag(const char* scr, int j, int lane) {
  const int g = lane >> 4, i = lane & 15;
  const char* p = scr + (8 * g + (i >> 2)) * RT_STR + 32 * j + (i & 3) * 8;
  const u32x2 a = __builtin_bit_cast(u32x2, __builtin_amdgcn_ds_read_tr16_b64_v4i16((lds_s16x4*)p));
  const u32x2 b = __builtin_bit_cast(u32x2, __builtin_amdgcn_ds_read_tr16_b64_v4i16((lds_s16x4*)(p + 4 * RT_STR)));
  return (u32x4){a[0], a[1], b[0], b[1]};
}
struct cf { float re, im; };
DEV cf cmul(cf a, cf b) {
  cf r;
  r.re = a.re * b.re - a.im * b.im;
  r.im = a.re * b.im + a.im * b.re;
  return r;
}
DEV cf cexp_(float a2, float br, float m, float m0) {
  float mag = __builtin_amdgcn_exp2f(a2 * (m - m0));
  float ph = __builtin_amdgcn_fractf(br * m);
  cf r; r.re = mag * __builtin_amdgcn_cosf(ph); r.im = mag * __builtin_amdgcn_sinf(ph);
  return r;
}
DEV unsigned pk_h(float a, float b) {
  f32x2 v = {a, b};
  h16x2 r = __builtin_convertvector(v, h16x2);
  return __builtin_bit_cast(unsigned, r);
}
DEV f32x4 mfma_f16(u32x4 a, u32x4 b, f32x4 c) {
  return __builtin_amdgcn_mfma_f32_16x16x32_f16(__builtin_bit_cast(h16x8, a), __builtin_bit_cast(h16x8, b), c, 0, 0, 0);
}
DEV void put2(char* p, cf a, cf b) {
  u32x2 o = {pk_h(a.re, a.im), pk_h(b.re, b.im)};
  *(u32x2*)p = o;
}

constexpr int KG_RS = 544;
DEV void k1_kgen(const K1Args& p, int d, char* smem) {
  constexpr int RS = KG_RS;
  const int tid = threadIdx.x;
  const int lane = tid & 63;
  const int w = __builtin_amdgcn_readfirstlane(tid >> 6);
  const int g = lane >> 4, i = lane & 15;
  char* stamp_ws = p.ws; (void)stamp_ws;
  const size_t po = (size_t)d * NS + 2 * lane;
  const unsigned rar = *(const unsigned*)(p.are + po), rai = *(const unsigned*)(p.aim + po);
  const unsigned rbr = *(const unsigned*)(p.bre + po), rbi = *(const unsigned*)(p.bim + po);
  const unsigned rcr = *(const unsigned*)(p.cre + po), rci = *(const unsigned*)(p.cim + po);
  const float ldt = ldbf(p.logdt + d);
  asm volatile("" ::: "memory");
  SB();
  const float dt = rbf(expf(ldt));
  STAMP(0, 1);
  float a2[2], bv[2], m0[2];
  a2[0] = (dt * bf_lo(rar)) * 1.44269504088896341f; a2[1] = (dt * bf_hi(rar)) * 1.44269504088896341f;
  bv[0] = (dt * bf_lo(rai)) * 0.15915494309189534f; bv[1] = (dt * bf_hi(rai)) * 0.15915494309189534f;
  m0[0] = a2[0] > 0.f ? 2048.f : 0.f; m0[1] = a2[1] > 0.f ? 2048.f : 0.f;
  char* my = smem + lane * 8;
  if (w == 0) {
    cf v[2], v8[2], e8[2];
    float bound = 0.f;
#pragma unroll
    for (int q = 0; q < 2; ++q) {
      const float dbr = dt * (q ? bf_hi(rbr) : bf_lo(rbr)), dbi = dt * (q ? bf_hi(rbi) : bf_lo(rbi));
      const float cr = q ? bf_hi(rcr) : bf_lo(rcr), ci = q ? bf_hi(rci) : bf_lo(rci);
      const float gsh = __builtin_amdgcn_exp2f(a2[q] * m0[q]);
      v[q].re = (cr * dbr - ci * dbi) * gsh; v[q].im = -(cr * dbi + ci * dbr) * gsh;
      bound += (fabsf(v[q].re) + fabsf(v[q].im)) * __builtin_amdgcn_exp2f(fmaxf(a2[q], 0.f) * 120.f);
      e8[q] = cexp_(a2[q], -bv[q], 8.f, 0.f);
      v8[q] = cexp_(a2[q], -bv[q], 64.f, 0.f);
    }
#pragma unroll
    for (int o = 1; o < 64; o <<= 1) bound += __shfl_xor(bound, o, 64);
    int ex = (int)((__float_as_uint(bound) >> 23) & 0xff);
    ex = ex < 20 ? 20 : (ex > 240 ? 240 : ex);
    const float sv = __uint_as_float((unsigned)(127 + 13 + 127 - ex) << 23);
    if (lane == 0) ((float*)(p.ws + WS_KSC))[d] = __uint_as_float((unsigned)(ex - 13) << 23);
#pragma unroll
    for (int q = 0; q < 2; ++q) { v[q].re *= sv; v[q].im *= sv; v8[q] = cmul(v[q], v8[q]); }
#pragma unroll
    for (int j = 0; j < 8; ++j) {
      put2(my + (41 + j) * RS, v[0], v[1]);
      put2(my + (49 + j) * RS, v8[0], v8[1]);
      if (j < 7) {
#pragma unroll
        for (int q = 0; q < 2; ++q) { v[q] = cmul(v[q], e8[q]); v8[q] = cmul(v8[q], e8[q]); }
      }
    }
  } else if (w == 1) {
    cf e1[2], u[2], u2[2];
#pragma unroll
    for (int q = 0; q < 2; ++q) {
      e1[q] = cexp_(a2[q], bv[q], 1.f, 0.f);
      u[q].re = __builtin_amdgcn_exp2f(-a2[q] * m0[q]); u[q].im = 0.f;
      u2[q] = cexp_(a2[q], bv[q], 128.f, m0[q]);
    }
#pragma unroll
    for (int r = 0; r < 8; ++r) {
      put2(my + r * RS, u[0], u[1]);
      put2(my + (8 + r) * RS, u2[0], u2[1]);
      if (r < 7) {
#pragma unroll
        for (int q = 0; q < 2; ++q) { u[q] = cmul(u[q], e1[q]); u2[q] = cmul(u2[q], e1[q]); }
      }
    }
  } else if (w == 2) {
    cf e2[2], u[2], u2[2];
#pragma unroll
    for (int q = 0; q < 2; ++q) {
      e2[q] = cexp_(a2[q], bv[q], 2.f, 0.f);
      u[q] = cexp_(a2[q], bv[q], 256.f, m0[q]);
      u2[q] = cexp_(a2[q], bv[q], 384.f, m0[q]);
    }
#pragma unroll
    for (int r = 0; r < 4; ++r) {
      put2(my + (16 + r) * RS, u[0], u[1]);
      put2(my + (20 + r) * RS, u2[0], u2[1]);
      if (r < 3) {
#pragma unroll
        for (int q = 0; q < 2; ++q) { u[q] = cmul(u[q], e2[q]); u2[q] = cmul(u2[q], e2[q]); }
      }
    }
    cf e4[2] = {cmul(e2[0], e2[0]), cmul(e2[1], e2[1])};
#pragma unroll
    for (int r = 0; r < 2; ++r) {
      cf c0 = cexp_(a2[0], bv[0], 512.f + 128.f * r, m0[0]), c1 = cexp_(a2[1], bv[1], 512.f + 128.f * r, m0[1]);
      put2(my + (24 + 2 * r) * RS, c0, c1);
      put2(my + (25 + 2 * r) * RS, cmul(c0, e4[0]), cmul(c1, e4[1]));
    }
  } else {
    cf e4[2], e128[2], u[2], u2[2];
#pragma unroll
    for (int q = 0; q < 2; ++q) {
      e4[q] = cexp_(a2[q], bv[q], 4.f, 0.f);
      e128[q] = cexp_(a2[q], bv[q], 128.f, 0.f);
      u[q] = cexp_(a2[q], bv[q], 1024.f, m0[q]);
      u2[q] = cexp_(a2[q], bv[q], 1536.f, m0[q]);
    }
#pragma unroll
    for (int r = 2; r < 4; ++r) {
      cf c0 = cexp_(a2[0], bv[0], 512.f + 128.f * r, m0[0]), c1 = cexp_(a2[1], bv[1], 512.f + 128.f * r, m0[1]);
      put2(my + (24 + 2 * r) * RS, c0, c1);
      put2(my + (25 + 2 * r) * RS, cmul(c0, e4[0]), cmul(c1, e4[1]));
    }
#pragma unroll
    for (int r = 0; r < 4; ++r) {
      put2(my + (32 + r) * RS, u[0], u[1]);
      put2(my + (36 + r) * RS, u2[0], u2[1]);
#pragma unroll
      for (int q = 0; q < 2; ++q) { if (r < 3) u[q] = cmul(u[q], e128[q]); u2[q] = cmul(u2[q], e128[q]); }
    }
    put2(my + 40 * RS, u2[0], u2[1]);
  }
  __syncthreads();
  STAMP(0, 2);
  _Float16* kh = (_Float16*)(smem + 57 * RS);
  if (w < 3) {
    const char* ua = smem + (16 * w + i) * RS + g * 16;
    const char* va = smem + (41 + i) * RS + g * 16;
    f32x4 acc = {0.f, 0.f, 0.f, 0.f};
#pragma unroll
    for (int ks = 0; ks < 8; ++ks)
      acc = mfma_f16(*(const u32x4*)(ua + ks * 64), *(const u32x4*)(va + ks * 64), acc);
    int idx0, st;
    if (w == 0) { idx0 = (g < 2 ? 4 * g : 120 + 4 * g) + 8 * i; st = 1; }
    else if (w == 1) { idx0 = g < 2 ? 256 + 64 * g + 4 * i : 384 + 64 * (g - 2) + 2 * i; st = 1; }
    else { idx0 = 512 + 64 * g + i; st = 16; }
#pragma unroll
    for (int rg = 0; rg < 4; ++rg) {
      int idx = idx0 + st * rg;
      if (w == 1 && g >= 2) idx = idx0 + 32 * (rg >> 1) + (rg & 1);
      if (w == 2 && g >= 2) idx = (g == 2 && rg == 0 && i == 0) ? 640 : 644;
      kh[idx] = (_Float16)acc[rg];
    }
  }
  __syncthreads();
  STAMP(0, 3);
  const unsigned short* ku = (const unsigned short*)kh;
  const size_t kout = WS_KREV + (size_t)d * KREV * 2;
  {
    const int l0 = 2040 - 8 * tid;
    u32x4 o;
    if (w < 2) {
      const int q = l0 >> 3;
      const unsigned a = ku[384 + q], b = ku[385 + q];
      const unsigned t = (q & 1) ? b : a;
      o[0] = b | (b << 16); o[1] = b | (t << 16); o[2] = a | (a << 16); o[3] = o[2];
    } else if (w == 2) {
      const int q = l0 >> 2;
      const unsigned a = ku[256 + q], b = ku[257 + q], c = ku[258 + q];
      o[0] = c | (c << 16); o[1] = b | (b << 16); o[2] = b | (a << 16); o[3] = a | (a << 16);
    } else {
      unsigned v[8];
#pragma unroll
      for (int e = 0; e < 8; ++e) {
        const int l = l0 + 7 - e;
        const int idx = l < 256 ? l : (int)(pk_bf((float)l, 0.f) & 0xffffu) - 17024;
        v[e] = ku[idx];
      }
#pragma unroll
      for (int q = 0; q < 4; ++q) o[q] = v[2 * q] | (v[2 * q + 1] << 16);
    }
    st16(ws_rsrc(p.ws), kout + 16 * tid, o);
  }
}

__global__ NOPK __launch_bounds__(256) void k1(K1Args p) {
  __shared__ __attribute__((aligned(16))) char smem[57 * KG_RS + 1312];
  int b = blockIdx.x;
  char* stamp_ws = p.ws; (void)stamp_ws;
  STAMP(0, 0);
  if (b < NB_PF) { k1_prefetch(p, b); STAMP(0, 7); return; }
  b -= NB_PF;
  if (b < NB_K) { k1_kgen(p, b, smem); STAMP(0, 7); return; }
  b -= NB_K;
#define K1_PRIO 0
  __builtin_amdgcn_s_setprio(K1_PRIO);
#define K1_DELAY 12
  if (K1_DELAY) { asm volatile("" :: "s"(p.ws), "s"(p.x)); __builtin_amdgcn_s_sleep(K1_DELAY); }
  if (b < NB_LN) {
    k1_ln(p, b, smem);
    STAMP(0, 7);
    return;
  }
  b -= NB_LN;
  k1_mod(p, b, smem);
  STAMP(0, 7);
}

constexpr int K2_CS = 4160;
constexpr int K2_KOFF = 4 * K2_CS;
constexpr int K2_XOFF = K2_CS;
constexpr int K2_CH = K2_KOFF + 4608;
constexpr int CV_RED = 3 * K2_CH;
constexpr int CV_RT2 = 86016;
constexpr int CV_XCH = CV_RT2 + 2 * 32 * 176;
constexpr int CV_FL = CV_RED + 20480;
constexpr int CV_RT = 151552;

struct Chan { float sa, ca1, ga, dsk, hs, hinv, yinv; };
struct ChanRaw { ModP msa, mca, mga; float kinv; unsigned short rds; };
DEV ChanRaw chan_ld(const unsigned short* dskip, const char* ws, int d) {
  ChanRaw c;
  c.rds = dskip[d];
  c.msa = mod_ld(ws, d); c.mca = mod_ld(ws, D_ + d); c.mga = mod_ld(ws, 2 * D_ + d);
  c.kinv = ((const float*)(ws + WS_KSC))[d];
  return c;
}
DEV Chan chan_fin(const ChanRaw& r) {
  Chan c;
  c.sa = mod_fin(r.msa); c.ca1 = rbf(1.f + mod_fin(r.mca)); c.ga = mod_fin(r.mga);
  c.dsk = __uint_as_float((unsigned)r.rds << 16);
  const float hb = 26.f * fabsf(c.ca1) + fabsf(c.sa);
  int ex = (int)((__float_as_uint(hb) >> 23) & 0xff);
  ex = ex < 20 ? 20 : (ex > 240 ? 240 : ex);
  c.hs = __uint_as_float((unsigned)(127 + 14 + 127 - ex) << 23);
  c.hinv = __uint_as_float((unsigned)(ex - 14) << 23);
  c.yinv = r.kinv * c.hinv;
  return c;
}
DEV size_t tl_off(int d, int t) { return (((size_t)(t >> 5) * D_ + d) * 32 + (t & 31)) * 2; }
struct StageRaw { u32x4 xa0, xa1, kv0, kv1; u32x2 xb; };
DEV StageRaw stage_ld(const char* ws, int d, int th) {
  StageRaw s;
  const unsigned short* krev = (const unsigned short*)(ws + WS_KREV) + (size_t)d * KREV;
  const size_t xo = tl_off(d, 16 * th);
  s.xa0 = *(const u32x4*)(ws + WS_XNT + xo); s.xa1 = *(const u32x4*)(ws + WS_XNT + xo + 16);
  s.xb = *(const u32x2*)(ws + WS_XNT + tl_off(d, min(16 * th + 16, L_ - 4)));
  s.kv0 = *(const u32x4*)(krev + 8 * th); s.kv1 = *(const u32x4*)(krev + 1024 + 8 * th);
  return s;
}
DEV void stage_wr(const StageRaw& s, const Chan& c, char* smem, int th) {
  constexpr int CS = K2_CS, KOFF = K2_KOFF;
  unsigned hh[10];
#pragma unroll
  for (int q = 0; q < 10; ++q) {
    unsigned u = q < 4 ? s.xa0[q] : (q < 8 ? s.xa1[q - 4] : s.xb[q - 8]);
    unsigned m = pk_bf(bf_lo(u) * c.ca1, bf_hi(u) * c.ca1);
    unsigned h = pk_bf(bf_lo(m) + c.sa, bf_hi(m) + c.sa);
    hh[q] = pk_h(bf_lo(h) * c.hs, bf_hi(h) * c.hs);
  }
  char* dst = smem + (2 * th + 2) * 16;
  unsigned s1[9];
#pragma unroll
  for (int q = 0; q < 9; ++q) s1[q] = __builtin_amdgcn_alignbit(hh[q + 1], hh[q], 16);
#pragma unroll
  for (int e = 0; e < 2; ++e) {
    *(u32x4*)(dst + 16 * e) = (u32x4){hh[4 * e], hh[4 * e + 1], hh[4 * e + 2], hh[4 * e + 3]};
    *(u32x4*)(dst + 16 * e + CS) = (u32x4){s1[4 * e], s1[4 * e + 1], s1[4 * e + 2], s1[4 * e + 3]};
    *(u32x4*)(dst + 16 * e + 2 * CS) = (u32x4){hh[4 * e + 1], hh[4 * e + 2], hh[4 * e + 3], hh[4 * e + 4]};
    *(u32x4*)(dst + 16 * e + 3 * CS) = (u32x4){s1[4 * e + 1], s1[4 * e + 2], s1[4 * e + 3], s1[4 * e + 4]};
  }
  if (th == 0) {
    u32x4 z = {0u, 0u, 0u, 0u};
    *(u32x4*)(smem) = z; *(u32x4*)(smem + CS) = z; *(u32x4*)(smem + 2 * CS) = z; *(u32x4*)(smem + 3 * CS) = z;
    *(u32x4*)(smem + 16) = z;
    *(u32x4*)(smem + CS + 16) = (u32x4){0u, 0u, 0u, hh[0] << 16};
    *(u32x4*)(smem + 2 * CS + 16) = (u32x4){0u, 0u, 0u, hh[0]};
    *(u32x4*)(smem + 3 * CS + 16) = (u32x4){0u, 0u, hh[0] << 16, s1[0]};
  }
  *(u32x4*)(smem + KOFF + 16 * th) = s.kv0;
  *(u32x4*)(smem + KOFF + 2048 + 16 * th) = s.kv1;
  if (th < 32) *(u32x4*)(smem + KOFF + 4096 + 16 * th) = (u32x4){0u, 0u, 0u, 0u};
}
DEV u32x2 conv_out(const f32x4& y, const Chan& c, const char* smem, int t, const u32x2& xres) {
  const u32x2 hv = *(const u32x2*)(smem + 2 * (t + 16));
  u32x2 o;
#pragma unroll
  for (int e = 0; e < 2; ++e) {
    const unsigned hu = hv[e];
    const h16x2 h2 = __builtin_bit_cast(h16x2, hu);
    const float h0 = (float)h2[0] * c.hinv, h1 = (float)h2[1] * c.hinv;
    unsigned yb = pk_bf(y[2 * e] * c.yinv, y[2 * e + 1] * c.yinv);
    unsigned hd = pk_bf(h0 * c.dsk, h1 * c.dsk);
    unsigned y2 = pk_bf(bf_lo(yb) + bf_lo(hd), bf_hi(yb) + bf_hi(hd));
    unsigned gy = pk_bf(bf_lo(y2) * c.ga, bf_hi(y2) * c.ga);
    o[e] = pk_bf(bf_lo(xres[e]) + bf_lo(gy), bf_hi(xres[e]) + bf_hi(gy));
  }
  return o;
}
#define K2_LOAD(buf, cc, u)                                                                                     \
  asm volatile("ds_read_b64 %0, %3 offset:%5\n\tds_read_b64 %1, %3 offset:%6\n\tds_read_b128 %2, %4 offset:%7"   \
               : "=&v"(Al[buf][u]), "=&v"(Ah[buf][u]), "=&v"(Bf[buf][u])                                          \
               : "v"(ap0), "v"(bp0), "i"(64 * (cc) + 512 * (u)), "i"(64 * (cc) + 512 * (u) + 8),                 \
                 "i"(64 * (cc) + 3584 - 512 * (u)))
template <int SET> DEV constexpr int half_tile(int j) {
  return SET == 0 ? (j == 0 ? 7 : (j == 1 ? 6 : 2)) : (j < 3 ? 5 - j : 4 - j);
}
template <int SET>
DEV void conv_half(unsigned ap0, unsigned bp0, char* redw) {
  constexpr int NU = SET == 0 ? 8 : 6, NT = SET == 0 ? 3 : 5;
  u32x2 Al[2][NU], Ah[2][NU];
  u32x4 Bf[2][NU];
#pragma unroll
  for (int u = 0; u < NU; ++u) K2_LOAD(0, 0, u);
  f32x4 acc[NT];
#pragma unroll
  for (int j = 0; j < NT; ++j) acc[j] = (f32x4){0.f, 0.f, 0.f, 0.f};
#pragma unroll
  for (int cc = 0; cc < 2; ++cc) {
    lgkm0();
    if (cc == 0) {
#pragma unroll
      for (int u = 0; u < NU; ++u) K2_LOAD(1, 1, u);
    }
#pragma unroll
    for (int sp = 0; sp < NU; ++sp)
#pragma unroll
      for (int j = 0; j < NT; ++j) {
        constexpr int dummy = 0; (void)dummy;
        const int q = half_tile<SET>(j);
        if (sp <= q) {
          const u32x4 A = {Al[cc][sp][0], Al[cc][sp][1], Ah[cc][sp][0], Ah[cc][sp][1]};
          acc[j] = mfma_f16(A, Bf[cc][q - sp], acc[j]);
        }
      }
    SB();
  }
#pragma unroll
  for (int j = 0; j < NT; ++j) *(f32x4*)(redw + j * 4096) = acc[j];
}

template <int NF>
DEV void w1_fin(const RtRaw<NF>& t, const ModP& mp, char* ws, int kk, int ct0, int lane, char* scr) {
  const int g = lane >> 4, i = lane & 15;
  float* tb = (float*)(scr + 32 * RT_STR);
  const float mv = mod_fin(mp);
  tb[lane] = lane < 32 ? rbf(1.f + mv) : mv;
  rt_park<NF>(t, scr, lane);
  lgkm0();
  const f32x4 ca = *(const f32x4*)(tb + 8 * g), cb = *(const f32x4*)(tb + 8 * g + 4);
  const f32x4 sa = *(const f32x4*)(tb + 32 + 8 * g), sb = *(const f32x4*)(tb + 36 + 8 * g);
  const u32x4 sv = {pk_bf(sa[0], sa[1]), pk_bf(sa[2], sa[3]), pk_bf(sb[0], sb[1]), pk_bf(sb[2], sb[3])};
  const u32x4 ones = {0x3f803f80u, 0x3f803f80u, 0x3f803f80u, 0x3f803f80u};
  const f32x4 z = {0.f, 0.f, 0.f, 0.f};
  const rsrc_t rs = ws_rsrc(ws);
#pragma unroll
  for (int j = 0; j < NF; ++j) {
    const u32x4 f = rt_frag(scr, j, lane);
    const u32x4 sc = {pk_bf(bf_lo(f[0]) * ca[0], bf_hi(f[0]) * ca[1]), pk_bf(bf_lo(f[1]) * ca[2], bf_hi(f[1]) * ca[3]),
                      pk_bf(bf_lo(f[2]) * cb[0], bf_hi(f[2]) * cb[1]), pk_bf(bf_lo(f[3]) * cb[2], bf_hi(f[3]) * cb[3])};
    st16(rs, WS_W1F + ((size_t)(kk * 108 + ct0 + j) * 64 + lane) * 16, sc);
    const f32x4 cs = mfma_bf16(sc, ones, z), b1 = mfma_bf16(f, sv, z);
    if (i < 2) st16(rs, WS_CSP + ((size_t)(2 * kk + i) * FF + 16 * (ct0 + j) + 4 * g) * 4, __builtin_bit_cast(u32x4, i ? b1 : cs));
  }
}

DEV void conv_phase(const unsigned short* __restrict__ dskip, char* __restrict__ ws,
                    const unsigned short* __restrict__ w1, const unsigned short* __restrict__ w2, char* smem_all) {
  constexpr int CS = K2_CS, KOFF = K2_KOFF;
  const int tid = threadIdx.x, lane = tid & 63;
  const int w = __builtin_amdgcn_readfirstlane(tid >> 6);
  char* stamp_ws = ws; (void)stamp_ws;
  STAMP(1, 0);
  const int bx = blockIdx.x;
  const int pj = (bx & 7) * 16 + (bx >> 4), par = (bx >> 3) & 1;
  const int r = lane & 15, g = lane >> 4, th = tid & 127;
  const int s4 = (r + 1) & 3;
  const rsrc_t rs = ws_rsrc(ws);
  unsigned* fl = (unsigned*)(smem_all + CV_FL);
  if (tid < 5) fl[tid] = 0u;
  if (w < 4) {
    const int d = 5 * pj + 2 * par + (w >> 1), hw = w & 1;
    char* smem = smem_all + (w >> 1) * K2_CH;
    const StageRaw sr = stage_ld(ws, d, th);
    const ChanRaw cr = chan_ld(dskip, ws, d);
    u32x2 xres[4];
#pragma unroll
    for (int q = 0; q < 4; ++q)
      xres[q] = *(const u32x2*)(ws + WS_XT + tl_off(d, 256 * (4 * hw + q) + 16 * r + 4 * g));
    SB();
    const Chan c = chan_fin(cr);
    stage_wr(sr, c, smem, th);
    __syncthreads();
    STAMP(1, 2);
    f32x4 acc[8];
#pragma unroll
    for (int q = 0; q < 8; ++q) acc[q] = (f32x4){0.f, 0.f, 0.f, 0.f};
    {
      const unsigned ap0 = (unsigned)(size_t)(smem + s4 * CS + 2 * (128 * hw + 8 * g + (r + 1) - s4));
      const unsigned bp0 = (unsigned)(size_t)(smem + KOFF + 2 * (2032 + 128 * hw - 16 * r + 8 * g) - 3584);
      u32x2 Al[2][8], Ah[2][8];
      u32x4 Bf[2][8];
#pragma unroll
      for (int u = 0; u < 8; ++u) K2_LOAD(0, 0, u);
#pragma unroll
      for (int cc = 0; cc < 4; ++cc) {
        lgkm0();
#pragma unroll
        for (int q = 0; q < 8; ++q) {
          if (cc < 3) K2_LOAD((cc + 1) & 1, cc + 1, q);
#pragma unroll
          for (int sp = 0; sp <= q; ++sp) {
            const u32x4 A = {Al[cc & 1][sp][0], Al[cc & 1][sp][1], Ah[cc & 1][sp][0], Ah[cc & 1][sp][1]};
            acc[q] = mfma_f16(A, Bf[cc & 1][q - sp], acc[q]);
          }
          SB();
        }
      }
    }
    STAMP(1, 3);
    f32x4 keep[4];
    {
      f32x4* xw = (f32x4*)(smem_all + CV_XCH + w * 4096) + lane;
      if (hw) {
#pragma unroll
        for (int q = 0; q < 4; ++q) { xw[64 * q] = acc[q]; keep[q] = acc[4 + q]; }
      } else {
#pragma unroll
        for (int q = 0; q < 4; ++q) { xw[64 * q] = acc[4 + q]; keep[q] = acc[q]; }
      }
    }
    lds_post(fl + w, lane);
    lds_wait(fl + (w ^ 1), 1u);
    const f32x4* xrd = (const f32x4*)(smem_all + CV_XCH + (w ^ 1) * 4096) + lane;
#pragma unroll
    for (int qp = 0; qp < 2; ++qp) {
      u32x2 o[2];
#pragma unroll
      for (int h = 0; h < 2; ++h) {
        const int q = 2 * qp + h;
        o[h] = conv_out(keep[q] + xrd[64 * q], c, smem, 256 * (4 * hw + q) + 16 * r + 4 * g, xres[q]);
      }
      const auto s0 = __builtin_amdgcn_permlane16_swap(o[0][0], o[1][0], false, false);
      const auto s1 = __builtin_amdgcn_permlane16_swap(o[0][1], o[1][1], false, false);
      const int t = 256 * (4 * hw + 2 * qp + (g & 1)) + 16 * r + 8 * (g >> 1);
      st16(rs, WS_X1T + (((size_t)(t >> 5) * D_ + d) * 32 + (t & 31)) * 2, (u32x4){s0[0], s1[0], s0[1], s1[1]});
    }
  } else {
    __builtin_amdgcn_s_setprio(2);
    const int d = 5 * pj + 4, wq = w - 4;
    char* smem = smem_all + 2 * K2_CH;
    const ChanRaw cr = chan_ld(dskip, ws, d);
    const int tl0 = par ? (wq < 3 ? 5 - wq : 1) : (wq == 0 ? 7 : (wq == 1 ? 6 : 2)), tl1 = 0;
    const unsigned ap0 = (unsigned)(size_t)(smem + s4 * CS + 2 * (64 * wq + 8 * g + (r + 1) - s4));
    const unsigned bp0 = (unsigned)(size_t)(smem + KOFF + 2 * (2032 + 64 * wq - 16 * r + 8 * g) - 3584);
    char* redw = smem_all + CV_RED + wq * 1024 + lane * 16;
    u32x2 xres[2];
    xres[0] = *(const u32x2*)(ws + WS_XT + tl_off(d, 256 * tl0 + 16 * r + 4 * g));
    xres[1] = *(const u32x2*)(ws + WS_XT + tl_off(d, 256 * tl1 + 16 * r + 4 * g));
    Chan c;
    if (wq < 2) {
      const StageRaw sr = stage_ld(ws, d, th);
      SB();
      c = chan_fin(cr);
      stage_wr(sr, c, smem, th);
    } else {
      c = chan_fin(cr);
#define RT_DELAY 32
      __builtin_amdgcn_s_sleep(RT_DELAY);
    }
    const bool isw1 = wq >= 2;
    const int b2 = min(bx, 215), b1 = min(bx, 239);
    const int jk = isw1 ? b1 / 12 : b2 >> 2;
    const int jc = isw1 ? (b1 % 12) * 9 + (wq - 2) * 4 : (b2 & 3) * 10 + 5 * wq;
    RtRaw<5> rt;
    rt_load<5>(rt, isw1 ? w1 : w2, isw1 ? FF : D_, jk, jc, lane);
    const ModP mp = mod_ld(ws, (lane < 32 ? 4 * D_ : 3 * D_ - 32) + 32 * min(jk, 19) + lane);
    SB();
    bar_lds();
    if (par) conv_half<1>(ap0, bp0, redw); else conv_half<0>(ap0, bp0, redw);
    lds_post(fl + 4, lane);
    if (isw1) {
      if (bx < 240) w1_fin<5>(rt, mp, ws, jk, jc, lane, smem_all + CV_RT + (wq - 2) * (32 * RT_STR + 256));
    } else if (bx < 216) {
      char* scr = smem_all + CV_RT2 + wq * (32 * RT_STR);
      rt_park<5>(rt, scr, lane);
      lgkm0();
#pragma unroll
      for (int j = 0; j < 5; ++j) st16(rs, WS_W2F + ((size_t)((jc + j) * 54 + jk) * 64 + lane) * 16, rt_frag(scr, j, lane));
    }
    lds_wait(fl + 4, 4u);
    const char* rd = smem_all + CV_RED + lane * 16;
#pragma unroll
    for (int u = 0; u < 2; ++u) {
      const bool on = u == 0 ? (par || wq < 3) : (par && wq == 0);
      if (on) {
        const int sl = u == 0 ? wq : 4, tl = u == 0 ? tl0 : tl1;
        const f32x4 y = (*(const f32x4*)(rd + sl * 4096) + *(const f32x4*)(rd + sl * 4096 + 1024))
                      + (*(const f32x4*)(rd + sl * 4096 + 2048) + *(const f32x4*)(rd + sl * 4096 + 3072));
        const int t = 256 * tl + 16 * r + 4 * g;
        st8(rs, WS_X1T + (((size_t)(t >> 5) * D_ + d) * 32 + (t & 31)) * 2, conv_out(y, c, smem, t, xres[u]));
      }
    }
  }
  STAMP(1, 7);
}

__global__ NOPK __launch_bounds__(512, 2) void k2(const unsigned short* __restrict__ dskip, char* __restrict__ ws,
                                                  const unsigned short* __restrict__ w1, const unsigned short* __restrict__ w2) {
  __shared__ __attribute__((aligned(16))) char smem[CV_RT + 2 * (32 * RT_STR + 256)];
  conv_phase(dskip, ws, w1, w2, smem);
}

DEV u32x4 ld16_sc1(rsrc_t r, int voff, int soff) { return __builtin_amdgcn_raw_buffer_load_b128(r, voff, soff, 16); }
DEV u32x4 ld16(rsrc_t r, int voff, int soff) { return __builtin_amdgcn_raw_buffer_load_b128(r, voff, soff, 0); }
DEV u32x4 tr_frag(const char* p) {
  const u32x2 a = __builtin_bit_cast(u32x2, __builtin_amdgcn_ds_read_tr16_b64_v4i16((lds_s16x4*)p));
  const u32x2 b = __builtin_bit_cast(u32x2, __builtin_amdgcn_ds_read_tr16_b64_v4i16((lds_s16x4*)(p + 256)));
  return (u32x4){a[0], a[1], b[0], b[1]};
}

constexpr int KF_RS = 84;
constexpr int KF_RB = 8 * 16 * KF_RS * 4;
constexpr int KF_BIG = 2 * KF_RB;
#define NW2L_ 8
constexpr int NW2L = NW2L_;
constexpr int NW2R = 35 - NW2L;
constexpr int KF_W2L = KF_BIG;
constexpr int KF_TAB = KF_W2L + 8 * NW2L * 1024;
constexpr int KF_PS = KF_TAB + 320;
constexpr int KF_XR = KF_PS + 512;
constexpr int KF_CNT = KF_XR + 64 * 176;
constexpr int KF_LDS = KF_CNT + 16;
#define W2_START 10
#define W2_RATE 3
#define W2_INL 28
#define SIG_SPLIT 4
#define H1_B0 1
#define H1_BATCH 7
#define W2_MID 4
#define W1_PRE 10
#define W1_EARLY 3
#define STAMP_EXTRA 0
#define H1_MODE 0
#define H1_PRE 17

DEV void kf_body(char* __restrict__ ws, unsigned short* __restrict__ out, char* smem) {
  char* xs = smem;
  float* tab = (float*)(smem + KF_TAB);
  float* ps = (float*)(smem + KF_PS);
  char* xr = smem + KF_XR;
  unsigned* lcnt = (unsigned*)(smem + KF_CNT);
  float* cst = (float*)(smem + 81920);
  const int tid = threadIdx.x, lane = tid & 63;
  const int w = __builtin_amdgcn_readfirstlane(tid >> 6);
  const int bx = blockIdx.x;
#define KF_MAP 0
  const int ng = (bx >> 3) & 7, mt = (bx & 7) * 4 + (bx >> 6);
  const int g = lane >> 4, i = lane & 15;
  char* stamp_ws = ws; (void)stamp_ws;
  STAMP(2, 0); STAMP(5, 0);
  const rsrc_t rs_ = ws_rsrc(ws);

  unsigned* flags = (unsigned*)(ws + WS_CNT) + mt * 128;
  const int ntl = ng < 6 ? 14 : 12;
  const int ctb = ng < 6 ? 14 * ng : 84 + 12 * (ng - 6);
  const bool two = w < ntl - 8;
  const bool full = w < 6;
  const int ks0 = w < 6 ? 7 * w : 42 + 6 * (w - 6);
  const int ct0 = ctb + (two ? 2 * w : (ntl - 8) + w);
  const int lo16 = lane * 16;
  const int w1s = (int)WS_W1F + ct0 * 1024;
  const int w2s = (int)WS_W2F + (ng * 5 * 54 + ks0) * 1024;

  u32x4 ring[32];
  const char* bb0 = xs + (8 * g + (i >> 2)) * 64 + ((g & 1) << 5) + (i & 3) * 8;
  const char* bb1 = xs + (8 * g + (i >> 2)) * 64 + (((g & 1) ^ 1) << 5) + (i & 3) * 8;
  auto w2_off = [&](int e) __attribute__((always_inline)) {
    const int s = e < 27 ? e / 4 : (e < 34 ? e - 27 : 6), c = e < 27 ? e % 4 : (e < 34 ? 4 : 3);
    const int sn = (s == 6 && !full) ? 5 : s;
    return w2s + (c * 54 + sn) * 1024;
  };

  const unsigned w2lb = (unsigned)(size_t)(smem + KF_W2L) + w * NW2L * 1024;
  auto w2_dma = [&](int n) __attribute__((always_inline)) {
    asm volatile("s_mov_b32 m0, %0\n\ts_nop 0\n\tbuffer_load_dwordx4 %1, %2, %3 offen lds"
                 :: "s"(w2lb + n * 1024), "v"(lo16), "s"(rs_), "s"(w2_off(NW2R + n)) : "memory");
  };

  auto gemm1 = [&](auto two_tag) __attribute__((always_inline)) {
    constexpr bool TWO = decltype(two_tag)::value;
    if (TWO) asm volatile("; two tiles" ::: "memory"); else asm volatile("; one tile" ::: "memory");
    u32x4 t[10];
#pragma unroll
    for (int it = 0; it < 10; ++it) t[it] = ld16(rs_, tid * 16, (int)WS_X1T + mt * 81920 + 8192 * it);
#pragma unroll
    for (int k = 0; k < W1_EARLY; ++k) {
      ring[2 * k] = ld16(rs_, lo16, w1s + k * 108 * 1024);
      if (TWO) ring[2 * k + 1] = ld16(rs_, lo16, w1s + (k * 108 + 1) * 1024);
    }
    SB();
    __builtin_amdgcn_s_barrier();
    SB();
#pragma unroll
    for (int k = W1_EARLY; k < W1_PRE; ++k) {
      ring[2 * k] = ld16(rs_, lo16, w1s + k * 108 * 1024);
      if (TWO) ring[2 * k + 1] = ld16(rs_, lo16, w1s + (k * 108 + 1) * 1024);
    }
    SB();
#pragma unroll
    for (int it = 0; it < 10; ++it) {
      const int c = tid + 512 * it;
      const int row = c >> 2;
      *(u32x4*)(xs + row * 64 + (((c & 3) ^ ((row >> 2) & 2)) << 4)) = t[it];
    }
    SB();
    if (tid == 0) *lcnt = 0u;
    ModP gbp;
    if (!TWO) gbp = mod_ld(ws, 5 * D_ + 80 * ng + min((w & 1) * 64 + lane, 79));
    SB();
    __syncthreads();
    STAMP(2, 1);
    {
      const int kb = (80 * ng) >> 5, tq = w & 3;
#pragma unroll
      for (int u = 0; u < 2; ++u) {
        const int kk = min(kb + 2 * (w >> 2) + u, 19);
        const u32x4 f = tr_frag(((tq & 1) ? bb1 : bb0) + (tq >> 1) * 40960 + kk * 2048);
        const unsigned dl = (unsigned)(32 * kk + 8 * g - 80 * ng);
        if (dl < 80u) *(u32x4*)(xr + (tq * 16 + i) * 176 + dl * 2) = f;
      }
    }
    const int hs = TWO ? 0 : (w & 1);
    const char* ba[4] = {bb0 + hs * 40960, bb1 + hs * 40960, bb0 + (hs ^ 1) * 40960, bb1 + (hs ^ 1) * 40960};
    const u32x4 ones = {0x3f803f80u, 0x3f803f80u, 0x3f803f80u, 0x3f803f80u};
    f32x4 acc[2][4];
#pragma unroll
    for (int c = 0; c < 2; ++c)
#pragma unroll
      for (int q = 0; q < 4; ++q) acc[c][q] = (f32x4){0.f, 0.f, 0.f, 0.f};
    u32x4 csp[20];
    const int cso = ((w & 1) * FF + 16 * ctb + 4 * min(lane, 4 * ntl - 1)) * 4;
    u32x4 bn[2][4];
#pragma unroll
    for (int q = 0; q < 4; ++q) bn[0][q] = tr_frag(ba[q]);
#pragma unroll
    for (int q = 0; q < 4; ++q) bn[1][q] = tr_frag(ba[q] + 2048);
#pragma unroll
    for (int kk = 0; kk < 20; ++kk) {
      u32x4 b[4];
#pragma unroll
      for (int q = 0; q < 4; ++q) b[q] = bn[kk & 1][q];
      SB();
#pragma unroll
      for (int q = 0; q < 4; ++q) acc[0][q] = mfma_bf16(ring[(2 * kk) % 32], b[q], acc[0][q]);
      if (TWO) {
#pragma unroll
        for (int q = 0; q < 4; ++q) acc[1][q] = mfma_bf16(ring[(2 * kk + 1) % 32], b[q], acc[1][q]);
      } else {
        acc[1][0] = mfma_bf16(ones, b[0], acc[1][0]); acc[1][1] = mfma_bf16(b[0], b[0], acc[1][1]);
        acc[1][2] = mfma_bf16(ones, b[1], acc[1][2]); acc[1][3] = mfma_bf16(b[1], b[1], acc[1][3]);
      }
      SB();
      if (kk < 18) {
#pragma unroll
        for (int q = 0; q < 4; ++q) bn[kk & 1][q] = tr_frag(ba[q] + (kk + 2) * 2048);
      }
      if (kk + W1_PRE < 20) {
        const int kn = kk + W1_PRE;
        ring[(2 * kn) % 32] = ld16(rs_, lo16, w1s + kn * 108 * 1024);
        if (TWO) ring[(2 * kn + 1) % 32] = ld16(rs_, lo16, w1s + (kn * 108 + 1) * 1024);
      }
#define LOCK_MASK 0x55554
      if ((LOCK_MASK >> kk) & 1) { SB(); __builtin_amdgcn_s_barrier(); SB(); }
      if (kk >= W2_START) {
#pragma unroll
        for (int u = 0; u < W2_RATE; ++u) {
          const int n = W2_RATE * (kk - W2_START) + u;
          if (n < NW2L) w2_dma(n);
          else if (n < W2_INL) ring[8 + n - NW2L] = ld16(rs_, lo16, w2_off(n - NW2L));
        }
      }
      if (!TWO) {
        if (kk < 10) {
#pragma unroll
          for (int u = 0; u < 2; ++u)
            csp[2 * kk + u] = ld16(rs_, cso, (int)WS_CSP + (2 * kk + u) * 2 * FF * 4);
        }
        if (kk == 14) {
          f32x4 t = __builtin_bit_cast(f32x4, csp[0]);
#pragma unroll
          for (int u = 1; u < 20; ++u) t += __builtin_bit_cast(f32x4, csp[u]);
          if (w >= 6 && lane < 56) *(f32x4*)(cst + (w & 1) * 224 + 4 * lane) = t;
        }
      }
      if (!TWO && kk == 12) { if ((w & 1) * 64 + lane < 80) tab[(w & 1) * 64 + lane] = mod_fin(gbp); }
      SB();
    }
    if (!TWO) {
#pragma unroll
      for (int h = 0; h < 2; ++h) {
        const f32x4 aq = acc[1][2 * h + 1];
        const float dq = (i & 2) ? ((i & 1) ? aq[3] : aq[2]) : ((i & 1) ? aq[1] : aq[0]);
        const int tk = (2 * hs + h) * 16 + i;
        if (g == (i >> 2)) {
          const float mean = acc[1][2 * h][0] * (1.f / D_);
          const float var = fmaxf(dq * (1.f / D_) - mean * mean, 0.f);
          const float sd = rbf(sqrtf(rbf(rbf(var) + 1e-5f)));
          const float rs = 1.f / sd;
          ps[tk] = rs;
          ps[64 + tk] = -rbf(mean) * rs;
        }
      }
    }
    __syncthreads();
    STAMP(2, 2);
    f32x4 csv[2], b1v[2];
#pragma unroll
    for (int c = 0; c < (TWO ? 2 : 1); ++c) {
      csv[c] = *(const f32x4*)(cst + 16 * (ct0 - ctb + c) + 4 * g);
      b1v[c] = *(const f32x4*)(cst + 224 + 16 * (ct0 - ctb + c) + 4 * g);
    }
#pragma unroll
    for (int qp = 0; qp < 2; ++qp) {
      u32x2 o[2][2];
#pragma unroll
      for (int h = 0; h < 2; ++h) {
        const int q = 2 * qp + h, tq = q ^ (2 * hs);
        const float rs = ps[tq * 16 + i], nm = ps[64 + tq * 16 + i];
#pragma unroll
        for (int c = 0; c < (TWO ? 2 : 1); ++c) {
          float v[4];
#pragma unroll
          for (int r = 0; r < 4; ++r) v[r] = fmaxf(fmaf(acc[c][q][r], rs, fmaf(nm, csv[c][r], b1v[c][r])), 0.f);
          o[c][h] = (u32x2){pk_bf(v[0], v[1]), pk_bf(v[2], v[3])};
        }
      }
      const int tl = ((2 * qp) ^ (2 * hs)) + (g & 1);
#pragma unroll
      for (int c = 0; c < (TWO ? 2 : 1); ++c) {
        const int ct = ct0 + c;
        const auto s0 = __builtin_amdgcn_permlane16_swap(o[c][0][0], o[c][1][0], false, false);
        const auto s1 = __builtin_amdgcn_permlane16_swap(o[c][0][1], o[c][1][1], false, false);
        const size_t off = (((size_t)(4 * mt + tl) * 54 + (ct >> 1)) * 64 + i + 16 * (2 * (ct & 1) + (g >> 1))) * 16;
#define H1_ST 16
        __builtin_amdgcn_raw_buffer_store_b128((u32x4){s0[0], s1[0], s0[1], s1[1]}, rs_, (int)(WS_H1F + off), 0, H1_ST);
      }
    }
    SB();
    STAMP(5, 1);
    __builtin_amdgcn_s_barrier();
    SB();
    constexpr int E0 = W2_INL - NW2L;
    auto signal = [&]() __attribute__((always_inline)) {
      if (lane == 0) {
        if (__hip_atomic_fetch_add(lcnt, 1u, __ATOMIC_RELAXED, __HIP_MEMORY_SCOPE_WORKGROUP) == 7u)
          __hip_atomic_store(flags + 16 * ng, 1u, __ATOMIC_RELAXED, __HIP_MEMORY_SCOPE_AGENT);
      }
    };
    if (w >= SIG_SPLIT) { asm volatile("s_waitcnt vmcnt(0)" ::: "memory"); signal(); }
    SB();
#pragma unroll
    for (int e = E0; e < NW2R; ++e) ring[(8 + e) % 32] = ld16(rs_, lo16, w2_off(e));
    SB();
    STAMP(5, 2);
    if (w < SIG_SPLIT) { asm volatile("s_waitcnt vmcnt(%0)" :: "n"(NW2R - E0) : "memory"); signal(); }
    SB();
  };
  if (two) gemm1(std::true_type{});
  else gemm1(std::false_type{});
  STAMP(2, 3);
#pragma nounroll
  for (unsigned spin = 0; spin < (1u << 20); ++spin) {
    if (__hip_atomic_load(flags + 16 * w, __ATOMIC_RELAXED, __HIP_MEMORY_SCOPE_AGENT) != 0u) break;
    __builtin_amdgcn_s_sleep(1);
  }
  asm volatile("" ::: "memory");
  SB();
  STAMP(2, 4);

  const int h1s = (int)WS_H1F + (mt * 4 * 54 + ks0) * 1024;
  u32x4 hb[28];
  auto load_h1 = [&](int n) __attribute__((always_inline)) {
    const int q = n / 7, s = n % 7;
    const int sn = (s == 6 && !full) ? 5 : s;
    hb[n] = ld16_sc1(rs_, lo16, h1s + (q * 54 + sn) * 1024);
  };
#pragma unroll
  for (int n = 0; n < H1_PRE; ++n) {
    if (H1_BATCH && n % H1_BATCH == 0 && n >= H1_B0) { SB(); __builtin_amdgcn_s_barrier(); SB(); }
    load_h1(n);
  }
  SB();
  float* red = (float*)smem;
  float* rb_ = red + (w * 16 + i) * KF_RS + 4 * g;
  const int re = w * 40 + min(lane, 39);
  const int rtk = re / 20, rq4 = re % 20;
  const float* rr_ = red + rtk * KF_RS + 4 * rq4;
  f32x4 ac2[2][5];
  f32x4 rv[4], rsum;
  const char* w2l = smem + KF_W2L + w * NW2L * 1024 + lo16;
  u32x4 wl[2], wl3;
  wl[0] = *(const u32x4*)w2l;
  auto red_wr = [&](int q, int c) __attribute__((always_inline)) {
    *(f32x4*)(rb_ + (q & 1) * (KF_RB / 4) + 16 * c) = ac2[q & 1][c];
  };
  auto red_rd = [&](int q, int h) __attribute__((always_inline)) {
#pragma unroll
    for (int ww = 0; ww < 4; ++ww) rv[ww] = *(const f32x4*)(rr_ + (q & 1) * (KF_RB / 4) + (4 * h + ww) * 16 * KF_RS);
  };
  auto red_out = [&](int q) __attribute__((always_inline)) {
    const f32x4 s = rsum + ((rv[0] + rv[1]) + (rv[2] + rv[3]));
    const u32x2 xv2 = *(const u32x2*)(xr + (16 * q + rtk) * 176 + rq4 * 8);
    const f32x4 gb = *(const f32x4*)(tab + 4 * rq4);
    u32x2 o;
#pragma unroll
    for (int h = 0; h < 2; ++h) {
      unsigned f = pk_bf(s[2 * h], s[2 * h + 1]);
      unsigned gf = pk_bf(bf_lo(f) * gb[2 * h], bf_hi(f) * gb[2 * h + 1]);
      o[h] = pk_bf(bf_lo(xv2[h]) + bf_lo(gf), bf_hi(xv2[h]) + bf_hi(gf));
    }
    if (lane < 40) *(u32x2*)(out + (size_t)(mt * 64 + 16 * q + rtk) * D_ + ng * 80 + 4 * rq4) = o;
  };
#pragma unroll
  for (int q = 0; q < 4; ++q) {
#pragma unroll
    for (int c = 0; c < 5; ++c) ac2[q & 1][c] = (f32x4){0.f, 0.f, 0.f, 0.f};
#pragma unroll
    for (int s = 0; s < 7; ++s) {
      if (q >= 1 && s >= 1 && s <= 5) red_wr(q - 1, s - 1);
      if (q >= 2) {
        if (s == 0) red_rd(q - 2, 0);
        if (s == 2) { rsum = (rv[0] + rv[1]) + (rv[2] + rv[3]); red_rd(q - 2, 1); }
        if (s == 4) red_out(q - 2);
      }
      SB();
      if (q < 3 || s < 6) wl[(7 * q + s + 1) & 1] = *(const u32x4*)(w2l + ((s + 1) % 7) * 1024);
      if (s == 5) wl3 = *(const u32x4*)(w2l + 7 * 1024);
      SB();
      if (s < 6 || full) {
#pragma unroll
        for (int c = 0; c < 5; ++c) {
          const u32x4 a = c == 4 ? wl[(7 * q + s) & 1] : ((s == 6 && c == 3) ? wl3 : ring[(8 + 4 * s + c) % 32]);
          ac2[q & 1][c] = mfma_bf16(a, hb[7 * q + s], ac2[q & 1][c]);
        }
      }
      SB();
      if (7 * q + s + H1_PRE < 28) { load_h1(7 * q + s + H1_PRE); SB(); }
    }
    if (q == 0) STAMP(2, 5);
    if (q >= 1) bar_lds();
  }
  STAMP(2, 6);
  red_rd(2, 0);
#pragma unroll
  for (int c = 0; c < 5; ++c) red_wr(3, c);
  rsum = (rv[0] + rv[1]) + (rv[2] + rv[3]);
  red_rd(2, 1);
  red_out(2);
  bar_lds();
  red_rd(3, 0);
  rsum = (rv[0] + rv[1]) + (rv[2] + rv[3]);
  red_rd(3, 1);
  red_out(3);
  STAMP(2, 7); STAMP(5, 7);
}

__global__ NOPK __launch_bounds__(512, 2) void kf(char* __restrict__ ws, unsigned short* __restrict__ out) {
  __shared__ __attribute__((aligned(16))) char smem[KF_LDS];
  kf_body(ws, out, smem);
}

__global__ void kend_() {}

extern "C" void kernel_launch(void* const* d_in, const int* in_sizes, int n_in,
                              void* d_out, int out_size, void* d_ws, size_t ws_size,
                              hipStream_t stream) {
  K1Args a;
  a.x = (const unsigned short*)d_in[0];
  a.are = (const unsigned short*)d_in[1];
  a.aim = (const unsigned short*)d_in[2];
  a.bre = (const unsigned short*)d_in[3];
  a.bim = (const unsigned short*)d_in[4];
  a.cre = (const unsigned short*)d_in[5];
  a.cim = (const unsigned short*)d_in[6];
  a.logdt = (const unsigned short*)d_in[7];
  a.w1 = (const unsigned short*)d_in[10];
  a.w2 = (const unsigned short*)d_in[11];
  a.c = (const unsigned short*)d_in[12];
  a.wmod = (const unsigned short*)d_in[13];
  a.ws = (char*)d_ws;
  a.dskip = (const unsigned short*)d_in[8];
  a.out = (const unsigned short*)d_out;
  k1<<<NB_PF + NB_MOD + NB_LN + NB_K, 256, 0, stream>>>(a);
  k2<<<256, 512, 0, stream>>>((const unsigned short*)d_in[8], (char*)d_ws, a.w1, a.w2);
  kf<<<256, 512, 0, stream>>>((char*)d_ws, (unsigned short*)d_out);
}
```

```cpp
#define PF 1
#include <hip/hip_runtime.h>
#include <hip/hip_bf16.h>
#include <stdint.h>
#include <type_traits>

typedef __attribute__((ext_vector_type(8))) __bf16 bf16x8;
typedef __attribute__((ext_vector_type(2))) __bf16 bf16x2;
typedef __attribute__((ext_vector_type(4))) float f32x4;
typedef __attribute__((ext_vector_type(2))) float f32x2;
typedef __attribute__((ext_vector_type(4))) unsigned u32x4;
typedef __attribute__((ext_vector_type(2))) unsigned u32x2;
typedef __attribute__((ext_vector_type(8))) _Float16 h16x8;
typedef __attribute__((ext_vector_type(2))) _Float16 h16x2;
typedef __attribute__((ext_vector_type(4))) short s16x4;
typedef __attribute__((address_space(3))) s16x4 lds_s16x4;

#if defined(__HIP_DEVICE_COMPILE__)
#define NOPK __attribute__((target("no-packed-fp32-ops")))
#else
#define NOPK
#endif
#define DEV __device__ __forceinline__

constexpr int L_ = 2048;
constexpr int D_ = 640;
constexpr int NS = 128;
constexpr int FF = 1728;
constexpr int KREV = 2304;

constexpr size_t WS_KSC  = 8192;
constexpr size_t WS_CNT  = 16384;
constexpr size_t WS_CSB  = 32768;
constexpr size_t WS_MOD  = 65536;
constexpr size_t WS_XNT  = 196608;
constexpr size_t WS_XT   = WS_XNT + (size_t)D_ * L_ * 2;
constexpr size_t WS_KREV = WS_XT + (size_t)D_ * L_ * 2;
constexpr size_t WS_W1F  = WS_KREV + (size_t)D_ * 2 * KREV * 2;
constexpr size_t WS_W2F  = WS_W1F + (size_t)D_ * FF * 2;
constexpr size_t WS_X1T  = WS_W2F + (size_t)D_ * FF * 2;
constexpr size_t WS_X1   = WS_X1T + (size_t)D_ * L_ * 2;
constexpr size_t WS_CSP  = WS_X1;
constexpr size_t WS_H1F  = WS_X1 + (size_t)D_ * L_ * 2;

#define STAMP(kid, k) do {} while (0)

DEV float bf_lo(unsigned u) { return __uint_as_float(u << 16); }
DEV float bf_hi(unsigned u) { return __uint_as_float(u & 0xffff0000u); }
DEV unsigned pk_bf(float a, float b) {
  f32x2 v = {a, b};
  bf16x2 r = __builtin_convertvector(v, bf16x2);
  return __builtin_bit_cast(unsigned, r);
}
DEV float rbf(float a) { return bf_lo(pk_bf(a, 0.f)); }
DEV float ldbf(const unsigned short* p) { return __uint_as_float(((unsigned)*p) << 16); }

typedef __amdgpu_buffer_rsrc_t rsrc_t;
DEV rsrc_t ws_rsrc(char* ws) { return __builtin_amdgcn_make_buffer_rsrc((void*)ws, 0, 0x7fffffff, 0x00020000); }
#define ST_AUX 16
DEV void st16(rsrc_t r, size_t off, u32x4 v) { __builtin_amdgcn_raw_buffer_store_b128(v, r, (int)off, 0, ST_AUX); }
#define LN_AUX 16
DEV void st16ln(rsrc_t r, size_t off, u32x4 v) { __builtin_amdgcn_raw_buffer_store_b128(v, r, (int)off, 0, LN_AUX); }
DEV void st8(rsrc_t r, size_t off, u32x2 v) { __builtin_amdgcn_raw_buffer_store_b64(v, r, (int)off, 0, 16); }

DEV u32x2 tr_read(unsigned lds_addr) {
  u32x2 r;
  asm volatile("ds_read_b64_tr_b16 %0, %1" : "=v"(r) : "v"(lds_addr) : "memory");
  return r;
}
DEV void bar_lds() { asm volatile("s_waitcnt lgkmcnt(0)\n\ts_barrier" ::: "memory"); }
DEV void lds_wait(const void* word, unsigned val) {
  unsigned tv, ts, cnt;
  asm volatile("s_mov_b32 %2, 0x100000\n"
               "1:\n\tds_read_b32 %0, %3\n\ts_waitcnt lgkmcnt(0)\n\tv_readfirstlane_b32 %1, %0\n\t"
               "s_cmp_ge_u32 %1, %4\n\ts_cbranch_scc1 2f\n\ts_sleep 1\n\ts_sub_u32 %2, %2, 1\n\t"
               "s_cmp_lg_u32 %2, 0\n\ts_cbranch_scc1 1b\n2:"
               : "=&v"(tv), "=&s"(ts), "=&s"(cnt) : "v"((unsigned)(size_t)word), "s"(val) : "memory", "scc");
}
DEV void lds_post(unsigned* word, int lane) {
  asm volatile("" ::: "memory");
  if (lane == 0) __hip_atomic_fetch_add(word, 1u, __ATOMIC_RELAXED, __HIP_MEMORY_SCOPE_WORKGROUP);
  asm volatile("" ::: "memory");
}
DEV void lgkm0() { asm volatile("s_waitcnt lgkmcnt(0)" ::: "memory"); __builtin_amdgcn_sched_barrier(0); }

DEV f32x4 mfma_bf16(u32x4 a, u32x4 b, f32x4 c) {
  return __builtin_amdgcn_mfma_f32_16x16x32_bf16(__builtin_bit_cast(bf16x8, a), __builtin_bit_cast(bf16x8, b), c, 0, 0, 0);
}

constexpr int NB_MOD = 120, NB_LN = 256, NB_W2 = 108, NB_K = 640;

struct K1Args {
  const unsigned short *x, *are, *aim, *bre, *bim, *cre, *cim, *logdt, *w1, *w2, *c, *wmod;
  const unsigned short *dskip, *out;
  char* ws;
};

typedef __attribute__((address_space(1))) char gchar;
struct Ptrs { gchar *ws, *out, *w1, *w2, *dskip; };
__device__ Ptrs g_ptrs;
constexpr int NB_PF = 8;
DEV void k1_prefetch(const K1Args& p, int b) {
  const int tid = threadIdx.x;
  if (b == 0 && tid == 0) {
    g_ptrs.ws = (gchar*)p.ws; g_ptrs.out = (gchar*)p.out; g_ptrs.w1 = (gchar*)p.w1; g_ptrs.w2 = (gchar*)p.w2; g_ptrs.dskip = (gchar*)p.dskip;
  }
  unsigned long long pc;
  int o0, o1;
  asm volatile("s_getpc_b64 %0\n.Lpf_here%=:\n\ts_mov_b32 %1, _Z2k16K1Args - .Lpf_here%=\n\ts_mov_b32 %2, _Z5kend_v - .Lpf_here%="
               : "=s"(pc), "=s"(o0), "=s"(o1));
  const char* c0 = (const char*)(pc + (long long)o0);
  long n = o1 - o0;
  n = n < 0 ? 0 : (n > 98304 ? 98304 : n);
  unsigned acc = 0;
  {
    unsigned v[3];
#pragma unroll
    for (int it = 0; it < 3; ++it) {
      const long o = (long)tid * 128 + it * 32768;
      v[it] = o < n ? *(const __attribute__((address_space(1))) unsigned*)(c0 + o) : 0u;
    }
    acc = v[0] ^ v[1] ^ v[2];
  }
  asm volatile("" :: "v"(acc));
}

DEV void k1_mod(const K1Args& p, int b, char* smem) {
  const int tid = threadIdx.x;
  const int rg = b & 7, cgp = b >> 3;
  const int c32 = tid & 31, r8 = tid >> 5;
  float* sil = (float*)smem;
  float* red = (float*)(smem + 512);
  u32x4 w[10];
#pragma unroll
  for (int it = 0; it < 10; ++it)
    w[it] = *(const u32x4*)(p.wmod + (size_t)(rg * 80 + r8 + 8 * it) * 3840 + 256 * cgp + 8 * c32);
  if (tid < 80) {
    float cv = ldbf(p.c + rg * 80 + tid);
    sil[tid] = rbf(cv / (1.f + expf(-cv)));
  }
  if (tid >= 128 && tid < 132 && b < 64) ((unsigned*)(p.ws + WS_CNT))[(b * 4 + tid - 128) * 16] = 0u;
  __syncthreads();
  float acc[8];
#pragma unroll
  for (int j = 0; j < 8; ++j) acc[j] = 0.f;
#pragma unroll
  for (int it = 0; it < 10; ++it) {
    float s = sil[r8 + 8 * it];
#pragma unroll
    for (int q = 0; q < 4; ++q) {
      acc[2 * q] += s * bf_lo(w[it][q]);
      acc[2 * q + 1] += s * bf_hi(w[it][q]);
    }
  }
#pragma unroll
  for (int j = 0; j < 8; ++j) acc[j] += __shfl_xor(acc[j], 32, 64);
  if ((tid & 63) < 32) {
    *(f32x4*)(red + (tid >> 6) * 256 + 8 * c32) = (f32x4){acc[0], acc[1], acc[2], acc[3]};
    *(f32x4*)(red + (tid >> 6) * 256 + 8 * c32 + 4) = (f32x4){acc[4], acc[5], acc[6], acc[7]};
  }
  __syncthreads();
  ((float*)(p.ws + WS_MOD))[(256 * cgp + tid) * 8 + rg] = (red[tid] + red[256 + tid]) + (red[512 + tid] + red[768 + tid]);
}
struct ModP { f32x4 a, b; };
DEV ModP mod_ld(const char* ws, int idx) {
  const f32x4* mp = (const f32x4*)(ws + WS_MOD) + 2 * idx;
  ModP m; m.a = mp[0]; m.b = mp[1];
  return m;
}
DEV float mod_fin(const ModP& m) {
  return rbf(((m.a[0] + m.a[1]) + (m.a[2] + m.a[3])) + ((m.b[0] + m.b[1]) + (m.b[2] + m.b[3])));
}
DEV float mod_sum(const char* ws, int idx) { return mod_fin(mod_ld(ws, idx)); }
#define SB() __builtin_amdgcn_sched_barrier(0)

DEV void k1_ln(const K1Args& p, int b, char* smem) {
  constexpr int STR = 704;
  const int tid = threadIdx.x;
  const int tok = tid >> 3, sub = tid & 7;
  const int tile = (b & 7) * 8 + ((b >> 3) & 7), pass = b >> 6;
  const int hf = pass & 1;
  const bool norm = pass < 2;
  const int t0 = tile * 32;
  const unsigned short* xr = p.x + (size_t)(t0 + tok) * D_ + 8 * sub;
  u32x4 xv[5], xo[5];
#pragma unroll
  for (int m = 0; m < 5; ++m) xv[m] = *(const u32x4*)(xr + 320 * hf + 64 * m);
  if (norm) {
#pragma unroll
    for (int m = 0; m < 5; ++m) xo[m] = *(const u32x4*)(xr + 320 * (hf ^ 1) + 64 * m);
    float s = 0.f, v = 0.f;
    const bf16x2 one2 = __builtin_bit_cast(bf16x2, 0x3f803f80u);
#pragma unroll
    for (int m = 0; m < 10; ++m)
#pragma unroll
      for (int q = 0; q < 4; ++q) {
        const unsigned u = m < 5 ? xv[m][q] : xo[m - 5][q];
        const bf16x2 x2 = __builtin_bit_cast(bf16x2, u);
        s = __builtin_amdgcn_fdot2_f32_bf16(x2, one2, s, false);
        v = __builtin_amdgcn_fdot2_f32_bf16(x2, x2, v, false);
      }
#pragma unroll
    for (int o = 1; o < 8; o <<= 1) { s += __shfl_xor(s, o, 64); v += __shfl_xor(v, o, 64); }
    const float mean = s * (1.f / D_);
    const float var = fmaxf(v * (1.f / D_) - mean * mean, 0.f);
    const float sd = rbf(sqrtf(rbf(rbf(var) + 1e-5f)));
    const float rs = 1.f / sd;
    const float mb = rbf(mean);
#pragma unroll
    for (int m = 0; m < 5; ++m)
#pragma unroll
      for (int q = 0; q < 4; ++q) {
        unsigned d2 = pk_bf(bf_lo(xv[m][q]) - mb, bf_hi(xv[m][q]) - mb);
        xv[m][q] = pk_bf(bf_lo(d2) * rs, bf_hi(d2) * rs);
      }
  }
  const int lane = tid & 63, w = tid >> 6;
  const int g = lane >> 4, i = lane & 15;
  const rsrc_t rsrc = ws_rsrc(p.ws);
#pragma unroll
  for (int m = 0; m < 5; ++m) *(u32x4*)(smem + tok * STR + (sub + 8 * m) * 16) = xv[m];
  __syncthreads();
  const size_t dsto = (norm ? WS_XNT : WS_XT) + ((size_t)tile * D_ + 320 * hf) * 64;
#pragma unroll
  for (int it = 0; it < 5; ++it) {
    const int cb = w * 5 + it;
    const char* a = smem + (8 * g + (i >> 2)) * STR + cb * 32 + (i & 3) * 8;
    const u32x2 ra = __builtin_bit_cast(u32x2, __builtin_amdgcn_ds_read_tr16_b64_v4i16((lds_s16x4*)a));
    const u32x2 rb = __builtin_bit_cast(u32x2, __builtin_amdgcn_ds_read_tr16_b64_v4i16((lds_s16x4*)(a + 4 * STR)));
    st16ln(rsrc, dsto + (size_t)(cb * 16 + i) * 64 + 16 * g, (u32x4){ra[0], ra[1], rb[0], rb[1]});
  }
}

constexpr int RT_STR = 176;
template <int NF> struct RtRaw { u32x4 r[NF]; };
template <int NF>
DEV void rt_load(RtRaw<NF>& t, const unsigned short* W, int N, int kk, int ct0, int lane) {
#pragma unroll
  for (int j = 0; j < NF; ++j) {
    const int c = lane + 64 * j, row = c / (2 * NF), part = c % (2 * NF);
    t.r[j] = *(const u32x4*)(W + (size_t)(32 * kk + row) * N + 16 * ct0 + 8 * part);
  }
}
template <int NF>
DEV void rt_park(const RtRaw<NF>& t, char* scr, int lane) {
#pragma unroll
  for (int j = 0; j < NF; ++j) {
    const int c = lane + 64 * j, row = c / (2 * NF), part = c % (2 * NF);
    *(u32x4*)(scr + row * RT_STR + 16 * part) = t.r[j];
  }
}
DEV u32x4 rt_frag(const char* scr, int j, int lane) {
  const int g = lane >> 4, i = lane & 15;
  const char* p = scr + (8 * g + (i >> 2)) * RT_STR + 32 * j + (i & 3) * 8;
  const u32x2 a = __builtin_bit_cast(u32x2, __builtin_amdgcn_ds_read_tr16_b64_v4i16((lds_s16x4*)p));
  const u32x2 b = __builtin_bit_cast(u32x2, __builtin_amdgcn_ds_read_tr16_b64_v4i16((lds_s16x4*)(p + 4 * RT_STR)));
  return (u32x4){a[0], a[1], b[0], b[1]};
}
struct cf { float re, im; };
DEV cf cmul(cf a, cf b) {
  cf r;
  r.re = a.re * b.re - a.im * b.im;
  r.im = a.re * b.im + a.im * b.re;
  return r;
}
DEV cf cexp_(float a2, float br, float m, float m0) {
  float mag = __builtin_amdgcn_exp2f(a2 * (m - m0));
  float ph = __builtin_amdgcn_fractf(br * m);
  cf r; r.re = mag * __builtin_amdgcn_cosf(ph); r.im = mag * __builtin_amdgcn_sinf(ph);
  return r;
}
DEV unsigned pk_h(float a, float b) {
  f32x2 v = {a, b};
  h16x2 r = __builtin_convertvector(v, h16x2);
  return __builtin_bit_cast(unsigned, r);
}
DEV f32x4 mfma_f16(u32x4 a, u32x4 b, f32x4 c) {
  return __builtin_amdgcn_mfma_f32_16x16x32_f16(__builtin_bit_cast(h16x8, a), __builtin_bit_cast(h16x8, b), c, 0, 0, 0);
}
DEV void put2(char* p, cf a, cf b) {
  u32x2 o = {pk_h(a.re, a.im), pk_h(b.re, b.im)};
  *(u32x2*)p = o;
}

constexpr int KG_RS = 544;
DEV void k1_kgen(const K1Args& p, int d, char* smem) {
  constexpr int RS = KG_RS;
  const int tid = threadIdx.x;
  const int lane = tid & 63;
  const int w = __builtin_amdgcn_readfirstlane(tid >> 6);
  const int g = lane >> 4, i = lane & 15;
  char* stamp_ws = p.ws; (void)stamp_ws;
  const size_t po = (size_t)d * NS + 2 * lane;
  const unsigned rar = *(const unsigned*)(p.are + po), rai = *(const unsigned*)(p.aim + po);
  const unsigned rbr = *(const unsigned*)(p.bre + po), rbi = *(const unsigned*)(p.bim + po);
  const unsigned rcr = *(const unsigned*)(p.cre + po), rci = *(const unsigned*)(p.cim + po);
  const float ldt = ldbf(p.logdt + d);
  asm volatile("" ::: "memory");
  SB();
  const float dt = rbf(expf(ldt));
  STAMP(0, 1);
  float a2[2], bv[2], m0[2];
  a2[0] = (dt * bf_lo(rar)) * 1.44269504088896341f; a2[1] = (dt * bf_hi(rar)) * 1.44269504088896341f;
  bv[0] = (dt * bf_lo(rai)) * 0.15915494309189534f; bv[1] = (dt * bf_hi(rai)) * 0.15915494309189534f;
  m0[0] = a2[0] > 0.f ? 2048.f : 0.f; m0[1] = a2[1] > 0.f ? 2048.f : 0.f;
  char* my = smem + lane * 8;
  if (w == 0) {
    cf v[2], v8[2], e8[2];
    float bound = 0.f;
#pragma unroll
    for (int q = 0; q < 2; ++q) {
      const float dbr = dt * (q ? bf_hi(rbr) : bf_lo(rbr)), dbi = dt * (q ? bf_hi(rbi) : bf_lo(rbi));
      const float cr = q ? bf_hi(rcr) : bf_lo(rcr), ci = q ? bf_hi(rci) : bf_lo(rci);
      const float gsh = __builtin_amdgcn_exp2f(a2[q] * m0[q]);
      v[q].re = (cr * dbr - ci * dbi) * gsh; v[q].im = -(cr * dbi + ci * dbr) * gsh;
      bound += (fabsf(v[q].re) + fabsf(v[q].im)) * __builtin_amdgcn_exp2f(fmaxf(a2[q], 0.f) * 120.f);
      e8[q] = cexp_(a2[q], -bv[q], 8.f, 0.f);
      v8[q] = cexp_(a2[q], -bv[q], 64.f, 0.f);
    }
#pragma unroll
    for (int o = 1; o < 64; o <<= 1) bound += __shfl_xor(bound, o, 64);
    int ex = (int)((__float_as_uint(bound) >> 23) & 0xff);
    ex = ex < 20 ? 20 : (ex > 240 ? 240 : ex);
    const float sv = __uint_as_float((unsigned)(127 + 13 + 127 - ex) << 23);
    if (lane == 0) ((float*)(p.ws + WS_KSC))[d] = __uint_as_float((unsigned)(ex - 13) << 23);
#pragma unroll
    for (int q = 0; q < 2; ++q) { v[q].re *= sv; v[q].im *= sv; v8[q] = cmul(v[q], v8[q]); }
#pragma unroll
    for (int j = 0; j < 8; ++j) {
      put2(my + (41 + j) * RS, v[0], v[1]);
      put2(my + (49 + j) * RS, v8[0], v8[1]);
      if (j < 7) {
#pragma unroll
        for (int q = 0; q < 2; ++q) { v[q] = cmul(v[q], e8[q]); v8[q] = cmul(v8[q], e8[q]); }
      }
    }
  } else if (w == 1) {
    cf e1[2], u[2], u2[2];
#pragma unroll
    for (int q = 0; q < 2; ++q) {
      e1[q] = cexp_(a2[q], bv[q], 1.f, 0.f);
      u[q].re = __builtin_amdgcn_exp2f(-a2[q] * m0[q]); u[q].im = 0.f;
      u2[q] = cexp_(a2[q], bv[q], 128.f, m0[q]);
    }
#pragma unroll
    for (int r = 0; r < 8; ++r) {
      put2(my + r * RS, u[0], u[1]);
      put2(my + (8 + r) * RS, u2[0], u2[1]);
      if (r < 7) {
#pragma unroll
        for (int q = 0; q < 2; ++q) { u[q] = cmul(u[q], e1[q]); u2[q] = cmul(u2[q], e1[q]); }
      }
    }
  } else if (w == 2) {
    cf e2[2], u[2], u2[2];
#pragma unroll
    for (int q = 0; q < 2; ++q) {
      e2[q] = cexp_(a2[q], bv[q], 2.f, 0.f);
      u[q] = cexp_(a2[q], bv[q], 256.f, m0[q]);
      u2[q] = cexp_(a2[q], bv[q], 384.f, m0[q]);
    }
#pragma unroll
    for (int r = 0; r < 4; ++r) {
      put2(my + (16 + r) * RS, u[0], u[1]);
      put2(my + (20 + r) * RS, u2[0], u2[1]);
      if (r < 3) {
#pragma unroll
        for (int q = 0; q < 2; ++q) { u[q] = cmul(u[q], e2[q]); u2[q] = cmul(u2[q], e2[q]); }
      }
    }
    cf e4[2] = {cmul(e2[0], e2[0]), cmul(e2[1], e2[1])};
#pragma unroll
    for (int r = 0; r < 2; ++r) {
      cf c0 = cexp_(a2[0], bv[0], 512.f + 128.f * r, m0[0]), c1 = cexp_(a2[1], bv[1], 512.f + 128.f * r, m0[1]);
      put2(my + (24 + 2 * r) * RS, c0, c1);
      put2(my + (25 + 2 * r) * RS, cmul(c0, e4[0]), cmul(c1, e4[1]));
    }
  } else {
    cf e4[2], e128[2], u[2], u2[2];
#pragma unroll
    for (int q = 0; q < 2; ++q) {
      e4[q] = cexp_(a2[q], bv[q], 4.f, 0.f);
      e128[q] = cexp_(a2[q], bv[q], 128.f, 0.f);
      u[q] = cexp_(a2[q], bv[q], 1024.f, m0[q]);
      u2[q] = cexp_(a2[q], bv[q], 1536.f, m0[q]);
    }
#pragma unroll
    for (int r = 2; r < 4; ++r) {
      cf c0 = cexp_(a2[0], bv[0], 512.f + 128.f * r, m0[0]), c1 = cexp_(a2[1], bv[1], 512.f + 128.f * r, m0[1]);
      put2(my + (24 + 2 * r) * RS, c0, c1);
      put2(my + (25 + 2 * r) * RS, cmul(c0, e4[0]), cmul(c1, e4[1]));
    }
#pragma unroll
    for (int r = 0; r < 4; ++r) {
      put2(my + (32 + r) * RS, u[0], u[1]);
      put2(my + (36 + r) * RS, u2[0], u2[1]);
#pragma unroll
      for (int q = 0; q < 2; ++q) { if (r < 3) u[q] = cmul(u[q], e128[q]); u2[q] = cmul(u2[q], e128[q]); }
    }
    put2(my + 40 * RS, u2[0], u2[1]);
  }
  __syncthreads();
  STAMP(0, 2);
  _Float16* kh = (_Float16*)(smem + 57 * RS);
  if (w < 3) {
    const char* ua = smem + (16 * w + i) * RS + g * 16;
    const char* va = smem + (41 + i) * RS + g * 16;
    f32x4 acc = {0.f, 0.f, 0.f, 0.f};
#pragma unroll
    for (int ks = 0; ks < 8; ++ks)
      acc = mfma_f16(*(const u32x4*)(ua + ks * 64), *(const u32x4*)(va + ks * 64), acc);
    int idx0, st;
    if (w == 0) { idx0 = (g < 2 ? 4 * g : 120 + 4 * g) + 8 * i; st = 1; }
    else if (w == 1) { idx0 = g < 2 ? 256 + 64 * g + 4 * i : 384 + 64 * (g - 2) + 2 * i; st = 1; }
    else { idx0 = 512 + 64 * g + i; st = 16; }
#pragma unroll
    for (int rg = 0; rg < 4; ++rg) {
      int idx = idx0 + st * rg;
      if (w == 1 && g >= 2) idx = idx0 + 32 * (rg >> 1) + (rg & 1);
      if (w == 2 && g >= 2) idx = (g == 2 && rg == 0 && i == 0) ? 640 : 644;
      kh[idx] = (_Float16)acc[rg];
    }
  }
  __syncthreads();
  STAMP(0, 3);
  const unsigned short* ku = (const unsigned short*)kh;
  const size_t kout = WS_KREV + (size_t)d * KREV * 2;
  {
    const int l0 = 2040 - 8 * tid;
    u32x4 o;
    if (w < 2) {
      const int q = l0 >> 3;
      const unsigned a = ku[384 + q], b = ku[385 + q];
      const unsigned t = (q & 1) ? b : a;
      o[0] = b | (b << 16); o[1] = b | (t << 16); o[2] = a | (a << 16); o[3] = o[2];
    } else if (w == 2) {
      const int q = l0 >> 2;
      const unsigned a = ku[256 + q], b = ku[257 + q], c = ku[258 + q];
      o[0] = c | (c << 16); o[1] = b | (b << 16); o[2] = b | (a << 16); o[3] = a | (a << 16);
    } else {
      unsigned v[8];
#pragma unroll
      for (int e = 0; e < 8; ++e) {
        const int l = l0 + 7 - e;
        const int idx = l < 256 ? l : (int)(pk_bf((float)l, 0.f) & 0xffffu) - 17024;
        v[e] = ku[idx];
      }
#pragma unroll
      for (int q = 0; q < 4; ++q) o[q] = v[2 * q] | (v[2 * q + 1] << 16);
    }
    st16(ws_rsrc(p.ws), kout + 16 * tid, o);
  }
}

__global__ NOPK __launch_bounds__(256) void k1(K1Args p) {
  __shared__ __attribute__((aligned(16))) char smem[57 * KG_RS + 1312];
  int b = blockIdx.x;
  char* stamp_ws = p.ws; (void)stamp_ws;
  STAMP(0, 0);
  if (b < NB_PF) { k1_prefetch(p, b); STAMP(0, 7); return; }
  b -= NB_PF;
  if (b < NB_K) { k1_kgen(p, b, smem); STAMP(0, 7); return; }
  b -= NB_K;
#define K1_PRIO 0
  __builtin_amdgcn_s_setprio(K1_PRIO);
#define K1_DELAY 12
  if (K1_DELAY) { asm volatile("" :: "s"(p.ws), "s"(p.x)); __builtin_amdgcn_s_sleep(K1_DELAY); }
  if (b < NB_LN) {
    k1_ln(p, b, smem);
    STAMP(0, 7);
    return;
  }
  b -= NB_LN;
  k1_mod(p, b, smem);
  STAMP(0, 7);
}

constexpr int K2_CS = 4160;
constexpr int K2_KOFF = 4 * K2_CS;
constexpr int K2_XOFF = K2_CS;
constexpr int K2_CH = K2_KOFF + 4608;
constexpr int CV_RED = 3 * K2_CH;
constexpr int CV_RT2 = 86016;
constexpr int CV_XCH = CV_RT2 + 2 * 32 * 176;
constexpr int CV_FL = CV_RED + 20480;
constexpr int CV_RT = 151552;

struct Chan { float sa, ca1, ga, dsk, hs, hinv, yinv; };
struct ChanRaw { ModP msa, mca, mga; float kinv; unsigned short rds; };
DEV ChanRaw chan_ld(const unsigned short* dskip, const char* ws, int d) {
  ChanRaw c;
  c.rds = dskip[d];
  c.msa = mod_ld(ws, d); c.mca = mod_ld(ws, D_ + d); c.mga = mod_ld(ws, 2 * D_ + d);
  c.kinv = ((const float*)(ws + WS_KSC))[d];
  return c;
}
DEV Chan chan_fin(const ChanRaw& r) {
  Chan c;
  c.sa = mod_fin(r.msa); c.ca1 = rbf(1.f + mod_fin(r.mca)); c.ga = mod_fin(r.mga);
  c.dsk = __uint_as_float((unsigned)r.rds << 16);
  const float hb = 26.f * fabsf(c.ca1) + fabsf(c.sa);
  int ex = (int)((__float_as_uint(hb) >> 23) & 0xff);
  ex = ex < 20 ? 20 : (ex > 240 ? 240 : ex);
  c.hs = __uint_as_float((unsigned)(127 + 14 + 127 - ex) << 23);
  c.hinv = __uint_as_float((unsigned)(ex - 14) << 23);
  c.yinv = r.kinv * c.hinv;
  return c;
}
DEV size_t tl_off(int d, int t) { return (((size_t)(t >> 5) * D_ + d) * 32 + (t & 31)) * 2; }
struct StageRaw { u32x4 xa0, xa1, kv0, kv1; u32x2 xb; };
DEV StageRaw stage_ld(const char* ws, int d, int th) {
  StageRaw s;
  const unsigned short* krev = (const unsigned short*)(ws + WS_KREV) + (size_t)d * KREV;
  const size_t xo = tl_off(d, 16 * th);
  s.xa0 = *(const u32x4*)(ws + WS_XNT + xo); s.xa1 = *(const u32x4*)(ws + WS_XNT + xo + 16);
  s.xb = *(const u32x2*)(ws + WS_XNT + tl_off(d, min(16 * th + 16, L_ - 4)));
  s.kv0 = *(const u32x4*)(krev + 8 * th); s.kv1 = *(const u32x4*)(krev + 1024 + 8 * th);
  return s;
}
DEV void stage_wr(const StageRaw& s, const Chan& c, char* smem, int th) {
  constexpr int CS = K2_CS, KOFF = K2_KOFF;
  unsigned hh[10];
#pragma unroll
  for (int q = 0; q < 10; ++q) {
    unsigned u = q < 4 ? s.xa0[q] : (q < 8 ? s.xa1[q - 4] : s.xb[q - 8]);
    unsigned m = pk_bf(bf_lo(u) * c.ca1, bf_hi(u) * c.ca1);
    unsigned h = pk_bf(bf_lo(m) + c.sa, bf_hi(m) + c.sa);
    hh[q] = pk_h(bf_lo(h) * c.hs, bf_hi(h) * c.hs);
  }
  char* dst = smem + (2 * th + 2) * 16;
  unsigned s1[9];
#pragma unroll
  for (int q = 0; q < 9; ++q) s1[q] = __builtin_amdgcn_alignbit(hh[q + 1], hh[q], 16);
#pragma unroll
  for (int e = 0; e < 2; ++e) {
    *(u32x4*)(dst + 16 * e) = (u32x4){hh[4 * e], hh[4 * e + 1], hh[4 * e + 2], hh[4 * e + 3]};
    *(u32x4*)(dst + 16 * e + CS) = (u32x4){s1[4 * e], s1[4 * e + 1], s1[4 * e + 2], s1[4 * e + 3]};
    *(u32x4*)(dst + 16 * e + 2 * CS) = (u32x4){hh[4 * e + 1], hh[4 * e + 2], hh[4 * e + 3], hh[4 * e + 4]};
    *(u32x4*)(dst + 16 * e + 3 * CS) = (u32x4){s1[4 * e + 1], s1[4 * e + 2], s1[4 * e + 3], s1[4 * e + 4]};
  }
  if (th == 0) {
    u32x4 z = {0u, 0u, 0u, 0u};
    *(u32x4*)(smem) = z; *(u32x4*)(smem + CS) = z; *(u32x4*)(smem + 2 * CS) = z; *(u32x4*)(smem + 3 * CS) = z;
    *(u32x4*)(smem + 16) = z;
    *(u32x4*)(smem + CS + 16) = (u32x4){0u, 0u, 0u, hh[0] << 16};
    *(u32x4*)(smem + 2 * CS + 16) = (u32x4){0u, 0u, 0u, hh[0]};
    *(u32x4*)(smem + 3 * CS + 16) = (u32x4){0u, 0u, hh[0] << 16, s1[0]};
  }
  *(u32x4*)(smem + KOFF + 16 * th) = s.kv0;
  *(u32x4*)(smem + KOFF + 2048 + 16 * th) = s.kv1;
  if (th < 32) *(u32x4*)(smem + KOFF + 4096 + 16 * th) = (u32x4){0u, 0u, 0u, 0u};
}
DEV u32x2 conv_out(const f32x4& y, const Chan& c, const char* smem, int t, const u32x2& xres) {
  const u32x2 hv = *(const u32x2*)(smem + 2 * (t + 16));
  u32x2 o;
#pragma unroll
  for (int e = 0; e < 2; ++e) {
    const unsigned hu = hv[e];
    const h16x2 h2 = __builtin_bit_cast(h16x2, hu);
    const float h0 = (float)h2[0] * c.hinv, h1 = (float)h2[1] * c.hinv;
    unsigned yb = pk_bf(y[2 * e] * c.yinv, y[2 * e + 1] * c.yinv);
    unsigned hd = pk_bf(h0 * c.dsk, h1 * c.dsk);
    unsigned y2 = pk_bf(bf_lo(yb) + bf_lo(hd), bf_hi(yb) + bf_hi(hd));
    unsigned gy = pk_bf(bf_lo(y2) * c.ga, bf_hi(y2) * c.ga);
    o[e] = pk_bf(bf_lo(xres[e]) + bf_lo(gy), bf_hi(xres[e]) + bf_hi(gy));
  }
  return o;
}
#define K2_LOAD(buf, cc, u)                                                                                     \
  asm volatile("ds_read_b64 %0, %3 offset:%5\n\tds_read_b64 %1, %3 offset:%6\n\tds_read_b128 %2, %4 offset:%7"   \
               : "=&v"(Al[buf][u]), "=&v"(Ah[buf][u]), "=&v"(Bf[buf][u])                                          \
               : "v"(ap0), "v"(bp0), "i"(64 * (cc) + 512 * (u)), "i"(64 * (cc) + 512 * (u) + 8),                 \
                 "i"(64 * (cc) + 3584 - 512 * (u)))
template <int SET> DEV constexpr int half_tile(int j) {
  return SET == 0 ? (j == 0 ? 7 : (j == 1 ? 6 : 2)) : (j < 3 ? 5 - j : 4 - j);
}
template <int SET>
DEV void conv_half(unsigned ap0, unsigned bp0, char* redw) {
  constexpr int NU = SET == 0 ? 8 : 6, NT = SET == 0 ? 3 : 5;
  u32x2 Al[2][NU], Ah[2][NU];
  u32x4 Bf[2][NU];
#pragma unroll
  for (int u = 0; u < NU; ++u) K2_LOAD(0, 0, u);
  f32x4 acc[NT];
#pragma unroll
  for (int j = 0; j < NT; ++j) acc[j] = (f32x4){0.f, 0.f, 0.f, 0.f};
#pragma unroll
  for (int cc = 0; cc < 2; ++cc) {
    lgkm0();
    if (cc == 0) {
#pragma unroll
      for (int u = 0; u < NU; ++u) K2_LOAD(1, 1, u);
    }
#pragma unroll
    for (int sp = 0; sp < NU; ++sp)
#pragma unroll
      for (int j = 0; j < NT; ++j) {
        constexpr int dummy = 0; (void)dummy;
        const int q = half_tile<SET>(j);
        if (sp <= q) {
          const u32x4 A = {Al[cc][sp][0], Al[cc][sp][1], Ah[cc][sp][0], Ah[cc][sp][1]};
          acc[j] = mfma_f16(A, Bf[cc][q - sp], acc[j]);
        }
      }
    SB();
  }
#pragma unroll
  for (int j = 0; j < NT; ++j) *(f32x4*)(redw + j * 4096) = acc[j];
}

template <int NF>
DEV void w1_fin(const RtRaw<NF>& t, const ModP& mp, char* ws, int kk, int ct0, int lane, char* scr) {
  const int g = lane >> 4, i = lane & 15;
  float* tb = (float*)(scr + 32 * RT_STR);
  const float mv = mod_fin(mp);
  tb[lane] = lane < 32 ? rbf(1.f + mv) : mv;
  rt_park<NF>(t, scr, lane);
  lgkm0();
  const f32x4 ca = *(const f32x4*)(tb + 8 * g), cb = *(const f32x4*)(tb + 8 * g + 4);
  const f32x4 sa = *(const f32x4*)(tb + 32 + 8 * g), sb = *(const f32x4*)(tb + 36 + 8 * g);
  const u32x4 sv = {pk_bf(sa[0], sa[1]), pk_bf(sa[2], sa[3]), pk_bf(sb[0], sb[1]), pk_bf(sb[2], sb[3])};
  const u32x4 ones = {0x3f803f80u, 0x3f803f80u, 0x3f803f80u, 0x3f803f80u};
  const f32x4 z = {0.f, 0.f, 0.f, 0.f};
  const rsrc_t rs = ws_rsrc(ws);
#pragma unroll
  for (int j = 0; j < NF; ++j) {
    const u32x4 f = rt_frag(scr, j, lane);
    const u32x4 sc = {pk_bf(bf_lo(f[0]) * ca[0], bf_hi(f[0]) * ca[1]), pk_bf(bf_lo(f[1]) * ca[2], bf_hi(f[1]) * ca[3]),
                      pk_bf(bf_lo(f[2]) * cb[0], bf_hi(f[2]) * cb[1]), pk_bf(bf_lo(f[3]) * cb[2], bf_hi(f[3]) * cb[3])};
    st16(rs, WS_W1F + ((size_t)(kk * 108 + ct0 + j) * 64 + lane) * 16, sc);
    const f32x4 cs = mfma_bf16(sc, ones, z), b1 = mfma_bf16(f, sv, z);
    if (i < 2) st16(rs, WS_CSP + ((size_t)(2 * kk + i) * FF + 16 * (ct0 + j) + 4 * g) * 4, __builtin_bit_cast(u32x4, i ? b1 : cs));
  }
}

DEV void conv_phase(const unsigned short* __restrict__ dskip, char* __restrict__ ws,
                    const unsigned short* __restrict__ w1, const unsigned short* __restrict__ w2, char* smem_all) {
  constexpr int CS = K2_CS, KOFF = K2_KOFF;
  const int tid = threadIdx.x, lane = tid & 63;
  const int w = __builtin_amdgcn_readfirstlane(tid >> 6);
  char* stamp_ws = ws; (void)stamp_ws;
  STAMP(1, 0);
  const int bx = blockIdx.x;
  const int pj = (bx & 7) * 16 + (bx >> 4), par = (bx >> 3) & 1;
  const int r = lane & 15, g = lane >> 4, th = tid & 127;
  const int s4 = (r + 1) & 3;
  const rsrc_t rs = ws_rsrc(ws);
  unsigned* fl = (unsigned*)(smem_all + CV_FL);
  if (tid < 5) fl[tid] = 0u;
  if (w < 4) {
    const int d = 5 * pj + 2 * par + (w >> 1), hw = w & 1;
    char* smem = smem_all + (w >> 1) * K2_CH;
    const StageRaw sr = stage_ld(ws, d, th);
    const ChanRaw cr = chan_ld(dskip, ws, d);
    u32x2 xres[4];
#pragma unroll
    for (int q = 0; q < 4; ++q)
      xres[q] = *(const u32x2*)(ws + WS_XT + tl_off(d, 256 * (4 * hw + q) + 16 * r + 4 * g));
    SB();
    const Chan c = chan_fin(cr);
    stage_wr(sr, c, smem, th);
    __syncthreads();
    STAMP(1, 2);
    f32x4 acc[8];
#pragma unroll
    for (int q = 0; q < 8; ++q) acc[q] = (f32x4){0.f, 0.f, 0.f, 0.f};
    {
      const unsigned ap0 = (unsigned)(size_t)(smem + s4 * CS + 2 * (128 * hw + 8 * g + (r + 1) - s4));
      const unsigned bp0 = (unsigned)(size_t)(smem + KOFF + 2 * (2032 + 128 * hw - 16 * r + 8 * g) - 3584);
      u32x2 Al[2][8], Ah[2][8];
      u32x4 Bf[2][8];
#pragma unroll
      for (int u = 0; u < 8; ++u) K2_LOAD(0, 0, u);
#pragma unroll
      for (int cc = 0; cc < 4; ++cc) {
        lgkm0();
#pragma unroll
        for (int q = 0; q < 8; ++q) {
          if (cc < 3) K2_LOAD((cc + 1) & 1, cc + 1, q);
#pragma unroll
          for (int sp = 0; sp <= q; ++sp) {
            const u32x4 A = {Al[cc & 1][sp][0], Al[cc & 1][sp][1], Ah[cc & 1][sp][0], Ah[cc & 1][sp][1]};
            acc[q] = mfma_f16(A, Bf[cc & 1][q - sp], acc[q]);
          }
          SB();
        }
      }
    }
    STAMP(1, 3);
    f32x4 keep[4];
    {
      f32x4* xw = (f32x4*)(smem_all + CV_XCH + w * 4096) + lane;
      if (hw) {
#pragma unroll
        for (int q = 0; q < 4; ++q) { xw[64 * q] = acc[q]; keep[q] = acc[4 + q]; }
      } else {
#pragma unroll
        for (int q = 0; q < 4; ++q) { xw[64 * q] = acc[4 + q]; keep[q] = acc[q]; }
      }
    }
    lds_post(fl + w, lane);
    lds_wait(fl + (w ^ 1), 1u);
    const f32x4* xrd = (const f32x4*)(smem_all + CV_XCH + (w ^ 1) * 4096) + lane;
#pragma unroll
    for (int qp = 0; qp < 2; ++qp) {
      u32x2 o[2];
#pragma unroll
      for (int h = 0; h < 2; ++h) {
        const int q = 2 * qp + h;
        o[h] = conv_out(keep[q] + xrd[64 * q], c, smem, 256 * (4 * hw + q) + 16 * r + 4 * g, xres[q]);
      }
      const auto s0 = __builtin_amdgcn_permlane16_swap(o[0][0], o[1][0], false, false);
      const auto s1 = __builtin_amdgcn_permlane16_swap(o[0][1], o[1][1], false, false);
      const int t = 256 * (4 * hw + 2 * qp + (g & 1)) + 16 * r + 8 * (g >> 1);
      st16(rs, WS_X1T + (((size_t)(t >> 5) * D_ + d) * 32 + (t & 31)) * 2, (u32x4){s0[0], s1[0], s0[1], s1[1]});
    }
  } else {
    __builtin_amdgcn_s_setprio(2);
    const int d = 5 * pj + 4, wq = w - 4;
    char* smem = smem_all + 2 * K2_CH;
    StageRaw sr;
    if (wq < 2) sr = stage_ld(ws, d, th);
    asm volatile("" ::: "memory");
    const ChanRaw cr = chan_ld(dskip, ws, d);
    const int tl0 = par ? (wq < 3 ? 5 - wq : 1) : (wq == 0 ? 7 : (wq == 1 ? 6 : 2)), tl1 = 0;
    const unsigned ap0 = (unsigned)(size_t)(smem + s4 * CS + 2 * (64 * wq + 8 * g + (r + 1) - s4));
    const unsigned bp0 = (unsigned)(size_t)(smem + KOFF + 2 * (2032 + 64 * wq - 16 * r + 8 * g) - 3584);
    char* redw = smem_all + CV_RED + wq * 1024 + lane * 16;
    u32x2 xres[2];
    xres[0] = *(const u32x2*)(ws + WS_XT + tl_off(d, 256 * tl0 + 16 * r + 4 * g));
    xres[1] = *(const u32x2*)(ws + WS_XT + tl_off(d, 256 * tl1 + 16 * r + 4 * g));
    SB();
    const Chan c = chan_fin(cr);
    if (wq < 2) {
      stage_wr(sr, c, smem, th);
    } else {
#define RT_DELAY 32
      __builtin_amdgcn_s_sleep(RT_DELAY);
    }
    const bool isw1 = wq >= 2;
    const int b2 = min(bx, 215), b1 = min(bx, 239);
    const int jk = isw1 ? b1 / 12 : b2 >> 2;
    const int jc = isw1 ? (b1 % 12) * 9 + (wq - 2) * 4 : (b2 & 3) * 10 + 5 * wq;
    RtRaw<5> rt;
    rt_load<5>(rt, isw1 ? w1 : w2, isw1 ? FF : D_, jk, jc, lane);
    const ModP mp = mod_ld(ws, (lane < 32 ? 4 * D_ : 3 * D_ - 32) + 32 * min(jk, 19) + lane);
    SB();
    bar_lds();
    if (par) conv_half<1>(ap0, bp0, redw); else conv_half<0>(ap0, bp0, redw);
    lds_post(fl + 4, lane);
    if (isw1) {
      if (bx < 240) w1_fin<5>(rt, mp, ws, jk, jc, lane, smem_all + CV_RT + (wq - 2) * (32 * RT_STR + 256));
    } else if (bx < 216) {
      char* scr = smem_all + CV_RT2 + wq * (32 * RT_STR);
      rt_park<5>(rt, scr, lane);
      lgkm0();
#pragma unroll
      for (int j = 0; j < 5; ++j) st16(rs, WS_W2F + ((size_t)((jc + j) * 54 + jk) * 64 + lane) * 16, rt_frag(scr, j, lane));
    }
    lds_wait(fl + 4, 4u);
    const char* rd = smem_all + CV_RED + lane * 16;
#pragma unroll
    for (int u = 0; u < 2; ++u) {
      const bool on = u == 0 ? (par || wq < 3) : (par && wq == 0);
      if (on) {
        const int sl = u == 0 ? wq : 4, tl = u == 0 ? tl0 : tl1;
        const f32x4 y = (*(const f32x4*)(rd + sl * 4096) + *(const f32x4*)(rd + sl * 4096 + 1024))
                      + (*(const f32x4*)(rd + sl * 4096 + 2048) + *(const f32x4*)(rd + sl * 4096 + 3072));
        const int t = 256 * tl + 16 * r + 4 * g;
        st8(rs, WS_X1T + (((size_t)(t >> 5) * D_ + d) * 32 + (t & 31)) * 2, conv_out(y, c, smem, t, xres[u]));
      }
    }
  }
  STAMP(1, 7);
}

__global__ NOPK __launch_bounds__(512, 2) void k2() {
  __shared__ __attribute__((aligned(16))) char smem[CV_RT + 2 * (32 * RT_STR + 256)];
  const Ptrs p = g_ptrs;
  conv_phase((const unsigned short*)(char*)p.dskip, (char*)p.ws, (const unsigned short*)(char*)p.w1, (const unsigned short*)(char*)p.w2, smem);
}

DEV u32x4 ld16_sc1(rsrc_t r, int voff, int soff) { return __builtin_amdgcn_raw_buffer_load_b128(r, voff, soff, 16); }
DEV u32x4 ld16(rsrc_t r, int voff, int soff) { return __builtin_amdgcn_raw_buffer_load_b128(r, voff, soff, 0); }
DEV u32x4 tr_frag(const char* p) {
  const u32x2 a = __builtin_bit_cast(u32x2, __builtin_amdgcn_ds_read_tr16_b64_v4i16((lds_s16x4*)p));
  const u32x2 b = __builtin_bit_cast(u32x2, __builtin_amdgcn_ds_read_tr16_b64_v4i16((lds_s16x4*)(p + 256)));
  return (u32x4){a[0], a[1], b[0], b[1]};
}

constexpr int KF_RS = 84;
constexpr int KF_RB = 8 * 16 * KF_RS * 4;
constexpr int KF_BIG = 2 * KF_RB;
#define NW2L_ 8
constexpr int NW2L = NW2L_;
constexpr int NW2R = 35 - NW2L;
constexpr int KF_W2L = KF_BIG;
constexpr int KF_TAB = KF_W2L + 8 * NW2L * 1024;
constexpr int KF_PS = KF_TAB + 320;
constexpr int KF_XR = KF_PS + 512;
constexpr int KF_CNT = KF_XR + 64 * 176;
constexpr int KF_LDS = KF_CNT + 16;
#define W2_START 10
#define W2_RATE 3
#define W2_INL 28
#define SIG_SPLIT 4
#define H1_B0 1
#define H1_BATCH 7
#define W2_MID 4
#define W1_PRE 10
#define W1_EARLY 3
#define STAMP_EXTRA 0
#define H1_MODE 0
#define H1_PRE 17

DEV void kf_body(char* __restrict__ ws, unsigned short* __restrict__ out, char* smem) {
  char* xs = smem;
  float* tab = (float*)(smem + KF_TAB);
  float* ps = (float*)(smem + KF_PS);
  char* xr = smem + KF_XR;
  unsigned* lcnt = (unsigned*)(smem + KF_CNT);
  float* cst = (float*)(smem + 81920);
  const int tid = threadIdx.x, lane = tid & 63;
  const int w = __builtin_amdgcn_readfirstlane(tid >> 6);
  const int bx = blockIdx.x;
#define KF_MAP 0
  const int ng = (bx >> 3) & 7, mt = (bx & 7) * 4 + (bx >> 6);
  const int g = lane >> 4, i = lane & 15;
  char* stamp_ws = ws; (void)stamp_ws;
  STAMP(2, 0); STAMP(5, 0);
  const rsrc_t rs_ = ws_rsrc(ws);

  unsigned* flags = (unsigned*)(ws + WS_CNT) + mt * 128;
  const int ntl = ng < 6 ? 14 : 12;
  const int ctb = ng < 6 ? 14 * ng : 84 + 12 * (ng - 6);
  const bool two = w < ntl - 8;
  const bool full = w < 6;
  const int ks0 = w < 6 ? 7 * w : 42 + 6 * (w - 6);
  const int ct0 = ctb + (two ? 2 * w : (ntl - 8) + w);
  const int lo16 = lane * 16;
  const int w1s = (int)WS_W1F + ct0 * 1024;
  const int w2s = (int)WS_W2F + (ng * 5 * 54 + ks0) * 1024;

  u32x4 ring[32];
  const char* bb0 = xs + (8 * g + (i >> 2)) * 64 + ((g & 1) << 5) + (i & 3) * 8;
  const char* bb1 = xs + (8 * g + (i >> 2)) * 64 + (((g & 1) ^ 1) << 5) + (i & 3) * 8;
  auto w2_off = [&](int e) __attribute__((always_inline)) {
    const int s = e < 27 ? e / 4 : (e < 34 ? e - 27 : 6), c = e < 27 ? e % 4 : (e < 34 ? 4 : 3);
    const int sn = (s == 6 && !full) ? 5 : s;
    return w2s + (c * 54 + sn) * 1024;
  };

  const unsigned w2lb = (unsigned)(size_t)(smem + KF_W2L) + w * NW2L * 1024;
  auto w2_dma = [&](int n) __attribute__((always_inline)) {
    asm volatile("s_mov_b32 m0, %0\n\ts_nop 0\n\tbuffer_load_dwordx4 %1, %2, %3 offen lds"
                 :: "s"(w2lb + n * 1024), "v"(lo16), "s"(rs_), "s"(w2_off(NW2R + n)) : "memory");
  };

  auto gemm1 = [&](auto two_tag) __attribute__((always_inline)) {
    constexpr bool TWO = decltype(two_tag)::value;
    if (TWO) asm volatile("; two tiles" ::: "memory"); else asm volatile("; one tile" ::: "memory");
    u32x4 t[10];
#pragma unroll
    for (int it = 0; it < 10; ++it) t[it] = ld16(rs_, tid * 16, (int)WS_X1T + mt * 81920 + 8192 * it);
#pragma unroll
    for (int k = 0; k < W1_EARLY; ++k) {
      ring[2 * k] = ld16(rs_, lo16, w1s + k * 108 * 1024);
      if (TWO) ring[2 * k + 1] = ld16(rs_, lo16, w1s + (k * 108 + 1) * 1024);
    }
    SB();
    __builtin_amdgcn_s_barrier();
    SB();
#pragma unroll
    for (int k = W1_EARLY; k < W1_PRE; ++k) {
      ring[2 * k] = ld16(rs_, lo16, w1s + k * 108 * 1024);
      if (TWO) ring[2 * k + 1] = ld16(rs_, lo16, w1s + (k * 108 + 1) * 1024);
    }
    SB();
#pragma unroll
    for (int it = 0; it < 10; ++it) {
      const int c = tid + 512 * it;
      const int row = c >> 2;
      *(u32x4*)(xs + row * 64 + (((c & 3) ^ ((row >> 2) & 2)) << 4)) = t[it];
    }
    SB();
    if (tid == 0) *lcnt = 0u;
    ModP gbp;
    if (!TWO) gbp = mod_ld(ws, 5 * D_ + 80 * ng + min((w & 1) * 64 + lane, 79));
    SB();
    __syncthreads();
    STAMP(2, 1);
    {
      const int kb = (80 * ng) >> 5, tq = w & 3;
#pragma unroll
      for (int u = 0; u < 2; ++u) {
        const int kk = min(kb + 2 * (w >> 2) + u, 19);
        const u32x4 f = tr_frag(((tq & 1) ? bb1 : bb0) + (tq >> 1) * 40960 + kk * 2048);
        const unsigned dl = (unsigned)(32 * kk + 8 * g - 80 * ng);
        if (dl < 80u) *(u32x4*)(xr + (tq * 16 + i) * 176 + dl * 2) = f;
      }
    }
    const int hs = TWO ? 0 : (w & 1);
    const char* ba[4] = {bb0 + hs * 40960, bb1 + hs * 40960, bb0 + (hs ^ 1) * 40960, bb1 + (hs ^ 1) * 40960};
    const u32x4 ones = {0x3f803f80u, 0x3f803f80u, 0x3f803f80u, 0x3f803f80u};
    f32x4 acc[2][4];
#pragma unroll
    for (int c = 0; c < 2; ++c)
#pragma unroll
      for (int q = 0; q < 4; ++q) acc[c][q] = (f32x4){0.f, 0.f, 0.f, 0.f};
    u32x4 csp[20];
    const int cso = ((w & 1) * FF + 16 * ctb + 4 * min(lane, 4 * ntl - 1)) * 4;
    u32x4 bn[2][4];
#pragma unroll
    for (int q = 0; q < 4; ++q) bn[0][q] = tr_frag(ba[q]);
#pragma unroll
    for (int q = 0; q < 4; ++q) bn[1][q] = tr_frag(ba[q] + 2048);
#pragma unroll
    for (int kk = 0; kk < 20; ++kk) {
      u32x4 b[4];
#pragma unroll
      for (int q = 0; q < 4; ++q) b[q] = bn[kk & 1][q];
      SB();
#pragma unroll
      for (int q = 0; q < 4; ++q) acc[0][q] = mfma_bf16(ring[(2 * kk) % 32], b[q], acc[0][q]);
      if (TWO) {
#pragma unroll
        for (int q = 0; q < 4; ++q) acc[1][q] = mfma_bf16(ring[(2 * kk + 1) % 32], b[q], acc[1][q]);
      } else {
        acc[1][0] = mfma_bf16(ones, b[0], acc[1][0]); acc[1][1] = mfma_bf16(b[0], b[0], acc[1][1]);
        acc[1][2] = mfma_bf16(ones, b[1], acc[1][2]); acc[1][3] = mfma_bf16(b[1], b[1], acc[1][3]);
      }
      SB();
      if (kk < 18) {
#pragma unroll
        for (int q = 0; q < 4; ++q) bn[kk & 1][q] = tr_frag(ba[q] + (kk + 2) * 2048);
      }
      if (kk + W1_PRE < 20) {
        const int kn = kk + W1_PRE;
        ring[(2 * kn) % 32] = ld16(rs_, lo16, w1s + kn * 108 * 1024);
        if (TWO) ring[(2 * kn + 1) % 32] = ld16(rs_, lo16, w1s + (kn * 108 + 1) * 1024);
      }
#define LOCK_MASK 0x55554
      if ((LOCK_MASK >> kk) & 1) { SB(); __builtin_amdgcn_s_barrier(); SB(); }
      if (kk >= W2_START) {
#pragma unroll
        for (int u = 0; u < W2_RATE; ++u) {
          const int n = W2_RATE * (kk - W2_START) + u;
          if (n < NW2L) w2_dma(n);
          else if (n < W2_INL) ring[8 + n - NW2L] = ld16(rs_, lo16, w2_off(n - NW2L));
        }
      }
      if (!TWO) {
        if (kk < 10) {
#pragma unroll
          for (int u = 0; u < 2; ++u)
            csp[2 * kk + u] = ld16(rs_, cso, (int)WS_CSP + (2 * kk + u) * 2 * FF * 4);
        }
        if (kk == 14) {
          f32x4 t = __builtin_bit_cast(f32x4, csp[0]);
#pragma unroll
          for (int u = 1; u < 20; ++u) t += __builtin_bit_cast(f32x4, csp[u]);
          if (w >= 6 && lane < 56) *(f32x4*)(cst + (w & 1) * 224 + 4 * lane) = t;
        }
      }
      if (!TWO && kk == 12) { if ((w & 1) * 64 + lane < 80) tab[(w & 1) * 64 + lane] = mod_fin(gbp); }
      SB();
    }
    if (!TWO) {
#pragma unroll
      for (int h = 0; h < 2; ++h) {
        const f32x4 aq = acc[1][2 * h + 1];
        const float dq = (i & 2) ? ((i & 1) ? aq[3] : aq[2]) : ((i & 1) ? aq[1] : aq[0]);
        const int tk = (2 * hs + h) * 16 + i;
        if (g == (i >> 2)) {
          const float mean = acc[1][2 * h][0] * (1.f / D_);
          const float var = fmaxf(dq * (1.f / D_) - mean * mean, 0.f);
          const float sd = rbf(sqrtf(rbf(rbf(var) + 1e-5f)));
          const float rs = 1.f / sd;
          ps[tk] = rs;
          ps[64 + tk] = -rbf(mean) * rs;
        }
      }
    }
    __syncthreads();
    STAMP(2, 2);
    f32x4 csv[2], b1v[2];
#pragma unroll
    for (int c = 0; c < (TWO ? 2 : 1); ++c) {
      csv[c] = *(const f32x4*)(cst + 16 * (ct0 - ctb + c) + 4 * g);
      b1v[c] = *(const f32x4*)(cst + 224 + 16 * (ct0 - ctb + c) + 4 * g);
    }
#pragma unroll
    for (int qp = 0; qp < 2; ++qp) {
      u32x2 o[2][2];
#pragma unroll
      for (int h = 0; h < 2; ++h) {
        const int q = 2 * qp + h, tq = q ^ (2 * hs);
        const float rs = ps[tq * 16 + i], nm = ps[64 + tq * 16 + i];
#pragma unroll
        for (int c = 0; c < (TWO ? 2 : 1); ++c) {
          float v[4];
#pragma unroll
          for (int r = 0; r < 4; ++r) v[r] = fmaxf(fmaf(acc[c][q][r], rs, fmaf(nm, csv[c][r], b1v[c][r])), 0.f);
          o[c][h] = (u32x2){pk_bf(v[0], v[1]), pk_bf(v[2], v[3])};
        }
      }
      const int tl = ((2 * qp) ^ (2 * hs)) + (g & 1);
#pragma unroll
      for (int c = 0; c < (TWO ? 2 : 1); ++c) {
        const int ct = ct0 + c;
        const auto s0 = __builtin_amdgcn_permlane16_swap(o[c][0][0], o[c][1][0], false, false);
        const auto s1 = __builtin_amdgcn_permlane16_swap(o[c][0][1], o[c][1][1], false, false);
        const size_t off = (((size_t)(4 * mt + tl) * 54 + (ct >> 1)) * 64 + i + 16 * (2 * (ct & 1) + (g >> 1))) * 16;
#define H1_ST 16
        __builtin_amdgcn_raw_buffer_store_b128((u32x4){s0[0], s1[0], s0[1], s1[1]}, rs_, (int)(WS_H1F + off), 0, H1_ST);
      }
    }
    SB();
    STAMP(5, 1);
    __builtin_amdgcn_s_barrier();
    SB();
    constexpr int E0 = W2_INL - NW2L;
    auto signal = [&]() __attribute__((always_inline)) {
      if (lane == 0) {
        if (__hip_atomic_fetch_add(lcnt, 1u, __ATOMIC_RELAXED, __HIP_MEMORY_SCOPE_WORKGROUP) == 7u)
          __hip_atomic_store(flags + 16 * ng, 1u, __ATOMIC_RELAXED, __HIP_MEMORY_SCOPE_AGENT);
      }
    };
    if (w >= SIG_SPLIT) { asm volatile("s_waitcnt vmcnt(0)" ::: "memory"); signal(); }
    SB();
#pragma unroll
    for (int e = E0; e < NW2R; ++e) ring[(8 + e) % 32] = ld16(rs_, lo16, w2_off(e));
    SB();
    STAMP(5, 2);
    if (w < SIG_SPLIT) { asm volatile("s_waitcnt vmcnt(%0)" :: "n"(NW2R - E0) : "memory"); signal(); }
    SB();
  };
  if (two) gemm1(std::true_type{});
  else gemm1(std::false_type{});
  STAMP(2, 3);
#pragma nounroll
  for (unsigned spin = 0; spin < (1u << 20); ++spin) {
    if (__hip_atomic_load(flags + 16 * w, __ATOMIC_RELAXED, __HIP_MEMORY_SCOPE_AGENT) != 0u) break;
    __builtin_amdgcn_s_sleep(1);
  }
  asm volatile("" ::: "memory");
  SB();
  STAMP(2, 4);

  const int h1s = (int)WS_H1F + (mt * 4 * 54 + ks0) * 1024;
  u32x4 hb[28];
  auto load_h1 = [&](int n) __attribute__((always_inline)) {
    const int q = n / 7, s = n % 7;
    const int sn = (s == 6 && !full) ? 5 : s;
    hb[n] = ld16_sc1(rs_, lo16, h1s + (q * 54 + sn) * 1024);
  };
#pragma unroll
  for (int n = 0; n < H1_PRE; ++n) {
    if (H1_BATCH && n % H1_BATCH == 0 && n >= H1_B0) { SB(); __builtin_amdgcn_s_barrier(); SB(); }
    load_h1(n);
  }
  SB();
  float* red = (float*)smem;
  float* rb_ = red + (w * 16 + i) * KF_RS + 4 * g;
  const int re = w * 40 + min(lane, 39);
  const int rtk = re / 20, rq4 = re % 20;
  const float* rr_ = red + rtk * KF_RS + 4 * rq4;
  f32x4 ac2[2][5];
  f32x4 rv[4], rsum;
  const char* w2l = smem + KF_W2L + w * NW2L * 1024 + lo16;
  u32x4 wl[2], wl3;
  wl[0] = *(const u32x4*)w2l;
  auto red_wr = [&](int q, int c) __attribute__((always_inline)) {
    *(f32x4*)(rb_ + (q & 1) * (KF_RB / 4) + 16 * c) = ac2[q & 1][c];
  };
  auto red_rd = [&](int q, int h) __attribute__((always_inline)) {
#pragma unroll
    for (int ww = 0; ww < 4; ++ww) rv[ww] = *(const f32x4*)(rr_ + (q & 1) * (KF_RB / 4) + (4 * h + ww) * 16 * KF_RS);
  };
  auto red_out = [&](int q) __attribute__((always_inline)) {
    const f32x4 s = rsum + ((rv[0] + rv[1]) + (rv[2] + rv[3]));
    const u32x2 xv2 = *(const u32x2*)(xr + (16 * q + rtk) * 176 + rq4 * 8);
    const f32x4 gb = *(const f32x4*)(tab + 4 * rq4);
    u32x2 o;
#pragma unroll
    for (int h = 0; h < 2; ++h) {
      unsigned f = pk_bf(s[2 * h], s[2 * h + 1]);
      unsigned gf = pk_bf(bf_lo(f) * gb[2 * h], bf_hi(f) * gb[2 * h + 1]);
      o[h] = pk_bf(bf_lo(xv2[h]) + bf_lo(gf), bf_hi(xv2[h]) + bf_hi(gf));
    }
    if (lane < 40) *(u32x2*)(out + (size_t)(mt * 64 + 16 * q + rtk) * D_ + ng * 80 + 4 * rq4) = o;
  };
#pragma unroll
  for (int q = 0; q < 4; ++q) {
#pragma unroll
    for (int c = 0; c < 5; ++c) ac2[q & 1][c] = (f32x4){0.f, 0.f, 0.f, 0.f};
#pragma unroll
    for (int s = 0; s < 7; ++s) {
      if (q >= 1 && s >= 1 && s <= 5) red_wr(q - 1, s - 1);
      if (q >= 2) {
        if (s == 0) red_rd(q - 2, 0);
        if (s == 2) { rsum = (rv[0] + rv[1]) + (rv[2] + rv[3]); red_rd(q - 2, 1); }
        if (s == 4) red_out(q - 2);
      }
      SB();
      if (q < 3 || s < 6) wl[(7 * q + s + 1) & 1] = *(const u32x4*)(w2l + ((s + 1) % 7) * 1024);
      if (s == 5) wl3 = *(const u32x4*)(w2l + 7 * 1024);
      SB();
      if (s < 6 || full) {
#pragma unroll
        for (int c = 0; c < 5; ++c) {
          const u32x4 a = c == 4 ? wl[(7 * q + s) & 1] : ((s == 6 && c == 3) ? wl3 : ring[(8 + 4 * s + c) % 32]);
          ac2[q & 1][c] = mfma_bf16(a, hb[7 * q + s], ac2[q & 1][c]);
        }
      }
      SB();
      if (7 * q + s + H1_PRE < 28) { load_h1(7 * q + s + H1_PRE); SB(); }
    }
    if (q == 0) STAMP(2, 5);
    if (q >= 1) bar_lds();
  }
  STAMP(2, 6);
  red_rd(2, 0);
#pragma unroll
  for (int c = 0; c < 5; ++c) red_wr(3, c);
  rsum = (rv[0] + rv[1]) + (rv[2] + rv[3]);
  red_rd(2, 1);
  red_out(2);
  bar_lds();
  red_rd(3, 0);
  rsum = (rv[0] + rv[1]) + (rv[2] + rv[3]);
  red_rd(3, 1);
  red_out(3);
  STAMP(2, 7); STAMP(5, 7);
}

__global__ NOPK __launch_bounds__(512, 2) void kf() {
  __shared__ __attribute__((aligned(16))) char smem[KF_LDS];
  const Ptrs p = g_ptrs;
  kf_body((char*)p.ws, (unsigned short*)(char*)p.out, smem);
}

__global__ void kend_() {}

extern "C" void kernel_launch(void* const* d_in, const int* in_sizes, int n_in,
                              void* d_out, int out_size, void* d_ws, size_t ws_size,
                              hipStream_t stream) {
  K1Args a;
  a.x = (const unsigned short*)d_in[0];
  a.are = (const unsigned short*)d_in[1];
  a.aim = (const unsigned short*)d_in[2];
  a.bre = (const unsigned short*)d_in[3];
  a.bim = (const unsigned short*)d_in[4];
  a.cre = (const unsigned short*)d_in[5];
  a.cim = (const unsigned short*)d_in[6];
  a.logdt = (const unsigned short*)d_in[7];
  a.w1 = (const unsigned short*)d_in[10];
  a.w2 = (const unsigned short*)d_in[11];
  a.c = (const unsigned short*)d_in[12];
  a.wmod = (const unsigned short*)d_in[13];
  a.ws = (char*)d_ws;
  a.dskip = (const unsigned short*)d_in[8];
  a.out = (const unsigned short*)d_out;
  k1<<<NB_PF + NB_MOD + NB_LN + NB_K, 256, 0, stream>>>(a);
  k2<<<256, 512, 0, stream>>>();
  kf<<<256, 512, 0, stream>>>();
}
```

```cpp
#define PF 1
#include <hip/hip_runtime.h>
#include <hip/hip_bf16.h>
#include <stdint.h>
#include <type_traits>

typedef __attribute__((ext_vector_type(8))) __bf16 bf16x8;
typedef __attribute__((ext_vector_type(2))) __bf16 bf16x2;
typedef __attribute__((ext_vector_type(4))) float f32x4;
typedef __attribute__((ext_vector_type(2))) float f32x2;
typedef __attribute__((ext_vector_type(4))) unsigned u32x4;
typedef __attribute__((ext_vector_type(2))) unsigned u32x2;
typedef __attribute__((ext_vector_type(8))) _Float16 h16x8;
typedef __attribute__((ext_vector_type(2))) _Float16 h16x2;
typedef __attribute__((ext_vector_type(4))) short s16x4;
typedef __attribute__((address_space(3))) s16x4 lds_s16x4;

#if defined(__HIP_DEVICE_COMPILE__)
#define NOPK __attribute__((target("no-packed-fp32-ops")))
#else
#define NOPK
#endif
#define DEV __device__ __forceinline__

constexpr int L_ = 2048;
constexpr int D_ = 640;
constexpr int NS = 128;
constexpr int FF = 1728;
constexpr int KREV = 2304;

constexpr size_t WS_KSC  = 8192;
constexpr size_t WS_CNT  = 16384;
constexpr size_t WS_CSB  = 32768;
constexpr size_t WS_MOD  = 65536;
constexpr size_t WS_XNT  = 196608;
constexpr size_t WS_XT   = WS_XNT + (size_t)D_ * L_ * 2;
constexpr size_t WS_KREV = WS_XT + (size_t)D_ * L_ * 2;
constexpr size_t WS_W1F  = WS_KREV + (size_t)D_ * 2 * KREV * 2;
constexpr size_t WS_W2F  = WS_W1F + (size_t)D_ * FF * 2;
constexpr size_t WS_X1T  = WS_W2F + (size_t)D_ * FF * 2;
constexpr size_t WS_X1   = WS_X1T + (size_t)D_ * L_ * 2;
constexpr size_t WS_CSP  = WS_X1;
constexpr size_t WS_H1F  = WS_X1 + (size_t)D_ * L_ * 2;

#define STAMP(kid, k) do {} while (0)

DEV float bf_lo(unsigned u) { return __uint_as_float(u << 16); }
DEV float bf_hi(unsigned u) { return __uint_as_float(u & 0xffff0000u); }
DEV unsigned pk_bf(float a, float b) {
  f32x2 v = {a, b};
  bf16x2 r = __builtin_convertvector(v, bf16x2);
  return __builtin_bit_cast(unsigned, r);
}
DEV float rbf(float a) { return bf_lo(pk_bf(a, 0.f)); }
DEV float ldbf(const unsigned short* p) { return __uint_as_float(((unsigned)*p) << 16); }

typedef __amdgpu_buffer_rsrc_t rsrc_t;
DEV rsrc_t ws_rsrc(char* ws) { return __builtin_amdgcn_make_buffer_rsrc((void*)ws, 0, 0x7fffffff, 0x00020000); }
#define ST_AUX 16
DEV void st16(rsrc_t r, size_t off, u32x4 v) { __builtin_amdgcn_raw_buffer_store_b128(v, r, (int)off, 0, ST_AUX); }
#define LN_AUX 16
DEV void st16ln(rsrc_t r, size_t off, u32x4 v) { __builtin_amdgcn_raw_buffer_store_b128(v, r, (int)off, 0, LN_AUX); }
DEV void st8(rsrc_t r, size_t off, u32x2 v) { __builtin_amdgcn_raw_buffer_store_b64(v, r, (int)off, 0, 16); }

DEV u32x2 tr_read(unsigned lds_addr) {
  u32x2 r;
  asm volatile("ds_read_b64_tr_b16 %0, %1" : "=v"(r) : "v"(lds_addr) : "memory");
  return r;
}
DEV void bar_lds() { asm volatile("s_waitcnt lgkmcnt(0)\n\ts_barrier" ::: "memory"); }
DEV void lds_wait(const void* word, unsigned val) {
  unsigned tv, ts, cnt;
  asm volatile("s_mov_b32 %2, 0x100000\n"
               "1:\n\tds_read_b32 %0, %3\n\ts_waitcnt lgkmcnt(0)\n\tv_readfirstlane_b32 %1, %0\n\t"
               "s_cmp_ge_u32 %1, %4\n\ts_cbranch_scc1 2f\n\ts_sleep 1\n\ts_sub_u32 %2, %2, 1\n\t"
               "s_cmp_lg_u32 %2, 0\n\ts_cbranch_scc1 1b\n2:"
               : "=&v"(tv), "=&s"(ts), "=&s"(cnt) : "v"((unsigned)(size_t)word), "s"(val) : "memory", "scc");
}
DEV void lds_post(unsigned* word, int lane) {
  asm volatile("" ::: "memory");
  if (lane == 0) __hip_atomic_fetch_add(word, 1u, __ATOMIC_RELAXED, __HIP_MEMORY_SCOPE_WORKGROUP);
  asm volatile("" ::: "memory");
}
DEV void lgkm0() { asm volatile("s_waitcnt lgkmcnt(0)" ::: "memory"); __builtin_amdgcn_sched_barrier(0); }

DEV f32x4 mfma_bf16(u32x4 a, u32x4 b, f32x4 c) {
  return __builtin_amdgcn_mfma_f32_16x16x32_bf16(__builtin_bit_cast(bf16x8, a), __builtin_bit_cast(bf16x8, b), c, 0, 0, 0);
}

constexpr int NB_MOD = 120, NB_LN = 256, NB_W2 = 108, NB_K = 640;

struct K1Args {
  const unsigned short *x, *are, *aim, *bre, *bim, *cre, *cim, *logdt, *w1, *w2, *c, *wmod;
  const unsigned short *dskip, *out;
  char* ws;
};

constexpr int NB_PF = 8;
DEV void k1_prefetch(const K1Args& p, int b) {
  const int tid = threadIdx.x;
  unsigned long long pc;
  int o0, o1;
  asm volatile("s_getpc_b64 %0\n.Lpf_here%=:\n\ts_mov_b32 %1, _Z2k16K1Args - .Lpf_here%=\n\ts_mov_b32 %2, _Z5kend_v - .Lpf_here%="
               : "=s"(pc), "=s"(o0), "=s"(o1));
  const char* c0 = (const char*)(pc + (long long)o0);
  long n = o1 - o0;
  n = n < 0 ? 0 : (n > 98304 ? 98304 : n);
  unsigned acc = 0;
  {
    unsigned v[3];
#pragma unroll
    for (int it = 0; it < 3; ++it) {
      const long o = (long)tid * 128 + it * 32768;
      v[it] = o < n ? *(const __attribute__((address_space(1))) unsigned*)(c0 + o) : 0u;
    }
    acc = v[0] ^ v[1] ^ v[2];
  }
  asm volatile("" :: "v"(acc));
}

DEV void k1_mod(const K1Args& p, int b, char* smem) {
  const int tid = threadIdx.x;
  const int rg = b & 7, cgp = b >> 3;
  const int c32 = tid & 31, r8 = tid >> 5;
  float* sil = (float*)smem;
  float* red = (float*)(smem + 512);
  u32x4 w[10];
#pragma unroll
  for (int it = 0; it < 10; ++it)
    w[it] = *(const u32x4*)(p.wmod + (size_t)(rg * 80 + r8 + 8 * it) * 3840 + 256 * cgp + 8 * c32);
  if (tid < 80) {
    float cv = ldbf(p.c + rg * 80 + tid);
    sil[tid] = rbf(cv / (1.f + expf(-cv)));
  }
  if (tid >= 128 && tid < 132 && b < 64) ((unsigned*)(p.ws + WS_CNT))[(b * 4 + tid - 128) * 16] = 0u;
  __syncthreads();
  float acc[8];
#pragma unroll
  for (int j = 0; j < 8; ++j) acc[j] = 0.f;
#pragma unroll
  for (int it = 0; it < 10; ++it) {
    float s = sil[r8 + 8 * it];
#pragma unroll
    for (int q = 0; q < 4; ++q) {
      acc[2 * q] += s * bf_lo(w[it][q]);
      acc[2 * q + 1] += s * bf_hi(w[it][q]);
    }
  }
#pragma unroll
  for (int j = 0; j < 8; ++j) acc[j] += __shfl_xor(acc[j], 32, 64);
  if ((tid & 63) < 32) {
    *(f32x4*)(red + (tid >> 6) * 256 + 8 * c32) = (f32x4){acc[0], acc[1], acc[2], acc[3]};
    *(f32x4*)(red + (tid >> 6) * 256 + 8 * c32 + 4) = (f32x4){acc[4], acc[5], acc[6], acc[7]};
  }
  __syncthreads();
  ((float*)(p.ws + WS_MOD))[(256 * cgp + tid) * 8 + rg] = (red[tid] + red[256 + tid]) + (red[512 + tid] + red[768 + tid]);
}
struct ModP { f32x4 a, b; };
DEV ModP mod_ld(const char* ws, int idx) {
  const f32x4* mp = (const f32x4*)(ws + WS_MOD) + 2 * idx;
  ModP m; m.a = mp[0]; m.b = mp[1];
  return m;
}
DEV float mod_fin(const ModP& m) {
  return rbf(((m.a[0] + m.a[1]) + (m.a[2] + m.a[3])) + ((m.b[0] + m.b[1]) + (m.b[2] + m.b[3])));
}
DEV float mod_sum(const char* ws, int idx) { return mod_fin(mod_ld(ws, idx)); }
#define SB() __builtin_amdgcn_sched_barrier(0)

DEV void k1_ln(const K1Args& p, int b, char* smem) {
  constexpr int STR = 704;
  const int tid = threadIdx.x;
  const int tok = tid >> 3, sub = tid & 7;
  const int tile = (b & 7) * 8 + ((b >> 3) & 7), pass = b >> 6;
  const int hf = pass & 1;
  const bool norm = pass < 2;
  const int t0 = tile * 32;
  const unsigned short* xr = p.x + (size_t)(t0 + tok) * D_ + 8 * sub;
  u32x4 xv[5], xo[5];
#pragma unroll
  for (int m = 0; m < 5; ++m) xv[m] = *(const u32x4*)(xr + 320 * hf + 64 * m);
  if (norm) {
#pragma unroll
    for (int m = 0; m < 5; ++m) xo[m] = *(const u32x4*)(xr + 320 * (hf ^ 1) + 64 * m);
    float s = 0.f, v = 0.f;
    const bf16x2 one2 = __builtin_bit_cast(bf16x2, 0x3f803f80u);
#pragma unroll
    for (int m = 0; m < 10; ++m)
#pragma unroll
      for (int q = 0; q < 4; ++q) {
        const unsigned u = m < 5 ? xv[m][q] : xo[m - 5][q];
        const bf16x2 x2 = __builtin_bit_cast(bf16x2, u);
        s = __builtin_amdgcn_fdot2_f32_bf16(x2, one2, s, false);
        v = __builtin_amdgcn_fdot2_f32_bf16(x2, x2, v, false);
      }
#pragma unroll
    for (int o = 1; o < 8; o <<= 1) { s += __shfl_xor(s, o, 64); v += __shfl_xor(v, o, 64); }
    const float mean = s * (1.f / D_);
    const float var = fmaxf(v * (1.f / D_) - mean * mean, 0.f);
    const float sd = rbf(sqrtf(rbf(rbf(var) + 1e-5f)));
    const float rs = 1.f / sd;
    const float mb = rbf(mean);
#pragma unroll
    for (int m = 0; m < 5; ++m)
#pragma unroll
      for (int q = 0; q < 4; ++q) {
        unsigned d2 = pk_bf(bf_lo(xv[m][q]) - mb, bf_hi(xv[m][q]) - mb);
        xv[m][q] = pk_bf(bf_lo(d2) * rs, bf_hi(d2) * rs);
      }
  }
  const int lane = tid & 63, w = tid >> 6;
  const int g = lane >> 4, i = lane & 15;
  const rsrc_t rsrc = ws_rsrc(p.ws);
#pragma unroll
  for (int m = 0; m < 5; ++m) *(u32x4*)(smem + tok * STR + (sub + 8 * m) * 16) = xv[m];
  __syncthreads();
  const size_t dsto = (norm ? WS_XNT : WS_XT) + ((size_t)tile * D_ + 320 * hf) * 64;
#pragma unroll
  for (int it = 0; it < 5; ++it) {
    const int cb = w * 5 + it;
    const char* a = smem + (8 * g + (i >> 2)) * STR + cb * 32 + (i & 3) * 8;
    const u32x2 ra = __builtin_bit_cast(u32x2, __builtin_amdgcn_ds_read_tr16_b64_v4i16((lds_s16x4*)a));
    const u32x2 rb = __builtin_bit_cast(u32x2, __builtin_amdgcn_ds_read_tr16_b64_v4i16((lds_s16x4*)(a + 4 * STR)));
    st16ln(rsrc, dsto + (size_t)(cb * 16 + i) * 64 + 16 * g, (u32x4){ra[0], ra[1], rb[0], rb[1]});
  }
}

constexpr int RT_STR = 176;
template <int NF> struct RtRaw { u32x4 r[NF]; };
template <int NF>
DEV void rt_load(RtRaw<NF>& t, const unsigned short* W, int N, int kk, int ct0, int lane) {
#pragma unroll
  for (int j = 0; j < NF; ++j) {
    const int c = lane + 64 * j, row = c / (2 * NF), part = c % (2 * NF);
    t.r[j] = *(const u32x4*)(W + (size_t)(32 * kk + row) * N + 16 * ct0 + 8 * part);
  }
}
template <int NF>
DEV void rt_park(const RtRaw<NF>& t, char* scr, int lane) {
#pragma unroll
  for (int j = 0; j < NF; ++j) {
    const int c = lane + 64 * j, row = c / (2 * NF), part = c % (2 * NF);
    *(u32x4*)(scr + row * RT_STR + 16 * part) = t.r[j];
  }
}
DEV u32x4 rt_frag(const char* scr, int j, int lane) {
  const int g = lane >> 4, i = lane & 15;
  const char* p = scr + (8 * g + (i >> 2)) * RT_STR + 32 * j + (i & 3) * 8;
  const u32x2 a = __builtin_bit_cast(u32x2, __builtin_amdgcn_ds_read_tr16_b64_v4i16((lds_s16x4*)p));
  const u32x2 b = __builtin_bit_cast(u32x2, __builtin_amdgcn_ds_read_tr16_b64_v4i16((lds_s16x4*)(p + 4 * RT_STR)));
  return (u32x4){a[0], a[1], b[0], b[1]};
}
struct cf { float re, im; };
DEV cf cmul(cf a, cf b) {
  cf r;
  r.re = a.re * b.re - a.im * b.im;
  r.im = a.re * b.im + a.im * b.re;
  return r;
}
DEV cf cexp_(float a2, float br, float m, float m0) {
  float mag = __builtin_amdgcn_exp2f(a2 * (m - m0));
  float ph = __builtin_amdgcn_fractf(br * m);
  cf r; r.re = mag * __builtin_amdgcn_cosf(ph); r.im = mag * __builtin_amdgcn_sinf(ph);
  return r;
}
DEV unsigned pk_h(float a, float b) {
  f32x2 v = {a, b};
  h16x2 r = __builtin_convertvector(v, h16x2);
  return __builtin_bit_cast(unsigned, r);
}
DEV f32x4 mfma_f16(u32x4 a, u32x4 b, f32x4 c) {
  return __builtin_amdgcn_mfma_f32_16x16x32_f16(__builtin_bit_cast(h16x8, a), __builtin_bit_cast(h16x8, b), c, 0, 0, 0);
}
DEV void put2(char* p, cf a, cf b) {
  u32x2 o = {pk_h(a.re, a.im), pk_h(b.re, b.im)};
  *(u32x2*)p = o;
}

constexpr int KG_RS = 544;
DEV void k1_kgen(const K1Args& p, int d, char* smem) {
  constexpr int RS = KG_RS;
  const int tid = threadIdx.x;
  const int lane = tid & 63;
  const int w = __builtin_amdgcn_readfirstlane(tid >> 6);
  const int g = lane >> 4, i = lane & 15;
  char* stamp_ws = p.ws; (void)stamp_ws;
  const size_t po = (size_t)d * NS + 2 * lane;
  const unsigned rar = *(const unsigned*)(p.are + po), rai = *(const unsigned*)(p.aim + po);
  const unsigned rbr = *(const unsigned*)(p.bre + po), rbi = *(const unsigned*)(p.bim + po);
  const unsigned rcr = *(const unsigned*)(p.cre + po), rci = *(const unsigned*)(p.cim + po);
  const float ldt = ldbf(p.logdt + d);
  asm volatile("" ::: "memory");
  SB();
  const float dt = rbf(expf(ldt));
  STAMP(0, 1);
  float a2[2], bv[2], m0[2];
  a2[0] = (dt * bf_lo(rar)) * 1.44269504088896341f; a2[1] = (dt * bf_hi(rar)) * 1.44269504088896341f;
  bv[0] = (dt * bf_lo(rai)) * 0.15915494309189534f; bv[1] = (dt * bf_hi(rai)) * 0.15915494309189534f;
  m0[0] = a2[0] > 0.f ? 2048.f : 0.f; m0[1] = a2[1] > 0.f ? 2048.f : 0.f;
  char* my = smem + lane * 8;
  if (w == 0) {
    cf v[2], v8[2], e8[2];
    float bound = 0.f;
#pragma unroll
    for (int q = 0; q < 2; ++q) {
      const float dbr = dt * (q ? bf_hi(rbr) : bf_lo(rbr)), dbi = dt * (q ? bf_hi(rbi) : bf_lo(rbi));
      const float cr = q ? bf_hi(rcr) : bf_lo(rcr), ci = q ? bf_hi(rci) : bf_lo(rci);
      const float gsh = __builtin_amdgcn_exp2f(a2[q] * m0[q]);
      v[q].re = (cr * dbr - ci * dbi) * gsh; v[q].im = -(cr * dbi + ci * dbr) * gsh;
      bound += (fabsf(v[q].re) + fabsf(v[q].im)) * __builtin_amdgcn_exp2f(fmaxf(a2[q], 0.f) * 120.f);
      e8[q] = cexp_(a2[q], -bv[q], 8.f, 0.f);
      v8[q] = cexp_(a2[q], -bv[q], 64.f, 0.f);
    }
#pragma unroll
    for (int o = 1; o < 64; o <<= 1) bound += __shfl_xor(bound, o, 64);
    int ex = (int)((__float_as_uint(bound) >> 23) & 0xff);
    ex = ex < 20 ? 20 : (ex > 240 ? 240 : ex);
    const float sv = __uint_as_float((unsigned)(127 + 13 + 127 - ex) << 23);
    if (lane == 0) ((float*)(p.ws + WS_KSC))[d] = __uint_as_float((unsigned)(ex - 13) << 23);
#pragma unroll
    for (int q = 0; q < 2; ++q) { v[q].re *= sv; v[q].im *= sv; v8[q] = cmul(v[q], v8[q]); }
#pragma unroll
    for (int j = 0; j < 8; ++j) {
      put2(my + (41 + j) * RS, v[0], v[1]);
      put2(my + (49 + j) * RS, v8[0], v8[1]);
      if (j < 7) {
#pragma unroll
        for (int q = 0; q < 2; ++q) { v[q] = cmul(v[q], e8[q]); v8[q] = cmul(v8[q], e8[q]); }
      }
    }
  } else if (w == 1) {
    cf e1[2], u[2], u2[2];
#pragma unroll
    for (int q = 0; q < 2; ++q) {
      e1[q] = cexp_(a2[q], bv[q], 1.f, 0.f);
      u[q].re = __builtin_amdgcn_exp2f(-a2[q] * m0[q]); u[q].im = 0.f;
      u2[q] = cexp_(a2[q], bv[q], 128.f, m0[q]);
    }
#pragma unroll
    for (int r = 0; r < 8; ++r) {
      put2(my + r * RS, u[0], u[1]);
      put2(my + (8 + r) * RS, u2[0], u2[1]);
      if (r < 7) {
#pragma unroll
        for (int q = 0; q < 2; ++q) { u[q] = cmul(u[q], e1[q]); u2[q] = cmul(u2[q], e1[q]); }
      }
    }
  } else if (w == 2) {
    cf e2[2], u[2], u2[2];
#pragma unroll
    for (int q = 0; q < 2; ++q) {
      e2[q] = cexp_(a2[q], bv[q], 2.f, 0.f);
      u[q] = cexp_(a2[q], bv[q], 256.f, m0[q]);
      u2[q] = cexp_(a2[q], bv[q], 384.f, m0[q]);
    }
#pragma unroll
    for (int r = 0; r < 4; ++r) {
      put2(my + (16 + r) * RS, u[0], u[1]);
      put2(my + (20 + r) * RS, u2[0], u2[1]);
      if (r < 3) {
#pragma unroll
        for (int q = 0; q < 2; ++q) { u[q] = cmul(u[q], e2[q]); u2[q] = cmul(u2[q], e2[q]); }
      }
    }
    cf e4[2] = {cmul(e2[0], e2[0]), cmul(e2[1], e2[1])};
#pragma unroll
    for (int r = 0; r < 2; ++r) {
      cf c0 = cexp_(a2[0], bv[0], 512.f + 128.f * r, m0[0]), c1 = cexp_(a2[1], bv[1], 512.f + 128.f * r, m0[1]);
      put2(my + (24 + 2 * r) * RS, c0, c1);
      put2(my + (25 + 2 * r) * RS, cmul(c0, e4[0]), cmul(c1, e4[1]));
    }
  } else {
    cf e4[2], e128[2], u[2], u2[2];
#pragma unroll
    for (int q = 0; q < 2; ++q) {
      e4[q] = cexp_(a2[q], bv[q], 4.f, 0.f);
      e128[q] = cexp_(a2[q], bv[q], 128.f, 0.f);
      u[q] = cexp_(a2[q], bv[q], 1024.f, m0[q]);
      u2[q] = cexp_(a2[q], bv[q], 1536.f, m0[q]);
    }
#pragma unroll
    for (int r = 2; r < 4; ++r) {
      cf c0 = cexp_(a2[0], bv[0], 512.f + 128.f * r, m0[0]), c1 = cexp_(a2[1], bv[1], 512.f + 128.f * r, m0[1]);
      put2(my + (24 + 2 * r) * RS, c0, c1);
      put2(my + (25 + 2 * r) * RS, cmul(c0, e4[0]), cmul(c1, e4[1]));
    }
#pragma unroll
    for (int r = 0; r < 4; ++r) {
      put2(my + (32 + r) * RS, u[0], u[1]);
      put2(my + (36 + r) * RS, u2[0], u2[1]);
#pragma unroll
      for (int q = 0; q < 2; ++q) { if (r < 3) u[q] = cmul(u[q], e128[q]); u2[q] = cmul(u2[q], e128[q]); }
    }
    put2(my + 40 * RS, u2[0], u2[1]);
  }
  __syncthreads();
  STAMP(0, 2);
  _Float16* kh = (_Float16*)(smem + 57 * RS);
  if (w < 3) {
    const char* ua = smem + (16 * w + i) * RS + g * 16;
    const char* va = smem + (41 + i) * RS + g * 16;
    f32x4 acc = {0.f, 0.f, 0.f, 0.f};
#pragma unroll
    for (int ks = 0; ks < 8; ++ks)
      acc = mfma_f16(*(const u32x4*)(ua + ks * 64), *(const u32x4*)(va + ks * 64), acc);
    int idx0, st;
    if (w == 0) { idx0 = (g < 2 ? 4 * g : 120 + 4 * g) + 8 * i; st = 1; }
    else if (w == 1) { idx0 = g < 2 ? 256 + 64 * g + 4 * i : 384 + 64 * (g - 2) + 2 * i; st = 1; }
    else { idx0 = 512 + 64 * g + i; st = 16; }
#pragma unroll
    for (int rg = 0; rg < 4; ++rg) {
      int idx = idx0 + st * rg;
      if (w == 1 && g >= 2) idx = idx0 + 32 * (rg >> 1) + (rg & 1);
      if (w == 2 && g >= 2) idx = (g == 2 && rg == 0 && i == 0) ? 640 : 644;
      kh[idx] = (_Float16)acc[rg];
    }
  }
  __syncthreads();
  STAMP(0, 3);
  const unsigned short* ku = (const unsigned short*)kh;
  const size_t kout = WS_KREV + (size_t)d * KREV * 2;
  {
    const int l0 = 2040 - 8 * tid;
    u32x4 o;
    if (w < 2) {
      const int q = l0 >> 3;
      const unsigned a = ku[384 + q], b = ku[385 + q];
      const unsigned t = (q & 1) ? b : a;
      o[0] = b | (b << 16); o[1] = b | (t << 16); o[2] = a | (a << 16); o[3] = o[2];
    } else if (w == 2) {
      const int q = l0 >> 2;
      const unsigned a = ku[256 + q], b = ku[257 + q], c = ku[258 + q];
      o[0] = c | (c << 16); o[1] = b | (b << 16); o[2] = b | (a << 16); o[3] = a | (a << 16);
    } else {
      unsigned v[8];
#pragma unroll
      for (int e = 0; e < 8; ++e) {
        const int l = l0 + 7 - e;
        const int idx = l < 256 ? l : (int)(pk_bf((float)l, 0.f) & 0xffffu) - 17024;
        v[e] = ku[idx];
      }
#pragma unroll
      for (int q = 0; q < 4; ++q) o[q] = v[2 * q] | (v[2 * q + 1] << 16);
    }
    st16(ws_rsrc(p.ws), kout + 16 * tid, o);
  }
}

__global__ NOPK __launch_bounds__(256) void k1(K1Args p) {
  __shared__ __attribute__((aligned(16))) char smem[57 * KG_RS + 1312];
  int b = blockIdx.x;
  char* stamp_ws = p.ws; (void)stamp_ws;
  STAMP(0, 0);
  if (b < NB_PF) { k1_prefetch(p, b); STAMP(0, 7); return; }
  b -= NB_PF;
  if (b < NB_K) { k1_kgen(p, b, smem); STAMP(0, 7); return; }
  b -= NB_K;
#define K1_PRIO 0
  __builtin_amdgcn_s_setprio(K1_PRIO);
#define K1_DELAY 12
  if (K1_DELAY) { asm volatile("" :: "s"(p.ws), "s"(p.x)); __builtin_amdgcn_s_sleep(K1_DELAY); }
  if (b < NB_LN) {
    k1_ln(p, b, smem);
    STAMP(0, 7);
    return;
  }
  b -= NB_LN;
  k1_mod(p, b, smem);
  STAMP(0, 7);
}

constexpr int K2_CS = 4160;
constexpr int K2_KOFF = 4 * K2_CS;
constexpr int K2_XOFF = K2_CS;
constexpr int K2_CH = K2_KOFF + 4608;
constexpr int CV_RED = 3 * K2_CH;
constexpr int CV_RT2 = 86016;
constexpr int CV_XCH = CV_RT2 + 2 * 32 * 176;
constexpr int CV_FL = CV_RED + 20480;
constexpr int CV_RT = 151552;

struct Chan { float sa, ca1, ga, dsk, hs, hinv, yinv; };
struct ChanRaw { ModP msa, mca, mga; float kinv; unsigned short rds; };
DEV ChanRaw chan_ld(const unsigned short* dskip, const char* ws, int d) {
  ChanRaw c;
  c.rds = dskip[d];
  c.msa = mod_ld(ws, d); c.mca = mod_ld(ws, D_ + d); c.mga = mod_ld(ws, 2 * D_ + d);
  c.kinv = ((const float*)(ws + WS_KSC))[d];
  return c;
}
DEV Chan chan_fin(const ChanRaw& r) {
  Chan c;
  c.sa = mod_fin(r.msa); c.ca1 = rbf(1.f + mod_fin(r.mca)); c.ga = mod_fin(r.mga);
  c.dsk = __uint_as_float((unsigned)r.rds << 16);
  const float hb = 26.f * fabsf(c.ca1) + fabsf(c.sa);
  int ex = (int)((__float_as_uint(hb) >> 23) & 0xff);
  ex = ex < 20 ? 20 : (ex > 240 ? 240 : ex);
  c.hs = __uint_as_float((unsigned)(127 + 14 + 127 - ex) << 23);
  c.hinv = __uint_as_float((unsigned)(ex - 14) << 23);
  c.yinv = r.kinv * c.hinv;
  return c;
}
DEV size_t tl_off(int d, int t) { return (((size_t)(t >> 5) * D_ + d) * 32 + (t & 31)) * 2; }
struct StageRaw { u32x4 xa0, xa1, kv0, kv1; u32x2 xb; };
DEV StageRaw stage_ld(const char* ws, int d, int th) {
  StageRaw s;
  const unsigned short* krev = (const unsigned short*)(ws + WS_KREV) + (size_t)d * KREV;
  const size_t xo = tl_off(d, 16 * th);
  s.xa0 = *(const u32x4*)(ws + WS_XNT + xo); s.xa1 = *(const u32x4*)(ws + WS_XNT + xo + 16);
  s.xb = *(const u32x2*)(ws + WS_XNT + tl_off(d, min(16 * th + 16, L_ - 4)));
  s.kv0 = *(const u32x4*)(krev + 8 * th); s.kv1 = *(const u32x4*)(krev + 1024 + 8 * th);
  return s;
}
DEV void stage_wr(const StageRaw& s, const Chan& c, char* smem, int th) {
  constexpr int CS = K2_CS, KOFF = K2_KOFF;
  unsigned hh[10];
#pragma unroll
  for (int q = 0; q < 10; ++q) {
    unsigned u = q < 4 ? s.xa0[q] : (q < 8 ? s.xa1[q - 4] : s.xb[q - 8]);
    unsigned m = pk_bf(bf_lo(u) * c.ca1, bf_hi(u) * c.ca1);
    unsigned h = pk_bf(bf_lo(m) + c.sa, bf_hi(m) + c.sa);
    hh[q] = pk_h(bf_lo(h) * c.hs, bf_hi(h) * c.hs);
  }
  char* dst = smem + (2 * th + 2) * 16;
  unsigned s1[9];
#pragma unroll
  for (int q = 0; q < 9; ++q) s1[q] = __builtin_amdgcn_alignbit(hh[q + 1], hh[q], 16);
#pragma unroll
  for (int e = 0; e < 2; ++e) {
    *(u32x4*)(dst + 16 * e) = (u32x4){hh[4 * e], hh[4 * e + 1], hh[4 * e + 2], hh[4 * e + 3]};
    *(u32x4*)(dst + 16 * e + CS) = (u32x4){s1[4 * e], s1[4 * e + 1], s1[4 * e + 2], s1[4 * e + 3]};
    *(u32x4*)(dst + 16 * e + 2 * CS) = (u32x4){hh[4 * e + 1], hh[4 * e + 2], hh[4 * e + 3], hh[4 * e + 4]};
    *(u32x4*)(dst + 16 * e + 3 * CS) = (u32x4){s1[4 * e + 1], s1[4 * e + 2], s1[4 * e + 3], s1[4 * e + 4]};
  }
  if (th == 0) {
    u32x4 z = {0u, 0u, 0u, 0u};
    *(u32x4*)(smem) = z; *(u32x4*)(smem + CS) = z; *(u32x4*)(smem + 2 * CS) = z; *(u32x4*)(smem + 3 * CS) = z;
    *(u32x4*)(smem + 16) = z;
    *(u32x4*)(smem + CS + 16) = (u32x4){0u, 0u, 0u, hh[0] << 16};
    *(u32x4*)(smem + 2 * CS + 16) = (u32x4){0u, 0u, 0u, hh[0]};
    *(u32x4*)(smem + 3 * CS + 16) = (u32x4){0u, 0u, hh[0] << 16, s1[0]};
  }
  *(u32x4*)(smem + KOFF + 16 * th) = s.kv0;
  *(u32x4*)(smem + KOFF + 2048 + 16 * th) = s.kv1;
  if (th < 32) *(u32x4*)(smem + KOFF + 4096 + 16 * th) = (u32x4){0u, 0u, 0u, 0u};
}
DEV u32x2 conv_out(const f32x4& y, const Chan& c, const char* smem, int t, const u32x2& xres) {
  const u32x2 hv = *(const u32x2*)(smem + 2 * (t + 16));
  u32x2 o;
#pragma unroll
  for (int e = 0; e < 2; ++e) {
    const unsigned hu = hv[e];
    const h16x2 h2 = __builtin_bit_cast(h16x2, hu);
    const float h0 = (float)h2[0] * c.hinv, h1 = (float)h2[1] * c.hinv;
    unsigned yb = pk_bf(y[2 * e] * c.yinv, y[2 * e + 1] * c.yinv);
    unsigned hd = pk_bf(h0 * c.dsk, h1 * c.dsk);
    unsigned y2 = pk_bf(bf_lo(yb) + bf_lo(hd), bf_hi(yb) + bf_hi(hd));
    unsigned gy = pk_bf(bf_lo(y2) * c.ga, bf_hi(y2) * c.ga);
    o[e] = pk_bf(bf_lo(xres[e]) + bf_lo(gy), bf_hi(xres[e]) + bf_hi(gy));
  }
  return o;
}
#define K2_LOAD(buf, cc, u)                                                                                     \
  asm volatile("ds_read_b64 %0, %3 offset:%5\n\tds_read_b64 %1, %3 offset:%6\n\tds_read_b128 %2, %4 offset:%7"   \
               : "=&v"(Al[buf][u]), "=&v"(Ah[buf][u]), "=&v"(Bf[buf][u])                                          \
               : "v"(ap0), "v"(bp0), "i"(64 * (cc) + 512 * (u)), "i"(64 * (cc) + 512 * (u) + 8),                 \
                 "i"(64 * (cc) + 3584 - 512 * (u)))
template <int SET> DEV constexpr int half_tile(int j) {
  return SET == 0 ? (j == 0 ? 7 : (j == 1 ? 6 : 2)) : (j < 3 ? 5 - j : 4 - j);
}
template <int SET>
DEV void conv_half(unsigned ap0, unsigned bp0, char* redw) {
  constexpr int NU = SET == 0 ? 8 : 6, NT = SET == 0 ? 3 : 5;
  u32x2 Al[2][NU], Ah[2][NU];
  u32x4 Bf[2][NU];
#pragma unroll
  for (int u = 0; u < NU; ++u) K2_LOAD(0, 0, u);
  f32x4 acc[NT];
#pragma unroll
  for (int j = 0; j < NT; ++j) acc[j] = (f32x4){0.f, 0.f, 0.f, 0.f};
#pragma unroll
  for (int cc = 0; cc < 2; ++cc) {
    lgkm0();
    if (cc == 0) {
#pragma unroll
      for (int u = 0; u < NU; ++u) K2_LOAD(1, 1, u);
    }
#pragma unroll
    for (int sp = 0; sp < NU; ++sp)
#pragma unroll
      for (int j = 0; j < NT; ++j) {
        constexpr int dummy = 0; (void)dummy;
        const int q = half_tile<SET>(j);
        if (sp <= q) {
          const u32x4 A = {Al[cc][sp][0], Al[cc][sp][1], Ah[cc][sp][0], Ah[cc][sp][1]};
          acc[j] = mfma_f16(A, Bf[cc][q - sp], acc[j]);
        }
      }
    SB();
  }
#pragma unroll
  for (int j = 0; j < NT; ++j) *(f32x4*)(redw + j * 4096) = acc[j];
}

template <int NF>
DEV void w1_fin(const RtRaw<NF>& t, const ModP& mp, char* ws, int kk, int ct0, int lane, char* scr) {
  const int g = lane >> 4, i = lane & 15;
  float* tb = (float*)(scr + 32 * RT_STR);
  const float mv = mod_fin(mp);
  tb[lane] = lane < 32 ? rbf(1.f + mv) : mv;
  rt_park<NF>(t, scr, lane);
  lgkm0();
  const f32x4 ca = *(const f32x4*)(tb + 8 * g), cb = *(const f32x4*)(tb + 8 * g + 4);
  const f32x4 sa = *(const f32x4*)(tb + 32 + 8 * g), sb = *(const f32x4*)(tb + 36 + 8 * g);
  const u32x4 sv = {pk_bf(sa[0], sa[1]), pk_bf(sa[2], sa[3]), pk_bf(sb[0], sb[1]), pk_bf(sb[2], sb[3])};
  const u32x4 ones = {0x3f803f80u, 0x3f803f80u, 0x3f803f80u, 0x3f803f80u};
  const f32x4 z = {0.f, 0.f, 0.f, 0.f};
  const rsrc_t rs = ws_rsrc(ws);
#pragma unroll
  for (int j = 0; j < NF; ++j) {
    const u32x4 f = rt_frag(scr, j, lane);
    const u32x4 sc = {pk_bf(bf_lo(f[0]) * ca[0], bf_hi(f[0]) * ca[1]), pk_bf(bf_lo(f[1]) * ca[2], bf_hi(f[1]) * ca[3]),
                      pk_bf(bf_lo(f[2]) * cb[0], bf_hi(f[2]) * cb[1]), pk_bf(bf_lo(f[3]) * cb[2], bf_hi(f[3]) * cb[3])};
    st16(rs, WS_W1F + ((size_t)(kk * 108 + ct0 + j) * 64 + lane) * 16, sc);
    const f32x4 cs = mfma_bf16(sc, ones, z), b1 = mfma_bf16(f, sv, z);
    if (i < 2) st16(rs, WS_CSP + ((size_t)(2 * kk + i) * FF + 16 * (ct0 + j) + 4 * g) * 4, __builtin_bit_cast(u32x4, i ? b1 : cs));
  }
}

DEV void conv_phase(const unsigned short* __restrict__ dskip, char* __restrict__ ws,
                    const unsigned short* __restrict__ w1, const unsigned short* __restrict__ w2, char* smem_all) {
  constexpr int CS = K2_CS, KOFF = K2_KOFF;
  const int tid = threadIdx.x, lane = tid & 63;
  const int w = __builtin_amdgcn_readfirstlane(tid >> 6);
  char* stamp_ws = ws; (void)stamp_ws;
  STAMP(1, 0);
  const int bx = blockIdx.x;
  const int pj = (bx & 7) * 16 + (bx >> 4), par = (bx >> 3) & 1;
  const int r = lane & 15, g = lane >> 4, th = tid & 127;
  const int s4 = (r + 1) & 3;
  const rsrc_t rs = ws_rsrc(ws);
  unsigned* fl = (unsigned*)(smem_all + CV_FL);
  if (tid < 5) fl[tid] = 0u;
  if (w < 4) {
    const int d = 5 * pj + 2 * par + (w >> 1), hw = w & 1;
    char* smem = smem_all + (w >> 1) * K2_CH;
    const StageRaw sr = stage_ld(ws, d, th);
    const ChanRaw cr = chan_ld(dskip, ws, d);
    u32x2 xres[4];
#pragma unroll
    for (int q = 0; q < 4; ++q)
      xres[q] = *(const u32x2*)(ws + WS_XT + tl_off(d, 256 * (4 * hw + q) + 16 * r + 4 * g));
    SB();
    const Chan c = chan_fin(cr);
    stage_wr(sr, c, smem, th);
    __syncthreads();
    STAMP(1, 2);
    f32x4 acc[8];
#pragma unroll
    for (int q = 0; q < 8; ++q) acc[q] = (f32x4){0.f, 0.f, 0.f, 0.f};
    {
      const unsigned ap0 = (unsigned)(size_t)(smem + s4 * CS + 2 * (128 * hw + 8 * g + (r + 1) - s4));
      const unsigned bp0 = (unsigned)(size_t)(smem + KOFF + 2 * (2032 + 128 * hw - 16 * r + 8 * g) - 3584);
      u32x2 Al[2][8], Ah[2][8];
      u32x4 Bf[2][8];
#pragma unroll
      for (int u = 0; u < 8; ++u) K2_LOAD(0, 0, u);
#pragma unroll
      for (int cc = 0; cc < 4; ++cc) {
        lgkm0();
#pragma unroll
        for (int q = 0; q < 8; ++q) {
          if (cc < 3) K2_LOAD((cc + 1) & 1, cc + 1, q);
#pragma unroll
          for (int sp = 0; sp <= q; ++sp) {
            const u32x4 A = {Al[cc & 1][sp][0], Al[cc & 1][sp][1], Ah[cc & 1][sp][0], Ah[cc & 1][sp][1]};
            acc[q] = mfma_f16(A, Bf[cc & 1][q - sp], acc[q]);
          }
          SB();
        }
      }
    }
    STAMP(1, 3);
    f32x4 keep[4];
    {
      f32x4* xw = (f32x4*)(smem_all + CV_XCH + w * 4096) + lane;
      if (hw) {
#pragma unroll
        for (int q = 0; q < 4; ++q) { xw[64 * q] = acc[q]; keep[q] = acc[4 + q]; }
      } else {
#pragma unroll
        for (int q = 0; q < 4; ++q) { xw[64 * q] = acc[4 + q]; keep[q] = acc[q]; }
      }
    }
    lds_post(fl + w, lane);
    lds_wait(fl + (w ^ 1), 1u);
    const f32x4* xrd = (const f32x4*)(smem_all + CV_XCH + (w ^ 1) * 4096) + lane;
#pragma unroll
    for (int qp = 0; qp < 2; ++qp) {
      u32x2 o[2];
#pragma unroll
      for (int h = 0; h < 2; ++h) {
        const int q = 2 * qp + h;
        o[h] = conv_out(keep[q] + xrd[64 * q], c, smem, 256 * (4 * hw + q) + 16 * r + 4 * g, xres[q]);
      }
      const auto s0 = __builtin_amdgcn_permlane16_swap(o[0][0], o[1][0], false, false);
      const auto s1 = __builtin_amdgcn_permlane16_swap(o[0][1], o[1][1], false, false);
      const int t = 256 * (4 * hw + 2 * qp + (g & 1)) + 16 * r + 8 * (g >> 1);
      st16(rs, WS_X1T + (((size_t)(t >> 5) * D_ + d) * 32 + (t & 31)) * 2, (u32x4){s0[0], s1[0], s0[1], s1[1]});
    }
  } else {
    __builtin_amdgcn_s_setprio(2);
    const int d = 5 * pj + 4, wq = w - 4;
    char* smem = smem_all + 2 * K2_CH;
    StageRaw sr;
    if (wq < 2) sr = stage_ld(ws, d, th);
    asm volatile("" ::: "memory");
    const ChanRaw cr = chan_ld(dskip, ws, d);
    const int tl0 = par ? (wq < 3 ? 5 - wq : 1) : (wq == 0 ? 7 : (wq == 1 ? 6 : 2)), tl1 = 0;
    const unsigned ap0 = (unsigned)(size_t)(smem + s4 * CS + 2 * (64 * wq + 8 * g + (r + 1) - s4));
    const unsigned bp0 = (unsigned)(size_t)(smem + KOFF + 2 * (2032 + 64 * wq - 16 * r + 8 * g) - 3584);
    char* redw = smem_all + CV_RED + wq * 1024 + lane * 16;
    u32x2 xres[2];
    xres[0] = *(const u32x2*)(ws + WS_XT + tl_off(d, 256 * tl0 + 16 * r + 4 * g));
    xres[1] = *(const u32x2*)(ws + WS_XT + tl_off(d, 256 * tl1 + 16 * r + 4 * g));
    SB();
    const Chan c = chan_fin(cr);
    if (wq < 2) {
      stage_wr(sr, c, smem, th);
    } else {
#define RT_DELAY 32
      __builtin_amdgcn_s_sleep(RT_DELAY);
    }
    const bool isw1 = wq >= 2;
    const int b2 = min(bx, 215), b1 = min(bx, 239);
    const int jk = isw1 ? b1 / 12 : b2 >> 2;
    const int jc = isw1 ? (b1 % 12) * 9 + (wq - 2) * 4 : (b2 & 3) * 10 + 5 * wq;
    RtRaw<5> rt;
    rt_load<5>(rt, isw1 ? w1 : w2, isw1 ? FF : D_, jk, jc, lane);
    const ModP mp = mod_ld(ws, (lane < 32 ? 4 * D_ : 3 * D_ - 32) + 32 * min(jk, 19) + lane);
    SB();
    bar_lds();
    if (par) conv_half<1>(ap0, bp0, redw); else conv_half<0>(ap0, bp0, redw);
    lds_post(fl + 4, lane);
    if (isw1) {
      if (bx < 240) w1_fin<5>(rt, mp, ws, jk, jc, lane, smem_all + CV_RT + (wq - 2) * (32 * RT_STR + 256));
    } else if (bx < 216) {
      char* scr = smem_all + CV_RT2 + wq * (32 * RT_STR);
      rt_park<5>(rt, scr, lane);
      lgkm0();
#pragma unroll
      for (int j = 0; j < 5; ++j) st16(rs, WS_W2F + ((size_t)((jc + j) * 54 + jk) * 64 + lane) * 16, rt_frag(scr, j, lane));
    }
    lds_wait(fl + 4, 4u);
    const char* rd = smem_all + CV_RED + lane * 16;
#pragma unroll
    for (int u = 0; u < 2; ++u) {
      const bool on = u == 0 ? (par || wq < 3) : (par && wq == 0);
      if (on) {
        const int sl = u == 0 ? wq : 4, tl = u == 0 ? tl0 : tl1;
        const f32x4 y = (*(const f32x4*)(rd + sl * 4096) + *(const f32x4*)(rd + sl * 4096 + 1024))
                      + (*(const f32x4*)(rd + sl * 4096 + 2048) + *(const f32x4*)(rd + sl * 4096 + 3072));
        const int t = 256 * tl + 16 * r + 4 * g;
        st8(rs, WS_X1T + (((size_t)(t >> 5) * D_ + d) * 32 + (t & 31)) * 2, conv_out(y, c, smem, t, xres[u]));
      }
    }
  }
  STAMP(1, 7);
}

__global__ NOPK __launch_bounds__(512, 2) void k2(const unsigned short* __restrict__ dskip, char* __restrict__ ws,
                                                  const unsigned short* __restrict__ w1, const unsigned short* __restrict__ w2) {
  __shared__ __attribute__((aligned(16))) char smem[CV_RT + 2 * (32 * RT_STR + 256)];
  conv_phase(dskip, ws, w1, w2, smem);
}

DEV u32x4 ld16_sc1(rsrc_t r, int voff, int soff) { return __builtin_amdgcn_raw_buffer_load_b128(r, voff, soff, 16); }
DEV u32x4 ld16(rsrc_t r, int voff, int soff) { return __builtin_amdgcn_raw_buffer_load_b128(r, voff, soff, 0); }
DEV u32x4 tr_frag(const char* p) {
  const u32x2 a = __builtin_bit_cast(u32x2, __builtin_amdgcn_ds_read_tr16_b64_v4i16((lds_s16x4*)p));
  const u32x2 b = __builtin_bit_cast(u32x2, __builtin_amdgcn_ds_read_tr16_b64_v4i16((lds_s16x4*)(p + 256)));
  return (u32x4){a[0], a[1], b[0], b[1]};
}

constexpr int KF_RS = 84;
constexpr int KF_RB = 8 * 16 * KF_RS * 4;
constexpr int KF_BIG = 2 * KF_RB;
#define NW2L_ 8
constexpr int NW2L = NW2L_;
constexpr int NW2R = 35 - NW2L;
constexpr int KF_W2L = KF_BIG;
constexpr int KF_TAB = KF_W2L + 8 * NW2L * 1024;
constexpr int KF_PS = KF_TAB + 320;
constexpr int KF_XR = KF_PS + 512;
constexpr int KF_CNT = KF_XR + 64 * 176;
constexpr int KF_LDS = KF_CNT + 16;
#define W2_START 10
#define W2_RATE 3
#define W2_INL 28
#define SIG_SPLIT 4
#define H1_B0 1
#define H1_BATCH 7
#define W2_MID 4
#define W1_PRE 10
#define W1_EARLY 3
#define STAMP_EXTRA 0
#define H1_MODE 0
#define H1_PRE 17

DEV void kf_body(char* __restrict__ ws, unsigned short* __restrict__ out, char* smem) {
  char* xs = smem;
  float* tab = (float*)(smem + KF_TAB);
  float* ps = (float*)(smem + KF_PS);
  char* xr = smem + KF_XR;
  unsigned* lcnt = (unsigned*)(smem + KF_CNT);
  float* cst = (float*)(smem + 81920);
  const int tid = threadIdx.x, lane = tid & 63;
  const int w = __builtin_amdgcn_readfirstlane(tid >> 6);
  const int bx = blockIdx.x;
#define KF_MAP 0
  const int ng = (bx >> 3) & 7, mt = (bx & 7) * 4 + (bx >> 6);
  const int g = lane >> 4, i = lane & 15;
  char* stamp_ws = ws; (void)stamp_ws;
  STAMP(2, 0); STAMP(5, 0);
  const rsrc_t rs_ = ws_rsrc(ws);

  unsigned* flags = (unsigned*)(ws + WS_CNT) + mt * 128;
  const int ntl = ng < 6 ? 14 : 12;
  const int ctb = ng < 6 ? 14 * ng : 84 + 12 * (ng - 6);
  const bool two = w < ntl - 8;
  const bool full = w < 6;
  const int ks0 = w < 6 ? 7 * w : 42 + 6 * (w - 6);
  const int ct0 = ctb + (two ? 2 * w : (ntl - 8) + w);
  const int lo16 = lane * 16;
  const int w1s = (int)WS_W1F + ct0 * 1024;
  const int w2s = (int)WS_W2F + (ng * 5 * 54 + ks0) * 1024;

  u32x4 ring[32];
  const char* bb0 = xs + (8 * g + (i >> 2)) * 64 + ((g & 1) << 5) + (i & 3) * 8;
  const char* bb1 = xs + (8 * g + (i >> 2)) * 64 + (((g & 1) ^ 1) << 5) + (i & 3) * 8;
  auto w2_off = [&](int e) __attribute__((always_inline)) {
    const int s = e < 27 ? e / 4 : (e < 34 ? e - 27 : 6), c = e < 27 ? e % 4 : (e < 34 ? 4 : 3);
    const int sn = (s == 6 && !full) ? 5 : s;
    return w2s + (c * 54 + sn) * 1024;
  };

  const unsigned w2lb = (unsigned)(size_t)(smem + KF_W2L) + w * NW2L * 1024;
  auto w2_dma = [&](int n) __attribute__((always_inline)) {
    asm volatile("s_mov_b32 m0, %0\n\ts_nop 0\n\tbuffer_load_dwordx4 %1, %2, %3 offen lds"
                 :: "s"(w2lb + n * 1024), "v"(lo16), "s"(rs_), "s"(w2_off(NW2R + n)) : "memory");
  };

  auto gemm1 = [&](auto two_tag) __attribute__((always_inline)) {
    constexpr bool TWO = decltype(two_tag)::value;
    if (TWO) asm volatile("; two tiles" ::: "memory"); else asm volatile("; one tile" ::: "memory");
    u32x4 t[10];
#pragma unroll
    for (int it = 0; it < 10; ++it) t[it] = ld16(rs_, tid * 16, (int)WS_X1T + mt * 81920 + 8192 * it);
#pragma unroll
    for (int k = 0; k < W1_EARLY; ++k) {
      ring[2 * k] = ld16(rs_, lo16, w1s + k * 108 * 1024);
      if (TWO) ring[2 * k + 1] = ld16(rs_, lo16, w1s + (k * 108 + 1) * 1024);
    }
    SB();
    __builtin_amdgcn_s_barrier();
    SB();
#pragma unroll
    for (int k = W1_EARLY; k < W1_PRE; ++k) {
      ring[2 * k] = ld16(rs_, lo16, w1s + k * 108 * 1024);
      if (TWO) ring[2 * k + 1] = ld16(rs_, lo16, w1s + (k * 108 + 1) * 1024);
    }
    SB();
#pragma unroll
    for (int it = 0; it < 10; ++it) {
      const int c = tid + 512 * it;
      const int row = c >> 2;
      *(u32x4*)(xs + row * 64 + (((c & 3) ^ ((row >> 2) & 2)) << 4)) = t[it];
    }
    SB();
    if (tid == 0) *lcnt = 0u;
    ModP gbp;
    if (!TWO) gbp = mod_ld(ws, 5 * D_ + 80 * ng + min((w & 1) * 64 + lane, 79));
    SB();
    __syncthreads();
    STAMP(2, 1);
    {
      const int kb = (80 * ng) >> 5, tq = w & 3;
#pragma unroll
      for (int u = 0; u < 2; ++u) {
        const int kk = min(kb + 2 * (w >> 2) + u, 19);
        const u32x4 f = tr_frag(((tq & 1) ? bb1 : bb0) + (tq >> 1) * 40960 + kk * 2048);
        const unsigned dl = (unsigned)(32 * kk + 8 * g - 80 * ng);
        if (dl < 80u) *(u32x4*)(xr + (tq * 16 + i) * 176 + dl * 2) = f;
      }
    }
    const int hs = TWO ? 0 : (w & 1);
    const char* ba[4] = {bb0 + hs * 40960, bb1 + hs * 40960, bb0 + (hs ^ 1) * 40960, bb1 + (hs ^ 1) * 40960};
    const u32x4 ones = {0x3f803f80u, 0x3f803f80u, 0x3f803f80u, 0x3f803f80u};
    f32x4 acc[2][4];
#pragma unroll
    for (int c = 0; c < 2; ++c)
#pragma unroll
      for (int q = 0; q < 4; ++q) acc[c][q] = (f32x4){0.f, 0.f, 0.f, 0.f};
    u32x4 csp[20];
    const int cso = ((w & 1) * FF + 16 * ctb + 4 * min(lane, 4 * ntl - 1)) * 4;
    u32x4 bn[2][4];
#pragma unroll
    for (int q = 0; q < 4; ++q) bn[0][q] = tr_frag(ba[q]);
#pragma unroll
    for (int q = 0; q < 4; ++q) bn[1][q] = tr_frag(ba[q] + 2048);
#pragma unroll
    for (int kk = 0; kk < 20; ++kk) {
      u32x4 b[4];
#pragma unroll
      for (int q = 0; q < 4; ++q) b[q] = bn[kk & 1][q];
      SB();
#pragma unroll
      for (int q = 0; q < 4; ++q) acc[0][q] = mfma_bf16(ring[(2 * kk) % 32], b[q], acc[0][q]);
      if (TWO) {
#pragma unroll
        for (int q = 0; q < 4; ++q) acc[1][q] = mfma_bf16(ring[(2 * kk + 1) % 32], b[q], acc[1][q]);
      } else {
        acc[1][0] = mfma_bf16(ones, b[0], acc[1][0]); acc[1][1] = mfma_bf16(b[0], b[0], acc[1][1]);
        acc[1][2] = mfma_bf16(ones, b[1], acc[1][2]); acc[1][3] = mfma_bf16(b[1], b[1], acc[1][3]);
      }
      SB();
      if (kk < 18) {
#pragma unroll
        for (int q = 0; q < 4; ++q) bn[kk & 1][q] = tr_frag(ba[q] + (kk + 2) * 2048);
      }
      if (kk + W1_PRE < 20) {
        const int kn = kk + W1_PRE;
        ring[(2 * kn) % 32] = ld16(rs_, lo16, w1s + kn * 108 * 1024);
        if (TWO) ring[(2 * kn + 1) % 32] = ld16(rs_, lo16, w1s + (kn * 108 + 1) * 1024);
      }
#define LOCK_MASK 0x55554
      if ((LOCK_MASK >> kk) & 1) { SB(); __builtin_amdgcn_s_barrier(); SB(); }
      if (kk >= W2_START) {
#pragma unroll
        for (int u = 0; u < W2_RATE; ++u) {
          const int n = W2_RATE * (kk - W2_START) + u;
          if (n < NW2L) w2_dma(n);
          else if (n < W2_INL) ring[8 + n - NW2L] = ld16(rs_, lo16, w2_off(n - NW2L));
        }
      }
      if (!TWO) {
        if (kk < 10) {
#pragma unroll
          for (int u = 0; u < 2; ++u)
            csp[2 * kk + u] = ld16(rs_, cso, (int)WS_CSP + (2 * kk + u) * 2 * FF * 4);
        }
        if (kk == 14) {
          f32x4 t = __builtin_bit_cast(f32x4, csp[0]);
#pragma unroll
          for (int u = 1; u < 20; ++u) t += __builtin_bit_cast(f32x4, csp[u]);
          if (w >= 6 && lane < 56) *(f32x4*)(cst + (w & 1) * 224 + 4 * lane) = t;
        }
      }
      if (!TWO && kk == 12) { if ((w & 1) * 64 + lane < 80) tab[(w & 1) * 64 + lane] = mod_fin(gbp); }
      SB();
    }
    if (!TWO) {
#pragma unroll
      for (int h = 0; h < 2; ++h) {
        const f32x4 aq = acc[1][2 * h + 1];
        const float dq = (i & 2) ? ((i & 1) ? aq[3] : aq[2]) : ((i & 1) ? aq[1] : aq[0]);
        const int tk = (2 * hs + h) * 16 + i;
        if (g == (i >> 2)) {
          const float mean = acc[1][2 * h][0] * (1.f / D_);
          const float var = fmaxf(dq * (1.f / D_) - mean * mean, 0.f);
          const float sd = rbf(sqrtf(rbf(rbf(var) + 1e-5f)));
          const float rs = 1.f / sd;
          ps[tk] = rs;
          ps[64 + tk] = -rbf(mean) * rs;
        }
      }
    }
    __syncthreads();
    STAMP(2, 2);
    f32x4 csv[2], b1v[2];
#pragma unroll
    for (int c = 0; c < (TWO ? 2 : 1); ++c) {
      csv[c] = *(const f32x4*)(cst + 16 * (ct0 - ctb + c) + 4 * g);
      b1v[c] = *(const f32x4*)(cst + 224 + 16 * (ct0 - ctb + c) + 4 * g);
    }
#pragma unroll
    for (int qp = 0; qp < 2; ++qp) {
      u32x2 o[2][2];
#pragma unroll
      for (int h = 0; h < 2; ++h) {
        const int q = 2 * qp + h, tq = q ^ (2 * hs);
        const float rs = ps[tq * 16 + i], nm = ps[64 + tq * 16 + i];
#pragma unroll
        for (int c = 0; c < (TWO ? 2 : 1); ++c) {
          float v[4];
#pragma unroll
          for (int r = 0; r < 4; ++r) v[r] = fmaxf(fmaf(acc[c][q][r], rs, fmaf(nm, csv[c][r], b1v[c][r])), 0.f);
          o[c][h] = (u32x2){pk_bf(v[0], v[1]), pk_bf(v[2], v[3])};
        }
      }
      const int tl = ((2 * qp) ^ (2 * hs)) + (g & 1);
#pragma unroll
      for (int c = 0; c < (TWO ? 2 : 1); ++c) {
        const int ct = ct0 + c;
        const auto s0 = __builtin_amdgcn_permlane16_swap(o[c][0][0], o[c][1][0], false, false);
        const auto s1 = __builtin_amdgcn_permlane16_swap(o[c][0][1], o[c][1][1], false, false);
        const size_t off = (((size_t)(4 * mt + tl) * 54 + (ct >> 1)) * 64 + i + 16 * (2 * (ct & 1) + (g >> 1))) * 16;
#define H1_ST 16
        __builtin_amdgcn_raw_buffer_store_b128((u32x4){s0[0], s1[0], s0[1], s1[1]}, rs_, (int)(WS_H1F + off), 0, H1_ST);
      }
    }
    SB();
    STAMP(5, 1);
    __builtin_amdgcn_s_barrier();
    SB();
    constexpr int E0 = W2_INL - NW2L;
    auto signal = [&]() __attribute__((always_inline)) {
      if (lane == 0) {
        if (__hip_atomic_fetch_add(lcnt, 1u, __ATOMIC_RELAXED, __HIP_MEMORY_SCOPE_WORKGROUP) == 7u)
          __hip_atomic_store(flags + 16 * ng, 1u, __ATOMIC_RELAXED, __HIP_MEMORY_SCOPE_AGENT);
      }
    };
    if (w >= SIG_SPLIT) { asm volatile("s_waitcnt vmcnt(0)" ::: "memory"); signal(); }
    SB();
#pragma unroll
    for (int e = E0; e < NW2R; ++e) ring[(8 + e) % 32] = ld16(rs_, lo16, w2_off(e));
    SB();
    STAMP(5, 2);
    if (w < SIG_SPLIT) { asm volatile("s_waitcnt vmcnt(%0)" :: "n"(NW2R - E0) : "memory"); signal(); }
    SB();
  };
  if (two) gemm1(std::true_type{});
  else gemm1(std::false_type{});
  STAMP(2, 3);
#pragma nounroll
  for (unsigned spin = 0; spin < (1u << 20); ++spin) {
    if (__hip_atomic_load(flags + 16 * w, __ATOMIC_RELAXED, __HIP_MEMORY_SCOPE_AGENT) != 0u) break;
    __builtin_amdgcn_s_sleep(1);
  }
  asm volatile("" ::: "memory");
  SB();
  STAMP(2, 4);

  const int h1s = (int)WS_H1F + (mt * 4 * 54 + ks0) * 1024;
  u32x4 hb[28];
  auto load_h1 = [&](int n) __attribute__((always_inline)) {
    const int q = n / 7, s = n % 7;
    const int sn = (s == 6 && !full) ? 5 : s;
    hb[n] = ld16_sc1(rs_, lo16, h1s + (q * 54 + sn) * 1024);
  };
#pragma unroll
  for (int n = 0; n < H1_PRE; ++n) {
    if (H1_BATCH && n % H1_BATCH == 0 && n >= H1_B0) { SB(); __builtin_amdgcn_s_barrier(); SB(); }
    load_h1(n);
  }
  SB();
  float* red = (float*)smem;
  float* rb_ = red + (w * 16 + i) * KF_RS + 4 * g;
  const int re = w * 40 + min(lane, 39);
  const int rtk = re / 20, rq4 = re % 20;
  const float* rr_ = red + rtk * KF_RS + 4 * rq4;
  f32x4 ac2[2][5];
  f32x4 rv[4], rsum;
  const char* w2l = smem + KF_W2L + w * NW2L * 1024 + lo16;
  u32x4 wl[2], wl3;
  wl[0] = *(const u32x4*)w2l;
  auto red_wr = [&](int q, int c) __attribute__((always_inline)) {
    *(f32x4*)(rb_ + (q & 1) * (KF_RB / 4) + 16 * c) = ac2[q & 1][c];
  };
  auto red_rd = [&](int q, int h) __attribute__((always_inline)) {
#pragma unroll
    for (int ww = 0; ww < 4; ++ww) rv[ww] = *(const f32x4*)(rr_ + (q & 1) * (KF_RB / 4) + (4 * h + ww) * 16 * KF_RS);
  };
  auto red_out = [&](int q) __attribute__((always_inline)) {
    const f32x4 s = rsum + ((rv[0] + rv[1]) + (rv[2] + rv[3]));
    const u32x2 xv2 = *(const u32x2*)(xr + (16 * q + rtk) * 176 + rq4 * 8);
    const f32x4 gb = *(const f32x4*)(tab + 4 * rq4);
    u32x2 o;
#pragma unroll
    for (int h = 0; h < 2; ++h) {
      unsigned f = pk_bf(s[2 * h], s[2 * h + 1]);
      unsigned gf = pk_bf(bf_lo(f) * gb[2 * h], bf_hi(f) * gb[2 * h + 1]);
      o[h] = pk_bf(bf_lo(xv2[h]) + bf_lo(gf), bf_hi(xv2[h]) + bf_hi(gf));
    }
    if (lane < 40) *(u32x2*)(out + (size_t)(mt * 64 + 16 * q + rtk) * D_ + ng * 80 + 4 * rq4) = o;
  };
#pragma unroll
  for (int q = 0; q < 4; ++q) {
#pragma unroll
    for (int c = 0; c < 5; ++c) ac2[q & 1][c] = (f32x4){0.f, 0.f, 0.f, 0.f};
#pragma unroll
    for (int s = 0; s < 7; ++s) {
      if (q >= 1 && s >= 1 && s <= 5) red_wr(q - 1, s - 1);
      if (q >= 2) {
        if (s == 0) red_rd(q - 2, 0);
        if (s == 2) { rsum = (rv[0] + rv[1]) + (rv[2] + rv[3]); red_rd(q - 2, 1); }
        if (s == 4) red_out(q - 2);
      }
      SB();
      if (q < 3 || s < 6) wl[(7 * q + s + 1) & 1] = *(const u32x4*)(w2l + ((s + 1) % 7) * 1024);
      if (s == 5) wl3 = *(const u32x4*)(w2l + 7 * 1024);
      SB();
      if (s < 6 || full) {
#pragma unroll
        for (int c = 0; c < 5; ++c) {
          const u32x4 a = c == 4 ? wl[(7 * q + s) & 1] : ((s == 6 && c == 3) ? wl3 : ring[(8 + 4 * s + c) % 32]);
          ac2[q & 1][c] = mfma_bf16(a, hb[7 * q + s], ac2[q & 1][c]);
        }
      }
      SB();
      if (7 * q + s + H1_PRE < 28) { load_h1(7 * q + s + H1_PRE); SB(); }
    }
    if (q == 0) STAMP(2, 5);
    if (q >= 1) bar_lds();
  }
  STAMP(2, 6);
  red_rd(2, 0);
#pragma unroll
  for (int c = 0; c < 5; ++c) red_wr(3, c);
  rsum = (rv[0] + rv[1]) + (rv[2] + rv[3]);
  red_rd(2, 1);
  red_out(2);
  bar_lds();
  red_rd(3, 0);
  rsum = (rv[0] + rv[1]) + (rv[2] + rv[3]);
  red_rd(3, 1);
  red_out(3);
  STAMP(2, 7); STAMP(5, 7);
}

__global__ NOPK __launch_bounds__(512, 2) void kf(char* __restrict__ ws, unsigned short* __restrict__ out) {
  __shared__ __attribute__((aligned(16))) char smem[KF_LDS];
  kf_body(ws, out, smem);
}

__global__ void kend_() {}

extern "C" void kernel_launch(void* const* d_in, const int* in_sizes, int n_in,
                              void* d_out, int out_size, void* d_ws, size_t ws_size,
                              hipStream_t stream) {
  K1Args a;
  a.x = (const unsigned short*)d_in[0];
  a.are = (const unsigned short*)d_in[1];
  a.aim = (const unsigned short*)d_in[2];
  a.bre = (const unsigned short*)d_in[3];
  a.bim = (const unsigned short*)d_in[4];
  a.cre = (const unsigned short*)d_in[5];
  a.cim = (const unsigned short*)d_in[6];
  a.logdt = (const unsigned short*)d_in[7];
  a.w1 = (const unsigned short*)d_in[10];
  a.w2 = (const unsigned short*)d_in[11];
  a.c = (const unsigned short*)d_in[12];
  a.wmod = (const unsigned short*)d_in[13];
  a.ws = (char*)d_ws;
  a.dskip = (const unsigned short*)d_in[8];
  a.out = (const unsigned short*)d_out;
  k1<<<NB_PF + NB_MOD + NB_LN + NB_K, 256, 0, stream>>>(a);
  k2<<<256, 512, 0, stream>>>((const unsigned short*)d_in[8], (char*)d_ws, a.w1, a.w2);
  kf<<<256, 512, 0, stream>>>((char*)d_ws, (unsigned short*)d_out);
}
```

```cpp
#define PF 1
#include <hip/hip_runtime.h>
#include <hip/hip_bf16.h>
#include <stdint.h>
#include <type_traits>

typedef __attribute__((ext_vector_type(8))) __bf16 bf16x8;
typedef __attribute__((ext_vector_type(2))) __bf16 bf16x2;
typedef __attribute__((ext_vector_type(4))) float f32x4;
typedef __attribute__((ext_vector_type(2))) float f32x2;
typedef __attribute__((ext_vector_type(4))) unsigned u32x4;
typedef __attribute__((ext_vector_type(2))) unsigned u32x2;
typedef __attribute__((ext_vector_type(8))) _Float16 h16x8;
typedef __attribute__((ext_vector_type(2))) _Float16 h16x2;
typedef __attribute__((ext_vector_type(4))) short s16x4;
typedef __attribute__((address_space(3))) s16x4 lds_s16x4;

#if defined(__HIP_DEVICE_COMPILE__)
#define NOPK __attribute__((target("no-packed-fp32-ops")))
#else
#define NOPK
#endif
#define DEV __device__ __forceinline__

constexpr int L_ = 2048;
constexpr int D_ = 640;
constexpr int NS = 128;
constexpr int FF = 1728;
constexpr int KREV = 2304;

constexpr size_t WS_KSC  = 8192;
constexpr size_t WS_CNT  = 16384;
constexpr size_t WS_CSB  = 32768;
constexpr size_t WS_MOD  = 65536;
constexpr size_t WS_XNT  = 196608;
constexpr size_t WS_XT   = WS_XNT + (size_t)D_ * L_ * 2;
constexpr size_t WS_KREV = WS_XT + (size_t)D_ * L_ * 2;
constexpr size_t WS_W1F  = WS_KREV + (size_t)D_ * 2 * KREV * 2;
constexpr size_t WS_W2F  = WS_W1F + (size_t)D_ * FF * 2;
constexpr size_t WS_X1T  = WS_W2F + (size_t)D_ * FF * 2;
constexpr size_t WS_X1   = WS_X1T + (size_t)D_ * L_ * 2;
constexpr size_t WS_CSP  = WS_X1;
constexpr size_t WS_H1F  = WS_X1 + (size_t)D_ * L_ * 2;

#define STAMP(kid, k) do {} while (0)

#define TS(k, wsp) do { if (blockIdx.x == 0 && threadIdx.x == 0) ((unsigned long long*)((wsp) + (200u << 20)))[k] = __builtin_amdgcn_s_memrealtime(); } while (0)
DEV float bf_lo(unsigned u) { return __uint_as_float(u << 16); }
DEV float bf_hi(unsigned u) { return __uint_as_float(u & 0xffff0000u); }
DEV unsigned pk_bf(float a, float b) {
  f32x2 v = {a, b};
  bf16x2 r = __builtin_convertvector(v, bf16x2);
  return __builtin_bit_cast(unsigned, r);
}
DEV float rbf(float a) { return bf_lo(pk_bf(a, 0.f)); }
DEV float ldbf(const unsigned short* p) { return __uint_as_float(((unsigned)*p) << 16); }

typedef __amdgpu_buffer_rsrc_t rsrc_t;
DEV rsrc_t ws_rsrc(char* ws) { return __builtin_amdgcn_make_buffer_rsrc((void*)ws, 0, 0x7fffffff, 0x00020000); }
#define ST_AUX 16
DEV void st16(rsrc_t r, size_t off, u32x4 v) { __builtin_amdgcn_raw_buffer_store_b128(v, r, (int)off, 0, ST_AUX); }
#define LN_AUX 16
DEV void st16ln(rsrc_t r, size_t off, u32x4 v) { __builtin_amdgcn_raw_buffer_store_b128(v, r, (int)off, 0, LN_AUX); }
DEV void st8(rsrc_t r, size_t off, u32x2 v) { __builtin_amdgcn_raw_buffer_store_b64(v, r, (int)off, 0, 16); }

DEV u32x2 tr_read(unsigned lds_addr) {
  u32x2 r;
  asm volatile("ds_read_b64_tr_b16 %0, %1" : "=v"(r) : "v"(lds_addr) : "memory");
  return r;
}
DEV void bar_lds() { asm volatile("s_waitcnt lgkmcnt(0)\n\ts_barrier" ::: "memory"); }
DEV void lds_wait(const void* word, unsigned val) {
  unsigned tv, ts, cnt;
  asm volatile("s_mov_b32 %2, 0x100000\n"
               "1:\n\tds_read_b32 %0, %3\n\ts_waitcnt lgkmcnt(0)\n\tv_readfirstlane_b32 %1, %0\n\t"
               "s_cmp_ge_u32 %1, %4\n\ts_cbranch_scc1 2f\n\ts_sleep 1\n\ts_sub_u32 %2, %2, 1\n\t"
               "s_cmp_lg_u32 %2, 0\n\ts_cbranch_scc1 1b\n2:"
               : "=&v"(tv), "=&s"(ts), "=&s"(cnt) : "v"((unsigned)(size_t)word), "s"(val) : "memory", "scc");
}
DEV void lds_post(unsigned* word, int lane) {
  asm volatile("" ::: "memory");
  if (lane == 0) __hip_atomic_fetch_add(word, 1u, __ATOMIC_RELAXED, __HIP_MEMORY_SCOPE_WORKGROUP);
  asm volatile("" ::: "memory");
}
DEV void lgkm0() { asm volatile("s_waitcnt lgkmcnt(0)" ::: "memory"); __builtin_amdgcn_sched_barrier(0); }

DEV f32x4 mfma_bf16(u32x4 a, u32x4 b, f32x4 c) {
  return __builtin_amdgcn_mfma_f32_16x16x32_bf16(__builtin_bit_cast(bf16x8, a), __builtin_bit_cast(bf16x8, b), c, 0, 0, 0);
}

constexpr int NB_MOD = 120, NB_LN = 256, NB_W2 = 108, NB_K = 640;

struct K1Args {
  const unsigned short *x, *are, *aim, *bre, *bim, *cre, *cim, *logdt, *w1, *w2, *c, *wmod;
  const unsigned short *dskip, *out;
  char* ws;
};

constexpr int NB_PF = 8;
DEV void k1_prefetch(const K1Args& p, int b) {
  const int tid = threadIdx.x;
  unsigned long long pc;
  int o0, o1;
  asm volatile("s_getpc_b64 %0\n.Lpf_here%=:\n\ts_mov_b32 %1, _Z2k16K1Args - .Lpf_here%=\n\ts_mov_b32 %2, _Z5kend_v - .Lpf_here%="
               : "=s"(pc), "=s"(o0), "=s"(o1));
  const char* c0 = (const char*)(pc + (long long)o0);
  long n = o1 - o0;
  n = n < 0 ? 0 : (n > 98304 ? 98304 : n);
  unsigned acc = 0;
  {
    unsigned v[3];
#pragma unroll
    for (int it = 0; it < 3; ++it) {
      const long o = (long)tid * 128 + it * 32768;
      v[it] = o < n ? *(const __attribute__((address_space(1))) unsigned*)(c0 + o) : 0u;
    }
    acc = v[0] ^ v[1] ^ v[2];
  }
  asm volatile("" :: "v"(acc));
}

DEV void k1_mod(const K1Args& p, int b, char* smem) {
  const int tid = threadIdx.x;
  const int rg = b & 7, cgp = b >> 3;
  const int c32 = tid & 31, r8 = tid >> 5;
  float* sil = (float*)smem;
  float* red = (float*)(smem + 512);
  u32x4 w[10];
#pragma unroll
  for (int it = 0; it < 10; ++it)
    w[it] = *(const u32x4*)(p.wmod + (size_t)(rg * 80 + r8 + 8 * it) * 3840 + 256 * cgp + 8 * c32);
  if (tid < 80) {
    float cv = ldbf(p.c + rg * 80 + tid);
    sil[tid] = rbf(cv / (1.f + expf(-cv)));
  }
  if (tid >= 128 && tid < 132 && b < 64) ((unsigned*)(p.ws + WS_CNT))[(b * 4 + tid - 128) * 16] = 0u;
  __syncthreads();
  float acc[8];
#pragma unroll
  for (int j = 0; j < 8; ++j) acc[j] = 0.f;
#pragma unroll
  for (int it = 0; it < 10; ++it) {
    float s = sil[r8 + 8 * it];
#pragma unroll
    for (int q = 0; q < 4; ++q) {
      acc[2 * q] += s * bf_lo(w[it][q]);
      acc[2 * q + 1] += s * bf_hi(w[it][q]);
    }
  }
#pragma unroll
  for (int j = 0; j < 8; ++j) acc[j] += __shfl_xor(acc[j], 32, 64);
  if ((tid & 63) < 32) {
    *(f32x4*)(red + (tid >> 6) * 256 + 8 * c32) = (f32x4){acc[0], acc[1], acc[2], acc[3]};
    *(f32x4*)(red + (tid >> 6) * 256 + 8 * c32 + 4) = (f32x4){acc[4], acc[5], acc[6], acc[7]};
  }
  __syncthreads();
  ((float*)(p.ws + WS_MOD))[(256 * cgp + tid) * 8 + rg] = (red[tid] + red[256 + tid]) + (red[512 + tid] + red[768 + tid]);
}
struct ModP { f32x4 a, b; };
DEV ModP mod_ld(const char* ws, int idx) {
  const f32x4* mp = (const f32x4*)(ws + WS_MOD) + 2 * idx;
  ModP m; m.a = mp[0]; m.b = mp[1];
  return m;
}
DEV float mod_fin(const ModP& m) {
  return rbf(((m.a[0] + m.a[1]) + (m.a[2] + m.a[3])) + ((m.b[0] + m.b[1]) + (m.b[2] + m.b[3])));
}
DEV float mod_sum(const char* ws, int idx) { return mod_fin(mod_ld(ws, idx)); }
#define SB() __builtin_amdgcn_sched_barrier(0)

DEV void k1_ln(const K1Args& p, int b, char* smem) {
  constexpr int STR = 704;
  const int tid = threadIdx.x;
  const int tok = tid >> 3, sub = tid & 7;
  const int tile = (b & 7) * 8 + ((b >> 3) & 7), pass = b >> 6;
  const int hf = pass & 1;
  const bool norm = pass < 2;
  const int t0 = tile * 32;
  const unsigned short* xr = p.x + (size_t)(t0 + tok) * D_ + 8 * sub;
  u32x4 xv[5], xo[5];
#pragma unroll
  for (int m = 0; m < 5; ++m) xv[m] = *(const u32x4*)(xr + 320 * hf + 64 * m);
  if (norm) {
#pragma unroll
    for (int m = 0; m < 5; ++m) xo[m] = *(const u32x4*)(xr + 320 * (hf ^ 1) + 64 * m);
    float s = 0.f, v = 0.f;
    const bf16x2 one2 = __builtin_bit_cast(bf16x2, 0x3f803f80u);
#pragma unroll
    for (int m = 0; m < 10; ++m)
#pragma unroll
      for (int q = 0; q < 4; ++q) {
        const unsigned u = m < 5 ? xv[m][q] : xo[m - 5][q];
        const bf16x2 x2 = __builtin_bit_cast(bf16x2, u);
        s = __builtin_amdgcn_fdot2_f32_bf16(x2, one2, s, false);
        v = __builtin_amdgcn_fdot2_f32_bf16(x2, x2, v, false);
      }
#pragma unroll
    for (int o = 1; o < 8; o <<= 1) { s += __shfl_xor(s, o, 64); v += __shfl_xor(v, o, 64); }
    const float mean = s * (1.f / D_);
    const float var = fmaxf(v * (1.f / D_) - mean * mean, 0.f);
    const float sd = rbf(sqrtf(rbf(rbf(var) + 1e-5f)));
    const float rs = 1.f / sd;
    const float mb = rbf(mean);
#pragma unroll
    for (int m = 0; m < 5; ++m)
#pragma unroll
      for (int q = 0; q < 4; ++q) {
        unsigned d2 = pk_bf(bf_lo(xv[m][q]) - mb, bf_hi(xv[m][q]) - mb);
        xv[m][q] = pk_bf(bf_lo(d2) * rs, bf_hi(d2) * rs);
      }
  }
  const int lane = tid & 63, w = tid >> 6;
  const int g = lane >> 4, i = lane & 15;
  const rsrc_t rsrc = ws_rsrc(p.ws);
#pragma unroll
  for (int m = 0; m < 5; ++m) *(u32x4*)(smem + tok * STR + (sub + 8 * m) * 16) = xv[m];
  __syncthreads();
  const size_t dsto = (norm ? WS_XNT : WS_XT) + ((size_t)tile * D_ + 320 * hf) * 64;
#pragma unroll
  for (int it = 0; it < 5; ++it) {
    const int cb = w * 5 + it;
    const char* a = smem + (8 * g + (i >> 2)) * STR + cb * 32 + (i & 3) * 8;
    const u32x2 ra = __builtin_bit_cast(u32x2, __builtin_amdgcn_ds_read_tr16_b64_v4i16((lds_s16x4*)a));
    const u32x2 rb = __builtin_bit_cast(u32x2, __builtin_amdgcn_ds_read_tr16_b64_v4i16((lds_s16x4*)(a + 4 * STR)));
    st16ln(rsrc, dsto + (size_t)(cb * 16 + i) * 64 + 16 * g, (u32x4){ra[0], ra[1], rb[0], rb[1]});
  }
}

constexpr int RT_STR = 176;
template <int NF> struct RtRaw { u32x4 r[NF]; };
template <int NF>
DEV void rt_load(RtRaw<NF>& t, const unsigned short* W, int N, int kk, int ct0, int lane) {
#pragma unroll
  for (int j = 0; j < NF; ++j) {
    const int c = lane + 64 * j, row = c / (2 * NF), part = c % (2 * NF);
    t.r[j] = *(const u32x4*)(W + (size_t)(32 * kk + row) * N + 16 * ct0 + 8 * part);
  }
}
template <int NF>
DEV void rt_park(const RtRaw<NF>& t, char* scr, int lane) {
#pragma unroll
  for (int j = 0; j < NF; ++j) {
    const int c = lane + 64 * j, row = c / (2 * NF), part = c % (2 * NF);
    *(u32x4*)(scr + row * RT_STR + 16 * part) = t.r[j];
  }
}
DEV u32x4 rt_frag(const char* scr, int j, int lane) {
  const int g = lane >> 4, i = lane & 15;
  const char* p = scr + (8 * g + (i >> 2)) * RT_STR + 32 * j + (i & 3) * 8;
  const u32x2 a = __builtin_bit_cast(u32x2, __builtin_amdgcn_ds_read_tr16_b64_v4i16((lds_s16x4*)p));
  const u32x2 b = __builtin_bit_cast(u32x2, __builtin_amdgcn_ds_read_tr16_b64_v4i16((lds_s16x4*)(p + 4 * RT_STR)));
  return (u32x4){a[0], a[1], b[0], b[1]};
}
struct cf { float re, im; };
DEV cf cmul(cf a, cf b) {
  cf r;
  r.re = a.re * b.re - a.im * b.im;
  r.im = a.re * b.im + a.im * b.re;
  return r;
}
DEV cf cexp_(float a2, float br, float m, float m0) {
  float mag = __builtin_amdgcn_exp2f(a2 * (m - m0));
  float ph = __builtin_amdgcn_fractf(br * m);
  cf r; r.re = mag * __builtin_amdgcn_cosf(ph); r.im = mag * __builtin_amdgcn_sinf(ph);
  return r;
}
DEV unsigned pk_h(float a, float b) {
  f32x2 v = {a, b};
  h16x2 r = __builtin_convertvector(v, h16x2);
  return __builtin_bit_cast(unsigned, r);
}
DEV f32x4 mfma_f16(u32x4 a, u32x4 b, f32x4 c) {
  return __builtin_amdgcn_mfma_f32_16x16x32_f16(__builtin_bit_cast(h16x8, a), __builtin_bit_cast(h16x8, b), c, 0, 0, 0);
}
DEV void put2(char* p, cf a, cf b) {
  u32x2 o = {pk_h(a.re, a.im), pk_h(b.re, b.im)};
  *(u32x2*)p = o;
}

constexpr int KG_RS = 544;
DEV void k1_kgen(const K1Args& p, int d, char* smem) {
  constexpr int RS = KG_RS;
  const int tid = threadIdx.x;
  const int lane = tid & 63;
  const int w = __builtin_amdgcn_readfirstlane(tid >> 6);
  const int g = lane >> 4, i = lane & 15;
  char* stamp_ws = p.ws; (void)stamp_ws;
  const size_t po = (size_t)d * NS + 2 * lane;
  const unsigned rar = *(const unsigned*)(p.are + po), rai = *(const unsigned*)(p.aim + po);
  const unsigned rbr = *(const unsigned*)(p.bre + po), rbi = *(const unsigned*)(p.bim + po);
  const unsigned rcr = *(const unsigned*)(p.cre + po), rci = *(const unsigned*)(p.cim + po);
  const float ldt = ldbf(p.logdt + d);
  asm volatile("" ::: "memory");
  SB();
  const float dt = rbf(expf(ldt));
  STAMP(0, 1);
  float a2[2], bv[2], m0[2];
  a2[0] = (dt * bf_lo(rar)) * 1.44269504088896341f; a2[1] = (dt * bf_hi(rar)) * 1.44269504088896341f;
  bv[0] = (dt * bf_lo(rai)) * 0.15915494309189534f; bv[1] = (dt * bf_hi(rai)) * 0.15915494309189534f;
  m0[0] = a2[0] > 0.f ? 2048.f : 0.f; m0[1] = a2[1] > 0.f ? 2048.f : 0.f;
  char* my = smem + lane * 8;
  if (w == 0) {
    cf v[2], v8[2], e8[2];
    float bound = 0.f;
#pragma unroll
    for (int q = 0; q < 2; ++q) {
      const float dbr = dt * (q ? bf_hi(rbr) : bf_lo(rbr)), dbi = dt * (q ? bf_hi(rbi) : bf_lo(rbi));
      const float cr = q ? bf_hi(rcr) : bf_lo(rcr), ci = q ? bf_hi(rci) : bf_lo(rci);
      const float gsh = __builtin_amdgcn_exp2f(a2[q] * m0[q]);
      v[q].re = (cr * dbr - ci * dbi) * gsh; v[q].im = -(cr * dbi + ci * dbr) * gsh;
      bound += (fabsf(v[q].re) + fabsf(v[q].im)) * __builtin_amdgcn_exp2f(fmaxf(a2[q], 0.f) * 120.f);
      e8[q] = cexp_(a2[q], -bv[q], 8.f, 0.f);
      v8[q] = cexp_(a2[q], -bv[q], 64.f, 0.f);
    }
#pragma unroll
    for (int o = 1; o < 64; o <<= 1) bound += __shfl_xor(bound, o, 64);
    int ex = (int)((__float_as_uint(bound) >> 23) & 0xff);
    ex = ex < 20 ? 20 : (ex > 240 ? 240 : ex);
    const float sv = __uint_as_float((unsigned)(127 + 13 + 127 - ex) << 23);
    if (lane == 0) ((float*)(p.ws + WS_KSC))[d] = __uint_as_float((unsigned)(ex - 13) << 23);
#pragma unroll
    for (int q = 0; q < 2; ++q) { v[q].re *= sv; v[q].im *= sv; v8[q] = cmul(v[q], v8[q]); }
#pragma unroll
    for (int j = 0; j < 8; ++j) {
      put2(my + (41 + j) * RS, v[0], v[1]);
      put2(my + (49 + j) * RS, v8[0], v8[1]);
      if (j < 7) {
#pragma unroll
        for (int q = 0; q < 2; ++q) { v[q] = cmul(v[q], e8[q]); v8[q] = cmul(v8[q], e8[q]); }
      }
    }
  } else if (w == 1) {
    cf e1[2], u[2], u2[2];
#pragma unroll
    for (int q = 0; q < 2; ++q) {
      e1[q] = cexp_(a2[q], bv[q], 1.f, 0.f);
      u[q].re = __builtin_amdgcn_exp2f(-a2[q] * m0[q]); u[q].im = 0.f;
      u2[q] = cexp_(a2[q], bv[q], 128.f, m0[q]);
    }
#pragma unroll
    for (int r = 0; r < 8; ++r) {
      put2(my + r * RS, u[0], u[1]);
      put2(my + (8 + r) * RS, u2[0], u2[1]);
      if (r < 7) {
#pragma unroll
        for (int q = 0; q < 2; ++q) { u[q] = cmul(u[q], e1[q]); u2[q] = cmul(u2[q], e1[q]); }
      }
    }
  } else if (w == 2) {
    cf e2[2], u[2], u2[2];
#pragma unroll
    for (int q = 0; q < 2; ++q) {
      e2[q] = cexp_(a2[q], bv[q], 2.f, 0.f);
      u[q] = cexp_(a2[q], bv[q], 256.f, m0[q]);
      u2[q] = cexp_(a2[q], bv[q], 384.f, m0[q]);
    }
#pragma unroll
    for (int r = 0; r < 4; ++r) {
      put2(my + (16 + r) * RS, u[0], u[1]);
      put2(my + (20 + r) * RS, u2[0], u2[1]);
      if (r < 3) {
#pragma unroll
        for (int q = 0; q < 2; ++q) { u[q] = cmul(u[q], e2[q]); u2[q] = cmul(u2[q], e2[q]); }
      }
    }
    cf e4[2] = {cmul(e2[0], e2[0]), cmul(e2[1], e2[1])};
#pragma unroll
    for (int r = 0; r < 2; ++r) {
      cf c0 = cexp_(a2[0], bv[0], 512.f + 128.f * r, m0[0]), c1 = cexp_(a2[1], bv[1], 512.f + 128.f * r, m0[1]);
      put2(my + (24 + 2 * r) * RS, c0, c1);
      put2(my + (25 + 2 * r) * RS, cmul(c0, e4[0]), cmul(c1, e4[1]));
    }
  } else {
    cf e4[2], e128[2], u[2], u2[2];
#pragma unroll
    for (int q = 0; q < 2; ++q) {
      e4[q] = cexp_(a2[q], bv[q], 4.f, 0.f);
      e128[q] = cexp_(a2[q], bv[q], 128.f, 0.f);
      u[q] = cexp_(a2[q], bv[q], 1024.f, m0[q]);
      u2[q] = cexp_(a2[q], bv[q], 1536.f, m0[q]);
    }
#pragma unroll
    for (int r = 2; r < 4; ++r) {
      cf c0 = cexp_(a2[0], bv[0], 512.f + 128.f * r, m0[0]), c1 = cexp_(a2[1], bv[1], 512.f + 128.f * r, m0[1]);
      put2(my + (24 + 2 * r) * RS, c0, c1);
      put2(my + (25 + 2 * r) * RS, cmul(c0, e4[0]), cmul(c1, e4[1]));
    }
#pragma unroll
    for (int r = 0; r < 4; ++r) {
      put2(my + (32 + r) * RS, u[0], u[1]);
      put2(my + (36 + r) * RS, u2[0], u2[1]);
#pragma unroll
      for (int q = 0; q < 2; ++q) { if (r < 3) u[q] = cmul(u[q], e128[q]); u2[q] = cmul(u2[q], e128[q]); }
    }
    put2(my + 40 * RS, u2[0], u2[1]);
  }
  __syncthreads();
  STAMP(0, 2);
  _Float16* kh = (_Float16*)(smem + 57 * RS);
  if (w < 3) {
    const char* ua = smem + (16 * w + i) * RS + g * 16;
    const char* va = smem + (41 + i) * RS + g * 16;
    f32x4 acc = {0.f, 0.f, 0.f, 0.f};
#pragma unroll
    for (int ks = 0; ks < 8; ++ks)
      acc = mfma_f16(*(const u32x4*)(ua + ks * 64), *(const u32x4*)(va + ks * 64), acc);
    int idx0, st;
    if (w == 0) { idx0 = (g < 2 ? 4 * g : 120 + 4 * g) + 8 * i; st = 1; }
    else if (w == 1) { idx0 = g < 2 ? 256 + 64 * g + 4 * i : 384 + 64 * (g - 2) + 2 * i; st = 1; }
    else { idx0 = 512 + 64 * g + i; st = 16; }
#pragma unroll
    for (int rg = 0; rg < 4; ++rg) {
      int idx = idx0 + st * rg;
      if (w == 1 && g >= 2) idx = idx0 + 32 * (rg >> 1) + (rg & 1);
      if (w == 2 && g >= 2) idx = (g == 2 && rg == 0 && i == 0) ? 640 : 644;
      kh[idx] = (_Float16)acc[rg];
    }
  }
  __syncthreads();
  STAMP(0, 3);
  const unsigned short* ku = (const unsigned short*)kh;
  const size_t kout = WS_KREV + (size_t)d * KREV * 2;
  {
    const int l0 = 2040 - 8 * tid;
    u32x4 o;
    if (w < 2) {
      const int q = l0 >> 3;
      const unsigned a = ku[384 + q], b = ku[385 + q];
      const unsigned t = (q & 1) ? b : a;
      o[0] = b | (b << 16); o[1] = b | (t << 16); o[2] = a | (a << 16); o[3] = o[2];
    } else if (w == 2) {
      const int q = l0 >> 2;
      const unsigned a = ku[256 + q], b = ku[257 + q], c = ku[258 + q];
      o[0] = c | (c << 16); o[1] = b | (b << 16); o[2] = b | (a << 16); o[3] = a | (a << 16);
    } else {
      unsigned v[8];
#pragma unroll
      for (int e = 0; e < 8; ++e) {
        const int l = l0 + 7 - e;
        const int idx = l < 256 ? l : (int)(pk_bf((float)l, 0.f) & 0xffffu) - 17024;
        v[e] = ku[idx];
      }
#pragma unroll
      for (int q = 0; q < 4; ++q) o[q] = v[2 * q] | (v[2 * q + 1] << 16);
    }
    st16(ws_rsrc(p.ws), kout + 16 * tid, o);
  }
}

__global__ NOPK __launch_bounds__(256) void k1(K1Args p) {
  __shared__ __attribute__((aligned(16))) char smem[57 * KG_RS + 1312];
  int b = blockIdx.x;
  TS(0, p.ws);
  char* stamp_ws = p.ws; (void)stamp_ws;
  STAMP(0, 0);
  if (b < NB_PF) { k1_prefetch(p, b); STAMP(0, 7); return; }
  b -= NB_PF;
  if (b < NB_K) { k1_kgen(p, b, smem); STAMP(0, 7); return; }
  b -= NB_K;
#define K1_PRIO 0
  __builtin_amdgcn_s_setprio(K1_PRIO);
#define K1_DELAY 12
  if (K1_DELAY) { asm volatile("" :: "s"(p.ws), "s"(p.x)); __builtin_amdgcn_s_sleep(K1_DELAY); }
  if (b < NB_LN) {
    k1_ln(p, b, smem);
    STAMP(0, 7);
    return;
  }
  b -= NB_LN;
  k1_mod(p, b, smem);
  STAMP(0, 7);
}

constexpr int K2_CS = 4160;
constexpr int K2_KOFF = 4 * K2_CS;
constexpr int K2_XOFF = K2_CS;
constexpr int K2_CH = K2_KOFF + 4608;
constexpr int CV_RED = 3 * K2_CH;
constexpr int CV_RT2 = 86016;
constexpr int CV_XCH = CV_RT2 + 2 * 32 * 176;
constexpr int CV_FL = CV_RED + 20480;
constexpr int CV_RT = 151552;

struct Chan { float sa, ca1, ga, dsk, hs, hinv, yinv; };
struct ChanRaw { ModP msa, mca, mga; float kinv; unsigned short rds; };
DEV ChanRaw chan_ld(const unsigned short* dskip, const char* ws, int d) {
  ChanRaw c;
  c.rds = dskip[d];
  c.msa = mod_ld(ws, d); c.mca = mod_ld(ws, D_ + d); c.mga = mod_ld(ws, 2 * D_ + d);
  c.kinv = ((const float*)(ws + WS_KSC))[d];
  return c;
}
DEV Chan chan_fin(const ChanRaw& r) {
  Chan c;
  c.sa = mod_fin(r.msa); c.ca1 = rbf(1.f + mod_fin(r.mca)); c.ga = mod_fin(r.mga);
  c.dsk = __uint_as_float((unsigned)r.rds << 16);
  const float hb = 26.f * fabsf(c.ca1) + fabsf(c.sa);
  int ex = (int)((__float_as_uint(hb) >> 23) & 0xff);
  ex = ex < 20 ? 20 : (ex > 240 ? 240 : ex);
  c.hs = __uint_as_float((unsigned)(127 + 14 + 127 - ex) << 23);
  c.hinv = __uint_as_float((unsigned)(ex - 14) << 23);
  c.yinv = r.kinv * c.hinv;
  return c;
}
DEV size_t tl_off(int d, int t) { return (((size_t)(t >> 5) * D_ + d) * 32 + (t & 31)) * 2; }
struct StageRaw { u32x4 xa0, xa1, kv0, kv1; u32x2 xb; };
DEV StageRaw stage_ld(const char* ws, int d, int th) {
  StageRaw s;
  const unsigned short* krev = (const unsigned short*)(ws + WS_KREV) + (size_t)d * KREV;
  const size_t xo = tl_off(d, 16 * th);
  s.xa0 = *(const u32x4*)(ws + WS_XNT + xo); s.xa1 = *(const u32x4*)(ws + WS_XNT + xo + 16);
  s.xb = *(const u32x2*)(ws + WS_XNT + tl_off(d, min(16 * th + 16, L_ - 4)));
  s.kv0 = *(const u32x4*)(krev + 8 * th); s.kv1 = *(const u32x4*)(krev + 1024 + 8 * th);
  return s;
}
DEV void stage_wr(const StageRaw& s, const Chan& c, char* smem, int th) {
  constexpr int CS = K2_CS, KOFF = K2_KOFF;
  unsigned hh[10];
#pragma unroll
  for (int q = 0; q < 10; ++q) {
    unsigned u = q < 4 ? s.xa0[q] : (q < 8 ? s.xa1[q - 4] : s.xb[q - 8]);
    unsigned m = pk_bf(bf_lo(u) * c.ca1, bf_hi(u) * c.ca1);
    unsigned h = pk_bf(bf_lo(m) + c.sa, bf_hi(m) + c.sa);
    hh[q] = pk_h(bf_lo(h) * c.hs, bf_hi(h) * c.hs);
  }
  char* dst = smem + (2 * th + 2) * 16;
  unsigned s1[9];
#pragma unroll
  for (int q = 0; q < 9; ++q) s1[q] = __builtin_amdgcn_alignbit(hh[q + 1], hh[q], 16);
#pragma unroll
  for (int e = 0; e < 2; ++e) {
    *(u32x4*)(dst + 16 * e) = (u32x4){hh[4 * e], hh[4 * e + 1], hh[4 * e + 2], hh[4 * e + 3]};
    *(u32x4*)(dst + 16 * e + CS) = (u32x4){s1[4 * e], s1[4 * e + 1], s1[4 * e + 2], s1[4 * e + 3]};
    *(u32x4*)(dst + 16 * e + 2 * CS) = (u32x4){hh[4 * e + 1], hh[4 * e + 2], hh[4 * e + 3], hh[4 * e + 4]};
    *(u32x4*)(dst + 16 * e + 3 * CS) = (u32x4){s1[4 * e + 1], s1[4 * e + 2], s1[4 * e + 3], s1[4 * e + 4]};
  }
  if (th == 0) {
    u32x4 z = {0u, 0u, 0u, 0u};
    *(u32x4*)(smem) = z; *(u32x4*)(smem + CS) = z; *(u32x4*)(smem + 2 * CS) = z; *(u32x4*)(smem + 3 * CS) = z;
    *(u32x4*)(smem + 16) = z;
    *(u32x4*)(smem + CS + 16) = (u32x4){0u, 0u, 0u, hh[0] << 16};
    *(u32x4*)(smem + 2 * CS + 16) = (u32x4){0u, 0u, 0u, hh[0]};
    *(u32x4*)(smem + 3 * CS + 16) = (u32x4){0u, 0u, hh[0] << 16, s1[0]};
  }
  *(u32x4*)(smem + KOFF + 16 * th) = s.kv0;
  *(u32x4*)(smem + KOFF + 2048 + 16 * th) = s.kv1;
  if (th < 32) *(u32x4*)(smem + KOFF + 4096 + 16 * th) = (u32x4){0u, 0u, 0u, 0u};
}
DEV u32x2 conv_out(const f32x4& y, const Chan& c, const char* smem, int t, const u32x2& xres) {
  const u32x2 hv = *(const u32x2*)(smem + 2 * (t + 16));
  u32x2 o;
#pragma unroll
  for (int e = 0; e < 2; ++e) {
    const unsigned hu = hv[e];
    const h16x2 h2 = __builtin_bit_cast(h16x2, hu);
    const float h0 = (float)h2[0] * c.hinv, h1 = (float)h2[1] * c.hinv;
    unsigned yb = pk_bf(y[2 * e] * c.yinv, y[2 * e + 1] * c.yinv);
    unsigned hd = pk_bf(h0 * c.dsk, h1 * c.dsk);
    unsigned y2 = pk_bf(bf_lo(yb) + bf_lo(hd), bf_hi(yb) + bf_hi(hd));
    unsigned gy = pk_bf(bf_lo(y2) * c.ga, bf_hi(y2) * c.ga);
    o[e] = pk_bf(bf_lo(xres[e]) + bf_lo(gy), bf_hi(xres[e]) + bf_hi(gy));
  }
  return o;
}
#define K2_LOAD(buf, cc, u)                                                                                     \
  asm volatile("ds_read_b64 %0, %3 offset:%5\n\tds_read_b64 %1, %3 offset:%6\n\tds_read_b128 %2, %4 offset:%7"   \
               : "=&v"(Al[buf][u]), "=&v"(Ah[buf][u]), "=&v"(Bf[buf][u])                                          \
               : "v"(ap0), "v"(bp0), "i"(64 * (cc) + 512 * (u)), "i"(64 * (cc) + 512 * (u) + 8),                 \
                 "i"(64 * (cc) + 3584 - 512 * (u)))
template <int SET> DEV constexpr int half_tile(int j) {
  return SET == 0 ? (j == 0 ? 7 : (j == 1 ? 6 : 2)) : (j < 3 ? 5 - j : 4 - j);
}
template <int SET>
DEV void conv_half(unsigned ap0, unsigned bp0, char* redw) {
  constexpr int NU = SET == 0 ? 8 : 6, NT = SET == 0 ? 3 : 5;
  u32x2 Al[2][NU], Ah[2][NU];
  u32x4 Bf[2][NU];
#pragma unroll
  for (int u = 0; u < NU; ++u) K2_LOAD(0, 0, u);
  f32x4 acc[NT];
#pragma unroll
  for (int j = 0; j < NT; ++j) acc[j] = (f32x4){0.f, 0.f, 0.f, 0.f};
#pragma unroll
  for (int cc = 0; cc < 2; ++cc) {
    lgkm0();
    if (cc == 0) {
#pragma unroll
      for (int u = 0; u < NU; ++u) K2_LOAD(1, 1, u);
    }
#pragma unroll
    for (int sp = 0; sp < NU; ++sp)
#pragma unroll
      for (int j = 0; j < NT; ++j) {
        constexpr int dummy = 0; (void)dummy;
        const int q = half_tile<SET>(j);
        if (sp <= q) {
          const u32x4 A = {Al[cc][sp][0], Al[cc][sp][1], Ah[cc][sp][0], Ah[cc][sp][1]};
          acc[j] = mfma_f16(A, Bf[cc][q - sp], acc[j]);
        }
      }
    SB();
  }
#pragma unroll
  for (int j = 0; j < NT; ++j) *(f32x4*)(redw + j * 4096) = acc[j];
}

template <int NF>
DEV void w1_fin(const RtRaw<NF>& t, const ModP& mp, char* ws, int kk, int ct0, int lane, char* scr) {
  const int g = lane >> 4, i = lane & 15;
  float* tb = (float*)(scr + 32 * RT_STR);
  const float mv = mod_fin(mp);
  tb[lane] = lane < 32 ? rbf(1.f + mv) : mv;
  rt_park<NF>(t, scr, lane);
  lgkm0();
  const f32x4 ca = *(const f32x4*)(tb + 8 * g), cb = *(const f32x4*)(tb + 8 * g + 4);
  const f32x4 sa = *(const f32x4*)(tb + 32 + 8 * g), sb = *(const f32x4*)(tb + 36 + 8 * g);
  const u32x4 sv = {pk_bf(sa[0], sa[1]), pk_bf(sa[2], sa[3]), pk_bf(sb[0], sb[1]), pk_bf(sb[2], sb[3])};
  const u32x4 ones = {0x3f803f80u, 0x3f803f80u, 0x3f803f80u, 0x3f803f80u};
  const f32x4 z = {0.f, 0.f, 0.f, 0.f};
  const rsrc_t rs = ws_rsrc(ws);
#pragma unroll
  for (int j = 0; j < NF; ++j) {
    const u32x4 f = rt_frag(scr, j, lane);
    const u32x4 sc = {pk_bf(bf_lo(f[0]) * ca[0], bf_hi(f[0]) * ca[1]), pk_bf(bf_lo(f[1]) * ca[2], bf_hi(f[1]) * ca[3]),
                      pk_bf(bf_lo(f[2]) * cb[0], bf_hi(f[2]) * cb[1]), pk_bf(bf_lo(f[3]) * cb[2], bf_hi(f[3]) * cb[3])};
    st16(rs, WS_W1F + ((size_t)(kk * 108 + ct0 + j) * 64 + lane) * 16, sc);
    const f32x4 cs = mfma_bf16(sc, ones, z), b1 = mfma_bf16(f, sv, z);
    if (i < 2) st16(rs, WS_CSP + ((size_t)(2 * kk + i) * FF + 16 * (ct0 + j) + 4 * g) * 4, __builtin_bit_cast(u32x4, i ? b1 : cs));
  }
}

DEV void conv_phase(const unsigned short* __restrict__ dskip, char* __restrict__ ws,
                    const unsigned short* __restrict__ w1, const unsigned short* __restrict__ w2, char* smem_all) {
  constexpr int CS = K2_CS, KOFF = K2_KOFF;
  const int tid = threadIdx.x, lane = tid & 63;
  const int w = __builtin_amdgcn_readfirstlane(tid >> 6);
  char* stamp_ws = ws; (void)stamp_ws;
  STAMP(1, 0);
  const int bx = blockIdx.x;
  const int pj = (bx & 7) * 16 + (bx >> 4), par = (bx >> 3) & 1;
  const int r = lane & 15, g = lane >> 4, th = tid & 127;
  const int s4 = (r + 1) & 3;
  const rsrc_t rs = ws_rsrc(ws);
  unsigned* fl = (unsigned*)(smem_all + CV_FL);
  if (tid < 5) fl[tid] = 0u;
  if (w < 4) {
    const int d = 5 * pj + 2 * par + (w >> 1), hw = w & 1;
    char* smem = smem_all + (w >> 1) * K2_CH;
    const StageRaw sr = stage_ld(ws, d, th);
    const ChanRaw cr = chan_ld(dskip, ws, d);
    u32x2 xres[4];
#pragma unroll
    for (int q = 0; q < 4; ++q)
      xres[q] = *(const u32x2*)(ws + WS_XT + tl_off(d, 256 * (4 * hw + q) + 16 * r + 4 * g));
    SB();
    const Chan c = chan_fin(cr);
    stage_wr(sr, c, smem, th);
    __syncthreads();
    STAMP(1, 2);
    f32x4 acc[8];
#pragma unroll
    for (int q = 0; q < 8; ++q) acc[q] = (f32x4){0.f, 0.f, 0.f, 0.f};
    {
      const unsigned ap0 = (unsigned)(size_t)(smem + s4 * CS + 2 * (128 * hw + 8 * g + (r + 1) - s4));
      const unsigned bp0 = (unsigned)(size_t)(smem + KOFF + 2 * (2032 + 128 * hw - 16 * r + 8 * g) - 3584);
      u32x2 Al[2][8], Ah[2][8];
      u32x4 Bf[2][8];
#pragma unroll
      for (int u = 0; u < 8; ++u) K2_LOAD(0, 0, u);
#pragma unroll
      for (int cc = 0; cc < 4; ++cc) {
        lgkm0();
#pragma unroll
        for (int q = 0; q < 8; ++q) {
          if (cc < 3) K2_LOAD((cc + 1) & 1, cc + 1, q);
#pragma unroll
          for (int sp = 0; sp <= q; ++sp) {
            const u32x4 A = {Al[cc & 1][sp][0], Al[cc & 1][sp][1], Ah[cc & 1][sp][0], Ah[cc & 1][sp][1]};
            acc[q] = mfma_f16(A, Bf[cc & 1][q - sp], acc[q]);
          }
          SB();
        }
      }
    }
    STAMP(1, 3);
    f32x4 keep[4];
    {
      f32x4* xw = (f32x4*)(smem_all + CV_XCH + w * 4096) + lane;
      if (hw) {
#pragma unroll
        for (int q = 0; q < 4; ++q) { xw[64 * q] = acc[q]; keep[q] = acc[4 + q]; }
      } else {
#pragma unroll
        for (int q = 0; q < 4; ++q) { xw[64 * q] = acc[4 + q]; keep[q] = acc[q]; }
      }
    }
    lds_post(fl + w, lane);
    lds_wait(fl + (w ^ 1), 1u);
    const f32x4* xrd = (const f32x4*)(smem_all + CV_XCH + (w ^ 1) * 4096) + lane;
#pragma unroll
    for (int qp = 0; qp < 2; ++qp) {
      u32x2 o[2];
#pragma unroll
      for (int h = 0; h < 2; ++h) {
        const int q = 2 * qp + h;
        o[h] = conv_out(keep[q] + xrd[64 * q], c, smem, 256 * (4 * hw + q) + 16 * r + 4 * g, xres[q]);
      }
      const auto s0 = __builtin_amdgcn_permlane16_swap(o[0][0], o[1][0], false, false);
      const auto s1 = __builtin_amdgcn_permlane16_swap(o[0][1], o[1][1], false, false);
      const int t = 256 * (4 * hw + 2 * qp + (g & 1)) + 16 * r + 8 * (g >> 1);
      st16(rs, WS_X1T + (((size_t)(t >> 5) * D_ + d) * 32 + (t & 31)) * 2, (u32x4){s0[0], s1[0], s0[1], s1[1]});
    }
  } else {
    __builtin_amdgcn_s_setprio(2);
    const int d = 5 * pj + 4, wq = w - 4;
    char* smem = smem_all + 2 * K2_CH;
    StageRaw sr;
    if (wq < 2) sr = stage_ld(ws, d, th);
    asm volatile("" ::: "memory");
    const ChanRaw cr = chan_ld(dskip, ws, d);
    const int tl0 = par ? (wq < 3 ? 5 - wq : 1) : (wq == 0 ? 7 : (wq == 1 ? 6 : 2)), tl1 = 0;
    const unsigned ap0 = (unsigned)(size_t)(smem + s4 * CS + 2 * (64 * wq + 8 * g + (r + 1) - s4));
    const unsigned bp0 = (unsigned)(size_t)(smem + KOFF + 2 * (2032 + 64 * wq - 16 * r + 8 * g) - 3584);
    char* redw = smem_all + CV_RED + wq * 1024 + lane * 16;
    u32x2 xres[2];
    xres[0] = *(const u32x2*)(ws + WS_XT + tl_off(d, 256 * tl0 + 16 * r + 4 * g));
    xres[1] = *(const u32x2*)(ws + WS_XT + tl_off(d, 256 * tl1 + 16 * r + 4 * g));
    SB();
    const Chan c = chan_fin(cr);
    if (wq < 2) {
      stage_wr(sr, c, smem, th);
    } else {
#define RT_DELAY 32
      __builtin_amdgcn_s_sleep(RT_DELAY);
    }
    const bool isw1 = wq >= 2;
    const int b2 = min(bx, 215), b1 = min(bx, 239);
    const int jk = isw1 ? b1 / 12 : b2 >> 2;
    const int jc = isw1 ? (b1 % 12) * 9 + (wq - 2) * 4 : (b2 & 3) * 10 + 5 * wq;
    RtRaw<5> rt;
    rt_load<5>(rt, isw1 ? w1 : w2, isw1 ? FF : D_, jk, jc, lane);
    const ModP mp = mod_ld(ws, (lane < 32 ? 4 * D_ : 3 * D_ - 32) + 32 * min(jk, 19) + lane);
    SB();
    bar_lds();
    if (par) conv_half<1>(ap0, bp0, redw); else conv_half<0>(ap0, bp0, redw);
    lds_post(fl + 4, lane);
    if (isw1) {
      if (bx < 240) w1_fin<5>(rt, mp, ws, jk, jc, lane, smem_all + CV_RT + (wq - 2) * (32 * RT_STR + 256));
    } else if (bx < 216) {
      char* scr = smem_all + CV_RT2 + wq * (32 * RT_STR);
      rt_park<5>(rt, scr, lane);
      lgkm0();
#pragma unroll
      for (int j = 0; j < 5; ++j) st16(rs, WS_W2F + ((size_t)((jc + j) * 54 + jk) * 64 + lane) * 16, rt_frag(scr, j, lane));
    }
    lds_wait(fl + 4, 4u);
    const char* rd = smem_all + CV_RED + lane * 16;
#pragma unroll
    for (int u = 0; u < 2; ++u) {
      const bool on = u == 0 ? (par || wq < 3) : (par && wq == 0);
      if (on) {
        const int sl = u == 0 ? wq : 4, tl = u == 0 ? tl0 : tl1;
        const f32x4 y = (*(const f32x4*)(rd + sl * 4096) + *(const f32x4*)(rd + sl * 4096 + 1024))
                      + (*(const f32x4*)(rd + sl * 4096 + 2048) + *(const f32x4*)(rd + sl * 4096 + 3072));
        const int t = 256 * tl + 16 * r + 4 * g;
        st8(rs, WS_X1T + (((size_t)(t >> 5) * D_ + d) * 32 + (t & 31)) * 2, conv_out(y, c, smem, t, xres[u]));
      }
    }
  }
  STAMP(1, 7);
}

__global__ NOPK __launch_bounds__(512, 2) void k2(const unsigned short* __restrict__ dskip, char* __restrict__ ws,
                                                  const unsigned short* __restrict__ w1, const unsigned short* __restrict__ w2) {
  __shared__ __attribute__((aligned(16))) char smem[CV_RT + 2 * (32 * RT_STR + 256)];
  TS(1, ws);
  conv_phase(dskip, ws, w1, w2, smem);
}

DEV u32x4 ld16_sc1(rsrc_t r, int voff, int soff) { return __builtin_amdgcn_raw_buffer_load_b128(r, voff, soff, 16); }
DEV u32x4 ld16(rsrc_t r, int voff, int soff) { return __builtin_amdgcn_raw_buffer_load_b128(r, voff, soff, 0); }
DEV u32x4 tr_frag(const char* p) {
  const u32x2 a = __builtin_bit_cast(u32x2, __builtin_amdgcn_ds_read_tr16_b64_v4i16((lds_s16x4*)p));
  const u32x2 b = __builtin_bit_cast(u32x2, __builtin_amdgcn_ds_read_tr16_b64_v4i16((lds_s16x4*)(p + 256)));
  return (u32x4){a[0], a[1], b[0], b[1]};
}

constexpr int KF_RS = 84;
constexpr int KF_RB = 8 * 16 * KF_RS * 4;
constexpr int KF_BIG = 2 * KF_RB;
#define NW2L_ 8
constexpr int NW2L = NW2L_;
constexpr int NW2R = 35 - NW2L;
constexpr int KF_W2L = KF_BIG;
constexpr int KF_TAB = KF_W2L + 8 * NW2L * 1024;
constexpr int KF_PS = KF_TAB + 320;
constexpr int KF_XR = KF_PS + 512;
constexpr int KF_CNT = KF_XR + 64 * 176;
constexpr int KF_LDS = KF_CNT + 16;
#define W2_START 10
#define W2_RATE 3
#define W2_INL 28
#define SIG_SPLIT 4
#define H1_B0 1
#define H1_BATCH 7
#define W2_MID 4
#define W1_PRE 10
#define W1_EARLY 3
#define STAMP_EXTRA 0
#define H1_MODE 0
#define H1_PRE 17

DEV void kf_body(char* __restrict__ ws, unsigned short* __restrict__ out, char* smem) {
  char* xs = smem;
  float* tab = (float*)(smem + KF_TAB);
  float* ps = (float*)(smem + KF_PS);
  char* xr = smem + KF_XR;
  unsigned* lcnt = (unsigned*)(smem + KF_CNT);
  float* cst = (float*)(smem + 81920);
  const int tid = threadIdx.x, lane = tid & 63;
  const int w = __builtin_amdgcn_readfirstlane(tid >> 6);
  const int bx = blockIdx.x;
#define KF_MAP 0
  const int ng = (bx >> 3) & 7, mt = (bx & 7) * 4 + (bx >> 6);
  const int g = lane >> 4, i = lane & 15;
  char* stamp_ws = ws; (void)stamp_ws;
  STAMP(2, 0); STAMP(5, 0);
  const rsrc_t rs_ = ws_rsrc(ws);

  unsigned* flags = (unsigned*)(ws + WS_CNT) + mt * 128;
  const int ntl = ng < 6 ? 14 : 12;
  const int ctb = ng < 6 ? 14 * ng : 84 + 12 * (ng - 6);
  const bool two = w < ntl - 8;
  const bool full = w < 6;
  const int ks0 = w < 6 ? 7 * w : 42 + 6 * (w - 6);
  const int ct0 = ctb + (two ? 2 * w : (ntl - 8) + w);
  const int lo16 = lane * 16;
  const int w1s = (int)WS_W1F + ct0 * 1024;
  const int w2s = (int)WS_W2F + (ng * 5 * 54 + ks0) * 1024;

  u32x4 ring[32];
  const char* bb0 = xs + (8 * g + (i >> 2)) * 64 + ((g & 1) << 5) + (i & 3) * 8;
  const char* bb1 = xs + (8 * g + (i >> 2)) * 64 + (((g & 1) ^ 1) << 5) + (i & 3) * 8;
  auto w2_off = [&](int e) __attribute__((always_inline)) {
    const int s = e < 27 ? e / 4 : (e < 34 ? e - 27 : 6), c = e < 27 ? e % 4 : (e < 34 ? 4 : 3);
    const int sn = (s == 6 && !full) ? 5 : s;
    return w2s + (c * 54 + sn) * 1024;
  };

  const unsigned w2lb = (unsigned)(size_t)(smem + KF_W2L) + w * NW2L * 1024;
  auto w2_dma = [&](int n) __attribute__((always_inline)) {
    asm volatile("s_mov_b32 m0, %0\n\ts_nop 0\n\tbuffer_load_dwordx4 %1, %2, %3 offen lds"
                 :: "s"(w2lb + n * 1024), "v"(lo16), "s"(rs_), "s"(w2_off(NW2R + n)) : "memory");
  };

  auto gemm1 = [&](auto two_tag) __attribute__((always_inline)) {
    constexpr bool TWO = decltype(two_tag)::value;
    if (TWO) asm volatile("; two tiles" ::: "memory"); else asm volatile("; one tile" ::: "memory");
    u32x4 t[10];
#pragma unroll
    for (int it = 0; it < 10; ++it) t[it] = ld16(rs_, tid * 16, (int)WS_X1T + mt * 81920 + 8192 * it);
#pragma unroll
    for (int k = 0; k < W1_EARLY; ++k) {
      ring[2 * k] = ld16(rs_, lo16, w1s + k * 108 * 1024);
      if (TWO) ring[2 * k + 1] = ld16(rs_, lo16, w1s + (k * 108 + 1) * 1024);
    }
    SB();
    __builtin_amdgcn_s_barrier();
    SB();
#pragma unroll
    for (int k = W1_EARLY; k < W1_PRE; ++k) {
      ring[2 * k] = ld16(rs_, lo16, w1s + k * 108 * 1024);
      if (TWO) ring[2 * k + 1] = ld16(rs_, lo16, w1s + (k * 108 + 1) * 1024);
    }
    SB();
#pragma unroll
    for (int it = 0; it < 10; ++it) {
      const int c = tid + 512 * it;
      const int row = c >> 2;
      *(u32x4*)(xs + row * 64 + (((c & 3) ^ ((row >> 2) & 2)) << 4)) = t[it];
    }
    SB();
    if (tid == 0) *lcnt = 0u;
    ModP gbp;
    if (!TWO) gbp = mod_ld(ws, 5 * D_ + 80 * ng + min((w & 1) * 64 + lane, 79));
    SB();
    __syncthreads();
    STAMP(2, 1);
    {
      const int kb = (80 * ng) >> 5, tq = w & 3;
#pragma unroll
      for (int u = 0; u < 2; ++u) {
        const int kk = min(kb + 2 * (w >> 2) + u, 19);
        const u32x4 f = tr_frag(((tq & 1) ? bb1 : bb0) + (tq >> 1) * 40960 + kk * 2048);
        const unsigned dl = (unsigned)(32 * kk + 8 * g - 80 * ng);
        if (dl < 80u) *(u32x4*)(xr + (tq * 16 + i) * 176 + dl * 2) = f;
      }
    }
    const int hs = TWO ? 0 : (w & 1);
    const char* ba[4] = {bb0 + hs * 40960, bb1 + hs * 40960, bb0 + (hs ^ 1) * 40960, bb1 + (hs ^ 1) * 40960};
    const u32x4 ones = {0x3f803f80u, 0x3f803f80u, 0x3f803f80u, 0x3f803f80u};
    f32x4 acc[2][4];
#pragma unroll
    for (int c = 0; c < 2; ++c)
#pragma unroll
      for (int q = 0; q < 4; ++q) acc[c][q] = (f32x4){0.f, 0.f, 0.f, 0.f};
    u32x4 csp[20];
    const int cso = ((w & 1) * FF + 16 * ctb + 4 * min(lane, 4 * ntl - 1)) * 4;
    u32x4 bn[2][4];
#pragma unroll
    for (int q = 0; q < 4; ++q) bn[0][q] = tr_frag(ba[q]);
#pragma unroll
    for (int q = 0; q < 4; ++q) bn[1][q] = tr_frag(ba[q] + 2048);
#pragma unroll
    for (int kk = 0; kk < 20; ++kk) {
      u32x4 b[4];
#pragma unroll
      for (int q = 0; q < 4; ++q) b[q] = bn[kk & 1][q];
      SB();
#pragma unroll
      for (int q = 0; q < 4; ++q) acc[0][q] = mfma_bf16(ring[(2 * kk) % 32], b[q], acc[0][q]);
      if (TWO) {
#pragma unroll
        for (int q = 0; q < 4; ++q) acc[1][q] = mfma_bf16(ring[(2 * kk + 1) % 32], b[q], acc[1][q]);
      } else {
        acc[1][0] = mfma_bf16(ones, b[0], acc[1][0]); acc[1][1] = mfma_bf16(b[0], b[0], acc[1][1]);
        acc[1][2] = mfma_bf16(ones, b[1], acc[1][2]); acc[1][3] = mfma_bf16(b[1], b[1], acc[1][3]);
      }
      SB();
      if (kk < 18) {
#pragma unroll
        for (int q = 0; q < 4; ++q) bn[kk & 1][q] = tr_frag(ba[q] + (kk + 2) * 2048);
      }
      if (kk + W1_PRE < 20) {
        const int kn = kk + W1_PRE;
        ring[(2 * kn) % 32] = ld16(rs_, lo16, w1s + kn * 108 * 1024);
        if (TWO) ring[(2 * kn + 1) % 32] = ld16(rs_, lo16, w1s + (kn * 108 + 1) * 1024);
      }
#define LOCK_MASK 0x55554
      if ((LOCK_MASK >> kk) & 1) { SB(); __builtin_amdgcn_s_barrier(); SB(); }
      if (kk >= W2_START) {
#pragma unroll
        for (int u = 0; u < W2_RATE; ++u) {
          const int n = W2_RATE * (kk - W2_START) + u;
          if (n < NW2L) w2_dma(n);
          else if (n < W2_INL) ring[8 + n - NW2L] = ld16(rs_, lo16, w2_off(n - NW2L));
        }
      }
      if (!TWO) {
        if (kk < 10) {
#pragma unroll
          for (int u = 0; u < 2; ++u)
            csp[2 * kk + u] = ld16(rs_, cso, (int)WS_CSP + (2 * kk + u) * 2 * FF * 4);
        }
        if (kk == 14) {
          f32x4 t = __builtin_bit_cast(f32x4, csp[0]);
#pragma unroll
          for (int u = 1; u < 20; ++u) t += __builtin_bit_cast(f32x4, csp[u]);
          if (w >= 6 && lane < 56) *(f32x4*)(cst + (w & 1) * 224 + 4 * lane) = t;
        }
      }
      if (!TWO && kk == 12) { if ((w & 1) * 64 + lane < 80) tab[(w & 1) * 64 + lane] = mod_fin(gbp); }
      SB();
    }
    if (!TWO) {
#pragma unroll
      for (int h = 0; h < 2; ++h) {
        const f32x4 aq = acc[1][2 * h + 1];
        const float dq = (i & 2) ? ((i & 1) ? aq[3] : aq[2]) : ((i & 1) ? aq[1] : aq[0]);
        const int tk = (2 * hs + h) * 16 + i;
        if (g == (i >> 2)) {
          const float mean = acc[1][2 * h][0] * (1.f / D_);
          const float var = fmaxf(dq * (1.f / D_) - mean * mean, 0.f);
          const float sd = rbf(sqrtf(rbf(rbf(var) + 1e-5f)));
          const float rs = 1.f / sd;
          ps[tk] = rs;
          ps[64 + tk] = -rbf(mean) * rs;
        }
      }
    }
    __syncthreads();
    STAMP(2, 2);
    f32x4 csv[2], b1v[2];
#pragma unroll
    for (int c = 0; c < (TWO ? 2 : 1); ++c) {
      csv[c] = *(const f32x4*)(cst + 16 * (ct0 - ctb + c) + 4 * g);
      b1v[c] = *(const f32x4*)(cst + 224 + 16 * (ct0 - ctb + c) + 4 * g);
    }
#pragma unroll
    for (int qp = 0; qp < 2; ++qp) {
      u32x2 o[2][2];
#pragma unroll
      for (int h = 0; h < 2; ++h) {
        const int q = 2 * qp + h, tq = q ^ (2 * hs);
        const float rs = ps[tq * 16 + i], nm = ps[64 + tq * 16 + i];
#pragma unroll
        for (int c = 0; c < (TWO ? 2 : 1); ++c) {
          float v[4];
#pragma unroll
          for (int r = 0; r < 4; ++r) v[r] = fmaxf(fmaf(acc[c][q][r], rs, fmaf(nm, csv[c][r], b1v[c][r])), 0.f);
          o[c][h] = (u32x2){pk_bf(v[0], v[1]), pk_bf(v[2], v[3])};
        }
      }
      const int tl = ((2 * qp) ^ (2 * hs)) + (g & 1);
#pragma unroll
      for (int c = 0; c < (TWO ? 2 : 1); ++c) {
        const int ct = ct0 + c;
        const auto s0 = __builtin_amdgcn_permlane16_swap(o[c][0][0], o[c][1][0], false, false);
        const auto s1 = __builtin_amdgcn_permlane16_swap(o[c][0][1], o[c][1][1], false, false);
        const size_t off = (((size_t)(4 * mt + tl) * 54 + (ct >> 1)) * 64 + i + 16 * (2 * (ct & 1) + (g >> 1))) * 16;
#define H1_ST 16
        __builtin_amdgcn_raw_buffer_store_b128((u32x4){s0[0], s1[0], s0[1], s1[1]}, rs_, (int)(WS_H1F + off), 0, H1_ST);
      }
    }
    SB();
    STAMP(5, 1);
    __builtin_amdgcn_s_barrier();
    SB();
    constexpr int E0 = W2_INL - NW2L;
    auto signal = [&]() __attribute__((always_inline)) {
      if (lane == 0) {
        if (__hip_atomic_fetch_add(lcnt, 1u, __ATOMIC_RELAXED, __HIP_MEMORY_SCOPE_WORKGROUP) == 7u)
          __hip_atomic_store(flags + 16 * ng, 1u, __ATOMIC_RELAXED, __HIP_MEMORY_SCOPE_AGENT);
      }
    };
    if (w >= SIG_SPLIT) { asm volatile("s_waitcnt vmcnt(0)" ::: "memory"); signal(); }
    SB();
#pragma unroll
    for (int e = E0; e < NW2R; ++e) ring[(8 + e) % 32] = ld16(rs_, lo16, w2_off(e));
    SB();
    STAMP(5, 2);
    if (w < SIG_SPLIT) { asm volatile("s_waitcnt vmcnt(%0)" :: "n"(NW2R - E0) : "memory"); signal(); }
    SB();
  };
  if (two) gemm1(std::true_type{});
  else gemm1(std::false_type{});
  STAMP(2, 3);
#pragma nounroll
  for (unsigned spin = 0; spin < (1u << 20); ++spin) {
    if (__hip_atomic_load(flags + 16 * w, __ATOMIC_RELAXED, __HIP_MEMORY_SCOPE_AGENT) != 0u) break;
    __builtin_amdgcn_s_sleep(1);
  }
  asm volatile("" ::: "memory");
  SB();
  STAMP(2, 4);

  const int h1s = (int)WS_H1F + (mt * 4 * 54 + ks0) * 1024;
  u32x4 hb[28];
  auto load_h1 = [&](int n) __attribute__((always_inline)) {
    const int q = n / 7, s = n % 7;
    const int sn = (s == 6 && !full) ? 5 : s;
    hb[n] = ld16_sc1(rs_, lo16, h1s + (q * 54 + sn) * 1024);
  };
#pragma unroll
  for (int n = 0; n < H1_PRE; ++n) {
    if (H1_BATCH && n % H1_BATCH == 0 && n >= H1_B0) { SB(); __builtin_amdgcn_s_barrier(); SB(); }
    load_h1(n);
  }
  SB();
  float* red = (float*)smem;
  float* rb_ = red + (w * 16 + i) * KF_RS + 4 * g;
  const int re = w * 40 + min(lane, 39);
  const int rtk = re / 20, rq4 = re % 20;
  const float* rr_ = red + rtk * KF_RS + 4 * rq4;
  f32x4 ac2[2][5];
  f32x4 rv[4], rsum;
  const char* w2l = smem + KF_W2L + w * NW2L * 1024 + lo16;
  u32x4 wl[2], wl3;
  wl[0] = *(const u32x4*)w2l;
  auto red_wr = [&](int q, int c) __attribute__((always_inline)) {
    *(f32x4*)(rb_ + (q & 1) * (KF_RB / 4) + 16 * c) = ac2[q & 1][c];
  };
  auto red_rd = [&](int q, int h) __attribute__((always_inline)) {
#pragma unroll
    for (int ww = 0; ww < 4; ++ww) rv[ww] = *(const f32x4*)(rr_ + (q & 1) * (KF_RB / 4) + (4 * h + ww) * 16 * KF_RS);
  };
  auto red_out = [&](int q) __attribute__((always_inline)) {
    const f32x4 s = rsum + ((rv[0] + rv[1]) + (rv[2] + rv[3]));
    const u32x2 xv2 = *(const u32x2*)(xr + (16 * q + rtk) * 176 + rq4 * 8);
    const f32x4 gb = *(const f32x4*)(tab + 4 * rq4);
    u32x2 o;
#pragma unroll
    for (int h = 0; h < 2; ++h) {
      unsigned f = pk_bf(s[2 * h], s[2 * h + 1]);
      unsigned gf = pk_bf(bf_lo(f) * gb[2 * h], bf_hi(f) * gb[2 * h + 1]);
      o[h] = pk_bf(bf_lo(xv2[h]) + bf_lo(gf), bf_hi(xv2[h]) + bf_hi(gf));
    }
    if (lane < 40) *(u32x2*)(out + (size_t)(mt * 64 + 16 * q + rtk) * D_ + ng * 80 + 4 * rq4) = o;
  };
#pragma unroll
  for (int q = 0; q < 4; ++q) {
#pragma unroll
    for (int c = 0; c < 5; ++c) ac2[q & 1][c] = (f32x4){0.f, 0.f, 0.f, 0.f};
#pragma unroll
    for (int s = 0; s < 7; ++s) {
      if (q >= 1 && s >= 1 && s <= 5) red_wr(q - 1, s - 1);
      if (q >= 2) {
        if (s == 0) red_rd(q - 2, 0);
        if (s == 2) { rsum = (rv[0] + rv[1]) + (rv[2] + rv[3]); red_rd(q - 2, 1); }
        if (s == 4) red_out(q - 2);
      }
      SB();
      if (q < 3 || s < 6) wl[(7 * q + s + 1) & 1] = *(const u32x4*)(w2l + ((s + 1) % 7) * 1024);
      if (s == 5) wl3 = *(const u32x4*)(w2l + 7 * 1024);
      SB();
      if (s < 6 || full) {
#pragma unroll
        for (int c = 0; c < 5; ++c) {
          const u32x4 a = c == 4 ? wl[(7 * q + s) & 1] : ((s == 6 && c == 3) ? wl3 : ring[(8 + 4 * s + c) % 32]);
          ac2[q & 1][c] = mfma_bf16(a, hb[7 * q + s], ac2[q & 1][c]);
        }
      }
      SB();
      if (7 * q + s + H1_PRE < 28) { load_h1(7 * q + s + H1_PRE); SB(); }
    }
    if (q == 0) STAMP(2, 5);
    if (q >= 1) bar_lds();
  }
  STAMP(2, 6);
  red_rd(2, 0);
#pragma unroll
  for (int c = 0; c < 5; ++c) red_wr(3, c);
  rsum = (rv[0] + rv[1]) + (rv[2] + rv[3]);
  red_rd(2, 1);
  red_out(2);
  bar_lds();
  red_rd(3, 0);
  rsum = (rv[0] + rv[1]) + (rv[2] + rv[3]);
  red_rd(3, 1);
  red_out(3);
  STAMP(2, 7); STAMP(5, 7);
}

__global__ NOPK __launch_bounds__(512, 2) void kf(char* __restrict__ ws, unsigned short* __restrict__ out) {
  __shared__ __attribute__((aligned(16))) char smem[KF_LDS];
  TS(2, ws);
  kf_body(ws, out, smem);
}

__global__ void kprobe(char* ws, int which) {
  const unsigned long long now = __builtin_amdgcn_s_memrealtime();
  const unsigned long long* t = (const unsigned long long*)(ws + (200u << 20));
  unsigned long long d = which == 0 ? t[1] - t[0] : (which == 1 ? t[2] - t[1] : (which == 2 ? now - t[2] : 0ull));
  if (d > 10000ull) d = 10000ull;
  while (__builtin_amdgcn_s_memrealtime() - now < 10ull * d) __builtin_amdgcn_s_sleep(1);
}
__global__ void kend_() {}

extern "C" void kernel_launch(void* const* d_in, const int* in_sizes, int n_in,
                              void* d_out, int out_size, void* d_ws, size_t ws_size,
                              hipStream_t stream) {
  K1Args a;
  a.x = (const unsigned short*)d_in[0];
  a.are = (const unsigned short*)d_in[1];
  a.aim = (const unsigned short*)d_in[2];
  a.bre = (const unsigned short*)d_in[3];
  a.bim = (const unsigned short*)d_in[4];
  a.cre = (const unsigned short*)d_in[5];
  a.cim = (const unsigned short*)d_in[6];
  a.logdt = (const unsigned short*)d_in[7];
  a.w1 = (const unsigned short*)d_in[10];
  a.w2 = (const unsigned short*)d_in[11];
  a.c = (const unsigned short*)d_in[12];
  a.wmod = (const unsigned short*)d_in[13];
  a.ws = (char*)d_ws;
  a.dskip = (const unsigned short*)d_in[8];
  a.out = (const unsigned short*)d_out;
  k1<<<NB_PF + NB_MOD + NB_LN + NB_K, 256, 0, stream>>>(a);
  k2<<<256, 512, 0, stream>>>((const unsigned short*)d_in[8], (char*)d_ws, a.w1, a.w2);
  kf<<<256, 512, 0, stream>>>((char*)d_ws, (unsigned short*)d_out);
  kprobe<<<1, 64, 0, stream>>>((char*)d_ws, 3);
}
```

```cpp
#define PF 1
#include <hip/hip_runtime.h>
#include <hip/hip_bf16.h>
#include <stdint.h>
#include <type_traits>

typedef __attribute__((ext_vector_type(8))) __bf16 bf16x8;
typedef __attribute__((ext_vector_type(2))) __bf16 bf16x2;
typedef __attribute__((ext_vector_type(4))) float f32x4;
typedef __attribute__((ext_vector_type(2))) float f32x2;
typedef __attribute__((ext_vector_type(4))) unsigned u32x4;
typedef __attribute__((ext_vector_type(2))) unsigned u32x2;
typedef __attribute__((ext_vector_type(8))) _Float16 h16x8;
typedef __attribute__((ext_vector_type(2))) _Float16 h16x2;
typedef __attribute__((ext_vector_type(4))) short s16x4;
typedef __attribute__((address_space(3))) s16x4 lds_s16x4;

#if defined(__HIP_DEVICE_COMPILE__)
#define NOPK __attribute__((target("no-packed-fp32-ops")))
#else
#define NOPK
#endif
#define DEV __device__ __forceinline__

constexpr int L_ = 2048;
constexpr int D_ = 640;
constexpr int NS = 128;
constexpr int FF = 1728;
constexpr int KREV = 2304;

constexpr size_t WS_KSC  = 8192;
constexpr size_t WS_CNT  = 16384;
constexpr size_t WS_CSB  = 32768;
constexpr size_t WS_MOD  = 65536;
constexpr size_t WS_XNT  = 196608;
constexpr size_t WS_XT   = WS_XNT + (size_t)D_ * L_ * 2;
constexpr size_t WS_KREV = WS_XT + (size_t)D_ * L_ * 2;
constexpr size_t WS_W1F  = WS_KREV + (size_t)D_ * 2 * KREV * 2;
constexpr size_t WS_W2F  = WS_W1F + (size_t)D_ * FF * 2;
constexpr size_t WS_X1T  = WS_W2F + (size_t)D_ * FF * 2;
constexpr size_t WS_X1   = WS_X1T + (size_t)D_ * L_ * 2;
constexpr size_t WS_CSP  = WS_X1;
constexpr size_t WS_H1F  = WS_X1 + (size_t)D_ * L_ * 2;

#define STAMP(kid, k) do {} while (0)

DEV float bf_lo(unsigned u) { return __uint_as_float(u << 16); }
DEV float bf_hi(unsigned u) { return __uint_as_float(u & 0xffff0000u); }
DEV unsigned pk_bf(float a, float b) {
  f32x2 v = {a, b};
  bf16x2 r = __builtin_convertvector(v, bf16x2);
  return __builtin_bit_cast(unsigned, r);
}
DEV float rbf(float a) { return bf_lo(pk_bf(a, 0.f)); }
DEV float ldbf(const unsigned short* p) { return __uint_as_float(((unsigned)*p) << 16); }

typedef __amdgpu_buffer_rsrc_t rsrc_t;
DEV rsrc_t ws_rsrc(char* ws) { return __builtin_amdgcn_make_buffer_rsrc((void*)ws, 0, 0x7fffffff, 0x00020000); }
#define ST_AUX 16
DEV void st16(rsrc_t r, size_t off, u32x4 v) { __builtin_amdgcn_raw_buffer_store_b128(v, r, (int)off, 0, ST_AUX); }
#define LN_AUX 16
DEV void st16ln(rsrc_t r, size_t off, u32x4 v) { __builtin_amdgcn_raw_buffer_store_b128(v, r, (int)off, 0, LN_AUX); }
DEV void st8(rsrc_t r, size_t off, u32x2 v) { __builtin_amdgcn_raw_buffer_store_b64(v, r, (int)off, 0, 16); }

DEV u32x2 tr_read(unsigned lds_addr) {
  u32x2 r;
  asm volatile("ds_read_b64_tr_b16 %0, %1" : "=v"(r) : "v"(lds_addr) : "memory");
  return r;
}
DEV void bar_lds() { asm volatile("s_waitcnt lgkmcnt(0)\n\ts_barrier" ::: "memory"); }
DEV void lds_wait(const void* word, unsigned val) {
  unsigned tv, ts, cnt;
  asm volatile("s_mov_b32 %2, 0x100000\n"
               "1:\n\tds_read_b32 %0, %3\n\ts_waitcnt lgkmcnt(0)\n\tv_readfirstlane_b32 %1, %0\n\t"
               "s_cmp_ge_u32 %1, %4\n\ts_cbranch_scc1 2f\n\ts_sleep 1\n\ts_sub_u32 %2, %2, 1\n\t"
               "s_cmp_lg_u32 %2, 0\n\ts_cbranch_scc1 1b\n2:"
               : "=&v"(tv), "=&s"(ts), "=&s"(cnt) : "v"((unsigned)(size_t)word), "s"(val) : "memory", "scc");
}
DEV void lds_post(unsigned* word, int lane) {
  asm volatile("" ::: "memory");
  if (lane == 0) __hip_atomic_fetch_add(word, 1u, __ATOMIC_RELAXED, __HIP_MEMORY_SCOPE_WORKGROUP);
  asm volatile("" ::: "memory");
}
DEV void lgkm0() { asm volatile("s_waitcnt lgkmcnt(0)" ::: "memory"); __builtin_amdgcn_sched_barrier(0); }

DEV f32x4 mfma_bf16(u32x4 a, u32x4 b, f32x4 c) {
  return __builtin_amdgcn_mfma_f32_16x16x32_bf16(__builtin_bit_cast(bf16x8, a), __builtin_bit_cast(bf16x8, b), c, 0, 0, 0);
}

constexpr int NB_MOD = 120, NB_LN = 256, NB_W2 = 108, NB_K = 640;

struct K1Args {
  const unsigned short *x, *are, *aim, *bre, *bim, *cre, *cim, *logdt, *w1, *w2, *c, *wmod;
  const unsigned short *dskip, *out;
  char* ws;
};

constexpr int NB_PF = 8;
DEV void k1_prefetch(const K1Args& p, int b) {
  const int tid = threadIdx.x;
  unsigned long long pc;
  int o0, o1;
  asm volatile("s_getpc_b64 %0\n.Lpf_here%=:\n\ts_mov_b32 %1, _Z2k16K1Args - .Lpf_here%=\n\ts_mov_b32 %2, _Z5kend_v - .Lpf_here%="
               : "=s"(pc), "=s"(o0), "=s"(o1));
  const char* c0 = (const char*)(pc + (long long)o0);
  long n = o1 - o0;
  n = n < 0 ? 0 : (n > 98304 ? 98304 : n);
  unsigned acc = 0;
  {
    unsigned v[3];
#pragma unroll
    for (int it = 0; it < 3; ++it) {
      const long o = (long)tid * 128 + it * 32768;
      v[it] = o < n ? *(const __attribute__((address_space(1))) unsigned*)(c0 + o) : 0u;
    }
    acc = v[0] ^ v[1] ^ v[2];
  }
  asm volatile("" :: "v"(acc));
}

DEV void k1_mod(const K1Args& p, int b, char* smem) {
  const int tid = threadIdx.x;
  const int rg = b & 7, cgp = b >> 3;
  const int c32 = tid & 31, r8 = tid >> 5;
  float* sil = (float*)smem;
  float* red = (float*)(smem + 512);
  u32x4 w[10];
#pragma unroll
  for (int it = 0; it < 10; ++it)
    w[it] = *(const u32x4*)(p.wmod + (size_t)(rg * 80 + r8 + 8 * it) * 3840 + 256 * cgp + 8 * c32);
  if (tid < 80) {
    float cv = ldbf(p.c + rg * 80 + tid);
    sil[tid] = rbf(cv / (1.f + expf(-cv)));
  }
  if (tid >= 128 && tid < 132 && b < 64) ((unsigned*)(p.ws + WS_CNT))[(b * 4 + tid - 128) * 16] = 0u;
  __syncthreads();
  float acc[8];
#pragma unroll
  for (int j = 0; j < 8; ++j) acc[j] = 0.f;
#pragma unroll
  for (int it = 0; it < 10; ++it) {
    float s = sil[r8 + 8 * it];
#pragma unroll
    for (int q = 0; q < 4; ++q) {
      acc[2 * q] += s * bf_lo(w[it][q]);
      acc[2 * q + 1] += s * bf_hi(w[it][q]);
    }
  }
#pragma unroll
  for (int j = 0; j < 8; ++j) acc[j] += __shfl_xor(acc[j], 32, 64);
  if ((tid & 63) < 32) {
    *(f32x4*)(red + (tid >> 6) * 256 + 8 * c32) = (f32x4){acc[0], acc[1], acc[2], acc[3]};
    *(f32x4*)(red + (tid >> 6) * 256 + 8 * c32 + 4) = (f32x4){acc[4], acc[5], acc[6], acc[7]};
  }
  __syncthreads();
  ((float*)(p.ws + WS_MOD))[(256 * cgp + tid) * 8 + rg] = (red[tid] + red[256 + tid]) + (red[512 + tid] + red[768 + tid]);
}
struct ModP { f32x4 a, b; };
DEV ModP mod_ld(const char* ws, int idx) {
  const f32x4* mp = (const f32x4*)(ws + WS_MOD) + 2 * idx;
  ModP m; m.a = mp[0]; m.b = mp[1];
  return m;
}
DEV float mod_fin(const ModP& m) {
  return rbf(((m.a[0] + m.a[1]) + (m.a[2] + m.a[3])) + ((m.b[0] + m.b[1]) + (m.b[2] + m.b[3])));
}
DEV float mod_sum(const char* ws, int idx) { return mod_fin(mod_ld(ws, idx)); }
#define SB() __builtin_amdgcn_sched_barrier(0)

DEV void k1_ln(const K1Args& p, int b, char* smem) {
  constexpr int STR = 704;
  const int tid = threadIdx.x;
  const int tok = tid >> 3, sub = tid & 7;
  const int tile = (b & 7) * 8 + ((b >> 3) & 7), pass = b >> 6;
  const int hf = pass & 1;
  const bool norm = pass < 2;
  const int t0 = tile * 32;
  const unsigned short* xr = p.x + (size_t)(t0 + tok) * D_ + 8 * sub;
  u32x4 xv[5], xo[5];
#pragma unroll
  for (int m = 0; m < 5; ++m) xv[m] = *(const u32x4*)(xr + 320 * hf + 64 * m);
  if (norm) {
#pragma unroll
    for (int m = 0; m < 5; ++m) xo[m] = *(const u32x4*)(xr + 320 * (hf ^ 1) + 64 * m);
    float s = 0.f, v = 0.f;
    const bf16x2 one2 = __builtin_bit_cast(bf16x2, 0x3f803f80u);
#pragma unroll
    for (int m = 0; m < 10; ++m)
#pragma unroll
      for (int q = 0; q < 4; ++q) {
        const unsigned u = m < 5 ? xv[m][q] : xo[m - 5][q];
        const bf16x2 x2 = __builtin_bit_cast(bf16x2, u);
        s = __builtin_amdgcn_fdot2_f32_bf16(x2, one2, s, false);
        v = __builtin_amdgcn_fdot2_f32_bf16(x2, x2, v, false);
      }
#pragma unroll
    for (int o = 1; o < 8; o <<= 1) { s += __shfl_xor(s, o, 64); v += __shfl_xor(v, o, 64); }
    const float mean = s * (1.f / D_);
    const float var = fmaxf(v * (1.f / D_) - mean * mean, 0.f);
    const float sd = rbf(sqrtf(rbf(rbf(var) + 1e-5f)));
    const float rs = 1.f / sd;
    const float mb = rbf(mean);
#pragma unroll
    for (int m = 0; m < 5; ++m)
#pragma unroll
      for (int q = 0; q < 4; ++q) {
        unsigned d2 = pk_bf(bf_lo(xv[m][q]) - mb, bf_hi(xv[m][q]) - mb);
        xv[m][q] = pk_bf(bf_lo(d2) * rs, bf_hi(d2) * rs);
      }
  }
  const int lane = tid & 63, w = tid >> 6;
  const int g = lane >> 4, i = lane & 15;
  const rsrc_t rsrc = ws_rsrc(p.ws);
#pragma unroll
  for (int m = 0; m < 5; ++m) *(u32x4*)(smem + tok * STR + (sub + 8 * m) * 16) = xv[m];
  __syncthreads();
  const size_t dsto = (norm ? WS_XNT : WS_XT) + ((size_t)tile * D_ + 320 * hf) * 64;
#pragma unroll
  for (int it = 0; it < 5; ++it) {
    const int cb = w * 5 + it;
    const char* a = smem + (8 * g + (i >> 2)) * STR + cb * 32 + (i & 3) * 8;
    const u32x2 ra = __builtin_bit_cast(u32x2, __builtin_amdgcn_ds_read_tr16_b64_v4i16((lds_s16x4*)a));
    const u32x2 rb = __builtin_bit_cast(u32x2, __builtin_amdgcn_ds_read_tr16_b64_v4i16((lds_s16x4*)(a + 4 * STR)));
    st16ln(rsrc, dsto + (size_t)(cb * 16 + i) * 64 + 16 * g, (u32x4){ra[0], ra[1], rb[0], rb[1]});
  }
}

constexpr int RT_STR = 176;
template <int NF> struct RtRaw { u32x4 r[NF]; };
template <int NF>
DEV void rt_load(RtRaw<NF>& t, const unsigned short* W, int N, int kk, int ct0, int lane) {
#pragma unroll
  for (int j = 0; j < NF; ++j) {
    const int c = lane + 64 * j, row = c / (2 * NF), part = c % (2 * NF);
    t.r[j] = *(const u32x4*)(W + (size_t)(32 * kk + row) * N + 16 * ct0 + 8 * part);
  }
}
template <int NF>
DEV void rt_park(const RtRaw<NF>& t, char* scr, int lane) {
#pragma unroll
  for (int j = 0; j < NF; ++j) {
    const int c = lane + 64 * j, row = c / (2 * NF), part = c % (2 * NF);
    *(u32x4*)(scr + row * RT_STR + 16 * part) = t.r[j];
  }
}
DEV u32x4 rt_frag(const char* scr, int j, int lane) {
  const int g = lane >> 4, i = lane & 15;
  const char* p = scr + (8 * g + (i >> 2)) * RT_STR + 32 * j + (i & 3) * 8;
  const u32x2 a = __builtin_bit_cast(u32x2, __builtin_amdgcn_ds_read_tr16_b64_v4i16((lds_s16x4*)p));
  const u32x2 b = __builtin_bit_cast(u32x2, __builtin_amdgcn_ds_read_tr16_b64_v4i16((lds_s16x4*)(p + 4 * RT_STR)));
  return (u32x4){a[0], a[1], b[0], b[1]};
}
struct cf { float re, im; };
DEV cf cmul(cf a, cf b) {
  cf r;
  r.re = a.re * b.re - a.im * b.im;
  r.im = a.re * b.im + a.im * b.re;
  return r;
}
DEV cf cexp_(float a2, float br, float m, float m0) {
  float mag = __builtin_amdgcn_exp2f(a2 * (m - m0));
  float ph = __builtin_amdgcn_fractf(br * m);
  cf r; r.re = mag * __builtin_amdgcn_cosf(ph); r.im = mag * __builtin_amdgcn_sinf(ph);
  return r;
}
DEV unsigned pk_h(float a, float b) {
  f32x2 v = {a, b};
  h16x2 r = __builtin_convertvector(v, h16x2);
  return __builtin_bit_cast(unsigned, r);
}
DEV f32x4 mfma_f16(u32x4 a, u32x4 b, f32x4 c) {
  return __builtin_amdgcn_mfma_f32_16x16x32_f16(__builtin_bit_cast(h16x8, a), __builtin_bit_cast(h16x8, b), c, 0, 0, 0);
}
DEV void put2(char* p, cf a, cf b) {
  u32x2 o = {pk_h(a.re, a.im), pk_h(b.re, b.im)};
  *(u32x2*)p = o;
}

constexpr int KG_RS = 544;
DEV void k1_kgen(const K1Args& p, int d, char* smem) {
  constexpr int RS = KG_RS;
  const int tid = threadIdx.x;
  const int lane = tid & 63;
  const int w = __builtin_amdgcn_readfirstlane(tid >> 6);
  const int g = lane >> 4, i = lane & 15;
  char* stamp_ws = p.ws; (void)stamp_ws;
  const size_t po = (size_t)d * NS + 2 * lane;
  const unsigned rar = *(const unsigned*)(p.are + po), rai = *(const unsigned*)(p.aim + po);
  const unsigned rbr = *(const unsigned*)(p.bre + po), rbi = *(const unsigned*)(p.bim + po);
  const unsigned rcr = *(const unsigned*)(p.cre + po), rci = *(const unsigned*)(p.cim + po);
  const float ldt = ldbf(p.logdt + d);
  asm volatile("" ::: "memory");
  SB();
  const float dt = rbf(expf(ldt));
  STAMP(0, 1);
  float a2[2], bv[2], m0[2];
  a2[0] = (dt * bf_lo(rar)) * 1.44269504088896341f; a2[1] = (dt * bf_hi(rar)) * 1.44269504088896341f;
  bv[0] = (dt * bf_lo(rai)) * 0.15915494309189534f; bv[1] = (dt * bf_hi(rai)) * 0.15915494309189534f;
  m0[0] = a2[0] > 0.f ? 2048.f : 0.f; m0[1] = a2[1] > 0.f ? 2048.f : 0.f;
  char* my = smem + lane * 8;
  if (w == 0) {
    cf v[2], v8[2], e8[2];
    float bound = 0.f;
#pragma unroll
    for (int q = 0; q < 2; ++q) {
      const float dbr = dt * (q ? bf_hi(rbr) : bf_lo(rbr)), dbi = dt * (q ? bf_hi(rbi) : bf_lo(rbi));
      const float cr = q ? bf_hi(rcr) : bf_lo(rcr), ci = q ? bf_hi(rci) : bf_lo(rci);
      const float gsh = __builtin_amdgcn_exp2f(a2[q] * m0[q]);
      v[q].re = (cr * dbr - ci * dbi) * gsh; v[q].im = -(cr * dbi + ci * dbr) * gsh;
      bound += (fabsf(v[q].re) + fabsf(v[q].im)) * __builtin_amdgcn_exp2f(fmaxf(a2[q], 0.f) * 120.f);
      e8[q] = cexp_(a2[q], -bv[q], 8.f, 0.f);
      v8[q] = cexp_(a2[q], -bv[q], 64.f, 0.f);
    }
#pragma unroll
    for (int o = 1; o < 64; o <<= 1) bound += __shfl_xor(bound, o, 64);
    int ex = (int)((__float_as_uint(bound) >> 23) & 0xff);
    ex = ex < 20 ? 20 : (ex > 240 ? 240 : ex);
    const float sv = __uint_as_float((unsigned)(127 + 13 + 127 - ex) << 23);
    if (lane == 0) ((float*)(p.ws + WS_KSC))[d] = __uint_as_float((unsigned)(ex - 13) << 23);
#pragma unroll
    for (int q = 0; q < 2; ++q) { v[q].re *= sv; v[q].im *= sv; v8[q] = cmul(v[q], v8[q]); }
#pragma unroll
    for (int j = 0; j < 8; ++j) {
      put2(my + (41 + j) * RS, v[0], v[1]);
      put2(my + (49 + j) * RS, v8[0], v8[1]);
      if (j < 7) {
#pragma unroll
        for (int q = 0; q < 2; ++q) { v[q] = cmul(v[q], e8[q]); v8[q] = cmul(v8[q], e8[q]); }
      }
    }
  } else if (w == 1) {
    cf e1[2], u[2], u2[2];
#pragma unroll
    for (int q = 0; q < 2; ++q) {
      e1[q] = cexp_(a2[q], bv[q], 1.f, 0.f);
      u[q].re = __builtin_amdgcn_exp2f(-a2[q] * m0[q]); u[q].im = 0.f;
      u2[q] = cexp_(a2[q], bv[q], 128.f, m0[q]);
    }
#pragma unroll
    for (int r = 0; r < 8; ++r) {
      put2(my + r * RS, u[0], u[1]);
      put2(my + (8 + r) * RS, u2[0], u2[1]);
      if (r < 7) {
#pragma unroll
        for (int q = 0; q < 2; ++q) { u[q] = cmul(u[q], e1[q]); u2[q] = cmul(u2[q], e1[q]); }
      }
    }
  } else if (w == 2) {
    cf e2[2], u[2], u2[2];
#pragma unroll
    for (int q = 0; q < 2; ++q) {
      e2[q] = cexp_(a2[q], bv[q], 2.f, 0.f);
      u[q] = cexp_(a2[q], bv[q], 256.f, m0[q]);
      u2[q] = cexp_(a2[q], bv[q], 384.f, m0[q]);
    }
#pragma unroll
    for (int r = 0; r < 4; ++r) {
      put2(my + (16 + r) * RS, u[0], u[1]);
      put2(my + (20 + r) * RS, u2[0], u2[1]);
      if (r < 3) {
#pragma unroll
        for (int q = 0; q < 2; ++q) { u[q] = cmul(u[q], e2[q]); u2[q] = cmul(u2[q], e2[q]); }
      }
    }
    cf e4[2] = {cmul(e2[0], e2[0]), cmul(e2[1], e2[1])};
#pragma unroll
    for (int r = 0; r < 2; ++r) {
      cf c0 = cexp_(a2[0], bv[0], 512.f + 128.f * r, m0[0]), c1 = cexp_(a2[1], bv[1], 512.f + 128.f * r, m0[1]);
      put2(my + (24 + 2 * r) * RS, c0, c1);
      put2(my + (25 + 2 * r) * RS, cmul(c0, e4[0]), cmul(c1, e4[1]));
    }
  } else {
    cf e4[2], e128[2], u[2], u2[2];
#pragma unroll
    for (int q = 0; q < 2; ++q) {
      e4[q] = cexp_(a2[q], bv[q], 4.f, 0.f);
      e128[q] = cexp_(a2[q], bv[q], 128.f, 0.f);
      u[q] = cexp_(a2[q], bv[q], 1024.f, m0[q]);
      u2[q] = cexp_(a2[q], bv[q], 1536.f, m0[q]);
    }
#pragma unroll
    for (int r = 2; r < 4; ++r) {
      cf c0 = cexp_(a2[0], bv[0], 512.f + 128.f * r, m0[0]), c1 = cexp_(a2[1], bv[1], 512.f + 128.f * r, m0[1]);
      put2(my + (24 + 2 * r) * RS, c0, c1);
      put2(my + (25 + 2 * r) * RS, cmul(c0, e4[0]), cmul(c1, e4[1]));
    }
#pragma unroll
    for (int r = 0; r < 4; ++r) {
      put2(my + (32 + r) * RS, u[0], u[1]);
      put2(my + (36 + r) * RS, u2[0], u2[1]);
#pragma unroll
      for (int q = 0; q < 2; ++q) { if (r < 3) u[q] = cmul(u[q], e128[q]); u2[q] = cmul(u2[q], e128[q]); }
    }
    put2(my + 40 * RS, u2[0], u2[1]);
  }
  __syncthreads();
  STAMP(0, 2);
  _Float16* kh = (_Float16*)(smem + 57 * RS);
  if (w < 3) {
    const char* ua = smem + (16 * w + i) * RS + g * 16;
    const char* va = smem + (41 + i) * RS + g * 16;
    f32x4 acc = {0.f, 0.f, 0.f, 0.f};
#pragma unroll
    for (int ks = 0; ks < 8; ++ks)
      acc = mfma_f16(*(const u32x4*)(ua + ks * 64), *(const u32x4*)(va + ks * 64), acc);
    int idx0, st;
    if (w == 0) { idx0 = (g < 2 ? 4 * g : 120 + 4 * g) + 8 * i; st = 1; }
    else if (w == 1) { idx0 = g < 2 ? 256 + 64 * g + 4 * i : 384 + 64 * (g - 2) + 2 * i; st = 1; }
    else { idx0 = 512 + 64 * g + i; st = 16; }
#pragma unroll
    for (int rg = 0; rg < 4; ++rg) {
      int idx = idx0 + st * rg;
      if (w == 1 && g >= 2) idx = idx0 + 32 * (rg >> 1) + (rg & 1);
      if (w == 2 && g >= 2) idx = (g == 2 && rg == 0 && i == 0) ? 640 : 644;
      kh[idx] = (_Float16)acc[rg];
    }
  }
  __syncthreads();
  STAMP(0, 3);
  const unsigned short* ku = (const unsigned short*)kh;
  const size_t kout = WS_KREV + (size_t)d * KREV * 2;
  {
    const int l0 = 2040 - 8 * tid;
    u32x4 o;
    if (w < 2) {
      const int q = l0 >> 3;
      const unsigned a = ku[384 + q], b = ku[385 + q];
      const unsigned t = (q & 1) ? b : a;
      o[0] = b | (b << 16); o[1] = b | (t << 16); o[2] = a | (a << 16); o[3] = o[2];
    } else if (w == 2) {
      const int q = l0 >> 2;
      const unsigned a = ku[256 + q], b = ku[257 + q], c = ku[258 + q];
      o[0] = c | (c << 16); o[1] = b | (b << 16); o[2] = b | (a << 16); o[3] = a | (a << 16);
    } else {
      unsigned v[8];
#pragma unroll
      for (int e = 0; e < 8; ++e) {
        const int l = l0 + 7 - e;
        const int idx = l < 256 ? l : (int)(pk_bf((float)l, 0.f) & 0xffffu) - 17024;
        v[e] = ku[idx];
      }
#pragma unroll
      for (int q = 0; q < 4; ++q) o[q] = v[2 * q] | (v[2 * q + 1] << 16);
    }
    st16(ws_rsrc(p.ws), kout + 16 * tid, o);
  }
}

__global__ NOPK __launch_bounds__(256) void k1(K1Args p) {
  __shared__ __attribute__((aligned(16))) char smem[57 * KG_RS + 1312];
  int b = blockIdx.x;
  char* stamp_ws = p.ws; (void)stamp_ws;
  STAMP(0, 0);
  if (b < NB_PF) { k1_prefetch(p, b); STAMP(0, 7); return; }
  b -= NB_PF;
  if (b < NB_K) { k1_kgen(p, b, smem); STAMP(0, 7); return; }
  b -= NB_K;
#define K1_PRIO 0
  __builtin_amdgcn_s_setprio(K1_PRIO);
#define K1_DELAY 12
  if (K1_DELAY) { asm volatile("" :: "s"(p.ws), "s"(p.x)); __builtin_amdgcn_s_sleep(K1_DELAY); }
  if (b < NB_LN) {
    k1_ln(p, b, smem);
    STAMP(0, 7);
    return;
  }
  b -= NB_LN;
  k1_mod(p, b, smem);
  STAMP(0, 7);
}

constexpr int K2_CS = 4160;
constexpr int K2_KOFF = 4 * K2_CS;
constexpr int K2_XOFF = K2_CS;
constexpr int K2_CH = K2_KOFF + 4608;
constexpr int CV_RED = 3 * K2_CH;
constexpr int CV_RT2 = 86016;
constexpr int CV_XCH = CV_RT2 + 2 * 32 * 176;
constexpr int CV_FL = CV_RED + 20480;
constexpr int CV_RT = 151552;

struct Chan { float sa, ca1, ga, dsk, hs, hinv, yinv; };
struct ChanRaw { ModP msa, mca, mga; float kinv; unsigned short rds; };
DEV ChanRaw chan_ld(const unsigned short* dskip, const char* ws, int d) {
  ChanRaw c;
  c.rds = dskip[d];
  c.msa = mod_ld(ws, d); c.mca = mod_ld(ws, D_ + d); c.mga = mod_ld(ws, 2 * D_ + d);
  c.kinv = ((const float*)(ws + WS_KSC))[d];
  return c;
}
DEV Chan chan_fin(const ChanRaw& r) {
  Chan c;
  c.sa = mod_fin(r.msa); c.ca1 = rbf(1.f + mod_fin(r.mca)); c.ga = mod_fin(r.mga);
  c.dsk = __uint_as_float((unsigned)r.rds << 16);
  const float hb = 26.f * fabsf(c.ca1) + fabsf(c.sa);
  int ex = (int)((__float_as_uint(hb) >> 23) & 0xff);
  ex = ex < 20 ? 20 : (ex > 240 ? 240 : ex);
  c.hs = __uint_as_float((unsigned)(127 + 14 + 127 - ex) << 23);
  c.hinv = __uint_as_float((unsigned)(ex - 14) << 23);
  c.yinv = r.kinv * c.hinv;
  return c;
}
DEV size_t tl_off(int d, int t) { return (((size_t)(t >> 5) * D_ + d) * 32 + (t & 31)) * 2; }
struct StageRaw { u32x4 xa0, xa1, kv0, kv1; u32x2 xb; };
DEV StageRaw stage_ld(const char* ws, int d, int th) {
  StageRaw s;
  const unsigned short* krev = (const unsigned short*)(ws + WS_KREV) + (size_t)d * KREV;
  const size_t xo = tl_off(d, 16 * th);
  s.xa0 = *(const u32x4*)(ws + WS_XNT + xo); s.xa1 = *(const u32x4*)(ws + WS_XNT + xo + 16);
  s.xb = *(const u32x2*)(ws + WS_XNT + tl_off(d, min(16 * th + 16, L_ - 4)));
  s.kv0 = *(const u32x4*)(krev + 8 * th); s.kv1 = *(const u32x4*)(krev + 1024 + 8 * th);
  return s;
}
DEV void stage_wr(const StageRaw& s, const Chan& c, char* smem, int th) {
  constexpr int CS = K2_CS, KOFF = K2_KOFF;
  unsigned hh[10];
#pragma unroll
  for (int q = 0; q < 10; ++q) {
    unsigned u = q < 4 ? s.xa0[q] : (q < 8 ? s.xa1[q - 4] : s.xb[q - 8]);
    unsigned m = pk_bf(bf_lo(u) * c.ca1, bf_hi(u) * c.ca1);
    unsigned h = pk_bf(bf_lo(m) + c.sa, bf_hi(m) + c.sa);
    hh[q] = pk_h(bf_lo(h) * c.hs, bf_hi(h) * c.hs);
  }
  char* dst = smem + (2 * th + 2) * 16;
  unsigned s1[9];
#pragma unroll
  for (int q = 0; q < 9; ++q) s1[q] = __builtin_amdgcn_alignbit(hh[q + 1], hh[q], 16);
#pragma unroll
  for (int e = 0; e < 2; ++e) {
    *(u32x4*)(dst + 16 * e) = (u32x4){hh[4 * e], hh[4 * e + 1], hh[4 * e + 2], hh[4 * e + 3]};
    *(u32x4*)(dst + 16 * e + CS) = (u32x4){s1[4 * e], s1[4 * e + 1], s1[4 * e + 2], s1[4 * e + 3]};
    *(u32x4*)(dst + 16 * e + 2 * CS) = (u32x4){hh[4 * e + 1], hh[4 * e + 2], hh[4 * e + 3], hh[4 * e + 4]};
    *(u32x4*)(dst + 16 * e + 3 * CS) = (u32x4){s1[4 * e + 1], s1[4 * e + 2], s1[4 * e + 3], s1[4 * e + 4]};
  }
  if (th == 0) {
    u32x4 z = {0u, 0u, 0u, 0u};
    *(u32x4*)(smem) = z; *(u32x4*)(smem + CS) = z; *(u32x4*)(smem + 2 * CS) = z; *(u32x4*)(smem + 3 * CS) = z;
    *(u32x4*)(smem + 16) = z;
    *(u32x4*)(smem + CS + 16) = (u32x4){0u, 0u, 0u, hh[0] << 16};
    *(u32x4*)(smem + 2 * CS + 16) = (u32x4){0u, 0u, 0u, hh[0]};
    *(u32x4*)(smem + 3 * CS + 16) = (u32x4){0u, 0u, hh[0] << 16, s1[0]};
  }
  *(u32x4*)(smem + KOFF + 16 * th) = s.kv0;
  *(u32x4*)(smem + KOFF + 2048 + 16 * th) = s.kv1;
  if (th < 32) *(u32x4*)(smem + KOFF + 4096 + 16 * th) = (u32x4){0u, 0u, 0u, 0u};
}
DEV u32x2 conv_out(const f32x4& y, const Chan& c, const char* smem, int t, const u32x2& xres) {
  const u32x2 hv = *(const u32x2*)(smem + 2 * (t + 16));
  u32x2 o;
#pragma unroll
  for (int e = 0; e < 2; ++e) {
    const unsigned hu = hv[e];
    const h16x2 h2 = __builtin_bit_cast(h16x2, hu);
    const float h0 = (float)h2[0] * c.hinv, h1 = (float)h2[1] * c.hinv;
    unsigned yb = pk_bf(y[2 * e] * c.yinv, y[2 * e + 1] * c.yinv);
    unsigned hd = pk_bf(h0 * c.dsk, h1 * c.dsk);
    unsigned y2 = pk_bf(bf_lo(yb) + bf_lo(hd), bf_hi(yb) + bf_hi(hd));
    unsigned gy = pk_bf(bf_lo(y2) * c.ga, bf_hi(y2) * c.ga);
    o[e] = pk_bf(bf_lo(xres[e]) + bf_lo(gy), bf_hi(xres[e]) + bf_hi(gy));
  }
  return o;
}
#define K2_LOAD(buf, cc, u)                                                                                     \
  asm volatile("ds_read_b64 %0, %3 offset:%5\n\tds_read_b64 %1, %3 offset:%6\n\tds_read_b128 %2, %4 offset:%7"   \
               : "=&v"(Al[buf][u]), "=&v"(Ah[buf][u]), "=&v"(Bf[buf][u])                                          \
               : "v"(ap0), "v"(bp0), "i"(64 * (cc) + 512 * (u)), "i"(64 * (cc) + 512 * (u) + 8),                 \
                 "i"(64 * (cc) + 3584 - 512 * (u)))
template <int SET> DEV constexpr int half_tile(int j) {
  return SET == 0 ? (j == 0 ? 7 : (j == 1 ? 6 : 2)) : (j < 3 ? 5 - j : 4 - j);
}
template <int SET>
DEV void conv_half(unsigned ap0, unsigned bp0, char* redw) {
  constexpr int NU = SET == 0 ? 8 : 6, NT = SET == 0 ? 3 : 5;
  u32x2 Al[2][NU], Ah[2][NU];
  u32x4 Bf[2][NU];
#pragma unroll
  for (int u = 0; u < NU; ++u) K2_LOAD(0, 0, u);
  f32x4 acc[NT];
#pragma unroll
  for (int j = 0; j < NT; ++j) acc[j] = (f32x4){0.f, 0.f, 0.f, 0.f};
#pragma unroll
  for (int cc = 0; cc < 2; ++cc) {
    lgkm0();
    if (cc == 0) {
#pragma unroll
      for (int u = 0; u < NU; ++u) K2_LOAD(1, 1, u);
    }
#pragma unroll
    for (int sp = 0; sp < NU; ++sp)
#pragma unroll
      for (int j = 0; j < NT; ++j) {
        constexpr int dummy = 0; (void)dummy;
        const int q = half_tile<SET>(j);
        if (sp <= q) {
          const u32x4 A = {Al[cc][sp][0], Al[cc][sp][1], Ah[cc][sp][0], Ah[cc][sp][1]};
          acc[j] = mfma_f16(A, Bf[cc][q - sp], acc[j]);
        }
      }
    SB();
  }
#pragma unroll
  for (int j = 0; j < NT; ++j) *(f32x4*)(redw + j * 4096) = acc[j];
}

template <int NF>
DEV void w1_fin(const RtRaw<NF>& t, const ModP& mp, char* ws, int kk, int ct0, int lane, char* scr) {
  const int g = lane >> 4, i = lane & 15;
  float* tb = (float*)(scr + 32 * RT_STR);
  const float mv = mod_fin(mp);
  tb[lane] = lane < 32 ? rbf(1.f + mv) : mv;
  rt_park<NF>(t, scr, lane);
  lgkm0();
  const f32x4 ca = *(const f32x4*)(tb + 8 * g), cb = *(const f32x4*)(tb + 8 * g + 4);
  const f32x4 sa = *(const f32x4*)(tb + 32 + 8 * g), sb = *(const f32x4*)(tb + 36 + 8 * g);
  const u32x4 sv = {pk_bf(sa[0], sa[1]), pk_bf(sa[2], sa[3]), pk_bf(sb[0], sb[1]), pk_bf(sb[2], sb[3])};
  const u32x4 ones = {0x3f803f80u, 0x3f803f80u, 0x3f803f80u, 0x3f803f80u};
  const f32x4 z = {0.f, 0.f, 0.f, 0.f};
  const rsrc_t rs = ws_rsrc(ws);
#pragma unroll
  for (int j = 0; j < NF; ++j) {
    const u32x4 f = rt_frag(scr, j, lane);
    const u32x4 sc = {pk_bf(bf_lo(f[0]) * ca[0], bf_hi(f[0]) * ca[1]), pk_bf(bf_lo(f[1]) * ca[2], bf_hi(f[1]) * ca[3]),
                      pk_bf(bf_lo(f[2]) * cb[0], bf_hi(f[2]) * cb[1]), pk_bf(bf_lo(f[3]) * cb[2], bf_hi(f[3]) * cb[3])};
    st16(rs, WS_W1F + ((size_t)(kk * 108 + ct0 + j) * 64 + lane) * 16, sc);
    const f32x4 cs = mfma_bf16(sc, ones, z), b1 = mfma_bf16(f, sv, z);
    if (i < 2) st16(rs, WS_CSP + ((size_t)(2 * kk + i) * FF + 16 * (ct0 + j) + 4 * g) * 4, __builtin_bit_cast(u32x4, i ? b1 : cs));
  }
}

DEV void conv_phase(const unsigned short* __restrict__ dskip, char* __restrict__ ws,
                    const unsigned short* __restrict__ w1, const unsigned short* __restrict__ w2, char* smem_all) {
  constexpr int CS = K2_CS, KOFF = K2_KOFF;
  const int tid = threadIdx.x, lane = tid & 63;
  const int w = __builtin_amdgcn_readfirstlane(tid >> 6);
  char* stamp_ws = ws; (void)stamp_ws;
  STAMP(1, 0);
  const int bx = blockIdx.x;
  const int pj = (bx & 7) * 16 + (bx >> 4), par = (bx >> 3) & 1;
  const int r = lane & 15, g = lane >> 4, th = tid & 127;
  const int s4 = (r + 1) & 3;
  const rsrc_t rs = ws_rsrc(ws);
  unsigned* fl = (unsigned*)(smem_all + CV_FL);
  if (tid < 5) fl[tid] = 0u;
  if (w < 4) {
    const int d = 5 * pj + 2 * par + (w >> 1), hw = w & 1;
    char* smem = smem_all + (w >> 1) * K2_CH;
    const StageRaw sr = stage_ld(ws, d, th);
    const ChanRaw cr = chan_ld(dskip, ws, d);
    u32x2 xres[4];
#pragma unroll
    for (int q = 0; q < 4; ++q)
      xres[q] = *(const u32x2*)(ws + WS_XT + tl_off(d, 256 * (4 * hw + q) + 16 * r + 4 * g));
    SB();
    const Chan c = chan_fin(cr);
    stage_wr(sr, c, smem, th);
    __syncthreads();
    STAMP(1, 2);
    f32x4 acc[8];
#pragma unroll
    for (int q = 0; q < 8; ++q) acc[q] = (f32x4){0.f, 0.f, 0.f, 0.f};
    {
      const unsigned ap0 = (unsigned)(size_t)(smem + s4 * CS + 2 * (128 * hw + 8 * g + (r + 1) - s4));
      const unsigned bp0 = (unsigned)(size_t)(smem + KOFF + 2 * (2032 + 128 * hw - 16 * r + 8 * g) - 3584);
      u32x2 Al[2][8], Ah[2][8];
      u32x4 Bf[2][8];
#pragma unroll
      for (int u = 0; u < 8; ++u) K2_LOAD(0, 0, u);
#pragma unroll
      for (int cc = 0; cc < 4; ++cc) {
        lgkm0();
#pragma unroll
        for (int q = 0; q < 8; ++q) {
          if (cc < 3) K2_LOAD((cc + 1) & 1, cc + 1, q);
#pragma unroll
          for (int sp = 0; sp <= q; ++sp) {
            const u32x4 A = {Al[cc & 1][sp][0], Al[cc & 1][sp][1], Ah[cc & 1][sp][0], Ah[cc & 1][sp][1]};
            acc[q] = mfma_f16(A, Bf[cc & 1][q - sp], acc[q]);
          }
          SB();
        }
      }
    }
    STAMP(1, 3);
    f32x4 keep[4];
    {
      f32x4* xw = (f32x4*)(smem_all + CV_XCH + w * 4096) + lane;
      if (hw) {
#pragma unroll
        for (int q = 0; q < 4; ++q) { xw[64 * q] = acc[q]; keep[q] = acc[4 + q]; }
      } else {
#pragma unroll
        for (int q = 0; q < 4; ++q) { xw[64 * q] = acc[4 + q]; keep[q] = acc[q]; }
      }
    }
    lds_post(fl + w, lane);
    lds_wait(fl + (w ^ 1), 1u);
    const f32x4* xrd = (const f32x4*)(smem_all + CV_XCH + (w ^ 1) * 4096) + lane;
#pragma unroll
    for (int qp = 0; qp < 2; ++qp) {
      u32x2 o[2];
#pragma unroll
      for (int h = 0; h < 2; ++h) {
        const int q = 2 * qp + h;
        o[h] = conv_out(keep[q] + xrd[64 * q], c, smem, 256 * (4 * hw + q) + 16 * r + 4 * g, xres[q]);
      }
      const auto s0 = __builtin_amdgcn_permlane16_swap(o[0][0], o[1][0], false, false);
      const auto s1 = __builtin_amdgcn_permlane16_swap(o[0][1], o[1][1], false, false);
      const int t = 256 * (4 * hw + 2 * qp + (g & 1)) + 16 * r + 8 * (g >> 1);
      st16(rs, WS_X1T + (((size_t)(t >> 5) * D_ + d) * 32 + (t & 31)) * 2, (u32x4){s0[0], s1[0], s0[1], s1[1]});
    }
  } else {
    __builtin_amdgcn_s_setprio(2);
    const int d = 5 * pj + 4, wq = w - 4;
    char* smem = smem_all + 2 * K2_CH;
    StageRaw sr;
    if (wq < 2) sr = stage_ld(ws, d, th);
    asm volatile("" ::: "memory");
    const ChanRaw cr = chan_ld(dskip, ws, d);
    const int tl0 = par ? (wq < 3 ? 5 - wq : 1) : (wq == 0 ? 7 : (wq == 1 ? 6 : 2)), tl1 = 0;
    const unsigned ap0 = (unsigned)(size_t)(smem + s4 * CS + 2 * (64 * wq + 8 * g + (r + 1) - s4));
    const unsigned bp0 = (unsigned)(size_t)(smem + KOFF + 2 * (2032 + 64 * wq - 16 * r + 8 * g) - 3584);
    char* redw = smem_all + CV_RED + wq * 1024 + lane * 16;
    u32x2 xres[2];
    xres[0] = *(const u32x2*)(ws + WS_XT + tl_off(d, 256 * tl0 + 16 * r + 4 * g));
    xres[1] = *(const u32x2*)(ws + WS_XT + tl_off(d, 256 * tl1 + 16 * r + 4 * g));
    SB();
    const Chan c = chan_fin(cr);
    if (wq < 2) {
      stage_wr(sr, c, smem, th);
    } else {
#define RT_DELAY 32
      __builtin_amdgcn_s_sleep(RT_DELAY);
    }
    const bool isw1 = wq >= 2;
    const int b2 = min(bx, 215), b1 = min(bx, 239);
    const int jk = isw1 ? b1 / 12 : b2 >> 2;
    const int jc = isw1 ? (b1 % 12) * 9 + (wq - 2) * 4 : (b2 & 3) * 10 + 5 * wq;
    RtRaw<5> rt;
    rt_load<5>(rt, isw1 ? w1 : w2, isw1 ? FF : D_, jk, jc, lane);
    const ModP mp = mod_ld(ws, (lane < 32 ? 4 * D_ : 3 * D_ - 32) + 32 * min(jk, 19) + lane);
    SB();
    bar_lds();
    if (par) conv_half<1>(ap0, bp0, redw); else conv_half<0>(ap0, bp0, redw);
    lds_post(fl + 4, lane);
    if (isw1) {
      if (bx < 240) w1_fin<5>(rt, mp, ws, jk, jc, lane, smem_all + CV_RT + (wq - 2) * (32 * RT_STR + 256));
    } else if (bx < 216) {
      char* scr = smem_all + CV_RT2 + wq * (32 * RT_STR);
      rt_park<5>(rt, scr, lane);
      lgkm0();
#pragma unroll
      for (int j = 0; j < 5; ++j) st16(rs, WS_W2F + ((size_t)((jc + j) * 54 + jk) * 64 + lane) * 16, rt_frag(scr, j, lane));
    }
    lds_wait(fl + 4, 4u);
    const char* rd = smem_all + CV_RED + lane * 16;
#pragma unroll
    for (int u = 0; u < 2; ++u) {
      const bool on = u == 0 ? (par || wq < 3) : (par && wq == 0);
      if (on) {
        const int sl = u == 0 ? wq : 4, tl = u == 0 ? tl0 : tl1;
        const f32x4 y = (*(const f32x4*)(rd + sl * 4096) + *(const f32x4*)(rd + sl * 4096 + 1024))
                      + (*(const f32x4*)(rd + sl * 4096 + 2048) + *(const f32x4*)(rd + sl * 4096 + 3072));
        const int t = 256 * tl + 16 * r + 4 * g;
        st8(rs, WS_X1T + (((size_t)(t >> 5) * D_ + d) * 32 + (t & 31)) * 2, conv_out(y, c, smem, t, xres[u]));
      }
    }
  }
  STAMP(1, 7);
}

__global__ NOPK __launch_bounds__(512, 2) void k2(const unsigned short* __restrict__ dskip, char* __restrict__ ws,
                                                  const unsigned short* __restrict__ w1, const unsigned short* __restrict__ w2) {
  __shared__ __attribute__((aligned(16))) char smem[CV_RT + 2 * (32 * RT_STR + 256)];
  conv_phase(dskip, ws, w1, w2, smem);
}

DEV u32x4 ld16_sc1(rsrc_t r, int voff, int soff) { return __builtin_amdgcn_raw_buffer_load_b128(r, voff, soff, 16); }
DEV u32x4 ld16(rsrc_t r, int voff, int soff) { return __builtin_amdgcn_raw_buffer_load_b128(r, voff, soff, 0); }
DEV u32x4 tr_frag(const char* p) {
  const u32x2 a = __builtin_bit_cast(u32x2, __builtin_amdgcn_ds_read_tr16_b64_v4i16((lds_s16x4*)p));
  const u32x2 b = __builtin_bit_cast(u32x2, __builtin_amdgcn_ds_read_tr16_b64_v4i16((lds_s16x4*)(p + 256)));
  return (u32x4){a[0], a[1], b[0], b[1]};
}

constexpr int KF_RS = 84;
constexpr int KF_RB = 8 * 16 * KF_RS * 4;
constexpr int KF_BIG = 2 * KF_RB;
#define NW2L_ 8
constexpr int NW2L = NW2L_;
constexpr int NW2R = 35 - NW2L;
constexpr int KF_W2L = KF_BIG;
constexpr int KF_TAB = KF_W2L + 8 * NW2L * 1024;
constexpr int KF_PS = KF_TAB + 320;
constexpr int KF_XR = KF_PS + 512;
constexpr int KF_CNT = KF_XR + 64 * 176;
constexpr int KF_LDS = KF_CNT + 16;
#define LOCK_MASK 0x55554
#define W2_START 10
#define W2_RATE 3
#define W2_INL 28
#define SIG_SPLIT 4
#define H1_B0 1
#define H1_BATCH 7
#define W2_MID 4
#define W1_PRE 10
#define W1_EARLY 3
#define STAMP_EXTRA 0
#define H1_MODE 0
#define H1_PRE 9

DEV void kf_body(char* __restrict__ ws, unsigned short* __restrict__ out, char* smem) {
  char* xs = smem;
  float* tab = (float*)(smem + KF_TAB);
  float* ps = (float*)(smem + KF_PS);
  char* xr = smem + KF_XR;
  unsigned* lcnt = (unsigned*)(smem + KF_CNT);
  float* cst = (float*)(smem + 81920);
  const int tid = threadIdx.x, lane = tid & 63;
  const int w = __builtin_amdgcn_readfirstlane(tid >> 6);
  const int bx = blockIdx.x;
#define KF_MAP 1
  const int ng = (bx & 1) * 4 + ((bx >> 3) & 3), mt = ((bx & 7) >> 1) * 8 + (bx >> 5);
  const int g = lane >> 4, i = lane & 15;
  char* stamp_ws = ws; (void)stamp_ws;
  STAMP(2, 0); STAMP(5, 0);
  const rsrc_t rs_ = ws_rsrc(ws);

  unsigned* flags = (unsigned*)(ws + WS_CNT) + mt * 128;
  const int ntl = ng < 6 ? 14 : 12;
  const int ctb = ng < 6 ? 14 * ng : 84 + 12 * (ng - 6);
  const bool two = w < ntl - 8;
  const bool full = w < 6;
  const int ks0 = w < 6 ? 7 * w : 42 + 6 * (w - 6);
  const int ct0 = ctb + (two ? 2 * w : (ntl - 8) + w);
  const int lo16 = lane * 16;
  const int w1s = (int)WS_W1F + ct0 * 1024;
  const int w2s = (int)WS_W2F + (ng * 5 * 54 + ks0) * 1024;

  u32x4 ring[32];
  const char* bb0 = xs + (8 * g + (i >> 2)) * 64 + ((g & 1) << 5) + (i & 3) * 8;
  const char* bb1 = xs + (8 * g + (i >> 2)) * 64 + (((g & 1) ^ 1) << 5) + (i & 3) * 8;
  auto w2_off = [&](int e) __attribute__((always_inline)) {
    const int s = e < 27 ? e / 4 : (e < 34 ? e - 27 : 6), c = e < 27 ? e % 4 : (e < 34 ? 4 : 3);
    const int sn = (s == 6 && !full) ? 5 : s;
    return w2s + (c * 54 + sn) * 1024;
  };

  const unsigned w2lb = (unsigned)(size_t)(smem + KF_W2L) + w * NW2L * 1024;
  auto w2_dma = [&](int n) __attribute__((always_inline)) {
    asm volatile("s_mov_b32 m0, %0\n\ts_nop 0\n\tbuffer_load_dwordx4 %1, %2, %3 offen lds"
                 :: "s"(w2lb + n * 1024), "v"(lo16), "s"(rs_), "s"(w2_off(NW2R + n)) : "memory");
  };

  auto gemm1 = [&](auto two_tag) __attribute__((always_inline)) {
    constexpr bool TWO = decltype(two_tag)::value;
    if (TWO) asm volatile("; two tiles" ::: "memory"); else asm volatile("; one tile" ::: "memory");
    u32x4 t[10];
#pragma unroll
    for (int it = 0; it < 10; ++it) t[it] = ld16(rs_, tid * 16, (int)WS_X1T + mt * 81920 + 8192 * it);
#pragma unroll
    for (int k = 0; k < W1_EARLY; ++k) {
      ring[2 * k] = ld16(rs_, lo16, w1s + k * 108 * 1024);
      if (TWO) ring[2 * k + 1] = ld16(rs_, lo16, w1s + (k * 108 + 1) * 1024);
    }
    SB();
    __builtin_amdgcn_s_barrier();
    SB();
#pragma unroll
    for (int k = W1_EARLY; k < W1_PRE; ++k) {
      ring[2 * k] = ld16(rs_, lo16, w1s + k * 108 * 1024);
      if (TWO) ring[2 * k + 1] = ld16(rs_, lo16, w1s + (k * 108 + 1) * 1024);
    }
    SB();
#pragma unroll
    for (int it = 0; it < 10; ++it) {
      const int c = tid + 512 * it;
      const int row = c >> 2;
      *(u32x4*)(xs + row * 64 + (((c & 3) ^ ((row >> 2) & 2)) << 4)) = t[it];
    }
    SB();
    if (tid == 0) *lcnt = 0u;
    ModP gbp;
    if (!TWO) gbp = mod_ld(ws, 5 * D_ + 80 * ng + min((w & 1) * 64 + lane, 79));
    SB();
    __syncthreads();
    STAMP(2, 1);
    {
      const int kb = (80 * ng) >> 5, tq = w & 3;
#pragma unroll
      for (int u = 0; u < 2; ++u) {
        const int kk = min(kb + 2 * (w >> 2) + u, 19);
        const u32x4 f = tr_frag(((tq & 1) ? bb1 : bb0) + (tq >> 1) * 40960 + kk * 2048);
        const unsigned dl = (unsigned)(32 * kk + 8 * g - 80 * ng);
        if (dl < 80u) *(u32x4*)(xr + (tq * 16 + i) * 176 + dl * 2) = f;
      }
    }
    const int hs = TWO ? 0 : (w & 1);
    const char* ba[4] = {bb0 + hs * 40960, bb1 + hs * 40960, bb0 + (hs ^ 1) * 40960, bb1 + (hs ^ 1) * 40960};
    const u32x4 ones = {0x3f803f80u, 0x3f803f80u, 0x3f803f80u, 0x3f803f80u};
    f32x4 acc[2][4];
#pragma unroll
    for (int c = 0; c < 2; ++c)
#pragma unroll
      for (int q = 0; q < 4; ++q) acc[c][q] = (f32x4){0.f, 0.f, 0.f, 0.f};
    u32x4 csp[20];
    const int cso = ((w & 1) * FF + 16 * ctb + 4 * min(lane, 4 * ntl - 1)) * 4;
    u32x4 bn[2][4];
#pragma unroll
    for (int q = 0; q < 4; ++q) bn[0][q] = tr_frag(ba[q]);
#pragma unroll
    for (int q = 0; q < 4; ++q) bn[1][q] = tr_frag(ba[q] + 2048);
#pragma unroll
    for (int kk = 0; kk < 20; ++kk) {
      if ((LOCK_MASK >> kk) & 1) { SB(); __builtin_amdgcn_s_barrier(); SB(); }
      auto gload = [&](int u) __attribute__((always_inline)) {
        if (kk + W1_PRE < 20) {
          const int kn = kk + W1_PRE;
          if (u == 2) ring[(2 * kn) % 32] = ld16(rs_, lo16, w1s + kn * 108 * 1024);
          if (u == 3 && TWO) ring[(2 * kn + 1) % 32] = ld16(rs_, lo16, w1s + (kn * 108 + 1) * 1024);
        }
        if (kk >= W2_START && u < W2_RATE) {
          const int n = W2_RATE * (kk - W2_START) + u;
          if (n < NW2L) w2_dma(n);
          else if (n < W2_INL) ring[8 + n - NW2L] = ld16(rs_, lo16, w2_off(n - NW2L));
        }
      };
      const u32x4 a0 = ring[(2 * kk) % 32], a1 = ring[(2 * kk + 1) % 32];
#pragma unroll
      for (int q = 0; q < 4; ++q) {
        const u32x4 bq = bn[kk & 1][q];
        acc[0][q] = mfma_bf16(a0, bq, acc[0][q]);
        if (TWO) acc[1][q] = mfma_bf16(a1, bq, acc[1][q]);
        else if (q < 2) {
          acc[1][2 * q] = mfma_bf16(ones, bq, acc[1][2 * q]);
          acc[1][2 * q + 1] = mfma_bf16(bq, bq, acc[1][2 * q + 1]);
        }
        SB();
        if (kk < 18) bn[kk & 1][q] = tr_frag(ba[q] + (kk + 2) * 2048);
        gload(q);
        SB();
      }
      if (!TWO) {
        if (kk < 10) {
#pragma unroll
          for (int u = 0; u < 2; ++u)
            csp[2 * kk + u] = ld16(rs_, cso, (int)WS_CSP + (2 * kk + u) * 2 * FF * 4);
        }
        if (kk == 14) {
          f32x4 t = __builtin_bit_cast(f32x4, csp[0]);
#pragma unroll
          for (int u = 1; u < 20; ++u) t += __builtin_bit_cast(f32x4, csp[u]);
          if (w >= 6 && lane < 56) *(f32x4*)(cst + (w & 1) * 224 + 4 * lane) = t;
        }
      }
      if (!TWO && kk == 12) { if ((w & 1) * 64 + lane < 80) tab[(w & 1) * 64 + lane] = mod_fin(gbp); }
      SB();
    }
    if (!TWO) {
#pragma unroll
      for (int h = 0; h < 2; ++h) {
        const f32x4 aq = acc[1][2 * h + 1];
        const float dq = (i & 2) ? ((i & 1) ? aq[3] : aq[2]) : ((i & 1) ? aq[1] : aq[0]);
        const int tk = (2 * hs + h) * 16 + i;
        if (g == (i >> 2)) {
          const float mean = acc[1][2 * h][0] * (1.f / D_);
          const float var = fmaxf(dq * (1.f / D_) - mean * mean, 0.f);
          const float sd = rbf(sqrtf(rbf(rbf(var) + 1e-5f)));
          const float rs = 1.f / sd;
          ps[tk] = rs;
          ps[64 + tk] = -rbf(mean) * rs;
        }
      }
    }
    __syncthreads();
    STAMP(2, 2);
    f32x4 csv[2], b1v[2];
#pragma unroll
    for (int c = 0; c < (TWO ? 2 : 1); ++c) {
      csv[c] = *(const f32x4*)(cst + 16 * (ct0 - ctb + c) + 4 * g);
      b1v[c] = *(const f32x4*)(cst + 224 + 16 * (ct0 - ctb + c) + 4 * g);
    }
#pragma unroll
    for (int qp = 0; qp < 2; ++qp) {
      u32x2 o[2][2];
#pragma unroll
      for (int h = 0; h < 2; ++h) {
        const int q = 2 * qp + h, tq = q ^ (2 * hs);
        const float rs = ps[tq * 16 + i], nm = ps[64 + tq * 16 + i];
#pragma unroll
        for (int c = 0; c < (TWO ? 2 : 1); ++c) {
          float v[4];
#pragma unroll
          for (int r = 0; r < 4; ++r) v[r] = fmaxf(fmaf(acc[c][q][r], rs, fmaf(nm, csv[c][r], b1v[c][r])), 0.f);
          o[c][h] = (u32x2){pk_bf(v[0], v[1]), pk_bf(v[2], v[3])};
        }
      }
      const int tl = ((2 * qp) ^ (2 * hs)) + (g & 1);
#pragma unroll
      for (int c = 0; c < (TWO ? 2 : 1); ++c) {
        const int ct = ct0 + c;
        const auto s0 = __builtin_amdgcn_permlane16_swap(o[c][0][0], o[c][1][0], false, false);
        const auto s1 = __builtin_amdgcn_permlane16_swap(o[c][0][1], o[c][1][1], false, false);
        const size_t off = (((size_t)(4 * mt + tl) * 54 + (ct >> 1)) * 64 + i + 16 * (2 * (ct & 1) + (g >> 1))) * 16;
#define H1_ST 16
        __builtin_amdgcn_raw_buffer_store_b128((u32x4){s0[0], s1[0], s0[1], s1[1]}, rs_, (int)(WS_H1F + off), 0, H1_ST);
      }
    }
    SB();
    STAMP(5, 1);
    __builtin_amdgcn_s_barrier();
    SB();
    constexpr int E0 = W2_INL - NW2L;
    auto signal = [&]() __attribute__((always_inline)) {
      if (lane == 0) {
        if (__hip_atomic_fetch_add(lcnt, 1u, __ATOMIC_RELAXED, __HIP_MEMORY_SCOPE_WORKGROUP) == 7u)
          __hip_atomic_store(flags + 16 * ng, 1u, __ATOMIC_RELAXED, __HIP_MEMORY_SCOPE_AGENT);
      }
    };
    if (w >= SIG_SPLIT) { asm volatile("s_waitcnt vmcnt(0)" ::: "memory"); signal(); }
    SB();
#pragma unroll
    for (int e = E0; e < NW2R; ++e) ring[(8 + e) % 32] = ld16(rs_, lo16, w2_off(e));
    SB();
    STAMP(5, 2);
    if (w < SIG_SPLIT) { asm volatile("s_waitcnt vmcnt(%0)" :: "n"(NW2R - E0) : "memory"); signal(); }
    SB();
  };
  if (two) gemm1(std::true_type{});
  else gemm1(std::false_type{});
  STAMP(2, 3);
  u32x4 wl[NW2L];
#pragma unroll
  for (int n = 0; n < NW2L; ++n) wl[n] = *(const u32x4*)(smem + KF_W2L + (w * NW2L + n) * 1024 + lo16);
  SB();
#pragma nounroll
  for (unsigned spin = 0; spin < (1u << 20); ++spin) {
    if (__hip_atomic_load(flags + 16 * w, __ATOMIC_RELAXED, __HIP_MEMORY_SCOPE_AGENT) != 0u) break;
    __builtin_amdgcn_s_sleep(1);
  }
  asm volatile("" ::: "memory");
  SB();
  STAMP(2, 4);

  const int h1s = (int)WS_H1F + (mt * 4 * 54 + ks0) * 1024;
  u32x4 hb[28];
  auto load_h1 = [&](int n) __attribute__((always_inline)) {
    const int q = n / 7, s = n % 7;
    const int sn = (s == 6 && !full) ? 5 : s;
    hb[n] = ld16_sc1(rs_, lo16, h1s + (q * 54 + sn) * 1024);
  };
#pragma unroll
  for (int n = 0; n < H1_PRE; ++n) {
    if (H1_BATCH && n % H1_BATCH == 0 && n >= H1_B0) { SB(); __builtin_amdgcn_s_barrier(); SB(); }
    load_h1(n);
  }
  SB();
  float* red = (float*)smem;
  float* rb_ = red + (w * 16 + i) * KF_RS + 4 * g;
  const int re = w * 40 + min(lane, 39);
  const int rtk = re / 20, rq4 = re % 20;
  const float* rr_ = red + rtk * KF_RS + 4 * rq4;
  f32x4 ac2[2][5];
  f32x4 rv[8], rsum;
  u32x2 xv2;
  const f32x4 gb = *(const f32x4*)(tab + 4 * rq4);
  auto red_wr = [&](int q, int c) __attribute__((always_inline)) {
    *(f32x4*)(rb_ + (q & 1) * (KF_RB / 4) + 16 * c) = ac2[q & 1][c];
  };
  auto red_rd = [&](int q, int ww) __attribute__((always_inline)) {
    rv[ww] = *(const f32x4*)(rr_ + (q & 1) * (KF_RB / 4) + ww * 16 * KF_RS);
  };
  auto red_x = [&](int q) __attribute__((always_inline)) { xv2 = *(const u32x2*)(xr + (16 * q + rtk) * 176 + rq4 * 8); };
  auto red_out = [&](int q) __attribute__((always_inline)) {
    const f32x4 s = rsum + (rv[6] + rv[7]);
    u32x2 o;
#pragma unroll
    for (int h = 0; h < 2; ++h) {
      unsigned f = pk_bf(s[2 * h], s[2 * h + 1]);
      unsigned gf = pk_bf(bf_lo(f) * gb[2 * h], bf_hi(f) * gb[2 * h + 1]);
      o[h] = pk_bf(bf_lo(xv2[h]) + bf_lo(gf), bf_hi(xv2[h]) + bf_hi(gf));
    }
    *(u32x2*)(out + (size_t)(mt * 64 + 16 * q + rtk) * D_ + ng * 80 + 4 * rq4) = o;
  };
#pragma unroll
  for (int q = 0; q < 3; ++q) {
#pragma unroll
    for (int c = 0; c < 5; ++c) ac2[q & 1][c] = (f32x4){0.f, 0.f, 0.f, 0.f};
#pragma unroll
    for (int s = 0; s < 7; ++s) {
      auto mm = [&](int c) __attribute__((always_inline)) {
        const u32x4 a = c == 4 ? wl[s] : ((s == 6 && c == 3) ? wl[7] : ring[(8 + 4 * s + c) % 32]);
        ac2[q & 1][c] = mfma_bf16(a, hb[7 * q + s], ac2[q & 1][c]);
      };
      if (s == 5 && q >= 2) {
        red_wr(q - 1, 4);
        SB();
        red_out(q - 2);
#pragma unroll
        for (int c = 0; c < 5; ++c) mm(c);
#pragma unroll
        for (int c = 0; c < 5; ++c) {
          __builtin_amdgcn_sched_group_barrier(0x008, 1, 0);
          __builtin_amdgcn_sched_group_barrier(0x002, 10, 0);
        }
        SB();
      } else if (s < 6) {
        mm(0); SB();
        if (q >= 1 && s >= 1) { red_wr(q - 1, s - 1); SB(); }
        mm(1); SB();
        if (q >= 2 && s < 4) { red_rd(q - 2, 2 * s); SB(); }
        mm(2); SB();
        if (q >= 2 && s < 4) { red_rd(q - 2, 2 * s + 1); SB(); }
        if (q >= 2 && s == 4) { red_x(q - 2); SB(); }
        mm(3); SB();
        if (q >= 2 && s >= 1 && s < 4) {
          const f32x4 t = rv[2 * s - 2] + rv[2 * s - 1];
          rsum = s == 1 ? t : rsum + t;
          asm volatile("" : "+v"(rsum));
          SB();
        }
        mm(4); SB();
      } else if (full) {
#pragma unroll
        for (int c = 0; c < 5; ++c) mm(c);
        SB();
      }
      if (7 * q + s + H1_PRE < 28) { load_h1(7 * q + s + H1_PRE); SB(); }
    }
    if (q == 0) STAMP(2, 5);
    if (q >= 1) bar_lds();
  }
  {
    auto m3 = [&](int c, int s) __attribute__((always_inline)) {
      const u32x4 a = c == 4 ? wl[s] : ((s == 6 && c == 3) ? wl[7] : ring[(8 + 4 * s + c) % 32]);
      ac2[1][c] = mfma_bf16(a, hb[21 + s], s == 0 ? (f32x4){0.f, 0.f, 0.f, 0.f} : ac2[1][c]);
    };
    auto sum6 = [&]() __attribute__((always_inline)) {
      rsum = ((rv[0] + rv[1]) + (rv[2] + rv[3])) + (rv[4] + rv[5]);
    };
    if (full) {
#pragma unroll
      for (int c = 0; c < 5; ++c) {
        const u32x4 a = c == 4 ? wl[6] : (c == 3 ? wl[7] : ring[(8 + 24 + c) % 32]);
        rv[c] = mfma_bf16(a, hb[27], (f32x4){0.f, 0.f, 0.f, 0.f});
      }
    } else {
#pragma unroll
      for (int c = 0; c < 5; ++c) rv[c] = (f32x4){0.f, 0.f, 0.f, 0.f};
    }
    f32x4 c0[5];
#pragma unroll
    for (int c = 0; c < 5; ++c) c0[c] = rv[c];
    SB();
    auto col = [&](int c, auto&& between) __attribute__((always_inline)) {
#pragma unroll
      for (int s = 0; s < 6; ++s) {
        const u32x4 a = c == 4 ? wl[s] : ring[(8 + 4 * s + c) % 32];
        c0[c] = mfma_bf16(a, hb[21 + s], c0[c]);
        SB();
        between(s);
        SB();
      }
    };
    (void)m3;
    col(0, [&](int s) __attribute__((always_inline)) { if (s < 5) red_wr(2, s); if (s < 4) { red_rd(1, 2 * s); red_rd(1, 2 * s + 1); } if (s == 4) red_x(1); });
    col(1, [&](int s) __attribute__((always_inline)) { if (s == 1) { sum6(); asm volatile("" : "+v"(rsum)); } if (s == 3) red_out(1); });
    bar_lds();
    STAMP(2, 6);
    auto wr3 = [&](int c) __attribute__((always_inline)) { *(f32x4*)(rb_ + (KF_RB / 4) + 16 * c) = c0[c]; };
    col(2, [&](int s) __attribute__((always_inline)) { if (s < 2) wr3(s); if (s >= 1 && s < 5) { red_rd(2, 2 * s - 2); red_rd(2, 2 * s - 1); } if (s == 5) red_x(2); });
    col(3, [&](int s) __attribute__((always_inline)) { if (s == 1) wr3(2); if (s == 2) { sum6(); asm volatile("" : "+v"(rsum)); } if (s == 4) red_out(2); });
    col(4, [&](int s) __attribute__((always_inline)) { if (s == 1) wr3(3); if (s == 2) red_x(3); });
    wr3(4);
    bar_lds();
#pragma unroll
    for (int ww = 0; ww < 8; ++ww) red_rd(3, ww);
    sum6();
    red_out(3);
  }
  STAMP(2, 7); STAMP(5, 7);
}

__global__ NOPK __launch_bounds__(512, 2) void kf(char* __restrict__ ws, unsigned short* __restrict__ out) {
  __shared__ __attribute__((aligned(16))) char smem[KF_LDS];
  kf_body(ws, out, smem);
}

__global__ void kend_() {}

extern "C" void kernel_launch(void* const* d_in, const int* in_sizes, int n_in,
                              void* d_out, int out_size, void* d_ws, size_t ws_size,
                              hipStream_t stream) {
  K1Args a;
  a.x = (const unsigned short*)d_in[0];
  a.are = (const unsigned short*)d_in[1];
  a.aim = (const unsigned short*)d_in[2];
  a.bre = (const unsigned short*)d_in[3];
  a.bim = (const unsigned short*)d_in[4];
  a.cre = (const unsigned short*)d_in[5];
  a.cim = (const unsigned short*)d_in[6];
  a.logdt = (const unsigned short*)d_in[7];
  a.w1 = (const unsigned short*)d_in[10];
  a.w2 = (const unsigned short*)d_in[11];
  a.c = (const unsigned short*)d_in[12];
  a.wmod = (const unsigned short*)d_in[13];
  a.ws = (char*)d_ws;
  a.dskip = (const unsigned short*)d_in[8];
  a.out = (const unsigned short*)d_out;
  k1<<<NB_PF + NB_MOD + NB_LN + NB_K, 256, 0, stream>>>(a);
  k2<<<256, 512, 0, stream>>>((const unsigned short*)d_in[8], (char*)d_ws, a.w1, a.w2);
  kf<<<256, 512, 0, stream>>>((char*)d_ws, (unsigned short*)d_out);
}
```

```cpp
#define PF 1
#include <hip/hip_runtime.h>
#include <hip/hip_bf16.h>
#include <stdint.h>
#include <type_traits>

typedef __attribute__((ext_vector_type(8))) __bf16 bf16x8;
typedef __attribute__((ext_vector_type(2))) __bf16 bf16x2;
typedef __attribute__((ext_vector_type(4))) float f32x4;
typedef __attribute__((ext_vector_type(2))) float f32x2;
typedef __attribute__((ext_vector_type(4))) unsigned u32x4;
typedef __attribute__((ext_vector_type(2))) unsigned u32x2;
typedef __attribute__((ext_vector_type(8))) _Float16 h16x8;
typedef __attribute__((ext_vector_type(2))) _Float16 h16x2;
typedef __attribute__((ext_vector_type(4))) short s16x4;
typedef __attribute__((address_space(3))) s16x4 lds_s16x4;

#if defined(__HIP_DEVICE_COMPILE__)
#define NOPK __attribute__((target("no-packed-fp32-ops")))
#else
#define NOPK
#endif
#define DEV __device__ __forceinline__

constexpr int L_ = 2048;
constexpr int D_ = 640;
constexpr int NS = 128;
constexpr int FF = 1728;
constexpr int KREV = 2304;

constexpr size_t WS_KSC  = 8192;
constexpr size_t WS_CNT  = 16384;
constexpr size_t WS_CSB  = 32768;
constexpr size_t WS_MOD  = 65536;
constexpr size_t WS_XNT  = 196608;
constexpr size_t WS_XT   = WS_XNT + (size_t)D_ * L_ * 2;
constexpr size_t WS_KREV = WS_XT + (size_t)D_ * L_ * 2;
constexpr size_t WS_W1F  = WS_KREV + (size_t)D_ * 2 * KREV * 2;
constexpr size_t WS_W2F  = WS_W1F + (size_t)D_ * FF * 2;
constexpr size_t WS_X1T  = WS_W2F + (size_t)D_ * FF * 2;
constexpr size_t WS_X1   = WS_X1T + (size_t)D_ * L_ * 2;
constexpr size_t WS_CSP  = WS_X1;
constexpr size_t WS_H1F  = WS_X1 + (size_t)D_ * L_ * 2;

#define STAMP(kid, k) do {} while (0)

DEV float bf_lo(unsigned u) { return __uint_as_float(u << 16); }
DEV float bf_hi(unsigned u) { return __uint_as_float(u & 0xffff0000u); }
DEV unsigned pk_bf(float a, float b) {
  f32x2 v = {a, b};
  bf16x2 r = __builtin_convertvector(v, bf16x2);
  return __builtin_bit_cast(unsigned, r);
}
DEV float rbf(float a) { return bf_lo(pk_bf(a, 0.f)); }
DEV float ldbf(const unsigned short* p) { return __uint_as_float(((unsigned)*p) << 16); }

typedef __amdgpu_buffer_rsrc_t rsrc_t;
DEV rsrc_t ws_rsrc(char* ws) { return __builtin_amdgcn_make_buffer_rsrc((void*)ws, 0, 0x7fffffff, 0x00020000); }
#define ST_AUX 16
DEV void st16(rsrc_t r, size_t off, u32x4 v) { __builtin_amdgcn_raw_buffer_store_b128(v, r, (int)off, 0, ST_AUX); }
#define LN_AUX 16
DEV void st16ln(rsrc_t r, size_t off, u32x4 v) { __builtin_amdgcn_raw_buffer_store_b128(v, r, (int)off, 0, LN_AUX); }
DEV void st8(rsrc_t r, size_t off, u32x2 v) { __builtin_amdgcn_raw_buffer_store_b64(v, r, (int)off, 0, 16); }

DEV u32x2 tr_read(unsigned lds_addr) {
  u32x2 r;
  asm volatile("ds_read_b64_tr_b16 %0, %1" : "=v"(r) : "v"(lds_addr) : "memory");
  return r;
}
DEV void bar_lds() { asm volatile("s_waitcnt lgkmcnt(0)\n\ts_barrier" ::: "memory"); }
DEV void lds_wait(const void* word, unsigned val) {
  unsigned tv, ts, cnt;
  asm volatile("s_mov_b32 %2, 0x100000\n"
               "1:\n\tds_read_b32 %0, %3\n\ts_waitcnt lgkmcnt(0)\n\tv_readfirstlane_b32 %1, %0\n\t"
               "s_cmp_ge_u32 %1, %4\n\ts_cbranch_scc1 2f\n\ts_sleep 1\n\ts_sub_u32 %2, %2, 1\n\t"
               "s_cmp_lg_u32 %2, 0\n\ts_cbranch_scc1 1b\n2:"
               : "=&v"(tv), "=&s"(ts), "=&s"(cnt) : "v"((unsigned)(size_t)word), "s"(val) : "memory", "scc");
}
DEV void lds_post(unsigned* word, int lane) {
  asm volatile("" ::: "memory");
  if (lane == 0) __hip_atomic_fetch_add(word, 1u, __ATOMIC_RELAXED, __HIP_MEMORY_SCOPE_WORKGROUP);
  asm volatile("" ::: "memory");
}
DEV void lgkm0() { asm volatile("s_waitcnt lgkmcnt(0)" ::: "memory"); __builtin_amdgcn_sched_barrier(0); }

DEV f32x4 mfma_bf16(u32x4 a, u32x4 b, f32x4 c) {
  return __builtin_amdgcn_mfma_f32_16x16x32_bf16(__builtin_bit_cast(bf16x8, a), __builtin_bit_cast(bf16x8, b), c, 0, 0, 0);
}

constexpr int NB_MOD = 120, NB_LN = 256, NB_W2 = 108, NB_K = 640;

struct K1Args {
  const unsigned short *x, *are, *aim, *bre, *bim, *cre, *cim, *logdt, *w1, *w2, *c, *wmod;
  const unsigned short *dskip, *out;
  char* ws;
};

constexpr int NB_PF = 8;
DEV void k1_prefetch(const K1Args& p, int b) {
  const int tid = threadIdx.x;
  unsigned long long pc;
  int o0, o1;
  asm volatile("s_getpc_b64 %0\n.Lpf_here%=:\n\ts_mov_b32 %1, _Z2k16K1Args - .Lpf_here%=\n\ts_mov_b32 %2, _Z5kend_v - .Lpf_here%="
               : "=s"(pc), "=s"(o0), "=s"(o1));
  const char* c0 = (const char*)(pc + (long long)o0);
  long n = o1 - o0;
  n = n < 0 ? 0 : (n > 98304 ? 98304 : n);
  unsigned acc = 0;
  {
    unsigned v[3];
#pragma unroll
    for (int it = 0; it < 3; ++it) {
      const long o = (long)tid * 128 + it * 32768;
      v[it] = o < n ? *(const __attribute__((address_space(1))) unsigned*)(c0 + o) : 0u;
    }
    acc = v[0] ^ v[1] ^ v[2];
  }
  asm volatile("" :: "v"(acc));
}

DEV void k1_mod(const K1Args& p, int b, char* smem) {
  const int tid = threadIdx.x;
  const int rg = b & 7, cgp = b >> 3;
  const int c32 = tid & 31, r8 = tid >> 5;
  float* sil = (float*)smem;
  float* red = (float*)(smem + 512);
  u32x4 w[10];
#pragma unroll
  for (int it = 0; it < 10; ++it)
    w[it] = *(const u32x4*)(p.wmod + (size_t)(rg * 80 + r8 + 8 * it) * 3840 + 256 * cgp + 8 * c32);
  if (tid < 80) {
    float cv = ldbf(p.c + rg * 80 + tid);
    sil[tid] = rbf(cv / (1.f + expf(-cv)));
  }
  if (tid >= 128 && tid < 132 && b < 64) ((unsigned*)(p.ws + WS_CNT))[(b * 4 + tid - 128) * 16] = 0u;
  __syncthreads();
  float acc[8];
#pragma unroll
  for (int j = 0; j < 8; ++j) acc[j] = 0.f;
#pragma unroll
  for (int it = 0; it < 10; ++it) {
    float s = sil[r8 + 8 * it];
#pragma unroll
    for (int q = 0; q < 4; ++q) {
      acc[2 * q] += s * bf_lo(w[it][q]);
      acc[2 * q + 1] += s * bf_hi(w[it][q]);
    }
  }
#pragma unroll
  for (int j = 0; j < 8; ++j) acc[j] += __shfl_xor(acc[j], 32, 64);
  if ((tid & 63) < 32) {
    *(f32x4*)(red + (tid >> 6) * 256 + 8 * c32) = (f32x4){acc[0], acc[1], acc[2], acc[3]};
    *(f32x4*)(red + (tid >> 6) * 256 + 8 * c32 + 4) = (f32x4){acc[4], acc[5], acc[6], acc[7]};
  }
  __syncthreads();
  ((float*)(p.ws + WS_MOD))[(256 * cgp + tid) * 8 + rg] = (red[tid] + red[256 + tid]) + (red[512 + tid] + red[768 + tid]);
}
struct ModP { f32x4 a, b; };
DEV ModP mod_ld(const char* ws, int idx) {
  const f32x4* mp = (const f32x4*)(ws + WS_MOD) + 2 * idx;
  ModP m; m.a = mp[0]; m.b = mp[1];
  return m;
}
DEV float mod_fin(const ModP& m) {
  return rbf(((m.a[0] + m.a[1]) + (m.a[2] + m.a[3])) + ((m.b[0] + m.b[1]) + (m.b[2] + m.b[3])));
}
DEV float mod_sum(const char* ws, int idx) { return mod_fin(mod_ld(ws, idx)); }
#define SB() __builtin_amdgcn_sched_barrier(0)

DEV void k1_ln(const K1Args& p, int b, char* smem) {
  constexpr int STR = 704;
  const int tid = threadIdx.x;
  const int tok = tid >> 3, sub = tid & 7;
  const int tile = (b & 7) * 8 + ((b >> 3) & 7), pass = b >> 6;
  const int hf = pass & 1;
  const bool norm = pass < 2;
  const int t0 = tile * 32;
  const unsigned short* xr = p.x + (size_t)(t0 + tok) * D_ + 8 * sub;
  u32x4 xv[5], xo[5];
#pragma unroll
  for (int m = 0; m < 5; ++m) xv[m] = *(const u32x4*)(xr + 320 * hf + 64 * m);
  if (norm) {
#pragma unroll
    for (int m = 0; m < 5; ++m) xo[m] = *(const u32x4*)(xr + 320 * (hf ^ 1) + 64 * m);
    float s = 0.f, v = 0.f;
    const bf16x2 one2 = __builtin_bit_cast(bf16x2, 0x3f803f80u);
#pragma unroll
    for (int m = 0; m < 10; ++m)
#pragma unroll
      for (int q = 0; q < 4; ++q) {
        const unsigned u = m < 5 ? xv[m][q] : xo[m - 5][q];
        const bf16x2 x2 = __builtin_bit_cast(bf16x2, u);
        s = __builtin_amdgcn_fdot2_f32_bf16(x2, one2, s, false);
        v = __builtin_amdgcn_fdot2_f32_bf16(x2, x2, v, false);
      }
#pragma unroll
    for (int o = 1; o < 8; o <<= 1) { s += __shfl_xor(s, o, 64); v += __shfl_xor(v, o, 64); }
    const float mean = s * (1.f / D_);
    const float var = fmaxf(v * (1.f / D_) - mean * mean, 0.f);
    const float sd = rbf(sqrtf(rbf(rbf(var) + 1e-5f)));
    const float rs = 1.f / sd;
    const float mb = rbf(mean);
#pragma unroll
    for (int m = 0; m < 5; ++m)
#pragma unroll
      for (int q = 0; q < 4; ++q) {
        unsigned d2 = pk_bf(bf_lo(xv[m][q]) - mb, bf_hi(xv[m][q]) - mb);
        xv[m][q] = pk_bf(bf_lo(d2) * rs, bf_hi(d2) * rs);
      }
  }
  const int lane = tid & 63, w = tid >> 6;
  const int g = lane >> 4, i = lane & 15;
  const rsrc_t rsrc = ws_rsrc(p.ws);
#pragma unroll
  for (int m = 0; m < 5; ++m) *(u32x4*)(smem + tok * STR + (sub + 8 * m) * 16) = xv[m];
  __syncthreads();
  const size_t dsto = (norm ? WS_XNT : WS_XT) + ((size_t)tile * D_ + 320 * hf) * 64;
#pragma unroll
  for (int it = 0; it < 5; ++it) {
    const int cb = w * 5 + it;
    const char* a = smem + (8 * g + (i >> 2)) * STR + cb * 32 + (i & 3) * 8;
    const u32x2 ra = __builtin_bit_cast(u32x2, __builtin_amdgcn_ds_read_tr16_b64_v4i16((lds_s16x4*)a));
    const u32x2 rb = __builtin_bit_cast(u32x2, __builtin_amdgcn_ds_read_tr16_b64_v4i16((lds_s16x4*)(a + 4 * STR)));
    st16ln(rsrc, dsto + (size_t)(cb * 16 + i) * 64 + 16 * g, (u32x4){ra[0], ra[1], rb[0], rb[1]});
  }
}

constexpr int RT_STR = 176;
template <int NF> struct RtRaw { u32x4 r[NF]; };
template <int NF>
DEV void rt_load(RtRaw<NF>& t, const unsigned short* W, int N, int kk, int ct0, int lane) {
#pragma unroll
  for (int j = 0; j < NF; ++j) {
    const int c = lane + 64 * j, row = c / (2 * NF), part = c % (2 * NF);
    t.r[j] = *(const u32x4*)(W + (size_t)(32 * kk + row) * N + 16 * ct0 + 8 * part);
  }
}
template <int NF>
DEV void rt_park(const RtRaw<NF>& t, char* scr, int lane) {
#pragma unroll
  for (int j = 0; j < NF; ++j) {
    const int c = lane + 64 * j, row = c / (2 * NF), part = c % (2 * NF);
    *(u32x4*)(scr + row * RT_STR + 16 * part) = t.r[j];
  }
}
DEV u32x4 rt_frag(const char* scr, int j, int lane) {
  const int g = lane >> 4, i = lane & 15;
  const char* p = scr + (8 * g + (i >> 2)) * RT_STR + 32 * j + (i & 3) * 8;
  const u32x2 a = __builtin_bit_cast(u32x2, __builtin_amdgcn_ds_read_tr16_b64_v4i16((lds_s16x4*)p));
  const u32x2 b = __builtin_bit_cast(u32x2, __builtin_amdgcn_ds_read_tr16_b64_v4i16((lds_s16x4*)(p + 4 * RT_STR)));
  return (u32x4){a[0], a[1], b[0], b[1]};
}
struct cf { float re, im; };
DEV cf cmul(cf a, cf b) {
  cf r;
  r.re = a.re * b.re - a.im * b.im;
  r.im = a.re * b.im + a.im * b.re;
  return r;
}
DEV cf cexp_(float a2, float br, float m, float m0) {
  float mag = __builtin_amdgcn_exp2f(a2 * (m - m0));
  float ph = __builtin_amdgcn_fractf(br * m);
  cf r; r.re = mag * __builtin_amdgcn_cosf(ph); r.im = mag * __builtin_amdgcn_sinf(ph);
  return r;
}
DEV unsigned pk_h(float a, float b) {
  f32x2 v = {a, b};
  h16x2 r = __builtin_convertvector(v, h16x2);
  return __builtin_bit_cast(unsigned, r);
}
DEV f32x4 mfma_f16(u32x4 a, u32x4 b, f32x4 c) {
  return __builtin_amdgcn_mfma_f32_16x16x32_f16(__builtin_bit_cast(h16x8, a), __builtin_bit_cast(h16x8, b), c, 0, 0, 0);
}
DEV void put2(char* p, cf a, cf b) {
  u32x2 o = {pk_h(a.re, a.im), pk_h(b.re, b.im)};
  *(u32x2*)p = o;
}

constexpr int KG_RS = 544;
DEV void k1_kgen(const K1Args& p, int d, char* smem) {
  constexpr int RS = KG_RS;
  const int tid = threadIdx.x;
  const int lane = tid & 63;
  const int w = __builtin_amdgcn_readfirstlane(tid >> 6);
  const int g = lane >> 4, i = lane & 15;
  char* stamp_ws = p.ws; (void)stamp_ws;
  const size_t po = (size_t)d * NS + 2 * lane;
  const unsigned rar = *(const unsigned*)(p.are + po), rai = *(const unsigned*)(p.aim + po);
  const unsigned rbr = *(const unsigned*)(p.bre + po), rbi = *(const unsigned*)(p.bim + po);
  const unsigned rcr = *(const unsigned*)(p.cre + po), rci = *(const unsigned*)(p.cim + po);
  const float ldt = ldbf(p.logdt + d);
  asm volatile("" ::: "memory");
  SB();
  const float dt = rbf(expf(ldt));
  STAMP(0, 1);
  float a2[2], bv[2], m0[2];
  a2[0] = (dt * bf_lo(rar)) * 1.44269504088896341f; a2[1] = (dt * bf_hi(rar)) * 1.44269504088896341f;
  bv[0] = (dt * bf_lo(rai)) * 0.15915494309189534f; bv[1] = (dt * bf_hi(rai)) * 0.15915494309189534f;
  m0[0] = a2[0] > 0.f ? 2048.f : 0.f; m0[1] = a2[1] > 0.f ? 2048.f : 0.f;
  char* my = smem + lane * 8;
  if (w == 0) {
    cf v[2], v8[2], e8[2];
    float bound = 0.f;
#pragma unroll
    for (int q = 0; q < 2; ++q) {
      const float dbr = dt * (q ? bf_hi(rbr) : bf_lo(rbr)), dbi = dt * (q ? bf_hi(rbi) : bf_lo(rbi));
      const float cr = q ? bf_hi(rcr) : bf_lo(rcr), ci = q ? bf_hi(rci) : bf_lo(rci);
      const float gsh = __builtin_amdgcn_exp2f(a2[q] * m0[q]);
      v[q].re = (cr * dbr - ci * dbi) * gsh; v[q].im = -(cr * dbi + ci * dbr) * gsh;
      bound += (fabsf(v[q].re) + fabsf(v[q].im)) * __builtin_amdgcn_exp2f(fmaxf(a2[q], 0.f) * 120.f);
      e8[q] = cexp_(a2[q], -bv[q], 8.f, 0.f);
      v8[q] = cexp_(a2[q], -bv[q], 64.f, 0.f);
    }
#pragma unroll
    for (int o = 1; o < 64; o <<= 1) bound += __shfl_xor(bound, o, 64);
    int ex = (int)((__float_as_uint(bound) >> 23) & 0xff);
    ex = ex < 20 ? 20 : (ex > 240 ? 240 : ex);
    const float sv = __uint_as_float((unsigned)(127 + 13 + 127 - ex) << 23);
    if (lane == 0) ((float*)(p.ws + WS_KSC))[d] = __uint_as_float((unsigned)(ex - 13) << 23);
#pragma unroll
    for (int q = 0; q < 2; ++q) { v[q].re *= sv; v[q].im *= sv; v8[q] = cmul(v[q], v8[q]); }
#pragma unroll
    for (int j = 0; j < 8; ++j) {
      put2(my + (41 + j) * RS, v[0], v[1]);
      put2(my + (49 + j) * RS, v8[0], v8[1]);
      if (j < 7) {
#pragma unroll
        for (int q = 0; q < 2; ++q) { v[q] = cmul(v[q], e8[q]); v8[q] = cmul(v8[q], e8[q]); }
      }
    }
  } else if (w == 1) {
    cf e1[2], u[2], u2[2];
#pragma unroll
    for (int q = 0; q < 2; ++q) {
      e1[q] = cexp_(a2[q], bv[q], 1.f, 0.f);
      u[q].re = __builtin_amdgcn_exp2f(-a2[q] * m0[q]); u[q].im = 0.f;
      u2[q] = cexp_(a2[q], bv[q], 128.f, m0[q]);
    }
#pragma unroll
    for (int r = 0; r < 8; ++r) {
      put2(my + r * RS, u[0], u[1]);
      put2(my + (8 + r) * RS, u2[0], u2[1]);
      if (r < 7) {
#pragma unroll
        for (int q = 0; q < 2; ++q) { u[q] = cmul(u[q], e1[q]); u2[q] = cmul(u2[q], e1[q]); }
      }
    }
  } else if (w == 2) {
    cf e2[2], u[2], u2[2];
#pragma unroll
    for (int q = 0; q < 2; ++q) {
      e2[q] = cexp_(a2[q], bv[q], 2.f, 0.f);
      u[q] = cexp_(a2[q], bv[q], 256.f, m0[q]);
      u2[q] = cexp_(a2[q], bv[q], 384.f, m0[q]);
    }
#pragma unroll
    for (int r = 0; r < 4; ++r) {
      put2(my + (16 + r) * RS, u[0], u[1]);
      put2(my + (20 + r) * RS, u2[0], u2[1]);
      if (r < 3) {
#pragma unroll
        for (int q = 0; q < 2; ++q) { u[q] = cmul(u[q], e2[q]); u2[q] = cmul(u2[q], e2[q]); }
      }
    }
    cf e4[2] = {cmul(e2[0], e2[0]), cmul(e2[1], e2[1])};
#pragma unroll
    for (int r = 0; r < 2; ++r) {
      cf c0 = cexp_(a2[0], bv[0], 512.f + 128.f * r, m0[0]), c1 = cexp_(a2[1], bv[1], 512.f + 128.f * r, m0[1]);
      put2(my + (24 + 2 * r) * RS, c0, c1);
      put2(my + (25 + 2 * r) * RS, cmul(c0, e4[0]), cmul(c1, e4[1]));
    }
  } else {
    cf e4[2], e128[2], u[2], u2[2];
#pragma unroll
    for (int q = 0; q < 2; ++q) {
      e4[q] = cexp_(a2[q], bv[q], 4.f, 0.f);
      e128[q] = cexp_(a2[q], bv[q], 128.f, 0.f);
      u[q] = cexp_(a2[q], bv[q], 1024.f, m0[q]);
      u2[q] = cexp_(a2[q], bv[q], 1536.f, m0[q]);
    }
#pragma unroll
    for (int r = 2; r < 4; ++r) {
      cf c0 = cexp_(a2[0], bv[0], 512.f + 128.f * r, m0[0]), c1 = cexp_(a2[1], bv[1], 512.f + 128.f * r, m0[1]);
      put2(my + (24 + 2 * r) * RS, c0, c1);
      put2(my + (25 + 2 * r) * RS, cmul(c0, e4[0]), cmul(c1, e4[1]));
    }
#pragma unroll
    for (int r = 0; r < 4; ++r) {
      put2(my + (32 + r) * RS, u[0], u[1]);
      put2(my + (36 + r) * RS, u2[0], u2[1]);
#pragma unroll
      for (int q = 0; q < 2; ++q) { if (r < 3) u[q] = cmul(u[q], e128[q]); u2[q] = cmul(u2[q], e128[q]); }
    }
    put2(my + 40 * RS, u2[0], u2[1]);
  }
  __syncthreads();
  STAMP(0, 2);
  _Float16* kh = (_Float16*)(smem + 57 * RS);
  if (w < 3) {
    const char* ua = smem + (16 * w + i) * RS + g * 16;
    const char* va = smem + (41 + i) * RS + g * 16;
    f32x4 acc = {0.f, 0.f, 0.f, 0.f};
#pragma unroll
    for (int ks = 0; ks < 8; ++ks)
      acc = mfma_f16(*(const u32x4*)(ua + ks * 64), *(const u32x4*)(va + ks * 64), acc);
    int idx0, st;
    if (w == 0) { idx0 = (g < 2 ? 4 * g : 120 + 4 * g) + 8 * i; st = 1; }
    else if (w == 1) { idx0 = g < 2 ? 256 + 64 * g + 4 * i : 384 + 64 * (g - 2) + 2 * i; st = 1; }
    else { idx0 = 512 + 64 * g + i; st = 16; }
#pragma unroll
    for (int rg = 0; rg < 4; ++rg) {
      int idx = idx0 + st * rg;
      if (w == 1 && g >= 2) idx = idx0 + 32 * (rg >> 1) + (rg & 1);
      if (w == 2 && g >= 2) idx = (g == 2 && rg == 0 && i == 0) ? 640 : 644;
      kh[idx] = (_Float16)acc[rg];
    }
  }
  __syncthreads();
  STAMP(0, 3);
  const unsigned short* ku = (const unsigned short*)kh;
  const size_t kout = WS_KREV + (size_t)d * KREV * 2;
  {
    const int l0 = 2040 - 8 * tid;
    u32x4 o;
    if (w < 2) {
      const int q = l0 >> 3;
      const unsigned a = ku[384 + q], b = ku[385 + q];
      const unsigned t = (q & 1) ? b : a;
      o[0] = b | (b << 16); o[1] = b | (t << 16); o[2] = a | (a << 16); o[3] = o[2];
    } else if (w == 2) {
      const int q = l0 >> 2;
      const unsigned a = ku[256 + q], b = ku[257 + q], c = ku[258 + q];
      o[0] = c | (c << 16); o[1] = b | (b << 16); o[2] = b | (a << 16); o[3] = a | (a << 16);
    } else {
      unsigned v[8];
#pragma unroll
      for (int e = 0; e < 8; ++e) {
        const int l = l0 + 7 - e;
        const int idx = l < 256 ? l : (int)(pk_bf((float)l, 0.f) & 0xffffu) - 17024;
        v[e] = ku[idx];
      }
#pragma unroll
      for (int q = 0; q < 4; ++q) o[q] = v[2 * q] | (v[2 * q + 1] << 16);
    }
    st16(ws_rsrc(p.ws), kout + 16 * tid, o);
  }
}

__global__ NOPK __launch_bounds__(256) void k1(K1Args p) {
  __shared__ __attribute__((aligned(16))) char smem[57 * KG_RS + 1312];
  int b = blockIdx.x;
  char* stamp_ws = p.ws; (void)stamp_ws;
  STAMP(0, 0);
  if (b < NB_PF) { k1_prefetch(p, b); STAMP(0, 7); return; }
  b -= NB_PF;
  if (b < NB_K) { k1_kgen(p, b, smem); STAMP(0, 7); return; }
  b -= NB_K;
#define K1_PRIO 0
  __builtin_amdgcn_s_setprio(K1_PRIO);
#define K1_DELAY 0
  if (K1_DELAY) { asm volatile("" :: "s"(p.ws), "s"(p.x)); __builtin_amdgcn_s_sleep(K1_DELAY); }
  if (b < NB_LN) {
    k1_ln(p, b, smem);
    STAMP(0, 7);
    return;
  }
  b -= NB_LN;
  k1_mod(p, b, smem);
  STAMP(0, 7);
}

constexpr int K2_CS = 4160;
constexpr int K2_KOFF = 4 * K2_CS;
constexpr int K2_XOFF = K2_CS;
constexpr int K2_CH = K2_KOFF + 4608;
constexpr int CV_RED = 3 * K2_CH;
constexpr int CV_RT2 = 86016;
constexpr int CV_SUM = CV_RT2 + 4 * (32 * 176 + 256);
constexpr int CV_XCH = CV_SUM + 2560;
constexpr int CV_FL = CV_RED + 20480;
constexpr int CV_RT = 151552;

struct Chan { float sa, ca1, ga, dsk, hs, hinv, yinv; };
struct ChanRaw { ModP msa, mca, mga; float kinv; unsigned short rds; };
DEV ChanRaw chan_ld(const unsigned short* dskip, const char* ws, int d) {
  ChanRaw c;
  c.rds = dskip[d];
  c.msa = mod_ld(ws, d); c.mca = mod_ld(ws, D_ + d); c.mga = mod_ld(ws, 2 * D_ + d);
  c.kinv = ((const float*)(ws + WS_KSC))[d];
  return c;
}
DEV Chan chan_fin(const ChanRaw& r) {
  Chan c;
  c.sa = mod_fin(r.msa); c.ca1 = rbf(1.f + mod_fin(r.mca)); c.ga = mod_fin(r.mga);
  c.dsk = __uint_as_float((unsigned)r.rds << 16);
  const float hb = 26.f * fabsf(c.ca1) + fabsf(c.sa);
  int ex = (int)((__float_as_uint(hb) >> 23) & 0xff);
  ex = ex < 20 ? 20 : (ex > 240 ? 240 : ex);
  c.hs = __uint_as_float((unsigned)(127 + 14 + 127 - ex) << 23);
  c.hinv = __uint_as_float((unsigned)(ex - 14) << 23);
  c.yinv = r.kinv * c.hinv;
  return c;
}
DEV size_t tl_off(int d, int t) { return (((size_t)(t >> 5) * D_ + d) * 32 + (t & 31)) * 2; }
struct StageRaw { u32x4 xa0, xa1, kv0, kv1; u32x2 xb; };
DEV StageRaw stage_ld(const char* ws, int d, int th) {
  StageRaw s;
  const unsigned short* krev = (const unsigned short*)(ws + WS_KREV) + (size_t)d * KREV;
  const size_t xo = tl_off(d, 16 * th);
  s.xa0 = *(const u32x4*)(ws + WS_XNT + xo); s.xa1 = *(const u32x4*)(ws + WS_XNT + xo + 16);
  s.xb = *(const u32x2*)(ws + WS_XNT + tl_off(d, min(16 * th + 16, L_ - 4)));
  s.kv0 = *(const u32x4*)(krev + 8 * th); s.kv1 = *(const u32x4*)(krev + 1024 + 8 * th);
  return s;
}
DEV void stage_wr(const StageRaw& s, const Chan& c, char* smem, int th) {
  constexpr int CS = K2_CS, KOFF = K2_KOFF;
  unsigned hh[10];
#pragma unroll
  for (int q = 0; q < 10; ++q) {
    unsigned u = q < 4 ? s.xa0[q] : (q < 8 ? s.xa1[q - 4] : s.xb[q - 8]);
    unsigned m = pk_bf(bf_lo(u) * c.ca1, bf_hi(u) * c.ca1);
    unsigned h = pk_bf(bf_lo(m) + c.sa, bf_hi(m) + c.sa);
    hh[q] = pk_h(bf_lo(h) * c.hs, bf_hi(h) * c.hs);
  }
  char* dst = smem + (2 * th + 2) * 16;
  unsigned s1[9];
#pragma unroll
  for (int q = 0; q < 9; ++q) s1[q] = __builtin_amdgcn_alignbit(hh[q + 1], hh[q], 16);
#pragma unroll
  for (int e = 0; e < 2; ++e) {
    *(u32x4*)(dst + 16 * e) = (u32x4){hh[4 * e], hh[4 * e + 1], hh[4 * e + 2], hh[4 * e + 3]};
    *(u32x4*)(dst + 16 * e + CS) = (u32x4){s1[4 * e], s1[4 * e + 1], s1[4 * e + 2], s1[4 * e + 3]};
    *(u32x4*)(dst + 16 * e + 2 * CS) = (u32x4){hh[4 * e + 1], hh[4 * e + 2], hh[4 * e + 3], hh[4 * e + 4]};
    *(u32x4*)(dst + 16 * e + 3 * CS) = (u32x4){s1[4 * e + 1], s1[4 * e + 2], s1[4 * e + 3], s1[4 * e + 4]};
  }
  if (th == 0) {
    u32x4 z = {0u, 0u, 0u, 0u};
    *(u32x4*)(smem) = z; *(u32x4*)(smem + CS) = z; *(u32x4*)(smem + 2 * CS) = z; *(u32x4*)(smem + 3 * CS) = z;
    *(u32x4*)(smem + 16) = z;
    *(u32x4*)(smem + CS + 16) = (u32x4){0u, 0u, 0u, hh[0] << 16};
    *(u32x4*)(smem + 2 * CS + 16) = (u32x4){0u, 0u, 0u, hh[0]};
    *(u32x4*)(smem + 3 * CS + 16) = (u32x4){0u, 0u, hh[0] << 16, s1[0]};
  }
  *(u32x4*)(smem + KOFF + 16 * th) = s.kv0;
  *(u32x4*)(smem + KOFF + 2048 + 16 * th) = s.kv1;
  if (th < 32) *(u32x4*)(smem + KOFF + 4096 + 16 * th) = (u32x4){0u, 0u, 0u, 0u};
}
DEV u32x2 conv_out(const f32x4& y, const Chan& c, const char* smem, int t, const u32x2& xres) {
  const u32x2 hv = *(const u32x2*)(smem + 2 * (t + 16));
  u32x2 o;
#pragma unroll
  for (int e = 0; e < 2; ++e) {
    const unsigned hu = hv[e];
    const h16x2 h2 = __builtin_bit_cast(h16x2, hu);
    const float h0 = (float)h2[0] * c.hinv, h1 = (float)h2[1] * c.hinv;
    unsigned yb = pk_bf(y[2 * e] * c.yinv, y[2 * e + 1] * c.yinv);
    unsigned hd = pk_bf(h0 * c.dsk, h1 * c.dsk);
    unsigned y2 = pk_bf(bf_lo(yb) + bf_lo(hd), bf_hi(yb) + bf_hi(hd));
    unsigned gy = pk_bf(bf_lo(y2) * c.ga, bf_hi(y2) * c.ga);
    o[e] = pk_bf(bf_lo(xres[e]) + bf_lo(gy), bf_hi(xres[e]) + bf_hi(gy));
  }
  return o;
}
#define K2_LOAD(buf, cc, u)                                                                                     \
  asm volatile("ds_read_b64 %0, %3 offset:%5\n\tds_read_b64 %1, %3 offset:%6\n\tds_read_b128 %2, %4 offset:%7"   \
               : "=&v"(Al[buf][u]), "=&v"(Ah[buf][u]), "=&v"(Bf[buf][u])                                          \
               : "v"(ap0), "v"(bp0), "i"(64 * (cc) + 512 * (u)), "i"(64 * (cc) + 512 * (u) + 8),                 \
                 "i"(64 * (cc) + 3584 - 512 * (u)))
template <int SET> DEV constexpr int half_tile(int j) {
  return SET == 0 ? (j == 0 ? 7 : (j == 1 ? 6 : 2)) : (j < 3 ? 5 - j : 4 - j);
}
template <int SET>
DEV void conv_half(unsigned ap0, unsigned bp0, char* redw) {
  constexpr int NU = SET == 0 ? 8 : 6, NT = SET == 0 ? 3 : 5;
  u32x2 Al[2][NU], Ah[2][NU];
  u32x4 Bf[2][NU];
#pragma unroll
  for (int u = 0; u < NU; ++u) K2_LOAD(0, 0, u);
  f32x4 acc[NT];
#pragma unroll
  for (int j = 0; j < NT; ++j) acc[j] = (f32x4){0.f, 0.f, 0.f, 0.f};
#pragma unroll
  for (int cc = 0; cc < 2; ++cc) {
    lgkm0();
    if (cc == 0) {
#pragma unroll
      for (int u = 0; u < NU; ++u) K2_LOAD(1, 1, u);
    }
#pragma unroll
    for (int sp = 0; sp < NU; ++sp)
#pragma unroll
      for (int j = 0; j < NT; ++j) {
        constexpr int dummy = 0; (void)dummy;
        const int q = half_tile<SET>(j);
        if (sp <= q) {
          const u32x4 A = {Al[cc][sp][0], Al[cc][sp][1], Ah[cc][sp][0], Ah[cc][sp][1]};
          acc[j] = mfma_f16(A, Bf[cc][q - sp], acc[j]);
        }
      }
    SB();
  }
#pragma unroll
  for (int j = 0; j < NT; ++j) *(f32x4*)(redw + j * 4096) = acc[j];
}

template <int NF>
DEV void w1_fin(const RtRaw<NF>& t, const ModP& mp, char* ws, int kk, int ct0, int lane, char* scr, float* sums) {
  const int g = lane >> 4, i = lane & 15;
  float* tb = (float*)(scr + 32 * RT_STR);
  const float mv = mod_fin(mp);
  tb[lane] = lane < 32 ? rbf(1.f + mv) : mv;
  rt_park<NF>(t, scr, lane);
  lgkm0();
  const f32x4 ca = *(const f32x4*)(tb + 8 * g), cb = *(const f32x4*)(tb + 8 * g + 4);
  const f32x4 sa = *(const f32x4*)(tb + 32 + 8 * g), sb = *(const f32x4*)(tb + 36 + 8 * g);
  const u32x4 sv = {pk_bf(sa[0], sa[1]), pk_bf(sa[2], sa[3]), pk_bf(sb[0], sb[1]), pk_bf(sb[2], sb[3])};
  const u32x4 ones = {0x3f803f80u, 0x3f803f80u, 0x3f803f80u, 0x3f803f80u};
  const f32x4 z = {0.f, 0.f, 0.f, 0.f};
  const rsrc_t rs = ws_rsrc(ws);
#pragma unroll
  for (int j = 0; j < NF; ++j) {
    const u32x4 f = rt_frag(scr, j, lane);
    const u32x4 sc = {pk_bf(bf_lo(f[0]) * ca[0], bf_hi(f[0]) * ca[1]), pk_bf(bf_lo(f[1]) * ca[2], bf_hi(f[1]) * ca[3]),
                      pk_bf(bf_lo(f[2]) * cb[0], bf_hi(f[2]) * cb[1]), pk_bf(bf_lo(f[3]) * cb[2], bf_hi(f[3]) * cb[3])};
    st16(rs, WS_W1F + ((size_t)(kk * 108 + ct0 + j) * 64 + lane) * 16, sc);
    const f32x4 cs = mfma_bf16(sc, ones, z), b1 = mfma_bf16(f, sv, z);
    if (i < 2) *(f32x4*)(sums + i * 80 + 16 * j + 4 * g) = i ? b1 : cs;
  }
}

DEV void conv_phase(const unsigned short* __restrict__ dskip, char* __restrict__ ws,
                    const unsigned short* __restrict__ w1, const unsigned short* __restrict__ w2, char* smem_all) {
  constexpr int CS = K2_CS, KOFF = K2_KOFF;
  const int tid = threadIdx.x, lane = tid & 63;
  const int w = __builtin_amdgcn_readfirstlane(tid >> 6);
  char* stamp_ws = ws; (void)stamp_ws;
  STAMP(1, 0);
  const int bx = blockIdx.x;
  const int pj = (bx & 7) * 16 + (bx >> 4), par = (bx >> 3) & 1;
  const int r = lane & 15, g = lane >> 4, th = tid & 127;
  const int s4 = (r + 1) & 3;
  const rsrc_t rs = ws_rsrc(ws);
  unsigned* fl = (unsigned*)(smem_all + CV_FL);
  if (tid < 6) fl[tid] = 0u;
  if (w < 4) {
    const int d = 5 * pj + 2 * par + (w >> 1), hw = w & 1;
    char* smem = smem_all + (w >> 1) * K2_CH;
    const StageRaw sr = stage_ld(ws, d, th);
    const ChanRaw cr = chan_ld(dskip, ws, d);
    u32x2 xres[4];
#pragma unroll
    for (int q = 0; q < 4; ++q)
      xres[q] = *(const u32x2*)(ws + WS_XT + tl_off(d, 256 * (4 * hw + q) + 16 * r + 4 * g));
    SB();
    const Chan c = chan_fin(cr);
    stage_wr(sr, c, smem, th);
    __syncthreads();
    STAMP(1, 2);
    f32x4 acc[8];
#pragma unroll
    for (int q = 0; q < 8; ++q) acc[q] = (f32x4){0.f, 0.f, 0.f, 0.f};
    {
      const unsigned ap0 = (unsigned)(size_t)(smem + s4 * CS + 2 * (128 * hw + 8 * g + (r + 1) - s4));
      const unsigned bp0 = (unsigned)(size_t)(smem + KOFF + 2 * (2032 + 128 * hw - 16 * r + 8 * g) - 3584);
      u32x2 Al[2][8], Ah[2][8];
      u32x4 Bf[2][8];
#pragma unroll
      for (int u = 0; u < 8; ++u) K2_LOAD(0, 0, u);
#pragma unroll
      for (int cc = 0; cc < 4; ++cc) {
        lgkm0();
#pragma unroll
        for (int q = 0; q < 8; ++q) {
          if (cc < 3) K2_LOAD((cc + 1) & 1, cc + 1, q);
#pragma unroll
          for (int sp = 0; sp <= q; ++sp) {
            const u32x4 A = {Al[cc & 1][sp][0], Al[cc & 1][sp][1], Ah[cc & 1][sp][0], Ah[cc & 1][sp][1]};
            acc[q] = mfma_f16(A, Bf[cc & 1][q - sp], acc[q]);
          }
          SB();
        }
      }
    }
    STAMP(1, 3);
    f32x4 keep[4];
    {
      f32x4* xw = (f32x4*)(smem_all + CV_XCH + w * 4096) + lane;
      if (hw) {
#pragma unroll
        for (int q = 0; q < 4; ++q) { xw[64 * q] = acc[q]; keep[q] = acc[4 + q]; }
      } else {
#pragma unroll
        for (int q = 0; q < 4; ++q) { xw[64 * q] = acc[4 + q]; keep[q] = acc[q]; }
      }
    }
    lds_post(fl + w, lane);
    lds_wait(fl + (w ^ 1), 1u);
    const f32x4* xrd = (const f32x4*)(smem_all + CV_XCH + (w ^ 1) * 4096) + lane;
#pragma unroll
    for (int qp = 0; qp < 2; ++qp) {
      u32x2 o[2];
#pragma unroll
      for (int h = 0; h < 2; ++h) {
        const int q = 2 * qp + h;
        o[h] = conv_out(keep[q] + xrd[64 * q], c, smem, 256 * (4 * hw + q) + 16 * r + 4 * g, xres[q]);
      }
      const auto s0 = __builtin_amdgcn_permlane16_swap(o[0][0], o[1][0], false, false);
      const auto s1 = __builtin_amdgcn_permlane16_swap(o[0][1], o[1][1], false, false);
      const int t = 256 * (4 * hw + 2 * qp + (g & 1)) + 16 * r + 8 * (g >> 1);
      st16(rs, WS_X1T + (((size_t)(t >> 6) * D_ + d) * 64 + (t & 63)) * 2, (u32x4){s0[0], s1[0], s0[1], s1[1]});
    }
  } else {
#define C_PRIO1 2
    __builtin_amdgcn_s_setprio(C_PRIO1);
    const int d = 5 * pj + 4, wq = w - 4;
    char* smem = smem_all + 2 * K2_CH;
    StageRaw sr;
    if (wq < 2) sr = stage_ld(ws, d, th);
    asm volatile("" ::: "memory");
    const ChanRaw cr = chan_ld(dskip, ws, d);
    const int tl0 = par ? (wq < 3 ? 5 - wq : 1) : (wq == 0 ? 7 : (wq == 1 ? 6 : 2)), tl1 = 0;
    const unsigned ap0 = (unsigned)(size_t)(smem + s4 * CS + 2 * (64 * wq + 8 * g + (r + 1) - s4));
    const unsigned bp0 = (unsigned)(size_t)(smem + KOFF + 2 * (2032 + 64 * wq - 16 * r + 8 * g) - 3584);
    char* redw = smem_all + CV_RED + wq * 1024 + lane * 16;
    u32x2 xres[2];
    xres[0] = *(const u32x2*)(ws + WS_XT + tl_off(d, 256 * tl0 + 16 * r + 4 * g));
    xres[1] = *(const u32x2*)(ws + WS_XT + tl_off(d, 256 * tl1 + 16 * r + 4 * g));
    SB();
    const Chan c = chan_fin(cr);
    if (wq < 2) {
      stage_wr(sr, c, smem, th);
    } else {
#define RT_DELAY 32
      __builtin_amdgcn_s_sleep(RT_DELAY);
    }
    const bool isw1 = bx < 110;
    const int b2 = min(max(bx - 110, 0), 107);
    const int jk = isw1 ? 4 * (bx / 22) + wq : b2 >> 1;
    const int jc = isw1 ? min(5 * (bx % 22), 103) : (b2 & 1) * 20 + 5 * wq;
    RtRaw<5> rt;
    rt_load<5>(rt, isw1 ? w1 : w2, isw1 ? FF : D_, jk, jc, lane);
    const ModP mp = mod_ld(ws, (lane < 32 ? 4 * D_ : 3 * D_ - 32) + 32 * min(jk, 19) + lane);
    SB();
    bar_lds();
    if (par) conv_half<1>(ap0, bp0, redw); else conv_half<0>(ap0, bp0, redw);
    lds_post(fl + 4, lane);
#define C_PRIO2 2
    __builtin_amdgcn_s_setprio(C_PRIO2);
    char* scr = smem_all + CV_RT2 + wq * (32 * RT_STR + 256);
    if (isw1) {
      float* sums = (float*)(smem_all + CV_SUM);
      w1_fin<5>(rt, mp, ws, jk, jc, lane, scr, sums + 160 * wq);
      asm volatile("" ::: "memory");
      unsigned old = 0u;
      if (lane == 0) old = __hip_atomic_fetch_add(fl + 5, 1u, __ATOMIC_RELAXED, __HIP_MEMORY_SCOPE_WORKGROUP);
      asm volatile("" ::: "memory");
      if (__builtin_amdgcn_readfirstlane(old) == 3u && lane < 40) {
        const f32x4* sp = (const f32x4*)sums + lane;
        const f32x4 v = (sp[0] + sp[40]) + (sp[80] + sp[120]);
        st16(rs, WS_CSP + ((size_t)(2 * (bx / 22) + lane / 20) * FF + 16 * jc + 4 * (lane % 20)) * 4, __builtin_bit_cast(u32x4, v));
      }
    } else if (bx < 218) {
      rt_park<5>(rt, scr, lane);
      lgkm0();
#pragma unroll
      for (int j = 0; j < 5; ++j) st16(rs, WS_W2F + ((size_t)((jc + j) * 54 + jk) * 64 + lane) * 16, rt_frag(scr, j, lane));
    }
    lds_wait(fl + 4, 4u);
    const char* rd = smem_all + CV_RED + lane * 16;
#pragma unroll
    for (int u = 0; u < 2; ++u) {
      const bool on = u == 0 ? (par || wq < 3) : (par && wq == 0);
      if (on) {
        const int sl = u == 0 ? wq : 4, tl = u == 0 ? tl0 : tl1;
        const f32x4 y = (*(const f32x4*)(rd + sl * 4096) + *(const f32x4*)(rd + sl * 4096 + 1024))
                      + (*(const f32x4*)(rd + sl * 4096 + 2048) + *(const f32x4*)(rd + sl * 4096 + 3072));
        const int t = 256 * tl + 16 * r + 4 * g;
        st8(rs, WS_X1T + (((size_t)(t >> 6) * D_ + d) * 64 + (t & 63)) * 2, conv_out(y, c, smem, t, xres[u]));
      }
    }
  }
  STAMP(1, 7);
}

__global__ NOPK __launch_bounds__(512, 2) void k2(const unsigned short* __restrict__ dskip, char* __restrict__ ws,
                                                  const unsigned short* __restrict__ w1, const unsigned short* __restrict__ w2) {
  __shared__ __attribute__((aligned(16))) char smem[CV_XCH + 16384];
  conv_phase(dskip, ws, w1, w2, smem);
}

DEV u32x4 ld16_sc1(rsrc_t r, int voff, int soff) { return __builtin_amdgcn_raw_buffer_load_b128(r, voff, soff, 16); }
DEV u32x4 ld16(rsrc_t r, int voff, int soff) { return __builtin_amdgcn_raw_buffer_load_b128(r, voff, soff, 0); }
DEV u32x4 tr_frag(const char* p) {
  const u32x2 a = __builtin_bit_cast(u32x2, __builtin_amdgcn_ds_read_tr16_b64_v4i16((lds_s16x4*)p));
  const u32x2 b = __builtin_bit_cast(u32x2, __builtin_amdgcn_ds_read_tr16_b64_v4i16((lds_s16x4*)(p + 256)));
  return (u32x4){a[0], a[1], b[0], b[1]};
}

constexpr int KF_RS = 84;
constexpr int KF_RB = 8 * 16 * KF_RS * 4;
constexpr int KF_BIG = 2 * KF_RB;
#define NW2L_ 8
constexpr int NW2L = NW2L_;
constexpr int NW2R = 35 - NW2L;
constexpr int KF_W2L = KF_BIG;
constexpr int KF_TAB = KF_W2L + 8 * NW2L * 1024;
constexpr int KF_PS = KF_TAB + 320;
constexpr int KF_XR = KF_PS + 512;
constexpr int KF_CNT = KF_XR + 64 * 176;
constexpr int KF_LDS = KF_CNT + 16;
#define LOCK_MASK 0x55554
#define CS_STEP 14
#define X_FIRST 5
#define W1_MID 7
#define X_STEP 4
#define W2_START 6
#define W2_RATE 2
#define W2_INL 28
#define SIG_SPLIT 4
#define H1_B0 1
#define H1_BATCH 7
#define W2_MID 4
#define W1_PRE 10
#define W1_EARLY 3
#define STAMP_EXTRA 0
#define H1_MODE 0
#define H1_PRE 9

DEV void kf_body(char* __restrict__ ws, unsigned short* __restrict__ out, char* smem) {
  char* xs = smem;
  float* tab = (float*)(smem + KF_TAB);
  float* ps = (float*)(smem + KF_PS);
  char* xr = smem + KF_XR;
  unsigned* lcnt = (unsigned*)(smem + KF_CNT);
  float* cst = (float*)(smem + 81920);
  const int tid = threadIdx.x, lane = tid & 63;
  const int w = __builtin_amdgcn_readfirstlane(tid >> 6);
  const int bx = blockIdx.x;
#define KF_MAP 1
  const int ng = (bx & 1) * 4 + ((bx >> 3) & 3), mt = ((bx & 7) >> 1) * 8 + (bx >> 5);
  const int g = lane >> 4, i = lane & 15;
  char* stamp_ws = ws; (void)stamp_ws;
  STAMP(2, 0); STAMP(5, 0);
  const rsrc_t rs_ = ws_rsrc(ws);

  unsigned* flags = (unsigned*)(ws + WS_CNT) + mt * 128;
  const int ntl = ng < 6 ? 14 : 12;
  const int ctb = ng < 6 ? 14 * ng : 84 + 12 * (ng - 6);
  const bool two = w < ntl - 8;
  const bool full = w < 6;
  const int ks0 = w < 6 ? 7 * w : 42 + 6 * (w - 6);
  const int ct0 = ctb + (two ? 2 * w : (ntl - 8) + w);
  const int lo16 = lane * 16;
  const int w1s = (int)WS_W1F + ct0 * 1024;
  const int w2s = (int)WS_W2F + (ng * 5 * 54 + ks0) * 1024;

  u32x4 ring[32], wl[NW2L];
  const char* bb0 = xs + (8 * g + (i >> 2)) * 64 + ((g & 1) << 5) + (i & 3) * 8;
  const char* bb1 = xs + (8 * g + (i >> 2)) * 64 + (((g & 1) ^ 1) << 5) + (i & 3) * 8;
  auto w2_off = [&](int e) __attribute__((always_inline)) {
    const int s = e < 27 ? e / 4 : (e < 34 ? e - 27 : 6), c = e < 27 ? e % 4 : (e < 34 ? 4 : 3);
    const int sn = (s == 6 && !full) ? 5 : s;
    return w2s + (c * 54 + sn) * 1024;
  };

  auto gemm1 = [&](auto two_tag) __attribute__((always_inline)) {
    constexpr bool TWO = decltype(two_tag)::value;
    if (TWO) asm volatile("; two tiles" ::: "memory"); else asm volatile("; one tile" ::: "memory");
    u32x4 t[10];
    auto st_x = [&](int i0, int i1) __attribute__((always_inline)) {
#pragma unroll
      for (int it = i0; it < i1; ++it) {
        const int c = tid + 512 * it;
        const int row = c >> 3;
        *(u32x4*)(xs + ((c >> 2) & 1) * 40960 + row * 64 + (((c & 3) ^ ((row >> 2) & 2)) << 4)) = t[it];
      }
    };
#pragma unroll
    for (int it = 0; it < X_FIRST; ++it) t[it] = ld16(rs_, tid * 16, (int)WS_X1T + mt * 81920 + 8192 * it);
#pragma unroll
    for (int k = 0; k < W1_EARLY; ++k) {
      ring[2 * k] = ld16(rs_, lo16, w1s + k * 108 * 1024);
      if (TWO) ring[2 * k + 1] = ld16(rs_, lo16, w1s + (k * 108 + 1) * 1024);
    }
    SB();
    __builtin_amdgcn_s_barrier();
    SB();
#pragma unroll
    for (int k = W1_EARLY; k < W1_MID; ++k) {
      ring[2 * k] = ld16(rs_, lo16, w1s + k * 108 * 1024);
      if (TWO) ring[2 * k + 1] = ld16(rs_, lo16, w1s + (k * 108 + 1) * 1024);
    }
    SB();
    st_x(0, X_FIRST);
    SB();
    if (tid == 0) *lcnt = 0u;
    ModP gbp;
    if (!TWO) gbp = mod_ld(ws, 5 * D_ + 80 * ng + min((w & 1) * 64 + lane, 79));
    SB();
    __syncthreads();
    STAMP(2, 1);
    auto resid = [&]() __attribute__((always_inline)) {
      const int kb = (80 * ng) >> 5, tq = w & 3;
#pragma unroll
      for (int u = 0; u < 2; ++u) {
        const int kk = min(kb + 2 * (w >> 2) + u, 19);
        const u32x4 f = tr_frag(((tq & 1) ? bb1 : bb0) + (tq >> 1) * 40960 + kk * 2048);
        const unsigned dl = (unsigned)(32 * kk + 8 * g - 80 * ng);
        if (dl < 80u) *(u32x4*)(xr + (tq * 16 + i) * 176 + dl * 2) = f;
      }
    };
    const int hs = TWO ? 0 : (w & 1);
    const char* ba[4] = {bb0 + hs * 40960, bb1 + hs * 40960, bb0 + (hs ^ 1) * 40960, bb1 + (hs ^ 1) * 40960};
    const u32x4 ones = {0x3f803f80u, 0x3f803f80u, 0x3f803f80u, 0x3f803f80u};
    f32x4 acc[2][4];
#pragma unroll
    for (int c = 0; c < 2; ++c)
#pragma unroll
      for (int q = 0; q < 4; ++q) acc[c][q] = (f32x4){0.f, 0.f, 0.f, 0.f};
    u32x4 csp[5];
    const int cso = ((w & 1) * FF + 16 * ctb + 4 * min(lane, 4 * ntl - 1)) * 4;
    u32x4 bn[2][4];
#pragma unroll
    for (int q = 0; q < 4; ++q) bn[0][q] = tr_frag(ba[q]);
#pragma unroll
    for (int q = 0; q < 4; ++q) bn[1][q] = tr_frag(ba[q] + 2048);
#pragma unroll
    for (int kk = 0; kk < 20; ++kk) {
      if (kk == X_STEP) { st_x(X_FIRST, 10); SB(); }
      if (kk == 2 * X_FIRST - 2) { SB(); bar_lds(); SB(); }
      else if ((LOCK_MASK >> kk) & 1) { SB(); __builtin_amdgcn_s_barrier(); SB(); }
      if (kk == 2 * X_FIRST) { resid(); SB(); }
      auto gload = [&](int u) __attribute__((always_inline)) {
        {
          constexpr int CATCH = W1_PRE - W1_MID;
          const int k0 = kk == 0 ? W1_MID : (kk <= CATCH ? W1_MID + 2 * kk - 1 : kk + W1_PRE);
          const int nk = (kk >= 1 && kk <= CATCH) ? 2 : 1;
          const int kn = k0 + (nk == 2 ? (u >> 1) : 0);
          const bool on = nk == 2 || u >= 2;
          if (on && kn < 20) {
            if ((u & 1) == 0) ring[(2 * kn) % 32] = ld16(rs_, lo16, w1s + kn * 108 * 1024);
            else if (TWO) ring[(2 * kn + 1) % 32] = ld16(rs_, lo16, w1s + (kn * 108 + 1) * 1024);
          }
          if (kk == 0 && u < 2) {
#pragma unroll
            for (int it = X_FIRST + u * 3; it < (u ? 10 : (X_FIRST + 3 < 10 ? X_FIRST + 3 : 10)); ++it)
              t[it] = ld16(rs_, tid * 16, (int)WS_X1T + mt * 81920 + 8192 * it);
          }
        }
        if (kk >= W2_START && u < W2_RATE) {
          const int n = W2_RATE * (kk - W2_START) + u;
          if (n < NW2L) wl[n] = ld16(rs_, lo16, w2_off(NW2R + n));
          else if (n < W2_INL) ring[8 + n - NW2L] = ld16(rs_, lo16, w2_off(n - NW2L));
        }
      };
      const u32x4 a0 = ring[(2 * kk) % 32], a1 = ring[(2 * kk + 1) % 32];
#pragma unroll
      for (int q = 0; q < 4; ++q) {
        const u32x4 bq = bn[kk & 1][q];
        acc[0][q] = mfma_bf16(a0, bq, acc[0][q]);
        if (TWO) acc[1][q] = mfma_bf16(a1, bq, acc[1][q]);
        else if (q < 2) {
          acc[1][2 * q] = mfma_bf16(ones, bq, acc[1][2 * q]);
          acc[1][2 * q + 1] = mfma_bf16(bq, bq, acc[1][2 * q + 1]);
        }
        SB();
        if (kk < 18) bn[kk & 1][q] = tr_frag(ba[q] + (kk + 2) * 2048);
        gload(q);
        SB();
      }
      if (!TWO) {
        if (kk < 5) csp[kk] = ld16(rs_, cso, (int)WS_CSP + kk * 2 * FF * 4);
        if (kk == CS_STEP) {
          f32x4 t = __builtin_bit_cast(f32x4, csp[0]);
#pragma unroll
          for (int u = 1; u < 5; ++u) t += __builtin_bit_cast(f32x4, csp[u]);
          if (w >= 6 && lane < 56) *(f32x4*)(cst + (w & 1) * 224 + 4 * lane) = t;
        }
      }
      if (!TWO && kk == 12) { if ((w & 1) * 64 + lane < 80) tab[(w & 1) * 64 + lane] = mod_fin(gbp); }
      SB();
    }
    if (!TWO) {
#pragma unroll
      for (int h = 0; h < 2; ++h) {
        const f32x4 aq = acc[1][2 * h + 1];
        const float dq = (i & 2) ? ((i & 1) ? aq[3] : aq[2]) : ((i & 1) ? aq[1] : aq[0]);
        const int tk = (2 * hs + h) * 16 + i;
        if (g == (i >> 2)) {
          const float mean = acc[1][2 * h][0] * (1.f / D_);
          const float var = fmaxf(dq * (1.f / D_) - mean * mean, 0.f);
          const float sd = rbf(sqrtf(rbf(rbf(var) + 1e-5f)));
          const float rs = 1.f / sd;
          ps[tk] = rs;
          ps[64 + tk] = -rbf(mean) * rs;
        }
      }
    }
    __syncthreads();
    STAMP(2, 2);
    f32x4 csv[2], b1v[2];
#pragma unroll
    for (int c = 0; c < (TWO ? 2 : 1); ++c) {
      csv[c] = *(const f32x4*)(cst + 16 * (ct0 - ctb + c) + 4 * g);
      b1v[c] = *(const f32x4*)(cst + 224 + 16 * (ct0 - ctb + c) + 4 * g);
    }
#pragma unroll
    for (int qp = 0; qp < 2; ++qp) {
      u32x2 o[2][2];
#pragma unroll
      for (int h = 0; h < 2; ++h) {
        const int q = 2 * qp + h, tq = q ^ (2 * hs);
        const float rs = ps[tq * 16 + i], nm = ps[64 + tq * 16 + i];
#pragma unroll
        for (int c = 0; c < (TWO ? 2 : 1); ++c) {
          float v[4];
#pragma unroll
          for (int r = 0; r < 4; ++r) v[r] = fmaxf(fmaf(acc[c][q][r], rs, fmaf(nm, csv[c][r], b1v[c][r])), 0.f);
          o[c][h] = (u32x2){pk_bf(v[0], v[1]), pk_bf(v[2], v[3])};
        }
      }
      const int tl = ((2 * qp) ^ (2 * hs)) + (g & 1);
#pragma unroll
      for (int c = 0; c < (TWO ? 2 : 1); ++c) {
        const int ct = ct0 + c;
        const auto s0 = __builtin_amdgcn_permlane16_swap(o[c][0][0], o[c][1][0], false, false);
        const auto s1 = __builtin_amdgcn_permlane16_swap(o[c][0][1], o[c][1][1], false, false);
        const size_t off = (((size_t)(4 * mt + tl) * 54 + (ct >> 1)) * 64 + i + 16 * (2 * (ct & 1) + (g >> 1))) * 16;
#define H1_ST 16
        __builtin_amdgcn_raw_buffer_store_b128((u32x4){s0[0], s1[0], s0[1], s1[1]}, rs_, (int)(WS_H1F + off), 0, H1_ST);
      }
    }
    SB();
    STAMP(5, 1);
    __builtin_amdgcn_s_barrier();
    SB();
    constexpr int E0 = W2_INL - NW2L;
    auto signal = [&]() __attribute__((always_inline)) {
      if (lane == 0) {
        if (__hip_atomic_fetch_add(lcnt, 1u, __ATOMIC_RELAXED, __HIP_MEMORY_SCOPE_WORKGROUP) == 7u)
          __hip_atomic_store(flags + 16 * ng, 1u, __ATOMIC_RELAXED, __HIP_MEMORY_SCOPE_AGENT);
      }
    };
    if (w >= SIG_SPLIT) { asm volatile("s_waitcnt vmcnt(0)" ::: "memory"); signal(); }
    SB();
#pragma unroll
    for (int e = E0; e < NW2R; ++e) ring[(8 + e) % 32] = ld16(rs_, lo16, w2_off(e));
    SB();
    STAMP(5, 2);
    if (w < SIG_SPLIT) { asm volatile("s_waitcnt vmcnt(%0)" :: "n"(NW2R - E0) : "memory"); signal(); }
    SB();
  };
  if (two) gemm1(std::true_type{});
  else gemm1(std::false_type{});
  STAMP(2, 3);
#pragma nounroll
  for (unsigned spin = 0; spin < (1u << 20); ++spin) {
    if (__hip_atomic_load(flags + 16 * w, __ATOMIC_RELAXED, __HIP_MEMORY_SCOPE_AGENT) != 0u) break;
    __builtin_amdgcn_s_sleep(1);
  }
  asm volatile("" ::: "memory");
  SB();
  STAMP(2, 4);

  const int h1s = (int)WS_H1F + (mt * 4 * 54 + ks0) * 1024;
  u32x4 hb[28];
  auto load_h1 = [&](int n) __attribute__((always_inline)) {
    const int q = n / 7, s = n % 7;
    const int sn = (s == 6 && !full) ? 5 : s;
    hb[n] = ld16_sc1(rs_, lo16, h1s + (q * 54 + sn) * 1024);
  };
#pragma unroll
  for (int n = 0; n < H1_PRE; ++n) {
    if (H1_BATCH && n % H1_BATCH == 0 && n >= H1_B0) { SB(); __builtin_amdgcn_s_barrier(); SB(); }
    load_h1(n);
  }
  SB();
  float* red = (float*)smem;
  float* rb_ = red + (w * 16 + i) * KF_RS + 4 * g;
  const int re = w * 40 + min(lane, 39);
  const int rtk = re / 20, rq4 = re % 20;
  const float* rr_ = red + rtk * KF_RS + 4 * rq4;
  f32x4 ac2[2][5];
  f32x4 rv[8], rsum;
  u32x2 xv2;
  const f32x4 gb = *(const f32x4*)(tab + 4 * rq4);
  auto red_wr = [&](int q, int c) __attribute__((always_inline)) {
    *(f32x4*)(rb_ + (q & 1) * (KF_RB / 4) + 16 * c) = ac2[q & 1][c];
  };
  auto red_rd = [&](int q, int ww) __attribute__((always_inline)) {
    rv[ww] = *(const f32x4*)(rr_ + (q & 1) * (KF_RB / 4) + ww * 16 * KF_RS);
  };
  auto red_x = [&](int q) __attribute__((always_inline)) { xv2 = *(const u32x2*)(xr + (16 * q + rtk) * 176 + rq4 * 8); };
  auto red_out = [&](int q) __attribute__((always_inline)) {
    const f32x4 s = rsum + (rv[6] + rv[7]);
    u32x2 o;
#pragma unroll
    for (int h = 0; h < 2; ++h) {
      unsigned f = pk_bf(s[2 * h], s[2 * h + 1]);
      unsigned gf = pk_bf(bf_lo(f) * gb[2 * h], bf_hi(f) * gb[2 * h + 1]);
      o[h] = pk_bf(bf_lo(xv2[h]) + bf_lo(gf), bf_hi(xv2[h]) + bf_hi(gf));
    }
    *(u32x2*)(out + (size_t)(mt * 64 + 16 * q + rtk) * D_ + ng * 80 + 4 * rq4) = o;
  };
#pragma unroll
  for (int q = 0; q < 3; ++q) {
#pragma unroll
    for (int c = 0; c < 5; ++c) ac2[q & 1][c] = (f32x4){0.f, 0.f, 0.f, 0.f};
#pragma unroll
    for (int s = 0; s < 7; ++s) {
      auto mm = [&](int c) __attribute__((always_inline)) {
        const u32x4 a = c == 4 ? wl[s] : ((s == 6 && c == 3) ? wl[7] : ring[(8 + 4 * s + c) % 32]);
        ac2[q & 1][c] = mfma_bf16(a, hb[7 * q + s], ac2[q & 1][c]);
      };
      if (s == 5 && q >= 2) {
        red_wr(q - 1, 4);
        SB();
        red_out(q - 2);
#pragma unroll
        for (int c = 0; c < 5; ++c) mm(c);
#pragma unroll
        for (int c = 0; c < 5; ++c) {
          __builtin_amdgcn_sched_group_barrier(0x008, 1, 0);
          __builtin_amdgcn_sched_group_barrier(0x002, 10, 0);
        }
        SB();
      } else if (s < 6) {
        mm(0); SB();
        if (q >= 1 && s >= 1) { red_wr(q - 1, s - 1); SB(); }
        mm(1); SB();
        if (q >= 2 && s < 4) { red_rd(q - 2, 2 * s); SB(); }
        mm(2); SB();
        if (q >= 2 && s < 4) { red_rd(q - 2, 2 * s + 1); SB(); }
        if (q >= 2 && s == 4) { red_x(q - 2); SB(); }
        mm(3); SB();
        if (q >= 2 && s >= 1 && s < 4) {
          const f32x4 t = rv[2 * s - 2] + rv[2 * s - 1];
          rsum = s == 1 ? t : rsum + t;
          asm volatile("" : "+v"(rsum));
          SB();
        }
        mm(4); SB();
      } else if (full) {
#pragma unroll
        for (int c = 0; c < 5; ++c) mm(c);
        SB();
      }
      if (7 * q + s + H1_PRE < 28) { load_h1(7 * q + s + H1_PRE); SB(); }
    }
    if (q == 0) STAMP(2, 5);
    if (q >= 1) bar_lds();
  }
  {
    auto m3 = [&](int c, int s) __attribute__((always_inline)) {
      const u32x4 a = c == 4 ? wl[s] : ((s == 6 && c == 3) ? wl[7] : ring[(8 + 4 * s + c) % 32]);
      ac2[1][c] = mfma_bf16(a, hb[21 + s], s == 0 ? (f32x4){0.f, 0.f, 0.f, 0.f} : ac2[1][c]);
    };
    auto sum6 = [&]() __attribute__((always_inline)) {
      rsum = ((rv[0] + rv[1]) + (rv[2] + rv[3])) + (rv[4] + rv[5]);
    };
    if (full) {
#pragma unroll
      for (int c = 0; c < 5; ++c) {
        const u32x4 a = c == 4 ? wl[6] : (c == 3 ? wl[7] : ring[(8 + 24 + c) % 32]);
        rv[c] = mfma_bf16(a, hb[27], (f32x4){0.f, 0.f, 0.f, 0.f});
      }
    } else {
#pragma unroll
      for (int c = 0; c < 5; ++c) rv[c] = (f32x4){0.f, 0.f, 0.f, 0.f};
    }
    f32x4 c0[5];
#pragma unroll
    for (int c = 0; c < 5; ++c) c0[c] = rv[c];
    SB();
    auto col = [&](int c, auto&& between) __attribute__((always_inline)) {
#pragma unroll
      for (int s = 0; s < 6; ++s) {
        const u32x4 a = c == 4 ? wl[s] : ring[(8 + 4 * s + c) % 32];
        c0[c] = mfma_bf16(a, hb[21 + s], c0[c]);
        SB();
        between(s);
        SB();
      }
    };
    (void)m3;
    col(0, [&](int s) __attribute__((always_inline)) { if (s < 5) red_wr(2, s); if (s < 4) { red_rd(1, 2 * s); red_rd(1, 2 * s + 1); } if (s == 4) red_x(1); });
    col(1, [&](int s) __attribute__((always_inline)) { if (s == 1) { sum6(); asm volatile("" : "+v"(rsum)); } if (s == 3) red_out(1); });
    bar_lds();
    STAMP(2, 6);
    auto wr3 = [&](int c) __attribute__((always_inline)) { *(f32x4*)(rb_ + (KF_RB / 4) + 16 * c) = c0[c]; };
    col(2, [&](int s) __attribute__((always_inline)) { if (s < 2) wr3(s); if (s >= 1 && s < 5) { red_rd(2, 2 * s - 2); red_rd(2, 2 * s - 1); } if (s == 5) red_x(2); });
    col(3, [&](int s) __attribute__((always_inline)) { if (s == 1) wr3(2); if (s == 2) { sum6(); asm volatile("" : "+v"(rsum)); } if (s == 4) red_out(2); });
    col(4, [&](int s) __attribute__((always_inline)) { if (s == 1) wr3(3); if (s == 2) red_x(3); });
    wr3(4);
    bar_lds();
#pragma unroll
    for (int ww = 0; ww < 8; ++ww) red_rd(3, ww);
    sum6();
    red_out(3);
  }
  STAMP(2, 7); STAMP(5, 7);
}

__global__ NOPK __launch_bounds__(512, 2) void kf(char* __restrict__ ws, unsigned short* __restrict__ out) {
  __shared__ __attribute__((aligned(16))) char smem[KF_LDS];
  kf_body(ws, out, smem);
}

__global__ void kend_() {}

extern "C" void kernel_launch(void* const* d_in, const int* in_sizes, int n_in,
                              void* d_out, int out_size, void* d_ws, size_t ws_size,
                              hipStream_t stream) {
  K1Args a;
  a.x = (const unsigned short*)d_in[0];
  a.are = (const unsigned short*)d_in[1];
  a.aim = (const unsigned short*)d_in[2];
  a.bre = (const unsigned short*)d_in[3];
  a.bim = (const unsigned short*)d_in[4];
  a.cre = (const unsigned short*)d_in[5];
  a.cim = (const unsigned short*)d_in[6];
  a.logdt = (const unsigned short*)d_in[7];
  a.w1 = (const unsigned short*)d_in[10];
  a.w2 = (const unsigned short*)d_in[11];
  a.c = (const unsigned short*)d_in[12];
  a.wmod = (const unsigned short*)d_in[13];
  a.ws = (char*)d_ws;
  a.dskip = (const unsigned short*)d_in[8];
  a.out = (const unsigned short*)d_out;
  k1<<<NB_PF + NB_MOD + NB_LN + NB_K, 256, 0, stream>>>(a);
  k2<<<256, 512, 0, stream>>>((const unsigned short*)d_in[8], (char*)d_ws, a.w1, a.w2);
  kf<<<256, 512, 0, stream>>>((char*)d_ws, (unsigned short*)d_out);
}
```

```cpp
#define K1_DELAY 4
#define PF 1
#include <hip/hip_runtime.h>
#include <hip/hip_bf16.h>
#include <stdint.h>
#include <type_traits>

typedef __attribute__((ext_vector_type(8))) __bf16 bf16x8;
typedef __attribute__((ext_vector_type(2))) __bf16 bf16x2;
typedef __attribute__((ext_vector_type(4))) float f32x4;
typedef __attribute__((ext_vector_type(2))) float f32x2;
typedef __attribute__((ext_vector_type(4))) unsigned u32x4;
typedef __attribute__((ext_vector_type(2))) unsigned u32x2;
typedef __attribute__((ext_vector_type(8))) _Float16 h16x8;
typedef __attribute__((ext_vector_type(2))) _Float16 h16x2;
typedef __attribute__((ext_vector_type(4))) short s16x4;
typedef __attribute__((address_space(3))) s16x4 lds_s16x4;

#if defined(__HIP_DEVICE_COMPILE__)
#define NOPK __attribute__((target("no-packed-fp32-ops")))
#else
#define NOPK
#endif
#define DEV __device__ __forceinline__

constexpr int L_ = 2048;
constexpr int D_ = 640;
constexpr int NS = 128;
constexpr int FF = 1728;
constexpr int KREV = 2304;

constexpr size_t WS_KSC  = 8192;
constexpr size_t WS_CNT  = 16384;
constexpr size_t WS_CSB  = 32768;
constexpr size_t WS_MOD  = 65536;
constexpr size_t WS_XNT  = 196608;
constexpr size_t WS_XT   = WS_XNT + (size_t)D_ * L_ * 2;
constexpr size_t WS_KREV = WS_XT + (size_t)D_ * L_ * 2;
constexpr size_t WS_W1F  = WS_KREV + (size_t)D_ * 2 * KREV * 2;
constexpr size_t WS_W2F  = WS_W1F + (size_t)D_ * FF * 2;
constexpr size_t WS_X1T  = WS_W2F + (size_t)D_ * FF * 2;
constexpr size_t WS_X1   = WS_X1T + (size_t)D_ * L_ * 2;
constexpr size_t WS_CSP  = WS_X1;
constexpr size_t WS_H1F  = WS_X1 + (size_t)D_ * L_ * 2;

#define STAMP(kid, k) do {} while (0)

#define TS(k, wsp) do { if (blockIdx.x == 0 && threadIdx.x == 0) ((unsigned long long*)((wsp) + (200u << 20)))[k] = __builtin_amdgcn_s_memrealtime(); } while (0)
DEV float bf_lo(unsigned u) { return __uint_as_float(u << 16); }
DEV float bf_hi(unsigned u) { return __uint_as_float(u & 0xffff0000u); }
DEV unsigned pk_bf(float a, float b) {
  f32x2 v = {a, b};
  bf16x2 r = __builtin_convertvector(v, bf16x2);
  return __builtin_bit_cast(unsigned, r);
}
DEV float rbf(float a) { return bf_lo(pk_bf(a, 0.f)); }
DEV float ldbf(const unsigned short* p) { return __uint_as_float(((unsigned)*p) << 16); }

typedef __amdgpu_buffer_rsrc_t rsrc_t;
DEV rsrc_t ws_rsrc(char* ws) { return __builtin_amdgcn_make_buffer_rsrc((void*)ws, 0, 0x7fffffff, 0x00020000); }
#define ST_AUX 16
DEV void st16(rsrc_t r, size_t off, u32x4 v) { __builtin_amdgcn_raw_buffer_store_b128(v, r, (int)off, 0, ST_AUX); }
#define LN_AUX 16
DEV void st16ln(rsrc_t r, size_t off, u32x4 v) { __builtin_amdgcn_raw_buffer_store_b128(v, r, (int)off, 0, LN_AUX); }
DEV void st8(rsrc_t r, size_t off, u32x2 v) { __builtin_amdgcn_raw_buffer_store_b64(v, r, (int)off, 0, 16); }

DEV u32x2 tr_read(unsigned lds_addr) {
  u32x2 r;
  asm volatile("ds_read_b64_tr_b16 %0, %1" : "=v"(r) : "v"(lds_addr) : "memory");
  return r;
}
DEV void bar_lds() { asm volatile("s_waitcnt lgkmcnt(0)\n\ts_barrier" ::: "memory"); }
DEV void lds_wait(const void* word, unsigned val) {
  unsigned tv, ts, cnt;
  asm volatile("s_mov_b32 %2, 0x100000\n"
               "1:\n\tds_read_b32 %0, %3\n\ts_waitcnt lgkmcnt(0)\n\tv_readfirstlane_b32 %1, %0\n\t"
               "s_cmp_ge_u32 %1, %4\n\ts_cbranch_scc1 2f\n\ts_sleep 1\n\ts_sub_u32 %2, %2, 1\n\t"
               "s_cmp_lg_u32 %2, 0\n\ts_cbranch_scc1 1b\n2:"
               : "=&v"(tv), "=&s"(ts), "=&s"(cnt) : "v"((unsigned)(size_t)word), "s"(val) : "memory", "scc");
}
DEV void lds_post(unsigned* word, int lane) {
  asm volatile("" ::: "memory");
  if (lane == 0) __hip_atomic_fetch_add(word, 1u, __ATOMIC_RELAXED, __HIP_MEMORY_SCOPE_WORKGROUP);
  asm volatile("" ::: "memory");
}
DEV void lgkm0() { asm volatile("s_waitcnt lgkmcnt(0)" ::: "memory"); __builtin_amdgcn_sched_barrier(0); }

DEV f32x4 mfma_bf16(u32x4 a, u32x4 b, f32x4 c) {
  return __builtin_amdgcn_mfma_f32_16x16x32_bf16(__builtin_bit_cast(bf16x8, a), __builtin_bit_cast(bf16x8, b), c, 0, 0, 0);
}

constexpr int NB_MOD = 120, NB_LN = 256, NB_W2 = 108, NB_K = 640;

struct K1Args {
  const unsigned short *x, *are, *aim, *bre, *bim, *cre, *cim, *logdt, *w1, *w2, *c, *wmod;
  const unsigned short *dskip, *out;
  char* ws;
};

constexpr int NB_PF = 8;
DEV void k1_prefetch(const K1Args& p, int b) {
  const int tid = threadIdx.x;
  unsigned long long pc;
  int o0, o1;
  asm volatile("s_getpc_b64 %0\n.Lpf_here%=:\n\ts_mov_b32 %1, _Z2k16K1Args - .Lpf_here%=\n\ts_mov_b32 %2, _Z5kend_v - .Lpf_here%="
               : "=s"(pc), "=s"(o0), "=s"(o1));
  const char* c0 = (const char*)(pc + (long long)o0);
  long n = o1 - o0;
  n = n < 0 ? 0 : (n > 98304 ? 98304 : n);
  unsigned acc = 0;
  {
    unsigned v[3];
#pragma unroll
    for (int it = 0; it < 3; ++it) {
      const long o = (long)tid * 128 + it * 32768;
      v[it] = o < n ? *(const __attribute__((address_space(1))) unsigned*)(c0 + o) : 0u;
    }
    acc = v[0] ^ v[1] ^ v[2];
  }
  asm volatile("" :: "v"(acc));
}

DEV void k1_mod(const K1Args& p, int b, char* smem) {
  const int tid = threadIdx.x;
  const int rg = b & 7, cgp = b >> 3;
  const int c32 = tid & 31, r8 = tid >> 5;
  float* sil = (float*)smem;
  float* red = (float*)(smem + 512);
  u32x4 w[10];
#pragma unroll
  for (int it = 0; it < 10; ++it)
    w[it] = *(const u32x4*)(p.wmod + (size_t)(rg * 80 + r8 + 8 * it) * 3840 + 256 * cgp + 8 * c32);
  if (tid < 80) {
    float cv = ldbf(p.c + rg * 80 + tid);
    sil[tid] = rbf(cv / (1.f + expf(-cv)));
  }
  if (tid >= 128 && tid < 132 && b < 64) ((unsigned*)(p.ws + WS_CNT))[(b * 4 + tid - 128) * 16] = 0u;
  __syncthreads();
  float acc[8];
#pragma unroll
  for (int j = 0; j < 8; ++j) acc[j] = 0.f;
#pragma unroll
  for (int it = 0; it < 10; ++it) {
    float s = sil[r8 + 8 * it];
#pragma unroll
    for (int q = 0; q < 4; ++q) {
      acc[2 * q] += s * bf_lo(w[it][q]);
      acc[2 * q + 1] += s * bf_hi(w[it][q]);
    }
  }
#pragma unroll
  for (int j = 0; j < 8; ++j) acc[j] += __shfl_xor(acc[j], 32, 64);
  if ((tid & 63) < 32) {
    *(f32x4*)(red + (tid >> 6) * 256 + 8 * c32) = (f32x4){acc[0], acc[1], acc[2], acc[3]};
    *(f32x4*)(red + (tid >> 6) * 256 + 8 * c32 + 4) = (f32x4){acc[4], acc[5], acc[6], acc[7]};
  }
  __syncthreads();
  ((float*)(p.ws + WS_MOD))[(256 * cgp + tid) * 8 + rg] = (red[tid] + red[256 + tid]) + (red[512 + tid] + red[768 + tid]);
}
struct ModP { f32x4 a, b; };
DEV ModP mod_ld(const char* ws, int idx) {
  const f32x4* mp = (const f32x4*)(ws + WS_MOD) + 2 * idx;
  ModP m; m.a = mp[0]; m.b = mp[1];
  return m;
}
DEV float mod_fin(const ModP& m) {
  return rbf(((m.a[0] + m.a[1]) + (m.a[2] + m.a[3])) + ((m.b[0] + m.b[1]) + (m.b[2] + m.b[3])));
}
DEV float mod_sum(const char* ws, int idx) { return mod_fin(mod_ld(ws, idx)); }
#define SB() __builtin_amdgcn_sched_barrier(0)

DEV void k1_ln(const K1Args& p, int b, char* smem) {
  constexpr int STR = 704;
  const int tid = threadIdx.x;
  const int tok = tid >> 3, sub = tid & 7;
  const int tile = (b & 7) * 8 + ((b >> 3) & 7), pass = b >> 6;
  const int hf = pass & 1;
  const bool norm = pass < 2;
  const int t0 = tile * 32;
  const unsigned short* xr = p.x + (size_t)(t0 + tok) * D_ + 8 * sub;
  u32x4 xv[5], xo[5];
#pragma unroll
  for (int m = 0; m < 5; ++m) xv[m] = *(const u32x4*)(xr + 320 * hf + 64 * m);
  if (norm) {
#pragma unroll
    for (int m = 0; m < 5; ++m) xo[m] = *(const u32x4*)(xr + 320 * (hf ^ 1) + 64 * m);
    float s = 0.f, v = 0.f;
    const bf16x2 one2 = __builtin_bit_cast(bf16x2, 0x3f803f80u);
#pragma unroll
    for (int m = 0; m < 10; ++m)
#pragma unroll
      for (int q = 0; q < 4; ++q) {
        const unsigned u = m < 5 ? xv[m][q] : xo[m - 5][q];
        const bf16x2 x2 = __builtin_bit_cast(bf16x2, u);
        s = __builtin_amdgcn_fdot2_f32_bf16(x2, one2, s, false);
        v = __builtin_amdgcn_fdot2_f32_bf16(x2, x2, v, false);
      }
#pragma unroll
    for (int o = 1; o < 8; o <<= 1) { s += __shfl_xor(s, o, 64); v += __shfl_xor(v, o, 64); }
    const float mean = s * (1.f / D_);
    const float var = fmaxf(v * (1.f / D_) - mean * mean, 0.f);
    const float sd = rbf(sqrtf(rbf(rbf(var) + 1e-5f)));
    const float rs = 1.f / sd;
    const float mb = rbf(mean);
#pragma unroll
    for (int m = 0; m < 5; ++m)
#pragma unroll
      for (int q = 0; q < 4; ++q) {
        unsigned d2 = pk_bf(bf_lo(xv[m][q]) - mb, bf_hi(xv[m][q]) - mb);
        xv[m][q] = pk_bf(bf_lo(d2) * rs, bf_hi(d2) * rs);
      }
  }
  const int lane = tid & 63, w = tid >> 6;
  const int g = lane >> 4, i = lane & 15;
  const rsrc_t rsrc = ws_rsrc(p.ws);
#pragma unroll
  for (int m = 0; m < 5; ++m) *(u32x4*)(smem + tok * STR + (sub + 8 * m) * 16) = xv[m];
  __syncthreads();
  const size_t dsto = (norm ? WS_XNT : WS_XT) + ((size_t)tile * D_ + 320 * hf) * 64;
#pragma unroll
  for (int it = 0; it < 5; ++it) {
    const int cb = w * 5 + it;
    const char* a = smem + (8 * g + (i >> 2)) * STR + cb * 32 + (i & 3) * 8;
    const u32x2 ra = __builtin_bit_cast(u32x2, __builtin_amdgcn_ds_read_tr16_b64_v4i16((lds_s16x4*)a));
    const u32x2 rb = __builtin_bit_cast(u32x2, __builtin_amdgcn_ds_read_tr16_b64_v4i16((lds_s16x4*)(a + 4 * STR)));
    st16ln(rsrc, dsto + (size_t)(cb * 16 + i) * 64 + 16 * g, (u32x4){ra[0], ra[1], rb[0], rb[1]});
  }
}

constexpr int RT_STR = 176;
template <int NF> struct RtRaw { u32x4 r[NF]; };
template <int NF>
DEV void rt_load(RtRaw<NF>& t, const unsigned short* W, int N, int kk, int ct0, int lane) {
#pragma unroll
  for (int j = 0; j < NF; ++j) {
    const int c = lane + 64 * j, row = c / (2 * NF), part = c % (2 * NF);
    t.r[j] = *(const u32x4*)(W + (size_t)(32 * kk + row) * N + 16 * ct0 + 8 * part);
  }
}
template <int NF>
DEV void rt_park(const RtRaw<NF>& t, char* scr, int lane) {
#pragma unroll
  for (int j = 0; j < NF; ++j) {
    const int c = lane + 64 * j, row = c / (2 * NF), part = c % (2 * NF);
    *(u32x4*)(scr + row * RT_STR + 16 * part) = t.r[j];
  }
}
DEV u32x4 rt_frag(const char* scr, int j, int lane) {
  const int g = lane >> 4, i = lane & 15;
  const char* p = scr + (8 * g + (i >> 2)) * RT_STR + 32 * j + (i & 3) * 8;
  const u32x2 a = __builtin_bit_cast(u32x2, __builtin_amdgcn_ds_read_tr16_b64_v4i16((lds_s16x4*)p));
  const u32x2 b = __builtin_bit_cast(u32x2, __builtin_amdgcn_ds_read_tr16_b64_v4i16((lds_s16x4*)(p + 4 * RT_STR)));
  return (u32x4){a[0], a[1], b[0], b[1]};
}
struct cf { float re, im; };
DEV cf cmul(cf a, cf b) {
  cf r;
  r.re = a.re * b.re - a.im * b.im;
  r.im = a.re * b.im + a.im * b.re;
  return r;
}
DEV cf cexp_(float a2, float br, float m, float m0) {
  float mag = __builtin_amdgcn_exp2f(a2 * (m - m0));
  float ph = __builtin_amdgcn_fractf(br * m);
  cf r; r.re = mag * __builtin_amdgcn_cosf(ph); r.im = mag * __builtin_amdgcn_sinf(ph);
  return r;
}
DEV unsigned pk_h(float a, float b) {
  f32x2 v = {a, b};
  h16x2 r = __builtin_convertvector(v, h16x2);
  return __builtin_bit_cast(unsigned, r);
}
DEV f32x4 mfma_f16(u32x4 a, u32x4 b, f32x4 c) {
  return __builtin_amdgcn_mfma_f32_16x16x32_f16(__builtin_bit_cast(h16x8, a), __builtin_bit_cast(h16x8, b), c, 0, 0, 0);
}
DEV void put2(char* p, cf a, cf b) {
  u32x2 o = {pk_h(a.re, a.im), pk_h(b.re, b.im)};
  *(u32x2*)p = o;
}

constexpr int KG_RS = 544;
DEV void k1_kgen(const K1Args& p, int d, char* smem) {
  constexpr int RS = KG_RS;
  const int tid = threadIdx.x;
  const int lane = tid & 63;
  const int w = __builtin_amdgcn_readfirstlane(tid >> 6);
  const int g = lane >> 4, i = lane & 15;
  char* stamp_ws = p.ws; (void)stamp_ws;
  const size_t po = (size_t)d * NS + 2 * lane;
  const unsigned rar = *(const unsigned*)(p.are + po), rai = *(const unsigned*)(p.aim + po);
  const unsigned rbr = *(const unsigned*)(p.bre + po), rbi = *(const unsigned*)(p.bim + po);
  const unsigned rcr = *(const unsigned*)(p.cre + po), rci = *(const unsigned*)(p.cim + po);
  const float ldt = ldbf(p.logdt + d);
  asm volatile("" ::: "memory");
  SB();
  const float dt = rbf(expf(ldt));
  STAMP(0, 1);
  float a2[2], bv[2], m0[2];
  a2[0] = (dt * bf_lo(rar)) * 1.44269504088896341f; a2[1] = (dt * bf_hi(rar)) * 1.44269504088896341f;
  bv[0] = (dt * bf_lo(rai)) * 0.15915494309189534f; bv[1] = (dt * bf_hi(rai)) * 0.15915494309189534f;
  m0[0] = a2[0] > 0.f ? 2048.f : 0.f; m0[1] = a2[1] > 0.f ? 2048.f : 0.f;
  char* my = smem + lane * 8;
  if (w == 0) {
    cf v[2], v8[2], e8[2];
    float bound = 0.f;
#pragma unroll
    for (int q = 0; q < 2; ++q) {
      const float dbr = dt * (q ? bf_hi(rbr) : bf_lo(rbr)), dbi = dt * (q ? bf_hi(rbi) : bf_lo(rbi));
      const float cr = q ? bf_hi(rcr) : bf_lo(rcr), ci = q ? bf_hi(rci) : bf_lo(rci);
      const float gsh = __builtin_amdgcn_exp2f(a2[q] * m0[q]);
      v[q].re = (cr * dbr - ci * dbi) * gsh; v[q].im = -(cr * dbi + ci * dbr) * gsh;
      bound += (fabsf(v[q].re) + fabsf(v[q].im)) * __builtin_amdgcn_exp2f(fmaxf(a2[q], 0.f) * 120.f);
      e8[q] = cexp_(a2[q], -bv[q], 8.f, 0.f);
      v8[q] = cexp_(a2[q], -bv[q], 64.f, 0.f);
    }
#pragma unroll
    for (int o = 1; o < 64; o <<= 1) bound += __shfl_xor(bound, o, 64);
    int ex = (int)((__float_as_uint(bound) >> 23) & 0xff);
    ex = ex < 20 ? 20 : (ex > 240 ? 240 : ex);
    const float sv = __uint_as_float((unsigned)(127 + 13 + 127 - ex) << 23);
    if (lane == 0) ((float*)(p.ws + WS_KSC))[d] = __uint_as_float((unsigned)(ex - 13) << 23);
#pragma unroll
    for (int q = 0; q < 2; ++q) { v[q].re *= sv; v[q].im *= sv; v8[q] = cmul(v[q], v8[q]); }
#pragma unroll
    for (int j = 0; j < 8; ++j) {
      put2(my + (41 + j) * RS, v[0], v[1]);
      put2(my + (49 + j) * RS, v8[0], v8[1]);
      if (j < 7) {
#pragma unroll
        for (int q = 0; q < 2; ++q) { v[q] = cmul(v[q], e8[q]); v8[q] = cmul(v8[q], e8[q]); }
      }
    }
  } else if (w == 1) {
    cf e1[2], u[2], u2[2];
#pragma unroll
    for (int q = 0; q < 2; ++q) {
      e1[q] = cexp_(a2[q], bv[q], 1.f, 0.f);
      u[q].re = __builtin_amdgcn_exp2f(-a2[q] * m0[q]); u[q].im = 0.f;
      u2[q] = cexp_(a2[q], bv[q], 128.f, m0[q]);
    }
#pragma unroll
    for (int r = 0; r < 8; ++r) {
      put2(my + r * RS, u[0], u[1]);
      put2(my + (8 + r) * RS, u2[0], u2[1]);
      if (r < 7) {
#pragma unroll
        for (int q = 0; q < 2; ++q) { u[q] = cmul(u[q], e1[q]); u2[q] = cmul(u2[q], e1[q]); }
      }
    }
  } else if (w == 2) {
    cf e2[2], u[2], u2[2];
#pragma unroll
    for (int q = 0; q < 2; ++q) {
      e2[q] = cexp_(a2[q], bv[q], 2.f, 0.f);
      u[q] = cexp_(a2[q], bv[q], 256.f, m0[q]);
      u2[q] = cexp_(a2[q], bv[q], 384.f, m0[q]);
    }
#pragma unroll
    for (int r = 0; r < 4; ++r) {
      put2(my + (16 + r) * RS, u[0], u[1]);
      put2(my + (20 + r) * RS, u2[0], u2[1]);
      if (r < 3) {
#pragma unroll
        for (int q = 0; q < 2; ++q) { u[q] = cmul(u[q], e2[q]); u2[q] = cmul(u2[q], e2[q]); }
      }
    }
    cf e4[2] = {cmul(e2[0], e2[0]), cmul(e2[1], e2[1])};
#pragma unroll
    for (int r = 0; r < 2; ++r) {
      cf c0 = cexp_(a2[0], bv[0], 512.f + 128.f * r, m0[0]), c1 = cexp_(a2[1], bv[1], 512.f + 128.f * r, m0[1]);
      put2(my + (24 + 2 * r) * RS, c0, c1);
      put2(my + (25 + 2 * r) * RS, cmul(c0, e4[0]), cmul(c1, e4[1]));
    }
  } else {
    cf e4[2], e128[2], u[2], u2[2];
#pragma unroll
    for (int q = 0; q < 2; ++q) {
      e4[q] = cexp_(a2[q], bv[q], 4.f, 0.f);
      e128[q] = cexp_(a2[q], bv[q], 128.f, 0.f);
      u[q] = cexp_(a2[q], bv[q], 1024.f, m0[q]);
      u2[q] = cexp_(a2[q], bv[q], 1536.f, m0[q]);
    }
#pragma unroll
    for (int r = 2; r < 4; ++r) {
      cf c0 = cexp_(a2[0], bv[0], 512.f + 128.f * r, m0[0]), c1 = cexp_(a2[1], bv[1], 512.f + 128.f * r, m0[1]);
      put2(my + (24 + 2 * r) * RS, c0, c1);
      put2(my + (25 + 2 * r) * RS, cmul(c0, e4[0]), cmul(c1, e4[1]));
    }
#pragma unroll
    for (int r = 0; r < 4; ++r) {
      put2(my + (32 + r) * RS, u[0], u[1]);
      put2(my + (36 + r) * RS, u2[0], u2[1]);
#pragma unroll
      for (int q = 0; q < 2; ++q) { if (r < 3) u[q] = cmul(u[q], e128[q]); u2[q] = cmul(u2[q], e128[q]); }
    }
    put2(my + 40 * RS, u2[0], u2[1]);
  }
  __syncthreads();
  STAMP(0, 2);
  _Float16* kh = (_Float16*)(smem + 57 * RS);
  if (w < 3) {
    const char* ua = smem + (16 * w + i) * RS + g * 16;
    const char* va = smem + (41 + i) * RS + g * 16;
    f32x4 acc = {0.f, 0.f, 0.f, 0.f};
#pragma unroll
    for (int ks = 0; ks < 8; ++ks)
      acc = mfma_f16(*(const u32x4*)(ua + ks * 64), *(const u32x4*)(va + ks * 64), acc);
    int idx0, st;
    if (w == 0) { idx0 = (g < 2 ? 4 * g : 120 + 4 * g) + 8 * i; st = 1; }
    else if (w == 1) { idx0 = g < 2 ? 256 + 64 * g + 4 * i : 384 + 64 * (g - 2) + 2 * i; st = 1; }
    else { idx0 = 512 + 64 * g + i; st = 16; }
#pragma unroll
    for (int rg = 0; rg < 4; ++rg) {
      int idx = idx0 + st * rg;
      if (w == 1 && g >= 2) idx = idx0 + 32 * (rg >> 1) + (rg & 1);
      if (w == 2 && g >= 2) idx = (g == 2 && rg == 0 && i == 0) ? 640 : 644;
      kh[idx] = (_Float16)acc[rg];
    }
  }
  __syncthreads();
  STAMP(0, 3);
  const unsigned short* ku = (const unsigned short*)kh;
  const size_t kout = WS_KREV + (size_t)d * KREV * 2;
  {
    const int l0 = 2040 - 8 * tid;
    u32x4 o;
    if (w < 2) {
      const int q = l0 >> 3;
      const unsigned a = ku[384 + q], b = ku[385 + q];
      const unsigned t = (q & 1) ? b : a;
      o[0] = b | (b << 16); o[1] = b | (t << 16); o[2] = a | (a << 16); o[3] = o[2];
    } else if (w == 2) {
      const int q = l0 >> 2;
      const unsigned a = ku[256 + q], b = ku[257 + q], c = ku[258 + q];
      o[0] = c | (c << 16); o[1] = b | (b << 16); o[2] = b | (a << 16); o[3] = a | (a << 16);
    } else {
      unsigned v[8];
#pragma unroll
      for (int e = 0; e < 8; ++e) {
        const int l = l0 + 7 - e;
        const int idx = l < 256 ? l : (int)(pk_bf((float)l, 0.f) & 0xffffu) - 17024;
        v[e] = ku[idx];
      }
#pragma unroll
      for (int q = 0; q < 4; ++q) o[q] = v[2 * q] | (v[2 * q + 1] << 16);
    }
    st16(ws_rsrc(p.ws), kout + 16 * tid, o);
  }
}

__global__ NOPK __launch_bounds__(256) void k1(K1Args p) {
  __shared__ __attribute__((aligned(16))) char smem[57 * KG_RS + 1312];
  int b = blockIdx.x;
  TS(0, p.ws);
  char* stamp_ws = p.ws; (void)stamp_ws;
  STAMP(0, 0);
  if (b < NB_PF) { k1_prefetch(p, b); STAMP(0, 7); return; }
  b -= NB_PF;
  if (b < NB_K) { k1_kgen(p, b, smem); STAMP(0, 7); return; }
  b -= NB_K;
#define K1_PRIO 0
  __builtin_amdgcn_s_setprio(K1_PRIO);
  if (K1_DELAY) { asm volatile("" :: "s"(p.ws), "s"(p.x)); __builtin_amdgcn_s_sleep(K1_DELAY); }
  if (b < NB_LN) {
    k1_ln(p, b, smem);
    STAMP(0, 7);
    return;
  }
  b -= NB_LN;
  k1_mod(p, b, smem);
  STAMP(0, 7);
}

constexpr int K2_CS = 4160;
constexpr int K2_KOFF = 4 * K2_CS;
constexpr int K2_XOFF = K2_CS;
constexpr int K2_CH = K2_KOFF + 4608;
constexpr int CV_RED = 3 * K2_CH;
constexpr int CV_RT2 = 86016;
constexpr int CV_SUM = CV_RT2 + 4 * (32 * 176 + 256);
constexpr int CV_XCH = CV_SUM + 2560;
constexpr int CV_FL = CV_RED + 20480;
constexpr int CV_RT = 151552;

struct Chan { float sa, ca1, ga, dsk, hs, hinv, yinv; };
struct ChanRaw { ModP msa, mca, mga; float kinv; unsigned short rds; };
DEV ChanRaw chan_ld(const unsigned short* dskip, const char* ws, int d) {
  ChanRaw c;
  c.rds = dskip[d];
  c.msa = mod_ld(ws, d); c.mca = mod_ld(ws, D_ + d); c.mga = mod_ld(ws, 2 * D_ + d);
  c.kinv = ((const float*)(ws + WS_KSC))[d];
  return c;
}
DEV Chan chan_fin(const ChanRaw& r) {
  Chan c;
  c.sa = mod_fin(r.msa); c.ca1 = rbf(1.f + mod_fin(r.mca)); c.ga = mod_fin(r.mga);
  c.dsk = __uint_as_float((unsigned)r.rds << 16);
  const float hb = 26.f * fabsf(c.ca1) + fabsf(c.sa);
  int ex = (int)((__float_as_uint(hb) >> 23) & 0xff);
  ex = ex < 20 ? 20 : (ex > 240 ? 240 : ex);
  c.hs = __uint_as_float((unsigned)(127 + 14 + 127 - ex) << 23);
  c.hinv = __uint_as_float((unsigned)(ex - 14) << 23);
  c.yinv = r.kinv * c.hinv;
  return c;
}
DEV size_t tl_off(int d, int t) { return (((size_t)(t >> 5) * D_ + d) * 32 + (t & 31)) * 2; }
struct StageRaw { u32x4 xa0, xa1, kv0, kv1; u32x2 xb; };
DEV StageRaw stage_ld(const char* ws, int d, int th) {
  StageRaw s;
  const unsigned short* krev = (const unsigned short*)(ws + WS_KREV) + (size_t)d * KREV;
  const size_t xo = tl_off(d, 16 * th);
  s.xa0 = *(const u32x4*)(ws + WS_XNT + xo); s.xa1 = *(const u32x4*)(ws + WS_XNT + xo + 16);
  s.xb = *(const u32x2*)(ws + WS_XNT + tl_off(d, min(16 * th + 16, L_ - 4)));
  s.kv0 = *(const u32x4*)(krev + 8 * th); s.kv1 = *(const u32x4*)(krev + 1024 + 8 * th);
  return s;
}
DEV void stage_wr(const StageRaw& s, const Chan& c, char* smem, int th) {
  constexpr int CS = K2_CS, KOFF = K2_KOFF;
  unsigned hh[10];
#pragma unroll
  for (int q = 0; q < 10; ++q) {
    unsigned u = q < 4 ? s.xa0[q] : (q < 8 ? s.xa1[q - 4] : s.xb[q - 8]);
    unsigned m = pk_bf(bf_lo(u) * c.ca1, bf_hi(u) * c.ca1);
    unsigned h = pk_bf(bf_lo(m) + c.sa, bf_hi(m) + c.sa);
    hh[q] = pk_h(bf_lo(h) * c.hs, bf_hi(h) * c.hs);
  }
  char* dst = smem + (2 * th + 2) * 16;
  unsigned s1[9];
#pragma unroll
  for (int q = 0; q < 9; ++q) s1[q] = __builtin_amdgcn_alignbit(hh[q + 1], hh[q], 16);
#pragma unroll
  for (int e = 0; e < 2; ++e) {
    *(u32x4*)(dst + 16 * e) = (u32x4){hh[4 * e], hh[4 * e + 1], hh[4 * e + 2], hh[4 * e + 3]};
    *(u32x4*)(dst + 16 * e + CS) = (u32x4){s1[4 * e], s1[4 * e + 1], s1[4 * e + 2], s1[4 * e + 3]};
    *(u32x4*)(dst + 16 * e + 2 * CS) = (u32x4){hh[4 * e + 1], hh[4 * e + 2], hh[4 * e + 3], hh[4 * e + 4]};
    *(u32x4*)(dst + 16 * e + 3 * CS) = (u32x4){s1[4 * e + 1], s1[4 * e + 2], s1[4 * e + 3], s1[4 * e + 4]};
  }
  if (th == 0) {
    u32x4 z = {0u, 0u, 0u, 0u};
    *(u32x4*)(smem) = z; *(u32x4*)(smem + CS) = z; *(u32x4*)(smem + 2 * CS) = z; *(u32x4*)(smem + 3 * CS) = z;
    *(u32x4*)(smem + 16) = z;
    *(u32x4*)(smem + CS + 16) = (u32x4){0u, 0u, 0u, hh[0] << 16};
    *(u32x4*)(smem + 2 * CS + 16) = (u32x4){0u, 0u, 0u, hh[0]};
    *(u32x4*)(smem + 3 * CS + 16) = (u32x4){0u, 0u, hh[0] << 16, s1[0]};
  }
  *(u32x4*)(smem + KOFF + 16 * th) = s.kv0;
  *(u32x4*)(smem + KOFF + 2048 + 16 * th) = s.kv1;
  if (th < 32) *(u32x4*)(smem + KOFF + 4096 + 16 * th) = (u32x4){0u, 0u, 0u, 0u};
}
DEV u32x2 conv_out(const f32x4& y, const Chan& c, const char* smem, int t, const u32x2& xres) {
  const u32x2 hv = *(const u32x2*)(smem + 2 * (t + 16));
  u32x2 o;
#pragma unroll
  for (int e = 0; e < 2; ++e) {
    const unsigned hu = hv[e];
    const h16x2 h2 = __builtin_bit_cast(h16x2, hu);
    const float h0 = (float)h2[0] * c.hinv, h1 = (float)h2[1] * c.hinv;
    unsigned yb = pk_bf(y[2 * e] * c.yinv, y[2 * e + 1] * c.yinv);
    unsigned hd = pk_bf(h0 * c.dsk, h1 * c.dsk);
    unsigned y2 = pk_bf(bf_lo(yb) + bf_lo(hd), bf_hi(yb) + bf_hi(hd));
    unsigned gy = pk_bf(bf_lo(y2) * c.ga, bf_hi(y2) * c.ga);
    o[e] = pk_bf(bf_lo(xres[e]) + bf_lo(gy), bf_hi(xres[e]) + bf_hi(gy));
  }
  return o;
}
#define K2_LOAD(buf, cc, u)                                                                                     \
  asm volatile("ds_read_b64 %0, %3 offset:%5\n\tds_read_b64 %1, %3 offset:%6\n\tds_read_b128 %2, %4 offset:%7"   \
               : "=&v"(Al[buf][u]), "=&v"(Ah[buf][u]), "=&v"(Bf[buf][u])                                          \
               : "v"(ap0), "v"(bp0), "i"(64 * (cc) + 512 * (u)), "i"(64 * (cc) + 512 * (u) + 8),                 \
                 "i"(64 * (cc) + 3584 - 512 * (u)))
template <int SET> DEV constexpr int half_tile(int j) {
  return SET == 0 ? (j == 0 ? 7 : (j == 1 ? 6 : 2)) : (j < 3 ? 5 - j : 4 - j);
}
template <int SET>
DEV void conv_half(unsigned ap0, unsigned bp0, char* redw) {
  constexpr int NU = SET == 0 ? 8 : 6, NT = SET == 0 ? 3 : 5;
  u32x2 Al[2][NU], Ah[2][NU];
  u32x4 Bf[2][NU];
#pragma unroll
  for (int u = 0; u < NU; ++u) K2_LOAD(0, 0, u);
  f32x4 acc[NT];
#pragma unroll
  for (int j = 0; j < NT; ++j) acc[j] = (f32x4){0.f, 0.f, 0.f, 0.f};
#pragma unroll
  for (int cc = 0; cc < 2; ++cc) {
    lgkm0();
    if (cc == 0) {
#pragma unroll
      for (int u = 0; u < NU; ++u) K2_LOAD(1, 1, u);
    }
#pragma unroll
    for (int sp = 0; sp < NU; ++sp)
#pragma unroll
      for (int j = 0; j < NT; ++j) {
        constexpr int dummy = 0; (void)dummy;
        const int q = half_tile<SET>(j);
        if (sp <= q) {
          const u32x4 A = {Al[cc][sp][0], Al[cc][sp][1], Ah[cc][sp][0], Ah[cc][sp][1]};
          acc[j] = mfma_f16(A, Bf[cc][q - sp], acc[j]);
        }
      }
    SB();
  }
#pragma unroll
  for (int j = 0; j < NT; ++j) *(f32x4*)(redw + j * 4096) = acc[j];
}

template <int NF>
DEV void w1_fin(const RtRaw<NF>& t, const ModP& mp, char* ws, int kk, int ct0, int lane, char* scr, float* sums) {
  const int g = lane >> 4, i = lane & 15;
  float* tb = (float*)(scr + 32 * RT_STR);
  const float mv = mod_fin(mp);
  tb[lane] = lane < 32 ? rbf(1.f + mv) : mv;
  rt_park<NF>(t, scr, lane);
  lgkm0();
  const f32x4 ca = *(const f32x4*)(tb + 8 * g), cb = *(const f32x4*)(tb + 8 * g + 4);
  const f32x4 sa = *(const f32x4*)(tb + 32 + 8 * g), sb = *(const f32x4*)(tb + 36 + 8 * g);
  const u32x4 sv = {pk_bf(sa[0], sa[1]), pk_bf(sa[2], sa[3]), pk_bf(sb[0], sb[1]), pk_bf(sb[2], sb[3])};
  const u32x4 ones = {0x3f803f80u, 0x3f803f80u, 0x3f803f80u, 0x3f803f80u};
  const f32x4 z = {0.f, 0.f, 0.f, 0.f};
  const rsrc_t rs = ws_rsrc(ws);
#pragma unroll
  for (int j = 0; j < NF; ++j) {
    const u32x4 f = rt_frag(scr, j, lane);
    const u32x4 sc = {pk_bf(bf_lo(f[0]) * ca[0], bf_hi(f[0]) * ca[1]), pk_bf(bf_lo(f[1]) * ca[2], bf_hi(f[1]) * ca[3]),
                      pk_bf(bf_lo(f[2]) * cb[0], bf_hi(f[2]) * cb[1]), pk_bf(bf_lo(f[3]) * cb[2], bf_hi(f[3]) * cb[3])};
    st16(rs, WS_W1F + ((size_t)(kk * 108 + ct0 + j) * 64 + lane) * 16, sc);
    const f32x4 cs = mfma_bf16(sc, ones, z), b1 = mfma_bf16(f, sv, z);
    if (i < 2) *(f32x4*)(sums + i * 80 + 16 * j + 4 * g) = i ? b1 : cs;
  }
}

DEV void conv_phase(const unsigned short* __restrict__ dskip, char* __restrict__ ws,
                    const unsigned short* __restrict__ w1, const unsigned short* __restrict__ w2, char* smem_all) {
  constexpr int CS = K2_CS, KOFF = K2_KOFF;
  const int tid = threadIdx.x, lane = tid & 63;
  const int w = __builtin_amdgcn_readfirstlane(tid >> 6);
  char* stamp_ws = ws; (void)stamp_ws;
  STAMP(1, 0);
  const int bx = blockIdx.x;
  const int pj = (bx & 7) * 16 + (bx >> 4), par = (bx >> 3) & 1;
  const int r = lane & 15, g = lane >> 4, th = tid & 127;
  const int s4 = (r + 1) & 3;
  const rsrc_t rs = ws_rsrc(ws);
  unsigned* fl = (unsigned*)(smem_all + CV_FL);
  if (tid < 6) fl[tid] = 0u;
  if (w < 4) {
    const int d = 5 * pj + 2 * par + (w >> 1), hw = w & 1;
    char* smem = smem_all + (w >> 1) * K2_CH;
    const StageRaw sr = stage_ld(ws, d, th);
    const ChanRaw cr = chan_ld(dskip, ws, d);
    u32x2 xres[4];
#pragma unroll
    for (int q = 0; q < 4; ++q)
      xres[q] = *(const u32x2*)(ws + WS_XT + tl_off(d, 256 * (4 * hw + q) + 16 * r + 4 * g));
    SB();
    const Chan c = chan_fin(cr);
    stage_wr(sr, c, smem, th);
    __syncthreads();
    STAMP(1, 2);
    f32x4 acc[8];
#pragma unroll
    for (int q = 0; q < 8; ++q) acc[q] = (f32x4){0.f, 0.f, 0.f, 0.f};
    {
      const unsigned ap0 = (unsigned)(size_t)(smem + s4 * CS + 2 * (128 * hw + 8 * g + (r + 1) - s4));
      const unsigned bp0 = (unsigned)(size_t)(smem + KOFF + 2 * (2032 + 128 * hw - 16 * r + 8 * g) - 3584);
      u32x2 Al[2][8], Ah[2][8];
      u32x4 Bf[2][8];
#pragma unroll
      for (int u = 0; u < 8; ++u) K2_LOAD(0, 0, u);
#pragma unroll
      for (int cc = 0; cc < 4; ++cc) {
        lgkm0();
#pragma unroll
        for (int q = 0; q < 8; ++q) {
          if (cc < 3) K2_LOAD((cc + 1) & 1, cc + 1, q);
#pragma unroll
          for (int sp = 0; sp <= q; ++sp) {
            const u32x4 A = {Al[cc & 1][sp][0], Al[cc & 1][sp][1], Ah[cc & 1][sp][0], Ah[cc & 1][sp][1]};
            acc[q] = mfma_f16(A, Bf[cc & 1][q - sp], acc[q]);
          }
          SB();
        }
      }
    }
    STAMP(1, 3);
    f32x4 keep[4];
    {
      f32x4* xw = (f32x4*)(smem_all + CV_XCH + w * 4096) + lane;
      if (hw) {
#pragma unroll
        for (int q = 0; q < 4; ++q) { xw[64 * q] = acc[q]; keep[q] = acc[4 + q]; }
      } else {
#pragma unroll
        for (int q = 0; q < 4; ++q) { xw[64 * q] = acc[4 + q]; keep[q] = acc[q]; }
      }
    }
    lds_post(fl + w, lane);
    lds_wait(fl + (w ^ 1), 1u);
    const f32x4* xrd = (const f32x4*)(smem_all + CV_XCH + (w ^ 1) * 4096) + lane;
#pragma unroll
    for (int qp = 0; qp < 2; ++qp) {
      u32x2 o[2];
#pragma unroll
      for (int h = 0; h < 2; ++h) {
        const int q = 2 * qp + h;
        o[h] = conv_out(keep[q] + xrd[64 * q], c, smem, 256 * (4 * hw + q) + 16 * r + 4 * g, xres[q]);
      }
      const auto s0 = __builtin_amdgcn_permlane16_swap(o[0][0], o[1][0], false, false);
      const auto s1 = __builtin_amdgcn_permlane16_swap(o[0][1], o[1][1], false, false);
      const int t = 256 * (4 * hw + 2 * qp + (g & 1)) + 16 * r + 8 * (g >> 1);
      st16(rs, WS_X1T + (((size_t)(t >> 6) * D_ + d) * 64 + (t & 63)) * 2, (u32x4){s0[0], s1[0], s0[1], s1[1]});
    }
  } else {
#define C_PRIO1 2
    __builtin_amdgcn_s_setprio(C_PRIO1);
    const int d = 5 * pj + 4, wq = w - 4;
    char* smem = smem_all + 2 * K2_CH;
    StageRaw sr;
    if (wq < 2) sr = stage_ld(ws, d, th);
    asm volatile("" ::: "memory");
    const ChanRaw cr = chan_ld(dskip, ws, d);
    const int tl0 = par ? (wq < 3 ? 5 - wq : 1) : (wq == 0 ? 7 : (wq == 1 ? 6 : 2)), tl1 = 0;
    const unsigned ap0 = (unsigned)(size_t)(smem + s4 * CS + 2 * (64 * wq + 8 * g + (r + 1) - s4));
    const unsigned bp0 = (unsigned)(size_t)(smem + KOFF + 2 * (2032 + 64 * wq - 16 * r + 8 * g) - 3584);
    char* redw = smem_all + CV_RED + wq * 1024 + lane * 16;
    u32x2 xres[2];
    xres[0] = *(const u32x2*)(ws + WS_XT + tl_off(d, 256 * tl0 + 16 * r + 4 * g));
    xres[1] = *(const u32x2*)(ws + WS_XT + tl_off(d, 256 * tl1 + 16 * r + 4 * g));
    SB();
    const Chan c = chan_fin(cr);
    if (wq < 2) {
      stage_wr(sr, c, smem, th);
    } else {
#define RT_DELAY 32
      __builtin_amdgcn_s_sleep(RT_DELAY);
    }
    const bool isw1 = bx < 110;
    const int b2 = min(max(bx - 110, 0), 107);
    const int jk = isw1 ? 4 * (bx / 22) + wq : b2 >> 1;
    const int jc = isw1 ? min(5 * (bx % 22), 103) : (b2 & 1) * 20 + 5 * wq;
    RtRaw<5> rt;
    rt_load<5>(rt, isw1 ? w1 : w2, isw1 ? FF : D_, jk, jc, lane);
    const ModP mp = mod_ld(ws, (lane < 32 ? 4 * D_ : 3 * D_ - 32) + 32 * min(jk, 19) + lane);
    SB();
    bar_lds();
    if (par) conv_half<1>(ap0, bp0, redw); else conv_half<0>(ap0, bp0, redw);
    lds_post(fl + 4, lane);
#define C_PRIO2 2
    __builtin_amdgcn_s_setprio(C_PRIO2);
    char* scr = smem_all + CV_RT2 + wq * (32 * RT_STR + 256);
    if (isw1) {
      float* sums = (float*)(smem_all + CV_SUM);
      w1_fin<5>(rt, mp, ws, jk, jc, lane, scr, sums + 160 * wq);
      asm volatile("" ::: "memory");
      unsigned old = 0u;
      if (lane == 0) old = __hip_atomic_fetch_add(fl + 5, 1u, __ATOMIC_RELAXED, __HIP_MEMORY_SCOPE_WORKGROUP);
      asm volatile("" ::: "memory");
      if (__builtin_amdgcn_readfirstlane(old) == 3u && lane < 40) {
        const f32x4* sp = (const f32x4*)sums + lane;
        const f32x4 v = (sp[0] + sp[40]) + (sp[80] + sp[120]);
        st16(rs, WS_CSP + ((size_t)(2 * (bx / 22) + lane / 20) * FF + 16 * jc + 4 * (lane % 20)) * 4, __builtin_bit_cast(u32x4, v));
      }
    } else if (bx < 218) {
      rt_park<5>(rt, scr, lane);
      lgkm0();
#pragma unroll
      for (int j = 0; j < 5; ++j) st16(rs, WS_W2F + ((size_t)((jc + j) * 54 + jk) * 64 + lane) * 16, rt_frag(scr, j, lane));
    }
    lds_wait(fl + 4, 4u);
    const char* rd = smem_all + CV_RED + lane * 16;
#pragma unroll
    for (int u = 0; u < 2; ++u) {
      const bool on = u == 0 ? (par || wq < 3) : (par && wq == 0);
      if (on) {
        const int sl = u == 0 ? wq : 4, tl = u == 0 ? tl0 : tl1;
        const f32x4 y = (*(const f32x4*)(rd + sl * 4096) + *(const f32x4*)(rd + sl * 4096 + 1024))
                      + (*(const f32x4*)(rd + sl * 4096 + 2048) + *(const f32x4*)(rd + sl * 4096 + 3072));
        const int t = 256 * tl + 16 * r + 4 * g;
        st8(rs, WS_X1T + (((size_t)(t >> 6) * D_ + d) * 64 + (t & 63)) * 2, conv_out(y, c, smem, t, xres[u]));
      }
    }
  }
  STAMP(1, 7);
}

__global__ NOPK __launch_bounds__(512, 2) void k2(const unsigned short* __restrict__ dskip, char* __restrict__ ws,
                                                  const unsigned short* __restrict__ w1, const unsigned short* __restrict__ w2) {
  __shared__ __attribute__((aligned(16))) char smem[CV_XCH + 16384];
  TS(1, ws);
  conv_phase(dskip, ws, w1, w2, smem);
}

DEV u32x4 ld16_sc1(rsrc_t r, int voff, int soff) { return __builtin_amdgcn_raw_buffer_load_b128(r, voff, soff, 16); }
DEV u32x4 ld16(rsrc_t r, int voff, int soff) { return __builtin_amdgcn_raw_buffer_load_b128(r, voff, soff, 0); }
DEV u32x4 tr_frag(const char* p) {
  const u32x2 a = __builtin_bit_cast(u32x2, __builtin_amdgcn_ds_read_tr16_b64_v4i16((lds_s16x4*)p));
  const u32x2 b = __builtin_bit_cast(u32x2, __builtin_amdgcn_ds_read_tr16_b64_v4i16((lds_s16x4*)(p + 256)));
  return (u32x4){a[0], a[1], b[0], b[1]};
}

constexpr int KF_RS = 84;
constexpr int KF_RB = 8 * 16 * KF_RS * 4;
constexpr int KF_BIG = 2 * KF_RB;
#define NW2L_ 8
constexpr int NW2L = NW2L_;
constexpr int NW2R = 35 - NW2L;
constexpr int KF_W2L = KF_BIG;
constexpr int KF_TAB = KF_W2L + 8 * NW2L * 1024;
constexpr int KF_PS = KF_TAB + 320;
constexpr int KF_XR = KF_PS + 512;
constexpr int KF_CNT = KF_XR + 64 * 176;
constexpr int KF_LDS = KF_CNT + 16;
#define LOCK_MASK 0x55554
#define CS_STEP 14
#define X_FIRST 5
#define W1_MID 7
#define X_STEP 4
#define W2_START 6
#define W2_RATE 2
#define W2_INL 28
#define SIG_SPLIT 4
#define H1_B0 1
#define H1_BATCH 7
#define W2_MID 4
#define W1_PRE 10
#define W1_EARLY 3
#define STAMP_EXTRA 0
#define H1_MODE 0
#define H1_PRE 9

DEV void kf_body(char* __restrict__ ws, unsigned short* __restrict__ out, char* smem) {
  char* xs = smem;
  float* tab = (float*)(smem + KF_TAB);
  float* ps = (float*)(smem + KF_PS);
  char* xr = smem + KF_XR;
  unsigned* lcnt = (unsigned*)(smem + KF_CNT);
  float* cst = (float*)(smem + 81920);
  const int tid = threadIdx.x, lane = tid & 63;
  const int w = __builtin_amdgcn_readfirstlane(tid >> 6);
  const int bx = blockIdx.x;
#define KF_MAP 1
  const int ng = (bx & 1) * 4 + ((bx >> 3) & 3), mt = ((bx & 7) >> 1) * 8 + (bx >> 5);
  const int g = lane >> 4, i = lane & 15;
  char* stamp_ws = ws; (void)stamp_ws;
  STAMP(2, 0); STAMP(5, 0);
  const rsrc_t rs_ = ws_rsrc(ws);

  unsigned* flags = (unsigned*)(ws + WS_CNT) + mt * 128;
  const int ntl = ng < 6 ? 14 : 12;
  const int ctb = ng < 6 ? 14 * ng : 84 + 12 * (ng - 6);
  const bool two = w < ntl - 8;
  const bool full = w < 6;
  const int ks0 = w < 6 ? 7 * w : 42 + 6 * (w - 6);
  const int ct0 = ctb + (two ? 2 * w : (ntl - 8) + w);
  const int lo16 = lane * 16;
  const int w1s = (int)WS_W1F + ct0 * 1024;
  const int w2s = (int)WS_W2F + (ng * 5 * 54 + ks0) * 1024;

  u32x4 ring[32], wl[NW2L];
  const char* bb0 = xs + (8 * g + (i >> 2)) * 64 + ((g & 1) << 5) + (i & 3) * 8;
  const char* bb1 = xs + (8 * g + (i >> 2)) * 64 + (((g & 1) ^ 1) << 5) + (i & 3) * 8;
  auto w2_off = [&](int e) __attribute__((always_inline)) {
    const int s = e < 27 ? e / 4 : (e < 34 ? e - 27 : 6), c = e < 27 ? e % 4 : (e < 34 ? 4 : 3);
    const int sn = (s == 6 && !full) ? 5 : s;
    return w2s + (c * 54 + sn) * 1024;
  };

  auto gemm1 = [&](auto two_tag) __attribute__((always_inline)) {
    constexpr bool TWO = decltype(two_tag)::value;
    if (TWO) asm volatile("; two tiles" ::: "memory"); else asm volatile("; one tile" ::: "memory");
    u32x4 t[10];
    auto st_x = [&](int i0, int i1) __attribute__((always_inline)) {
#pragma unroll
      for (int it = i0; it < i1; ++it) {
        const int c = tid + 512 * it;
        const int row = c >> 3;
        *(u32x4*)(xs + ((c >> 2) & 1) * 40960 + row * 64 + (((c & 3) ^ ((row >> 2) & 2)) << 4)) = t[it];
      }
    };
#pragma unroll
    for (int it = 0; it < X_FIRST; ++it) t[it] = ld16(rs_, tid * 16, (int)WS_X1T + mt * 81920 + 8192 * it);
#pragma unroll
    for (int k = 0; k < W1_EARLY; ++k) {
      ring[2 * k] = ld16(rs_, lo16, w1s + k * 108 * 1024);
      if (TWO) ring[2 * k + 1] = ld16(rs_, lo16, w1s + (k * 108 + 1) * 1024);
    }
    SB();
    __builtin_amdgcn_s_barrier();
    SB();
#pragma unroll
    for (int k = W1_EARLY; k < W1_MID; ++k) {
      ring[2 * k] = ld16(rs_, lo16, w1s + k * 108 * 1024);
      if (TWO) ring[2 * k + 1] = ld16(rs_, lo16, w1s + (k * 108 + 1) * 1024);
    }
    SB();
    st_x(0, X_FIRST);
    SB();
    if (tid == 0) *lcnt = 0u;
    ModP gbp;
    if (!TWO) gbp = mod_ld(ws, 5 * D_ + 80 * ng + min((w & 1) * 64 + lane, 79));
    SB();
    __syncthreads();
    STAMP(2, 1);
    auto resid = [&]() __attribute__((always_inline)) {
      const int kb = (80 * ng) >> 5, tq = w & 3;
#pragma unroll
      for (int u = 0; u < 2; ++u) {
        const int kk = min(kb + 2 * (w >> 2) + u, 19);
        const u32x4 f = tr_frag(((tq & 1) ? bb1 : bb0) + (tq >> 1) * 40960 + kk * 2048);
        const unsigned dl = (unsigned)(32 * kk + 8 * g - 80 * ng);
        if (dl < 80u) *(u32x4*)(xr + (tq * 16 + i) * 176 + dl * 2) = f;
      }
    };
    const int hs = TWO ? 0 : (w & 1);
    const char* ba[4] = {bb0 + hs * 40960, bb1 + hs * 40960, bb0 + (hs ^ 1) * 40960, bb1 + (hs ^ 1) * 40960};
    const u32x4 ones = {0x3f803f80u, 0x3f803f80u, 0x3f803f80u, 0x3f803f80u};
    f32x4 acc[2][4];
#pragma unroll
    for (int c = 0; c < 2; ++c)
#pragma unroll
      for (int q = 0; q < 4; ++q) acc[c][q] = (f32x4){0.f, 0.f, 0.f, 0.f};
    u32x4 csp[5];
    const int cso = ((w & 1) * FF + 16 * ctb + 4 * min(lane, 4 * ntl - 1)) * 4;
    u32x4 bn[2][4];
#pragma unroll
    for (int q = 0; q < 4; ++q) bn[0][q] = tr_frag(ba[q]);
#pragma unroll
    for (int q = 0; q < 4; ++q) bn[1][q] = tr_frag(ba[q] + 2048);
#pragma unroll
    for (int kk = 0; kk < 20; ++kk) {
      if (kk == X_STEP) { st_x(X_FIRST, 10); SB(); }
      if (kk == 2 * X_FIRST - 2) { SB(); bar_lds(); SB(); }
      else if ((LOCK_MASK >> kk) & 1) { SB(); __builtin_amdgcn_s_barrier(); SB(); }
      if (kk == 2 * X_FIRST) { resid(); SB(); }
      auto gload = [&](int u) __attribute__((always_inline)) {
        {
          constexpr int CATCH = W1_PRE - W1_MID;
          const int k0 = kk == 0 ? W1_MID : (kk <= CATCH ? W1_MID + 2 * kk - 1 : kk + W1_PRE);
          const int nk = (kk >= 1 && kk <= CATCH) ? 2 : 1;
          const int kn = k0 + (nk == 2 ? (u >> 1) : 0);
          const bool on = nk == 2 || u >= 2;
          if (on && kn < 20) {
            if ((u & 1) == 0) ring[(2 * kn) % 32] = ld16(rs_, lo16, w1s + kn * 108 * 1024);
            else if (TWO) ring[(2 * kn + 1) % 32] = ld16(rs_, lo16, w1s + (kn * 108 + 1) * 1024);
          }
          if (kk == 0 && u < 2) {
#pragma unroll
            for (int it = X_FIRST + u * 3; it < (u ? 10 : (X_FIRST + 3 < 10 ? X_FIRST + 3 : 10)); ++it)
              t[it] = ld16(rs_, tid * 16, (int)WS_X1T + mt * 81920 + 8192 * it);
          }
        }
        if (kk >= W2_START && u < W2_RATE) {
          const int n = W2_RATE * (kk - W2_START) + u;
          if (n < NW2L) wl[n] = ld16(rs_, lo16, w2_off(NW2R + n));
          else if (n < W2_INL) ring[8 + n - NW2L] = ld16(rs_, lo16, w2_off(n - NW2L));
        }
      };
      const u32x4 a0 = ring[(2 * kk) % 32], a1 = ring[(2 * kk + 1) % 32];
#pragma unroll
      for (int q = 0; q < 4; ++q) {
        const u32x4 bq = bn[kk & 1][q];
        acc[0][q] = mfma_bf16(a0, bq, acc[0][q]);
        if (TWO) acc[1][q] = mfma_bf16(a1, bq, acc[1][q]);
        else if (q < 2) {
          acc[1][2 * q] = mfma_bf16(ones, bq, acc[1][2 * q]);
          acc[1][2 * q + 1] = mfma_bf16(bq, bq, acc[1][2 * q + 1]);
        }
        SB();
        if (kk < 18) bn[kk & 1][q] = tr_frag(ba[q] + (kk + 2) * 2048);
        gload(q);
        SB();
      }
      if (!TWO) {
        if (kk < 5) csp[kk] = ld16(rs_, cso, (int)WS_CSP + kk * 2 * FF * 4);
        if (kk == CS_STEP) {
          f32x4 t = __builtin_bit_cast(f32x4, csp[0]);
#pragma unroll
          for (int u = 1; u < 5; ++u) t += __builtin_bit_cast(f32x4, csp[u]);
          if (w >= 6 && lane < 56) *(f32x4*)(cst + (w & 1) * 224 + 4 * lane) = t;
        }
      }
      if (!TWO && kk == 12) { if ((w & 1) * 64 + lane < 80) tab[(w & 1) * 64 + lane] = mod_fin(gbp); }
      SB();
    }
    if (!TWO) {
#pragma unroll
      for (int h = 0; h < 2; ++h) {
        const f32x4 aq = acc[1][2 * h + 1];
        const float dq = (i & 2) ? ((i & 1) ? aq[3] : aq[2]) : ((i & 1) ? aq[1] : aq[0]);
        const int tk = (2 * hs + h) * 16 + i;
        if (g == (i >> 2)) {
          const float mean = acc[1][2 * h][0] * (1.f / D_);
          const float var = fmaxf(dq * (1.f / D_) - mean * mean, 0.f);
          const float sd = rbf(sqrtf(rbf(rbf(var) + 1e-5f)));
          const float rs = 1.f / sd;
          ps[tk] = rs;
          ps[64 + tk] = -rbf(mean) * rs;
        }
      }
    }
    __syncthreads();
    STAMP(2, 2);
    f32x4 csv[2], b1v[2];
#pragma unroll
    for (int c = 0; c < (TWO ? 2 : 1); ++c) {
      csv[c] = *(const f32x4*)(cst + 16 * (ct0 - ctb + c) + 4 * g);
      b1v[c] = *(const f32x4*)(cst + 224 + 16 * (ct0 - ctb + c) + 4 * g);
    }
#pragma unroll
    for (int qp = 0; qp < 2; ++qp) {
      u32x2 o[2][2];
#pragma unroll
      for (int h = 0; h < 2; ++h) {
        const int q = 2 * qp + h, tq = q ^ (2 * hs);
        const float rs = ps[tq * 16 + i], nm = ps[64 + tq * 16 + i];
#pragma unroll
        for (int c = 0; c < (TWO ? 2 : 1); ++c) {
          float v[4];
#pragma unroll
          for (int r = 0; r < 4; ++r) v[r] = fmaxf(fmaf(acc[c][q][r], rs, fmaf(nm, csv[c][r], b1v[c][r])), 0.f);
          o[c][h] = (u32x2){pk_bf(v[0], v[1]), pk_bf(v[2], v[3])};
        }
      }
      const int tl = ((2 * qp) ^ (2 * hs)) + (g & 1);
#pragma unroll
      for (int c = 0; c < (TWO ? 2 : 1); ++c) {
        const int ct = ct0 + c;
        const auto s0 = __builtin_amdgcn_permlane16_swap(o[c][0][0], o[c][1][0], false, false);
        const auto s1 = __builtin_amdgcn_permlane16_swap(o[c][0][1], o[c][1][1], false, false);
        const size_t off = (((size_t)(4 * mt + tl) * 54 + (ct >> 1)) * 64 + i + 16 * (2 * (ct & 1) + (g >> 1))) * 16;
#define H1_ST 16
        __builtin_amdgcn_raw_buffer_store_b128((u32x4){s0[0], s1[0], s0[1], s1[1]}, rs_, (int)(WS_H1F + off), 0, H1_ST);
      }
    }
    SB();
    STAMP(5, 1);
    __builtin_amdgcn_s_barrier();
    SB();
    constexpr int E0 = W2_INL - NW2L;
    auto signal = [&]() __attribute__((always_inline)) {
      if (lane == 0) {
        if (__hip_atomic_fetch_add(lcnt, 1u, __ATOMIC_RELAXED, __HIP_MEMORY_SCOPE_WORKGROUP) == 7u)
          __hip_atomic_store(flags + 16 * ng, 1u, __ATOMIC_RELAXED, __HIP_MEMORY_SCOPE_AGENT);
      }
    };
    if (w >= SIG_SPLIT) { asm volatile("s_waitcnt vmcnt(0)" ::: "memory"); signal(); }
    SB();
#pragma unroll
    for (int e = E0; e < NW2R; ++e) ring[(8 + e) % 32] = ld16(rs_, lo16, w2_off(e));
    SB();
    STAMP(5, 2);
    if (w < SIG_SPLIT) { asm volatile("s_waitcnt vmcnt(%0)" :: "n"(NW2R - E0) : "memory"); signal(); }
    SB();
  };
  if (two) gemm1(std::true_type{});
  else gemm1(std::false_type{});
  STAMP(2, 3);
#pragma nounroll
  for (unsigned spin = 0; spin < (1u << 20); ++spin) {
    if (__hip_atomic_load(flags + 16 * w, __ATOMIC_RELAXED, __HIP_MEMORY_SCOPE_AGENT) != 0u) break;
    __builtin_amdgcn_s_sleep(1);
  }
  asm volatile("" ::: "memory");
  SB();
  STAMP(2, 4);

  const int h1s = (int)WS_H1F + (mt * 4 * 54 + ks0) * 1024;
  u32x4 hb[28];
  auto load_h1 = [&](int n) __attribute__((always_inline)) {
    const int q = n / 7, s = n % 7;
    const int sn = (s == 6 && !full) ? 5 : s;
    hb[n] = ld16_sc1(rs_, lo16, h1s + (q * 54 + sn) * 1024);
  };
#pragma unroll
  for (int n = 0; n < H1_PRE; ++n) {
    if (H1_BATCH && n % H1_BATCH == 0 && n >= H1_B0) { SB(); __builtin_amdgcn_s_barrier(); SB(); }
    load_h1(n);
  }
  SB();
  float* red = (float*)smem;
  float* rb_ = red + (w * 16 + i) * KF_RS + 4 * g;
  const int re = w * 40 + min(lane, 39);
  const int rtk = re / 20, rq4 = re % 20;
  const float* rr_ = red + rtk * KF_RS + 4 * rq4;
  f32x4 ac2[2][5];
  f32x4 rv[8], rsum;
  u32x2 xv2;
  const f32x4 gb = *(const f32x4*)(tab + 4 * rq4);
  auto red_wr = [&](int q, int c) __attribute__((always_inline)) {
    *(f32x4*)(rb_ + (q & 1) * (KF_RB / 4) + 16 * c) = ac2[q & 1][c];
  };
  auto red_rd = [&](int q, int ww) __attribute__((always_inline)) {
    rv[ww] = *(const f32x4*)(rr_ + (q & 1) * (KF_RB / 4) + ww * 16 * KF_RS);
  };
  auto red_x = [&](int q) __attribute__((always_inline)) { xv2 = *(const u32x2*)(xr + (16 * q + rtk) * 176 + rq4 * 8); };
  auto red_out = [&](int q) __attribute__((always_inline)) {
    const f32x4 s = rsum + (rv[6] + rv[7]);
    u32x2 o;
#pragma unroll
    for (int h = 0; h < 2; ++h) {
      unsigned f = pk_bf(s[2 * h], s[2 * h + 1]);
      unsigned gf = pk_bf(bf_lo(f) * gb[2 * h], bf_hi(f) * gb[2 * h + 1]);
      o[h] = pk_bf(bf_lo(xv2[h]) + bf_lo(gf), bf_hi(xv2[h]) + bf_hi(gf));
    }
    *(u32x2*)(out + (size_t)(mt * 64 + 16 * q + rtk) * D_ + ng * 80 + 4 * rq4) = o;
  };
#pragma unroll
  for (int q = 0; q < 3; ++q) {
#pragma unroll
    for (int c = 0; c < 5; ++c) ac2[q & 1][c] = (f32x4){0.f, 0.f, 0.f, 0.f};
#pragma unroll
    for (int s = 0; s < 7; ++s) {
      auto mm = [&](int c) __attribute__((always_inline)) {
        const u32x4 a = c == 4 ? wl[s] : ((s == 6 && c == 3) ? wl[7] : ring[(8 + 4 * s + c) % 32]);
        ac2[q & 1][c] = mfma_bf16(a, hb[7 * q + s], ac2[q & 1][c]);
      };
      if (s == 5 && q >= 2) {
        red_wr(q - 1, 4);
        SB();
        red_out(q - 2);
#pragma unroll
        for (int c = 0; c < 5; ++c) mm(c);
#pragma unroll
        for (int c = 0; c < 5; ++c) {
          __builtin_amdgcn_sched_group_barrier(0x008, 1, 0);
          __builtin_amdgcn_sched_group_barrier(0x002, 10, 0);
        }
        SB();
      } else if (s < 6) {
        mm(0); SB();
        if (q >= 1 && s >= 1) { red_wr(q - 1, s - 1); SB(); }
        mm(1); SB();
        if (q >= 2 && s < 4) { red_rd(q - 2, 2 * s); SB(); }
        mm(2); SB();
        if (q >= 2 && s < 4) { red_rd(q - 2, 2 * s + 1); SB(); }
        if (q >= 2 && s == 4) { red_x(q - 2); SB(); }
        mm(3); SB();
        if (q >= 2 && s >= 1 && s < 4) {
          const f32x4 t = rv[2 * s - 2] + rv[2 * s - 1];
          rsum = s == 1 ? t : rsum + t;
          asm volatile("" : "+v"(rsum));
          SB();
        }
        mm(4); SB();
      } else if (full) {
#pragma unroll
        for (int c = 0; c < 5; ++c) mm(c);
        SB();
      }
      if (7 * q + s + H1_PRE < 28) { load_h1(7 * q + s + H1_PRE); SB(); }
    }
    if (q == 0) STAMP(2, 5);
    if (q >= 1) bar_lds();
  }
  {
    auto m3 = [&](int c, int s) __attribute__((always_inline)) {
      const u32x4 a = c == 4 ? wl[s] : ((s == 6 && c == 3) ? wl[7] : ring[(8 + 4 * s + c) % 32]);
      ac2[1][c] = mfma_bf16(a, hb[21 + s], s == 0 ? (f32x4){0.f, 0.f, 0.f, 0.f} : ac2[1][c]);
    };
    auto sum6 = [&]() __attribute__((always_inline)) {
      rsum = ((rv[0] + rv[1]) + (rv[2] + rv[3])) + (rv[4] + rv[5]);
    };
    if (full) {
#pragma unroll
      for (int c = 0; c < 5; ++c) {
        const u32x4 a = c == 4 ? wl[6] : (c == 3 ? wl[7] : ring[(8 + 24 + c) % 32]);
        rv[c] = mfma_bf16(a, hb[27], (f32x4){0.f, 0.f, 0.f, 0.f});
      }
    } else {
#pragma unroll
      for (int c = 0; c < 5; ++c) rv[c] = (f32x4){0.f, 0.f, 0.f, 0.f};
    }
    f32x4 c0[5];
#pragma unroll
    for (int c = 0; c < 5; ++c) c0[c] = rv[c];
    SB();
    auto col = [&](int c, auto&& between) __attribute__((always_inline)) {
#pragma unroll
      for (int s = 0; s < 6; ++s) {
        const u32x4 a = c == 4 ? wl[s] : ring[(8 + 4 * s + c) % 32];
        c0[c] = mfma_bf16(a, hb[21 + s], c0[c]);
        SB();
        between(s);
        SB();
      }
    };
    (void)m3;
    col(0, [&](int s) __attribute__((always_inline)) { if (s < 5) red_wr(2, s); if (s < 4) { red_rd(1, 2 * s); red_rd(1, 2 * s + 1); } if (s == 4) red_x(1); });
    col(1, [&](int s) __attribute__((always_inline)) { if (s == 1) { sum6(); asm volatile("" : "+v"(rsum)); } if (s == 3) red_out(1); });
    bar_lds();
    STAMP(2, 6);
    auto wr3 = [&](int c) __attribute__((always_inline)) { *(f32x4*)(rb_ + (KF_RB / 4) + 16 * c) = c0[c]; };
    col(2, [&](int s) __attribute__((always_inline)) { if (s < 2) wr3(s); if (s >= 1 && s < 5) { red_rd(2, 2 * s - 2); red_rd(2, 2 * s - 1); } if (s == 5) red_x(2); });
    col(3, [&](int s) __attribute__((always_inline)) { if (s == 1) wr3(2); if (s == 2) { sum6(); asm volatile("" : "+v"(rsum)); } if (s == 4) red_out(2); });
    col(4, [&](int s) __attribute__((always_inline)) { if (s == 1) wr3(3); if (s == 2) red_x(3); });
    wr3(4);
    bar_lds();
#pragma unroll
    for (int ww = 0; ww < 8; ++ww) red_rd(3, ww);
    sum6();
    red_out(3);
  }
  STAMP(2, 7); STAMP(5, 7);
}

__global__ NOPK __launch_bounds__(512, 2) void kf(char* __restrict__ ws, unsigned short* __restrict__ out) {
  __shared__ __attribute__((aligned(16))) char smem[KF_LDS];
  TS(2, ws);
  kf_body(ws, out, smem);
}

__global__ void kprobe(char* ws, int which) {
  const unsigned long long now = __builtin_amdgcn_s_memrealtime();
  const unsigned long long* t = (const unsigned long long*)(ws + (200u << 20));
  unsigned long long d = which == 0 ? t[1] - t[0] : (which == 1 ? t[2] - t[1] : (which == 2 ? now - t[2] : 0ull));
  if (d > 10000ull) d = 10000ull;
  while (__builtin_amdgcn_s_memrealtime() - now < 10ull * d) __builtin_amdgcn_s_sleep(1);
}
__global__ void kend_() {}

extern "C" void kernel_launch(void* const* d_in, const int* in_sizes, int n_in,
                              void* d_out, int out_size, void* d_ws, size_t ws_size,
                              hipStream_t stream) {
  K1Args a;
  a.x = (const unsigned short*)d_in[0];
  a.are = (const unsigned short*)d_in[1];
  a.aim = (const unsigned short*)d_in[2];
  a.bre = (const unsigned short*)d_in[3];
  a.bim = (const unsigned short*)d_in[4];
  a.cre = (const unsigned short*)d_in[5];
  a.cim = (const unsigned short*)d_in[6];
  a.logdt = (const unsigned short*)d_in[7];
  a.w1 = (const unsigned short*)d_in[10];
  a.w2 = (const unsigned short*)d_in[11];
  a.c = (const unsigned short*)d_in[12];
  a.wmod = (const unsigned short*)d_in[13];
  a.ws = (char*)d_ws;
  a.dskip = (const unsigned short*)d_in[8];
  a.out = (const unsigned short*)d_out;
  k1<<<NB_PF + NB_MOD + NB_LN + NB_K, 256, 0, stream>>>(a);
  k2<<<256, 512, 0, stream>>>((const unsigned short*)d_in[8], (char*)d_ws, a.w1, a.w2);
  kf<<<256, 512, 0, stream>>>((char*)d_ws, (unsigned short*)d_out);
  kprobe<<<1, 64, 0, stream>>>((char*)d_ws, 0);
}
```

```cpp
#include <hip/hip_runtime.h>
#include <hip/hip_bf16.h>
#include <stdint.h>
#include <type_traits>

typedef __attribute__((ext_vector_type(8))) __bf16 bf16x8;
typedef __attribute__((ext_vector_type(2))) __bf16 bf16x2;
typedef __attribute__((ext_vector_type(4))) float f32x4;
typedef __attribute__((ext_vector_type(2))) float f32x2;
typedef __attribute__((ext_vector_type(4))) unsigned u32x4;
typedef __attribute__((ext_vector_type(2))) unsigned u32x2;
typedef __attribute__((ext_vector_type(8))) _Float16 h16x8;
typedef __attribute__((ext_vector_type(2))) _Float16 h16x2;
typedef __attribute__((ext_vector_type(4))) short s16x4;
typedef __attribute__((address_space(3))) s16x4 lds_s16x4;

#if defined(__HIP_DEVICE_COMPILE__)
#define NOPK __attribute__((target("no-packed-fp32-ops")))
#else
#define NOPK
#endif
#define DEV __device__ __forceinline__

constexpr int L_ = 2048;
constexpr int D_ = 640;
constexpr int NS = 128;
constexpr int FF = 1728;
constexpr int KREV = 2304;

constexpr size_t WS_KSC  = 8192;
constexpr size_t WS_CNT  = 16384;
constexpr size_t WS_MOD  = 65536;
constexpr size_t WS_CSP  = 196608;
constexpr size_t WS_XNT  = 327680;
constexpr size_t WS_XT   = WS_XNT + (size_t)D_ * L_ * 2;
constexpr size_t WS_KREV = WS_XT + (size_t)D_ * L_ * 2;
constexpr size_t WS_W1F  = WS_KREV + (size_t)D_ * KREV * 2;
constexpr size_t WS_W2F  = WS_W1F + (size_t)D_ * FF * 2;
constexpr size_t WS_X1T  = WS_W2F + (size_t)D_ * FF * 2;
constexpr size_t WS_H1F  = WS_X1T + (size_t)D_ * L_ * 2;
constexpr size_t WS_END  = WS_H1F + (size_t)L_ * FF * 2;

DEV float bf_lo(unsigned u) { return __uint_as_float(u << 16); }
DEV float bf_hi(unsigned u) { return __uint_as_float(u & 0xffff0000u); }
DEV unsigned pk_bf(float a, float b) {
  f32x2 v = {a, b};
  bf16x2 r = __builtin_convertvector(v, bf16x2);
  return __builtin_bit_cast(unsigned, r);
}
DEV float rbf(float a) { return bf_lo(pk_bf(a, 0.f)); }
DEV float ldbf(const unsigned short* p) { return __uint_as_float(((unsigned)*p) << 16); }

typedef __amdgpu_buffer_rsrc_t rsrc_t;
DEV rsrc_t ws_rsrc(char* ws) { return __builtin_amdgcn_make_buffer_rsrc((void*)ws, 0, (int)WS_END, 0x00020000); }
DEV void st16(rsrc_t r, size_t off, u32x4 v) { __builtin_amdgcn_raw_buffer_store_b128(v, r, (int)off, 0, 16); }
DEV void st8(rsrc_t r, size_t off, u32x2 v) { __builtin_amdgcn_raw_buffer_store_b64(v, r, (int)off, 0, 16); }

DEV void bar_lds() { asm volatile("s_waitcnt lgkmcnt(0)\n\ts_barrier" ::: "memory"); }
DEV void lds_wait(const void* word, unsigned val) {
  unsigned tv, ts, cnt;
  asm volatile("s_mov_b32 %2, 0x100000\n"
               "1:\n\tds_read_b32 %0, %3\n\ts_waitcnt lgkmcnt(0)\n\tv_readfirstlane_b32 %1, %0\n\t"
               "s_cmp_ge_u32 %1, %4\n\ts_cbranch_scc1 2f\n\ts_sleep 1\n\ts_sub_u32 %2, %2, 1\n\t"
               "s_cmp_lg_u32 %2, 0\n\ts_cbranch_scc1 1b\n2:"
               : "=&v"(tv), "=&s"(ts), "=&s"(cnt) : "v"((unsigned)(size_t)word), "s"(val) : "memory", "scc");
}
DEV void lds_post(unsigned* word, int lane) {
  asm volatile("" ::: "memory");
  if (lane == 0) __hip_atomic_fetch_add(word, 1u, __ATOMIC_RELAXED, __HIP_MEMORY_SCOPE_WORKGROUP);
  asm volatile("" ::: "memory");
}
DEV void lgkm0() { asm volatile("s_waitcnt lgkmcnt(0)" ::: "memory"); __builtin_amdgcn_sched_barrier(0); }

DEV f32x4 mfma_bf16(u32x4 a, u32x4 b, f32x4 c) {
  return __builtin_amdgcn_mfma_f32_16x16x32_bf16(__builtin_bit_cast(bf16x8, a), __builtin_bit_cast(bf16x8, b), c, 0, 0, 0);
}

constexpr int NB_PF = 8, NB_K = 640, NB_LN = 256, NB_MOD = 120;

struct K1Args {
  const unsigned short *x, *are, *aim, *bre, *bim, *cre, *cim, *logdt, *c, *wmod;
  char* ws;
};

DEV void k1_prefetch() {
  const int tid = threadIdx.x;
  unsigned long long pc;
  int o0, o1;
  asm volatile("s_getpc_b64 %0\n.Lpf_here%=:\n\ts_mov_b32 %1, _Z2k16K1Args - .Lpf_here%=\n\ts_mov_b32 %2, _Z5kend_v - .Lpf_here%="
               : "=s"(pc), "=s"(o0), "=s"(o1));
  const char* c0 = (const char*)(pc + (long long)o0);
  long n = o1 - o0;
  n = n < 0 ? 0 : (n > 98304 ? 98304 : n);
  unsigned v[3];
#pragma unroll
  for (int it = 0; it < 3; ++it) {
    const long o = (long)tid * 128 + it * 32768;
    v[it] = o < n ? *(const __attribute__((address_space(1))) unsigned*)(c0 + o) : 0u;
  }
  asm volatile("" :: "v"(v[0] ^ v[1] ^ v[2]));
}

DEV void k1_mod(const K1Args& p, int b, char* smem) {
  const int tid = threadIdx.x;
  const int rg = b & 7, cgp = b >> 3;
  const int c32 = tid & 31, r8 = tid >> 5;
  float* sil = (float*)smem;
  float* red = (float*)(smem + 512);
  u32x4 w[10];
#pragma unroll
  for (int it = 0; it < 10; ++it)
    w[it] = *(const u32x4*)(p.wmod + (size_t)(rg * 80 + r8 + 8 * it) * 3840 + 256 * cgp + 8 * c32);
  if (tid < 80) {
    float cv = ldbf(p.c + rg * 80 + tid);
    sil[tid] = rbf(cv / (1.f + expf(-cv)));
  }
  if (tid >= 128 && tid < 132 && b < 64) ((unsigned*)(p.ws + WS_CNT))[(b * 4 + tid - 128) * 16] = 0u;
  __syncthreads();
  float acc[8];
#pragma unroll
  for (int j = 0; j < 8; ++j) acc[j] = 0.f;
#pragma unroll
  for (int it = 0; it < 10; ++it) {
    float s = sil[r8 + 8 * it];
#pragma unroll
    for (int q = 0; q < 4; ++q) {
      acc[2 * q] += s * bf_lo(w[it][q]);
      acc[2 * q + 1] += s * bf_hi(w[it][q]);
    }
  }
#pragma unroll
  for (int j = 0; j < 8; ++j) acc[j] += __shfl_xor(acc[j], 32, 64);
  if ((tid & 63) < 32) {
    *(f32x4*)(red + (tid >> 6) * 256 + 8 * c32) = (f32x4){acc[0], acc[1], acc[2], acc[3]};
    *(f32x4*)(red + (tid >> 6) * 256 + 8 * c32 + 4) = (f32x4){acc[4], acc[5], acc[6], acc[7]};
  }
  __syncthreads();
  ((float*)(p.ws + WS_MOD))[(256 * cgp + tid) * 8 + rg] = (red[tid] + red[256 + tid]) + (red[512 + tid] + red[768 + tid]);
}
struct ModP { f32x4 a, b; };
DEV ModP mod_ld(const char* ws, int idx) {
  const f32x4* mp = (const f32x4*)(ws + WS_MOD) + 2 * idx;
  ModP m; m.a = mp[0]; m.b = mp[1];
  return m;
}
DEV float mod_fin(const ModP& m) {
  return rbf(((m.a[0] + m.a[1]) + (m.a[2] + m.a[3])) + ((m.b[0] + m.b[1]) + (m.b[2] + m.b[3])));
}
#define SB() __builtin_amdgcn_sched_barrier(0)

DEV void k1_ln(const K1Args& p, int b, char* smem) {
  constexpr int STR = 704;
  const int tid = threadIdx.x;
  const int tok = tid >> 3, sub = tid & 7;
  const int tile = (b & 7) * 8 + ((b >> 3) & 7), pass = b >> 6;
  const int hf = pass & 1;
  const bool norm = pass < 2;
  const int t0 = tile * 32;
  const unsigned short* xr = p.x + (size_t)(t0 + tok) * D_ + 8 * sub;
  u32x4 xv[5], xo[5];
#pragma unroll
  for (int m = 0; m < 5; ++m) xv[m] = *(const u32x4*)(xr + 320 * hf + 64 * m);
  if (norm) {
#pragma unroll
    for (int m = 0; m < 5; ++m) xo[m] = *(const u32x4*)(xr + 320 * (hf ^ 1) + 64 * m);
    float s = 0.f, v = 0.f;
    const bf16x2 one2 = __builtin_bit_cast(bf16x2, 0x3f803f80u);
#pragma unroll
    for (int m = 0; m < 10; ++m)
#pragma unroll
      for (int q = 0; q < 4; ++q) {
        const unsigned u = m < 5 ? xv[m][q] : xo[m - 5][q];
        const bf16x2 x2 = __builtin_bit_cast(bf16x2, u);
        s = __builtin_amdgcn_fdot2_f32_bf16(x2, one2, s, false);
        v = __builtin_amdgcn_fdot2_f32_bf16(x2, x2, v, false);
      }
#pragma unroll
    for (int o = 1; o < 8; o <<= 1) { s += __shfl_xor(s, o, 64); v += __shfl_xor(v, o, 64); }
    const float mean = s * (1.f / D_);
    const float var = fmaxf(v * (1.f / D_) - mean * mean, 0.f);
    const float sd = rbf(sqrtf(rbf(rbf(var) + 1e-5f)));
    const float rs = 1.f / sd;
    const float mb = rbf(mean);
#pragma unroll
    for (int m = 0; m < 5; ++m)
#pragma unroll
      for (int q = 0; q < 4; ++q) {
        unsigned d2 = pk_bf(bf_lo(xv[m][q]) - mb, bf_hi(xv[m][q]) - mb);
        xv[m][q] = pk_bf(bf_lo(d2) * rs, bf_hi(d2) * rs);
      }
  }
  const int lane = tid & 63, w = tid >> 6;
  const int g = lane >> 4, i = lane & 15;
  const rsrc_t rsrc = ws_rsrc(p.ws);
#pragma unroll
  for (int m = 0; m < 5; ++m) *(u32x4*)(smem + tok * STR + (sub + 8 * m) * 16) = xv[m];
  __syncthreads();
  const size_t dsto = (norm ? WS_XNT : WS_XT) + ((size_t)tile * D_ + 320 * hf) * 64;
#pragma unroll
  for (int it = 0; it < 5; ++it) {
    const int cb = w * 5 + it;
    const char* a = smem + (8 * g + (i >> 2)) * STR + cb * 32 + (i & 3) * 8;
    const u32x2 ra = __builtin_bit_cast(u32x2, __builtin_amdgcn_ds_read_tr16_b64_v4i16((lds_s16x4*)a));
    const u32x2 rb = __builtin_bit_cast(u32x2, __builtin_amdgcn_ds_read_tr16_b64_v4i16((lds_s16x4*)(a + 4 * STR)));
    st16(rsrc, dsto + (size_t)(cb * 16 + i) * 64 + 16 * g, (u32x4){ra[0], ra[1], rb[0], rb[1]});
  }
}

constexpr int RT_STR = 176;
template <int NF> struct RtRaw { u32x4 r[NF]; };
template <int NF>
DEV void rt_load(RtRaw<NF>& t, const unsigned short* W, int N, int kk, int ct0, int lane) {
#pragma unroll
  for (int j = 0; j < NF; ++j) {
    const int c = lane + 64 * j, row = c / (2 * NF), part = c % (2 * NF);
    t.r[j] = *(const u32x4*)(W + (size_t)(32 * kk + row) * N + 16 * ct0 + 8 * part);
  }
}
template <int NF>
DEV void rt_park(const RtRaw<NF>& t, char* scr, int lane) {
#pragma unroll
  for (int j = 0; j < NF; ++j) {
    const int c = lane + 64 * j, row = c / (2 * NF), part = c % (2 * NF);
    *(u32x4*)(scr + row * RT_STR + 16 * part) = t.r[j];
  }
}
DEV u32x4 rt_frag(const char* scr, int j, int lane) {
  const int g = lane >> 4, i = lane & 15;
  const char* p = scr + (8 * g + (i >> 2)) * RT_STR + 32 * j + (i & 3) * 8;
  const u32x2 a = __builtin_bit_cast(u32x2, __builtin_amdgcn_ds_read_tr16_b64_v4i16((lds_s16x4*)p));
  const u32x2 b = __builtin_bit_cast(u32x2, __builtin_amdgcn_ds_read_tr16_b64_v4i16((lds_s16x4*)(p + 4 * RT_STR)));
  return (u32x4){a[0], a[1], b[0], b[1]};
}
struct cf { float re, im; };
DEV cf cmul(cf a, cf b) {
  cf r;
  r.re = a.re * b.re - a.im * b.im;
  r.im = a.re * b.im + a.im * b.re;
  return r;
}
DEV cf cexp_(float a2, float br, float m, float m0) {
  float mag = __builtin_amdgcn_exp2f(a2 * (m - m0));
  float ph = __builtin_amdgcn_fractf(br * m);
  cf r; r.re = mag * __builtin_amdgcn_cosf(ph); r.im = mag * __builtin_amdgcn_sinf(ph);
  return r;
}
DEV unsigned pk_h(float a, float b) {
  f32x2 v = {a, b};
  h16x2 r = __builtin_convertvector(v, h16x2);
  return __builtin_bit_cast(unsigned, r);
}
DEV f32x4 mfma_f16(u32x4 a, u32x4 b, f32x4 c) {
  return __builtin_amdgcn_mfma_f32_16x16x32_f16(__builtin_bit_cast(h16x8, a), __builtin_bit_cast(h16x8, b), c, 0, 0, 0);
}
DEV void put2(char* p, cf a, cf b) {
  u32x2 o = {pk_h(a.re, a.im), pk_h(b.re, b.im)};
  *(u32x2*)p = o;
}

constexpr int KG_RS = 544;
DEV void k1_kgen(const K1Args& p, int d, char* smem) {
  constexpr int RS = KG_RS;
  const int tid = threadIdx.x;
  const int lane = tid & 63;
  const int w = __builtin_amdgcn_readfirstlane(tid >> 6);
  const int g = lane >> 4, i = lane & 15;
  const size_t po = (size_t)d * NS + 2 * lane;
  const unsigned rar = *(const unsigned*)(p.are + po), rai = *(const unsigned*)(p.aim + po);
  const unsigned rbr = *(const unsigned*)(p.bre + po), rbi = *(const unsigned*)(p.bim + po);
  const unsigned rcr = *(const unsigned*)(p.cre + po), rci = *(const unsigned*)(p.cim + po);
  const float ldt = ldbf(p.logdt + d);
  asm volatile("" ::: "memory");
  SB();
  const float dt = rbf(expf(ldt));
  float a2[2], bv[2], m0[2];
  a2[0] = (dt * bf_lo(rar)) * 1.44269504088896341f; a2[1] = (dt * bf_hi(rar)) * 1.44269504088896341f;
  bv[0] = (dt * bf_lo(rai)) * 0.15915494309189534f; bv[1] = (dt * bf_hi(rai)) * 0.15915494309189534f;
  m0[0] = a2[0] > 0.f ? 2048.f : 0.f; m0[1] = a2[1] > 0.f ? 2048.f : 0.f;
  char* my = smem + lane * 8;
  if (w == 0) {
    cf v[2], v8[2], e8[2];
    float bound = 0.f;
#pragma unroll
    for (int q = 0; q < 2; ++q) {
      const float dbr = dt * (q ? bf_hi(rbr) : bf_lo(rbr)), dbi = dt * (q ? bf_hi(rbi) : bf_lo(rbi));
      const float cr = q ? bf_hi(rcr) : bf_lo(rcr), ci = q ? bf_hi(rci) : bf_lo(rci);
      const float gsh = __builtin_amdgcn_exp2f(a2[q] * m0[q]);
      v[q].re = (cr * dbr - ci * dbi) * gsh; v[q].im = -(cr * dbi + ci * dbr) * gsh;
      bound += (fabsf(v[q].re) + fabsf(v[q].im)) * __builtin_amdgcn_exp2f(fmaxf(a2[q], 0.f) * 120.f);
      e8[q] = cexp_(a2[q], -bv[q], 8.f, 0.f);
      v8[q] = cexp_(a2[q], -bv[q], 64.f, 0.f);
    }
#pragma unroll
    for (int o = 1; o < 64; o <<= 1) bound += __shfl_xor(bound, o, 64);
    int ex = (int)((__float_as_uint(bound) >> 23) & 0xff);
    ex = ex < 20 ? 20 : (ex > 240 ? 240 : ex);
    const float sv = __uint_as_float((unsigned)(127 + 13 + 127 - ex) << 23);
    if (lane == 0) ((float*)(p.ws + WS_KSC))[d] = __uint_as_float((unsigned)(ex - 13) << 23);
#pragma unroll
    for (int q = 0; q < 2; ++q) { v[q].re *= sv; v[q].im *= sv; v8[q] = cmul(v[q], v8[q]); }
#pragma unroll
    for (int j = 0; j < 8; ++j) {
      put2(my + (41 + j) * RS, v[0], v[1]);
      put2(my + (49 + j) * RS, v8[0], v8[1]);
      if (j < 7) {
#pragma unroll
        for (int q = 0; q < 2; ++q) { v[q] = cmul(v[q], e8[q]); v8[q] = cmul(v8[q], e8[q]); }
      }
    }
  } else if (w == 1) {
    cf e1[2], u[2], u2[2];
#pragma unroll
    for (int q = 0; q < 2; ++q) {
      e1[q] = cexp_(a2[q], bv[q], 1.f, 0.f);
      u[q].re = __builtin_amdgcn_exp2f(-a2[q] * m0[q]); u[q].im = 0.f;
      u2[q] = cexp_(a2[q], bv[q], 128.f, m0[q]);
    }
#pragma unroll
    for (int r = 0; r < 8; ++r) {
      put2(my + r * RS, u[0], u[1]);
      put2(my + (8 + r) * RS, u2[0], u2[1]);
      if (r < 7) {
#pragma unroll
        for (int q = 0; q < 2; ++q) { u[q] = cmul(u[q], e1[q]); u2[q] = cmul(u2[q], e1[q]); }
      }
    }
  } else if (w == 2) {
    cf e2[2], u[2], u2[2];
#pragma unroll
    for (int q = 0; q < 2; ++q) {
      e2[q] = cexp_(a2[q], bv[q], 2.f, 0.f);
      u[q] = cexp_(a2[q], bv[q], 256.f, m0[q]);
      u2[q] = cexp_(a2[q], bv[q], 384.f, m0[q]);
    }
#pragma unroll
    for (int r = 0; r < 4; ++r) {
      put2(my + (16 + r) * RS, u[0], u[1]);
      put2(my + (20 + r) * RS, u2[0], u2[1]);
      if (r < 3) {
#pragma unroll
        for (int q = 0; q < 2; ++q) { u[q] = cmul(u[q], e2[q]); u2[q] = cmul(u2[q], e2[q]); }
      }
    }
    cf e4[2] = {cmul(e2[0], e2[0]), cmul(e2[1], e2[1])};
#pragma unroll
    for (int r = 0; r < 2; ++r) {
      cf c0 = cexp_(a2[0], bv[0], 512.f + 128.f * r, m0[0]), c1 = cexp_(a2[1], bv[1], 512.f + 128.f * r, m0[1]);
      put2(my + (24 + 2 * r) * RS, c0, c1);
      put2(my + (25 + 2 * r) * RS, cmul(c0, e4[0]), cmul(c1, e4[1]));
    }
  } else {
    cf e4[2], e128[2], u[2], u2[2];
#pragma unroll
    for (int q = 0; q < 2; ++q) {
      e4[q] = cexp_(a2[q], bv[q], 4.f, 0.f);
      e128[q] = cexp_(a2[q], bv[q], 128.f, 0.f);
      u[q] = cexp_(a2[q], bv[q], 1024.f, m0[q]);
      u2[q] = cexp_(a2[q], bv[q], 1536.f, m0[q]);
    }
#pragma unroll
    for (int r = 2; r < 4; ++r) {
      cf c0 = cexp_(a2[0], bv[0], 512.f + 128.f * r, m0[0]), c1 = cexp_(a2[1], bv[1], 512.f + 128.f * r, m0[1]);
      put2(my + (24 + 2 * r) * RS, c0, c1);
      put2(my + (25 + 2 * r) * RS, cmul(c0, e4[0]), cmul(c1, e4[1]));
    }
#pragma unroll
    for (int r = 0; r < 4; ++r) {
      put2(my + (32 + r) * RS, u[0], u[1]);
      put2(my + (36 + r) * RS, u2[0], u2[1]);
#pragma unroll
      for (int q = 0; q < 2; ++q) { if (r < 3) u[q] = cmul(u[q], e128[q]); u2[q] = cmul(u2[q], e128[q]); }
    }
    put2(my + 40 * RS, u2[0], u2[1]);
  }
  __syncthreads();
  _Float16* kh = (_Float16*)(smem + 57 * RS);
  if (w < 3) {
    const char* ua = smem + (16 * w + i) * RS + g * 16;
    const char* va = smem + (41 + i) * RS + g * 16;
    f32x4 acc = {0.f, 0.f, 0.f, 0.f};
#pragma unroll
    for (int ks = 0; ks < 8; ++ks)
      acc = mfma_f16(*(const u32x4*)(ua + ks * 64), *(const u32x4*)(va + ks * 64), acc);
    int idx0, st;
    if (w == 0) { idx0 = (g < 2 ? 4 * g : 120 + 4 * g) + 8 * i; st = 1; }
    else if (w == 1) { idx0 = g < 2 ? 256 + 64 * g + 4 * i : 384 + 64 * (g - 2) + 2 * i; st = 1; }
    else { idx0 = 512 + 64 * g + i; st = 16; }
#pragma unroll
    for (int rg = 0; rg < 4; ++rg) {
      int idx = idx0 + st * rg;
      if (w == 1 && g >= 2) idx = idx0 + 32 * (rg >> 1) + (rg & 1);
      if (w == 2 && g >= 2) idx = (g == 2 && rg == 0 && i == 0) ? 640 : 644;
      kh[idx] = (_Float16)acc[rg];
    }
  }
  __syncthreads();
  const unsigned short* ku = (const unsigned short*)kh;
  const size_t kout = WS_KREV + (size_t)d * KREV * 2;
  {
    const int l0 = 2040 - 8 * tid;
    u32x4 o;
    if (w < 2) {
      const int q = l0 >> 3;
      const unsigned a = ku[384 + q], b = ku[385 + q];
      const unsigned t = (q & 1) ? b : a;
      o[0] = b | (b << 16); o[1] = b | (t << 16); o[2] = a | (a << 16); o[3] = o[2];
    } else if (w == 2) {
      const int q = l0 >> 2;
      const unsigned a = ku[256 + q], b = ku[257 + q], c = ku[258 + q];
      o[0] = c | (c << 16); o[1] = b | (b << 16); o[2] = b | (a << 16); o[3] = a | (a << 16);
    } else {
      unsigned v[8];
#pragma unroll
      for (int e = 0; e < 8; ++e) {
        const int l = l0 + 7 - e;
        const int idx = l < 256 ? l : (int)(pk_bf((float)l, 0.f) & 0xffffu) - 17024;
        v[e] = ku[idx];
      }
#pragma unroll
      for (int q = 0; q < 4; ++q) o[q] = v[2 * q] | (v[2 * q + 1] << 16);
    }
    st16(ws_rsrc(p.ws), kout + 16 * tid, o);
  }
}

__global__ NOPK __launch_bounds__(256) void k1(K1Args p) {
  __shared__ __attribute__((aligned(16))) char smem[57 * KG_RS + 1312];
  int b = blockIdx.x;
  if (b < NB_PF) { k1_prefetch(); return; }
  b -= NB_PF;
  if (b < NB_K) { k1_kgen(p, b, smem); return; }
  b -= NB_K;
  asm volatile("" :: "s"(p.ws), "s"(p.x));
  __builtin_amdgcn_s_sleep(4);
  if (b < NB_LN) k1_ln(p, b, smem);
  else k1_mod(p, b - NB_LN, smem);
}

constexpr int K2_CS = 4160;
constexpr int K2_KOFF = 4 * K2_CS;
constexpr int K2_CH = K2_KOFF + 4608;
constexpr int CV_RED = 3 * K2_CH;
constexpr int CV_RT2 = 86016;
constexpr int CV_SUM = CV_RT2 + 4 * (32 * 176 + 256);
constexpr int CV_XCH = CV_SUM + 2560;
constexpr int CV_FL = CV_RED + 20480;

struct Chan { float sa, ca1, ga, dsk, hs, hinv, yinv; };
struct ChanRaw { ModP msa, mca, mga; float kinv; unsigned short rds; };
DEV ChanRaw chan_ld(const unsigned short* dskip, const char* ws, int d) {
  ChanRaw c;
  c.rds = dskip[d];
  c.msa = mod_ld(ws, d); c.mca = mod_ld(ws, D_ + d); c.mga = mod_ld(ws, 2 * D_ + d);
  c.kinv = ((const float*)(ws + WS_KSC))[d];
  return c;
}
DEV Chan chan_fin(const ChanRaw& r) {
  Chan c;
  c.sa = mod_fin(r.msa); c.ca1 = rbf(1.f + mod_fin(r.mca)); c.ga = mod_fin(r.mga);
  c.dsk = __uint_as_float((unsigned)r.rds << 16);
  const float hb = 26.f * fabsf(c.ca1) + fabsf(c.sa);
  int ex = (int)((__float_as_uint(hb) >> 23) & 0xff);
  ex = ex < 20 ? 20 : (ex > 240 ? 240 : ex);
  c.hs = __uint_as_float((unsigned)(127 + 14 + 127 - ex) << 23);
  c.hinv = __uint_as_float((unsigned)(ex - 14) << 23);
  c.yinv = r.kinv * c.hinv;
  return c;
}
DEV size_t tl_off(int d, int t) { return (((size_t)(t >> 5) * D_ + d) * 32 + (t & 31)) * 2; }
struct StageRaw { u32x4 xa0, xa1, kv0, kv1; u32x2 xb; };
DEV StageRaw stage_ld(const char* ws, int d, int th) {
  StageRaw s;
  const unsigned short* krev = (const unsigned short*)(ws + WS_KREV) + (size_t)d * KREV;
  const size_t xo = tl_off(d, 16 * th);
  s.xa0 = *(const u32x4*)(ws + WS_XNT + xo); s.xa1 = *(const u32x4*)(ws + WS_XNT + xo + 16);
  s.xb = *(const u32x2*)(ws + WS_XNT + tl_off(d, min(16 * th + 16, L_ - 4)));
  s.kv0 = *(const u32x4*)(krev + 8 * th); s.kv1 = *(const u32x4*)(krev + 1024 + 8 * th);
  return s;
}
DEV void stage_wr(const StageRaw& s, const Chan& c, char* smem, int th) {
  constexpr int CS = K2_CS, KOFF = K2_KOFF;
  unsigned hh[10];
#pragma unroll
  for (int q = 0; q < 10; ++q) {
    unsigned u = q < 4 ? s.xa0[q] : (q < 8 ? s.xa1[q - 4] : s.xb[q - 8]);
    unsigned m = pk_bf(bf_lo(u) * c.ca1, bf_hi(u) * c.ca1);
    unsigned h = pk_bf(bf_lo(m) + c.sa, bf_hi(m) + c.sa);
    hh[q] = pk_h(bf_lo(h) * c.hs, bf_hi(h) * c.hs);
  }
  char* dst = smem + (2 * th + 2) * 16;
  unsigned s1[9];
#pragma unroll
  for (int q = 0; q < 9; ++q) s1[q] = __builtin_amdgcn_alignbit(hh[q + 1], hh[q], 16);
#pragma unroll
  for (int e = 0; e < 2; ++e) {
    *(u32x4*)(dst + 16 * e) = (u32x4){hh[4 * e], hh[4 * e + 1], hh[4 * e + 2], hh[4 * e + 3]};
    *(u32x4*)(dst + 16 * e + CS) = (u32x4){s1[4 * e], s1[4 * e + 1], s1[4 * e + 2], s1[4 * e + 3]};
    *(u32x4*)(dst + 16 * e + 2 * CS) = (u32x4){hh[4 * e + 1], hh[4 * e + 2], hh[4 * e + 3], hh[4 * e + 4]};
    *(u32x4*)(dst + 16 * e + 3 * CS) = (u32x4){s1[4 * e + 1], s1[4 * e + 2], s1[4 * e + 3], s1[4 * e + 4]};
  }
  if (th == 0) {
    u32x4 z = {0u, 0u, 0u, 0u};
    *(u32x4*)(smem) = z; *(u32x4*)(smem + CS) = z; *(u32x4*)(smem + 2 * CS) = z; *(u32x4*)(smem + 3 * CS) = z;
    *(u32x4*)(smem + 16) = z;
    *(u32x4*)(smem + CS + 16) = (u32x4){0u, 0u, 0u, hh[0] << 16};
    *(u32x4*)(smem + 2 * CS + 16) = (u32x4){0u, 0u, 0u, hh[0]};
    *(u32x4*)(smem + 3 * CS + 16) = (u32x4){0u, 0u, hh[0] << 16, s1[0]};
  }
  *(u32x4*)(smem + KOFF + 16 * th) = s.kv0;
  *(u32x4*)(smem + KOFF + 2048 + 16 * th) = s.kv1;
  if (th < 32) *(u32x4*)(smem + KOFF + 4096 + 16 * th) = (u32x4){0u, 0u, 0u, 0u};
}
DEV u32x2 conv_out(const f32x4& y, const Chan& c, const char* smem, int t, const u32x2& xres) {
  const u32x2 hv = *(const u32x2*)(smem + 2 * (t + 16));
  u32x2 o;
#pragma unroll
  for (int e = 0; e < 2; ++e) {
    const unsigned hu = hv[e];
    const h16x2 h2 = __builtin_bit_cast(h16x2, hu);
    const float h0 = (float)h2[0] * c.hinv, h1 = (float)h2[1] * c.hinv;
    unsigned yb = pk_bf(y[2 * e] * c.yinv, y[2 * e + 1] * c.yinv);
    unsigned hd = pk_bf(h0 * c.dsk, h1 * c.dsk);
    unsigned y2 = pk_bf(bf_lo(yb) + bf_lo(hd), bf_hi(yb) + bf_hi(hd));
    unsigned gy = pk_bf(bf_lo(y2) * c.ga, bf_hi(y2) * c.ga);
    o[e] = pk_bf(bf_lo(xres[e]) + bf_lo(gy), bf_hi(xres[e]) + bf_hi(gy));
  }
  return o;
}
#define K2_LOAD(buf, cc, u)                                                                                     \
  asm volatile("ds_read_b64 %0, %3 offset:%5\n\tds_read_b64 %1, %3 offset:%6\n\tds_read_b128 %2, %4 offset:%7"   \
               : "=&v"(Al[buf][u]), "=&v"(Ah[buf][u]), "=&v"(Bf[buf][u])                                          \
               : "v"(ap0), "v"(bp0), "i"(64 * (cc) + 512 * (u)), "i"(64 * (cc) + 512 * (u) + 8),                 \
                 "i"(64 * (cc) + 3584 - 512 * (u)))
template <int SET> DEV constexpr int half_tile(int j) {
  return SET == 0 ? (j == 0 ? 7 : (j == 1 ? 6 : 2)) : (j < 3 ? 5 - j : 4 - j);
}
template <int SET>
DEV void conv_half(unsigned ap0, unsigned bp0, char* redw) {
  constexpr int NU = SET == 0 ? 8 : 6, NT = SET == 0 ? 3 : 5;
  u32x2 Al[2][NU], Ah[2][NU];
  u32x4 Bf[2][NU];
#pragma unroll
  for (int u = 0; u < NU; ++u) K2_LOAD(0, 0, u);
  f32x4 acc[NT];
#pragma unroll
  for (int j = 0; j < NT; ++j) acc[j] = (f32x4){0.f, 0.f, 0.f, 0.f};
#pragma unroll
  for (int cc = 0; cc < 2; ++cc) {
    lgkm0();
    if (cc == 0) {
#pragma unroll
      for (int u = 0; u < NU; ++u) K2_LOAD(1, 1, u);
    }
#pragma unroll
    for (int sp = 0; sp < NU; ++sp)
#pragma unroll
      for (int j = 0; j < NT; ++j) {
        const int q = half_tile<SET>(j);
        if (sp <= q) {
          const u32x4 A = {Al[cc][sp][0], Al[cc][sp][1], Ah[cc][sp][0], Ah[cc][sp][1]};
          acc[j] = mfma_f16(A, Bf[cc][q - sp], acc[j]);
        }
      }
    SB();
  }
#pragma unroll
  for (int j = 0; j < NT; ++j) *(f32x4*)(redw + j * 4096) = acc[j];
}

template <int NF>
DEV void w1_fin(const RtRaw<NF>& t, const ModP& mp, char* ws, int kk, int ct0, int lane, char* scr, float* sums) {
  const int g = lane >> 4, i = lane & 15;
  float* tb = (float*)(scr + 32 * RT_STR);
  const float mv = mod_fin(mp);
  tb[lane] = lane < 32 ? rbf(1.f + mv) : mv;
  rt_park<NF>(t, scr, lane);
  lgkm0();
  const f32x4 ca = *(const f32x4*)(tb + 8 * g), cb = *(const f32x4*)(tb + 8 * g + 4);
  const f32x4 sa = *(const f32x4*)(tb + 32 + 8 * g), sb = *(const f32x4*)(tb + 36 + 8 * g);
  const u32x4 sv = {pk_bf(sa[0], sa[1]), pk_bf(sa[2], sa[3]), pk_bf(sb[0], sb[1]), pk_bf(sb[2], sb[3])};
  const u32x4 ones = {0x3f803f80u, 0x3f803f80u, 0x3f803f80u, 0x3f803f80u};
  const f32x4 z = {0.f, 0.f, 0.f, 0.f};
  const rsrc_t rs = ws_rsrc(ws);
#pragma unroll
  for (int j = 0; j < NF; ++j) {
    const u32x4 f = rt_frag(scr, j, lane);
    const u32x4 sc = {pk_bf(bf_lo(f[0]) * ca[0], bf_hi(f[0]) * ca[1]), pk_bf(bf_lo(f[1]) * ca[2], bf_hi(f[1]) * ca[3]),
                      pk_bf(bf_lo(f[2]) * cb[0], bf_hi(f[2]) * cb[1]), pk_bf(bf_lo(f[3]) * cb[2], bf_hi(f[3]) * cb[3])};
    st16(rs, WS_W1F + ((size_t)(kk * 108 + ct0 + j) * 64 + lane) * 16, sc);
    const f32x4 cs = mfma_bf16(sc, ones, z), b1 = mfma_bf16(f, sv, z);
    if (i < 2) *(f32x4*)(sums + i * 80 + 16 * j + 4 * g) = i ? b1 : cs;
  }
}

DEV void conv_phase(const unsigned short* __restrict__ dskip, char* __restrict__ ws,
                    const unsigned short* __restrict__ w1, const unsigned short* __restrict__ w2, char* smem_all) {
  constexpr int CS = K2_CS, KOFF = K2_KOFF;
  const int tid = threadIdx.x, lane = tid & 63;
  const int w = __builtin_amdgcn_readfirstlane(tid >> 6);
  const int bx = blockIdx.x;
  const int pj = (bx & 7) * 16 + (bx >> 4), par = (bx >> 3) & 1;
  const int r = lane & 15, g = lane >> 4, th = tid & 127;
  const int s4 = (r + 1) & 3;
  const rsrc_t rs = ws_rsrc(ws);
  unsigned* fl = (unsigned*)(smem_all + CV_FL);
  if (tid < 6) fl[tid] = 0u;
  if (w < 4) {
    const int d = 5 * pj + 2 * par + (w >> 1), hw = w & 1;
    char* smem = smem_all + (w >> 1) * K2_CH;
    const StageRaw sr = stage_ld(ws, d, th);
    const ChanRaw cr = chan_ld(dskip, ws, d);
    u32x2 xres[4];
#pragma unroll
    for (int q = 0; q < 4; ++q)
      xres[q] = *(const u32x2*)(ws + WS_XT + tl_off(d, 256 * (4 * hw + q) + 16 * r + 4 * g));
    SB();
    const Chan c = chan_fin(cr);
    stage_wr(sr, c, smem, th);
    __syncthreads();
    f32x4 acc[8];
#pragma unroll
    for (int q = 0; q < 8; ++q) acc[q] = (f32x4){0.f, 0.f, 0.f, 0.f};
    {
      const unsigned ap0 = (unsigned)(size_t)(smem + s4 * CS + 2 * (128 * hw + 8 * g + (r + 1) - s4));
      const unsigned bp0 = (unsigned)(size_t)(smem + KOFF + 2 * (2032 + 128 * hw - 16 * r + 8 * g) - 3584);
      u32x2 Al[2][8], Ah[2][8];
      u32x4 Bf[2][8];
#pragma unroll
      for (int u = 0; u < 8; ++u) K2_LOAD(0, 0, u);
#pragma unroll
      for (int cc = 0; cc < 4; ++cc) {
        lgkm0();
#pragma unroll
        for (int q = 0; q < 8; ++q) {
          if (cc < 3) K2_LOAD((cc + 1) & 1, cc + 1, q);
#pragma unroll
          for (int sp = 0; sp <= q; ++sp) {
            const u32x4 A = {Al[cc & 1][sp][0], Al[cc & 1][sp][1], Ah[cc & 1][sp][0], Ah[cc & 1][sp][1]};
            acc[q] = mfma_f16(A, Bf[cc & 1][q - sp], acc[q]);
          }
          SB();
        }
      }
    }
    f32x4 keep[4];
    {
      f32x4* xw = (f32x4*)(smem_all + CV_XCH + w * 4096) + lane;
      if (hw) {
#pragma unroll
        for (int q = 0; q < 4; ++q) { xw[64 * q] = acc[q]; keep[q] = acc[4 + q]; }
      } else {
#pragma unroll
        for (int q = 0; q < 4; ++q) { xw[64 * q] = acc[4 + q]; keep[q] = acc[q]; }
      }
    }
    lds_post(fl + w, lane);
    lds_wait(fl + (w ^ 1), 1u);
    const f32x4* xrd = (const f32x4*)(smem_all + CV_XCH + (w ^ 1) * 4096) + lane;
#pragma unroll
    for (int qp = 0; qp < 2; ++qp) {
      u32x2 o[2];
#pragma unroll
      for (int h = 0; h < 2; ++h) {
        const int q = 2 * qp + h;
        o[h] = conv_out(keep[q] + xrd[64 * q], c, smem, 256 * (4 * hw + q) + 16 * r + 4 * g, xres[q]);
      }
      const auto s0 = __builtin_amdgcn_permlane16_swap(o[0][0], o[1][0], false, false);
      const auto s1 = __builtin_amdgcn_permlane16_swap(o[0][1], o[1][1], false, false);
      const int t = 256 * (4 * hw + 2 * qp + (g & 1)) + 16 * r + 8 * (g >> 1);
      st16(rs, WS_X1T + (((size_t)(t >> 6) * D_ + d) * 64 + (t & 63)) * 2, (u32x4){s0[0], s1[0], s0[1], s1[1]});
    }
  } else {
    __builtin_amdgcn_s_setprio(2);
    const int d = 5 * pj + 4, wq = w - 4;
    char* smem = smem_all + 2 * K2_CH;
    StageRaw sr;
    if (wq < 2) sr = stage_ld(ws, d, th);
    asm volatile("" ::: "memory");
    const ChanRaw cr = chan_ld(dskip, ws, d);
    const int tl0 = par ? (wq < 3 ? 5 - wq : 1) : (wq == 0 ? 7 : (wq == 1 ? 6 : 2)), tl1 = 0;
    const unsigned ap0 = (unsigned)(size_t)(smem + s4 * CS + 2 * (64 * wq + 8 * g + (r + 1) - s4));
    const unsigned bp0 = (unsigned)(size_t)(smem + KOFF + 2 * (2032 + 64 * wq - 16 * r + 8 * g) - 3584);
    char* redw = smem_all + CV_RED + wq * 1024 + lane * 16;
    u32x2 xres[2];
    xres[0] = *(const u32x2*)(ws + WS_XT + tl_off(d, 256 * tl0 + 16 * r + 4 * g));
    xres[1] = *(const u32x2*)(ws + WS_XT + tl_off(d, 256 * tl1 + 16 * r + 4 * g));
    SB();
    const Chan c = chan_fin(cr);
    if (wq < 2) {
      stage_wr(sr, c, smem, th);
    } else {
      __builtin_amdgcn_s_sleep(32);
    }
    const bool isw1 = bx < 110;
    const int b2 = min(max(bx - 110, 0), 107);
    const int jk = isw1 ? 4 * (bx / 22) + wq : b2 >> 1;
    const int jc = isw1 ? min(5 * (bx % 22), 103) : (b2 & 1) * 20 + 5 * wq;
    RtRaw<5> rt;
    rt_load<5>(rt, isw1 ? w1 : w2, isw1 ? FF : D_, jk, jc, lane);
    const ModP mp = mod_ld(ws, (lane < 32 ? 4 * D_ : 3 * D_ - 32) + 32 * min(jk, 19) + lane);
    SB();
    bar_lds();
    if (par) conv_half<1>(ap0, bp0, redw); else conv_half<0>(ap0, bp0, redw);
    lds_post(fl + 4, lane);
    char* scr = smem_all + CV_RT2 + wq * (32 * RT_STR + 256);
    if (isw1) {
      float* sums = (float*)(smem_all + CV_SUM);
      w1_fin<5>(rt, mp, ws, jk, jc, lane, scr, sums + 160 * wq);
      asm volatile("" ::: "memory");
      unsigned old = 0u;
      if (lane == 0) old = __hip_atomic_fetch_add(fl + 5, 1u, __ATOMIC_RELAXED, __HIP_MEMORY_SCOPE_WORKGROUP);
      asm volatile("" ::: "memory");
      if (__builtin_amdgcn_readfirstlane(old) == 3u && lane < 40) {
        const f32x4* sp = (const f32x4*)sums + lane;
        const f32x4 v = (sp[0] + sp[40]) + (sp[80] + sp[120]);
        st16(rs, WS_CSP + ((size_t)(2 * (bx / 22) + lane / 20) * FF + 16 * jc + 4 * (lane % 20)) * 4, __builtin_bit_cast(u32x4, v));
      }
    } else if (bx < 218) {
      rt_park<5>(rt, scr, lane);
      lgkm0();
#pragma unroll
      for (int j = 0; j < 5; ++j) st16(rs, WS_W2F + ((size_t)((jc + j) * 54 + jk) * 64 + lane) * 16, rt_frag(scr, j, lane));
    }
    lds_wait(fl + 4, 4u);
    const char* rd = smem_all + CV_RED + lane * 16;
#pragma unroll
    for (int u = 0; u < 2; ++u) {
      const bool on = u == 0 ? (par || wq < 3) : (par && wq == 0);
      if (on) {
        const int sl = u == 0 ? wq : 4, tl = u == 0 ? tl0 : tl1;
        const f32x4 y = (*(const f32x4*)(rd + sl * 4096) + *(const f32x4*)(rd + sl * 4096 + 1024))
                      + (*(const f32x4*)(rd + sl * 4096 + 2048) + *(const f32x4*)(rd + sl * 4096 + 3072));
        const int t = 256 * tl + 16 * r + 4 * g;
        st8(rs, WS_X1T + (((size_t)(t >> 6) * D_ + d) * 64 + (t & 63)) * 2, conv_out(y, c, smem, t, xres[u]));
      }
    }
  }
}

__global__ NOPK __launch_bounds__(512, 2) void k2(const unsigned short* __restrict__ dskip, char* __restrict__ ws,
                                                  const unsigned short* __restrict__ w1, const unsigned short* __restrict__ w2) {
  __shared__ __attribute__((aligned(16))) char smem[CV_XCH + 16384];
  conv_phase(dskip, ws, w1, w2, smem);
}

DEV u32x4 ld16_sc1(rsrc_t r, int voff, int soff) { return __builtin_amdgcn_raw_buffer_load_b128(r, voff, soff, 16); }
DEV u32x4 ld16(rsrc_t r, int voff, int soff) { return __builtin_amdgcn_raw_buffer_load_b128(r, voff, soff, 0); }
DEV u32x4 tr_frag(const char* p) {
  const u32x2 a = __builtin_bit_cast(u32x2, __builtin_amdgcn_ds_read_tr16_b64_v4i16((lds_s16x4*)p));
  const u32x2 b = __builtin_bit_cast(u32x2, __builtin_amdgcn_ds_read_tr16_b64_v4i16((lds_s16x4*)(p + 256)));
  return (u32x4){a[0], a[1], b[0], b[1]};
}

constexpr int KF_RS = 84;
constexpr int KF_RB = 8 * 16 * KF_RS * 4;
constexpr int KF_BIG = 2 * KF_RB;
constexpr int NW2L = 8, NW2R = 27;
constexpr int KF_TAB = KF_BIG;
constexpr int KF_PS = KF_TAB + 320;
constexpr int KF_XR = KF_PS + 512;
constexpr int KF_CNT = KF_XR + 64 * 176;
constexpr int KF_LDS = KF_CNT + 16;
constexpr int X_FIRST = 5, X_STEP = 4;
constexpr int W1_EARLY = 3, W1_MID = 7, W1_PRE = 10;
constexpr int W2_START = 6, W2_RATE = 2, W2_INL = 28;
constexpr int CS_STEP = 14, SIG_SPLIT = 4, H1_PRE = 9, H1_BATCH = 7;
constexpr unsigned LOCK_MASK = 0x55554;

DEV void kf_body(char* __restrict__ ws, unsigned short* __restrict__ out, char* smem) {
  char* xs = smem;
  float* tab = (float*)(smem + KF_TAB);
  float* ps = (float*)(smem + KF_PS);
  char* xr = smem + KF_XR;
  unsigned* lcnt = (unsigned*)(smem + KF_CNT);
  float* cst = (float*)(smem + 81920);
  const int tid = threadIdx.x, lane = tid & 63;
  const int w = __builtin_amdgcn_readfirstlane(tid >> 6);
  const int bx = blockIdx.x;
  const int ng = (bx & 1) * 4 + ((bx >> 3) & 3), mt = ((bx & 7) >> 1) * 8 + (bx >> 5);
  const int g = lane >> 4, i = lane & 15;
  const rsrc_t rs_ = ws_rsrc(ws);

  unsigned* flags = (unsigned*)(ws + WS_CNT) + mt * 128;
  const int ntl = ng < 6 ? 14 : 12;
  const int ctb = ng < 6 ? 14 * ng : 84 + 12 * (ng - 6);
  const bool two = w < ntl - 8;
  const bool full = w < 6;
  const int ks0 = w < 6 ? 7 * w : 42 + 6 * (w - 6);
  const int ct0 = ctb + (two ? 2 * w : (ntl - 8) + w);
  const int lo16 = lane * 16;
  const int w1s = (int)WS_W1F + ct0 * 1024;
  const int w2s = (int)WS_W2F + (ng * 5 * 54 + ks0) * 1024;

  u32x4 ring[32], wl[NW2L];
  const char* bb0 = xs + (8 * g + (i >> 2)) * 64 + ((g & 1) << 5) + (i & 3) * 8;
  const char* bb1 = xs + (8 * g + (i >> 2)) * 64 + (((g & 1) ^ 1) << 5) + (i & 3) * 8;
  auto w2_off = [&](int e) __attribute__((always_inline)) {
    const int s = e < 27 ? e / 4 : (e < 34 ? e - 27 : 6), c = e < 27 ? e % 4 : (e < 34 ? 4 : 3);
    const int sn = (s == 6 && !full) ? 5 : s;
    return w2s + (c * 54 + sn) * 1024;
  };

  auto gemm1 = [&](auto two_tag) __attribute__((always_inline)) {
    constexpr bool TWO = decltype(two_tag)::value;
    if (TWO) asm volatile("; two tiles" ::: "memory"); else asm volatile("; one tile" ::: "memory");
    u32x4 t[10];
    auto st_x = [&](int i0, int i1) __attribute__((always_inline)) {
#pragma unroll
      for (int it = i0; it < i1; ++it) {
        const int c = tid + 512 * it;
        const int row = c >> 3;
        *(u32x4*)(xs + ((c >> 2) & 1) * 40960 + row * 64 + (((c & 3) ^ ((row >> 2) & 2)) << 4)) = t[it];
      }
    };
#pragma unroll
    for (int it = 0; it < X_FIRST; ++it) t[it] = ld16(rs_, tid * 16, (int)WS_X1T + mt * 81920 + 8192 * it);
#pragma unroll
    for (int k = 0; k < W1_EARLY; ++k) {
      ring[2 * k] = ld16(rs_, lo16, w1s + k * 108 * 1024);
      if (TWO) ring[2 * k + 1] = ld16(rs_, lo16, w1s + (k * 108 + 1) * 1024);
    }
    SB();
    __builtin_amdgcn_s_barrier();
    SB();
#pragma unroll
    for (int k = W1_EARLY; k < W1_MID; ++k) {
      ring[2 * k] = ld16(rs_, lo16, w1s + k * 108 * 1024);
      if (TWO) ring[2 * k + 1] = ld16(rs_, lo16, w1s + (k * 108 + 1) * 1024);
    }
    SB();
    st_x(0, X_FIRST);
    SB();
    if (tid == 0) *lcnt = 0u;
    ModP gbp;
    if (!TWO) gbp = mod_ld(ws, 5 * D_ + 80 * ng + min((w & 1) * 64 + lane, 79));
    SB();
    __syncthreads();
    auto resid = [&]() __attribute__((always_inline)) {
      const int kb = (80 * ng) >> 5, tq = w & 3;
#pragma unroll
      for (int u = 0; u < 2; ++u) {
        const int kk = min(kb + 2 * (w >> 2) + u, 19);
        const u32x4 f = tr_frag(((tq & 1) ? bb1 : bb0) + (tq >> 1) * 40960 + kk * 2048);
        const unsigned dl = (unsigned)(32 * kk + 8 * g - 80 * ng);
        if (dl < 80u) *(u32x4*)(xr + (tq * 16 + i) * 176 + dl * 2) = f;
      }
    };
    const int hs = TWO ? 0 : (w & 1);
    const char* ba[4] = {bb0 + hs * 40960, bb1 + hs * 40960, bb0 + (hs ^ 1) * 40960, bb1 + (hs ^ 1) * 40960};
    const u32x4 ones = {0x3f803f80u, 0x3f803f80u, 0x3f803f80u, 0x3f803f80u};
    f32x4 acc[2][4];
#pragma unroll
    for (int c = 0; c < 2; ++c)
#pragma unroll
      for (int q = 0; q < 4; ++q) acc[c][q] = (f32x4){0.f, 0.f, 0.f, 0.f};
    u32x4 csp[5];
    const int cso = ((w & 1) * FF + 16 * ctb + 4 * min(lane, 4 * ntl - 1)) * 4;
    u32x4 bn[2][4];
#pragma unroll
    for (int q = 0; q < 4; ++q) bn[0][q] = tr_frag(ba[q]);
#pragma unroll
    for (int q = 0; q < 4; ++q) bn[1][q] = tr_frag(ba[q] + 2048);
#pragma unroll
    for (int kk = 0; kk < 20; ++kk) {
      if (kk == X_STEP) { st_x(X_FIRST, 10); SB(); }
      if (kk == 2 * X_FIRST - 2) { SB(); bar_lds(); SB(); }
      else if ((LOCK_MASK >> kk) & 1) { SB(); __builtin_amdgcn_s_barrier(); SB(); }
      if (kk == 2 * X_FIRST) { resid(); SB(); }
      auto gload = [&](int u) __attribute__((always_inline)) {
        {
          constexpr int CATCH = W1_PRE - W1_MID;
          const int k0 = kk == 0 ? W1_MID : (kk <= CATCH ? W1_MID + 2 * kk - 1 : kk + W1_PRE);
          const int nk = (kk >= 1 && kk <= CATCH) ? 2 : 1;
          const int kn = k0 + (nk == 2 ? (u >> 1) : 0);
          const bool on = nk == 2 || u >= 2;
          if (on && kn < 20) {
            if ((u & 1) == 0) ring[(2 * kn) % 32] = ld16(rs_, lo16, w1s + kn * 108 * 1024);
            else if (TWO) ring[(2 * kn + 1) % 32] = ld16(rs_, lo16, w1s + (kn * 108 + 1) * 1024);
          }
          if (kk == 0 && u < 2) {
#pragma unroll
            for (int it = X_FIRST + u * 3; it < (u ? 10 : (X_FIRST + 3 < 10 ? X_FIRST + 3 : 10)); ++it)
              t[it] = ld16(rs_, tid * 16, (int)WS_X1T + mt * 81920 + 8192 * it);
          }
        }
        if (kk >= W2_START && u < W2_RATE) {
          const int n = W2_RATE * (kk - W2_START) + u;
          if (n < NW2L) wl[n] = ld16(rs_, lo16, w2_off(NW2R + n));
          else if (n < W2_INL) ring[8 + n - NW2L] = ld16(rs_, lo16, w2_off(n - NW2L));
        }
      };
      const u32x4 a0 = ring[(2 * kk) % 32], a1 = ring[(2 * kk + 1) % 32];
#pragma unroll
      for (int q = 0; q < 4; ++q) {
        const u32x4 bq = bn[kk & 1][q];
        acc[0][q] = mfma_bf16(a0, bq, acc[0][q]);
        if (TWO) acc[1][q] = mfma_bf16(a1, bq, acc[1][q]);
        else if (q < 2) {
          acc[1][2 * q] = mfma_bf16(ones, bq, acc[1][2 * q]);
          acc[1][2 * q + 1] = mfma_bf16(bq, bq, acc[1][2 * q + 1]);
        }
        SB();
        if (kk < 18) bn[kk & 1][q] = tr_frag(ba[q] + (kk + 2) * 2048);
        gload(q);
        SB();
      }
      if (!TWO) {
        if (kk < 5) csp[kk] = ld16(rs_, cso, (int)WS_CSP + kk * 2 * FF * 4);
        if (kk == CS_STEP) {
          f32x4 t = __builtin_bit_cast(f32x4, csp[0]);
#pragma unroll
          for (int u = 1; u < 5; ++u) t += __builtin_bit_cast(f32x4, csp[u]);
          if (w >= 6 && lane < 56) *(f32x4*)(cst + (w & 1) * 224 + 4 * lane) = t;
        }
      }
      if (!TWO && kk == 12) { if ((w & 1) * 64 + lane < 80) tab[(w & 1) * 64 + lane] = mod_fin(gbp); }
      SB();
    }
    if (!TWO) {
#pragma unroll
      for (int h = 0; h < 2; ++h) {
        const f32x4 aq = acc[1][2 * h + 1];
        const float dq = (i & 2) ? ((i & 1) ? aq[3] : aq[2]) : ((i & 1) ? aq[1] : aq[0]);
        const int tk = (2 * hs + h) * 16 + i;
        if (g == (i >> 2)) {
          const float mean = acc[1][2 * h][0] * (1.f / D_);
          const float var = fmaxf(dq * (1.f / D_) - mean * mean, 0.f);
          const float sd = rbf(sqrtf(rbf(rbf(var) + 1e-5f)));
          const float rs = 1.f / sd;
          ps[tk] = rs;
          ps[64 + tk] = -rbf(mean) * rs;
        }
      }
    }
    __syncthreads();
    f32x4 csv[2], b1v[2];
#pragma unroll
    for (int c = 0; c < (TWO ? 2 : 1); ++c) {
      csv[c] = *(const f32x4*)(cst + 16 * (ct0 - ctb + c) + 4 * g);
      b1v[c] = *(const f32x4*)(cst + 224 + 16 * (ct0 - ctb + c) + 4 * g);
    }
#pragma unroll
    for (int qp = 0; qp < 2; ++qp) {
      u32x2 o[2][2];
#pragma unroll
      for (int h = 0; h < 2; ++h) {
        const int q = 2 * qp + h, tq = q ^ (2 * hs);
        const float rs = ps[tq * 16 + i], nm = ps[64 + tq * 16 + i];
#pragma unroll
        for (int c = 0; c < (TWO ? 2 : 1); ++c) {
          float v[4];
#pragma unroll
          for (int r = 0; r < 4; ++r) v[r] = fmaxf(fmaf(acc[c][q][r], rs, fmaf(nm, csv[c][r], b1v[c][r])), 0.f);
          o[c][h] = (u32x2){pk_bf(v[0], v[1]), pk_bf(v[2], v[3])};
        }
      }
      const int tl = ((2 * qp) ^ (2 * hs)) + (g & 1);
#pragma unroll
      for (int c = 0; c < (TWO ? 2 : 1); ++c) {
        const int ct = ct0 + c;
        const auto s0 = __builtin_amdgcn_permlane16_swap(o[c][0][0], o[c][1][0], false, false);
        const auto s1 = __builtin_amdgcn_permlane16_swap(o[c][0][1], o[c][1][1], false, false);
        const size_t off = (((size_t)(4 * mt + tl) * 54 + (ct >> 1)) * 64 + i + 16 * (2 * (ct & 1) + (g >> 1))) * 16;
        st16(rs_, WS_H1F + off, (u32x4){s0[0], s1[0], s0[1], s1[1]});
      }
    }
    SB();
    __builtin_amdgcn_s_barrier();
    SB();
    constexpr int E0 = W2_INL - NW2L;
    auto signal = [&]() __attribute__((always_inline)) {
      if (lane == 0) {
        if (__hip_atomic_fetch_add(lcnt, 1u, __ATOMIC_RELAXED, __HIP_MEMORY_SCOPE_WORKGROUP) == 7u)
          __hip_atomic_store(flags + 16 * ng, 1u, __ATOMIC_RELAXED, __HIP_MEMORY_SCOPE_AGENT);
      }
    };
    if (w >= SIG_SPLIT) { asm volatile("s_waitcnt vmcnt(0)" ::: "memory"); signal(); }
    SB();
#pragma unroll
    for (int e = E0; e < NW2R; ++e) ring[(8 + e) % 32] = ld16(rs_, lo16, w2_off(e));
    SB();
    if (w < SIG_SPLIT) { asm volatile("s_waitcnt vmcnt(%0)" :: "n"(NW2R - E0) : "memory"); signal(); }
    SB();
  };
  if (two) gemm1(std::true_type{});
  else gemm1(std::false_type{});
#pragma nounroll
  for (unsigned spin = 0; spin < (1u << 20); ++spin) {
    if (__hip_atomic_load(flags + 16 * w, __ATOMIC_RELAXED, __HIP_MEMORY_SCOPE_AGENT) != 0u) break;
    __builtin_amdgcn_s_sleep(1);
  }
  asm volatile("" ::: "memory");
  SB();

  const int h1s = (int)WS_H1F + (mt * 4 * 54 + ks0) * 1024;
  u32x4 hb[28];
  auto load_h1 = [&](int n) __attribute__((always_inline)) {
    const int q = n / 7, s = n % 7;
    const int sn = (s == 6 && !full) ? 5 : s;
    hb[n] = ld16_sc1(rs_, lo16, h1s + (q * 54 + sn) * 1024);
  };
#pragma unroll
  for (int n = 0; n < H1_PRE; ++n) {
    if (n == H1_BATCH) { SB(); __builtin_amdgcn_s_barrier(); SB(); }
    load_h1(n);
  }
  SB();
  float* red = (float*)smem;
  float* rb_ = red + (w * 16 + i) * KF_RS + 4 * g;
  const int re = w * 40 + min(lane, 39);
  const int rtk = re / 20, rq4 = re % 20;
  const float* rr_ = red + rtk * KF_RS + 4 * rq4;
  f32x4 ac2[2][5];
  f32x4 rv[8], rsum;
  u32x2 xv2;
  const f32x4 gb = *(const f32x4*)(tab + 4 * rq4);
  auto red_wr = [&](int q, int c) __attribute__((always_inline)) {
    *(f32x4*)(rb_ + (q & 1) * (KF_RB / 4) + 16 * c) = ac2[q & 1][c];
  };
  auto red_rd = [&](int q, int ww) __attribute__((always_inline)) {
    rv[ww] = *(const f32x4*)(rr_ + (q & 1) * (KF_RB / 4) + ww * 16 * KF_RS);
  };
  auto red_x = [&](int q) __attribute__((always_inline)) { xv2 = *(const u32x2*)(xr + (16 * q + rtk) * 176 + rq4 * 8); };
  auto red_out = [&](int q) __attribute__((always_inline)) {
    const f32x4 s = rsum + (rv[6] + rv[7]);
    u32x2 o;
#pragma unroll
    for (int h = 0; h < 2; ++h) {
      unsigned f = pk_bf(s[2 * h], s[2 * h + 1]);
      unsigned gf = pk_bf(bf_lo(f) * gb[2 * h], bf_hi(f) * gb[2 * h + 1]);
      o[h] = pk_bf(bf_lo(xv2[h]) + bf_lo(gf), bf_hi(xv2[h]) + bf_hi(gf));
    }
    *(u32x2*)(out + (size_t)(mt * 64 + 16 * q + rtk) * D_ + ng * 80 + 4 * rq4) = o;
  };
#pragma unroll
  for (int q = 0; q < 3; ++q) {
#pragma unroll
    for (int c = 0; c < 5; ++c) ac2[q & 1][c] = (f32x4){0.f, 0.f, 0.f, 0.f};
#pragma unroll
    for (int s = 0; s < 7; ++s) {
      auto mm = [&](int c) __attribute__((always_inline)) {
        const u32x4 a = c == 4 ? wl[s] : ((s == 6 && c == 3) ? wl[7] : ring[(8 + 4 * s + c) % 32]);
        ac2[q & 1][c] = mfma_bf16(a, hb[7 * q + s], ac2[q & 1][c]);
      };
      if (s == 5 && q >= 2) {
        red_wr(q - 1, 4);
        SB();
        red_out(q - 2);
#pragma unroll
        for (int c = 0; c < 5; ++c) mm(c);
#pragma unroll
        for (int c = 0; c < 5; ++c) {
          __builtin_amdgcn_sched_group_barrier(0x008, 1, 0);
          __builtin_amdgcn_sched_group_barrier(0x002, 10, 0);
        }
        SB();
      } else if (s < 6) {
        mm(0); SB();
        if (q >= 1 && s >= 1) { red_wr(q - 1, s - 1); SB(); }
        mm(1); SB();
        if (q >= 2 && s < 4) { red_rd(q - 2, 2 * s); SB(); }
        mm(2); SB();
        if (q >= 2 && s < 4) { red_rd(q - 2, 2 * s + 1); SB(); }
        if (q >= 2 && s == 4) { red_x(q - 2); SB(); }
        mm(3); SB();
        if (q >= 2 && s >= 1 && s < 4) {
          const f32x4 t = rv[2 * s - 2] + rv[2 * s - 1];
          rsum = s == 1 ? t : rsum + t;
          asm volatile("" : "+v"(rsum));
          SB();
        }
        mm(4); SB();
      } else if (full) {
#pragma unroll
        for (int c = 0; c < 5; ++c) mm(c);
        SB();
      }
      if (7 * q + s + H1_PRE < 28) { load_h1(7 * q + s + H1_PRE); SB(); }
    }
    if (q >= 1) bar_lds();
  }
  {
    auto sum6 = [&]() __attribute__((always_inline)) {
      rsum = ((rv[0] + rv[1]) + (rv[2] + rv[3])) + (rv[4] + rv[5]);
    };
    if (full) {
#pragma unroll
      for (int c = 0; c < 5; ++c) {
        const u32x4 a = c == 4 ? wl[6] : (c == 3 ? wl[7] : ring[(8 + 24 + c) % 32]);
        rv[c] = mfma_bf16(a, hb[27], (f32x4){0.f, 0.f, 0.f, 0.f});
      }
    } else {
#pragma unroll
      for (int c = 0; c < 5; ++c) rv[c] = (f32x4){0.f, 0.f, 0.f, 0.f};
    }
    f32x4 c0[5];
#pragma unroll
    for (int c = 0; c < 5; ++c) c0[c] = rv[c];
    SB();
    auto col = [&](int c, auto&& between) __attribute__((always_inline)) {
#pragma unroll
      for (int s = 0; s < 6; ++s) {
        const u32x4 a = c == 4 ? wl[s] : ring[(8 + 4 * s + c) % 32];
        c0[c] = mfma_bf16(a, hb[21 + s], c0[c]);
        SB();
        between(s);
        SB();
      }
    };
    col(0, [&](int s) __attribute__((always_inline)) { if (s < 5) red_wr(2, s); if (s < 4) { red_rd(1, 2 * s); red_rd(1, 2 * s + 1); } if (s == 4) red_x(1); });
    col(1, [&](int s) __attribute__((always_inline)) { if (s == 1) { sum6(); asm volatile("" : "+v"(rsum)); } if (s == 3) red_out(1); });
    bar_lds();
    auto wr3 = [&](int c) __attribute__((always_inline)) { *(f32x4*)(rb_ + (KF_RB / 4) + 16 * c) = c0[c]; };
    col(2, [&](int s) __attribute__((always_inline)) { if (s < 2) wr3(s); if (s >= 1 && s < 5) { red_rd(2, 2 * s - 2); red_rd(2, 2 * s - 1); } if (s == 5) red_x(2); });
    col(3, [&](int s) __attribute__((always_inline)) { if (s == 1) wr3(2); if (s == 2) { sum6(); asm volatile("" : "+v"(rsum)); } if (s == 4) red_out(2); });
    col(4, [&](int s) __attribute__((always_inline)) { if (s == 1) wr3(3); if (s == 2) red_x(3); });
    wr3(4);
    bar_lds();
#pragma unroll
    for (int ww = 0; ww < 8; ++ww) red_rd(3, ww);
    sum6();
    red_out(3);
  }
}

__global__ NOPK __launch_bounds__(512, 2) void kf(char* __restrict__ ws, unsigned short* __restrict__ out) {
  __shared__ __attribute__((aligned(16))) char smem[KF_LDS];
  kf_body(ws, out, smem);
}

__global__ void kend_() {}

extern "C" void kernel_launch(void* const* d_in, const int* in_sizes, int n_in,
                              void* d_out, int out_size, void* d_ws, size_t ws_size,
                              hipStream_t stream) {
  K1Args a;
  a.x = (const unsigned short*)d_in[0];
  a.are = (const unsigned short*)d_in[1];
  a.aim = (const unsigned short*)d_in[2];
  a.bre = (const unsigned short*)d_in[3];
  a.bim = (const unsigned short*)d_in[4];
  a.cre = (const unsigned short*)d_in[5];
  a.cim = (const unsigned short*)d_in[6];
  a.logdt = (const unsigned short*)d_in[7];
  a.c = (const unsigned short*)d_in[12];
  a.wmod = (const unsigned short*)d_in[13];
  a.ws = (char*)d_ws;
  k1<<<NB_PF + NB_K + NB_LN + NB_MOD, 256, 0, stream>>>(a);
  k2<<<256, 512, 0, stream>>>((const unsigned short*)d_in[8], (char*)d_ws, (const unsigned short*)d_in[10],
                              (const unsigned short*)d_in[11]);
  kf<<<256, 512, 0, stream>>>((char*)d_ws, (unsigned short*)d_out);
}
```

```cpp
#define PF 1
#include <hip/hip_runtime.h>
#include <hip/hip_bf16.h>
#include <stdint.h>
#include <type_traits>

typedef __attribute__((ext_vector_type(8))) __bf16 bf16x8;
typedef __attribute__((ext_vector_type(2))) __bf16 bf16x2;
typedef __attribute__((ext_vector_type(4))) float f32x4;
typedef __attribute__((ext_vector_type(2))) float f32x2;
typedef __attribute__((ext_vector_type(4))) unsigned u32x4;
typedef __attribute__((ext_vector_type(2))) unsigned u32x2;
typedef __attribute__((ext_vector_type(8))) _Float16 h16x8;
typedef __attribute__((ext_vector_type(2))) _Float16 h16x2;
typedef __attribute__((ext_vector_type(4))) short s16x4;
typedef __attribute__((address_space(3))) s16x4 lds_s16x4;

#if defined(__HIP_DEVICE_COMPILE__)
#define NOPK __attribute__((target("no-packed-fp32-ops")))
#else
#define NOPK
#endif
#define DEV __device__ __forceinline__

constexpr int L_ = 2048;
constexpr int D_ = 640;
constexpr int NS = 128;
constexpr int FF = 1728;
constexpr int KREV = 2304;

constexpr size_t WS_KSC  = 8192;
constexpr size_t WS_CNT  = 16384;
constexpr size_t WS_CSB  = 32768;
constexpr size_t WS_MOD  = 65536;
constexpr size_t WS_XNT  = 196608;
constexpr size_t WS_XT   = WS_XNT + (size_t)D_ * L_ * 2;
constexpr size_t WS_KREV = WS_XT + (size_t)D_ * L_ * 2;
constexpr size_t WS_W1F  = WS_KREV + (size_t)D_ * 2 * KREV * 2;
constexpr size_t WS_W2F  = WS_W1F + (size_t)D_ * FF * 2;
constexpr size_t WS_X1T  = WS_W2F + (size_t)D_ * FF * 2;
constexpr size_t WS_X1   = WS_X1T + (size_t)D_ * L_ * 2;
constexpr size_t WS_CSP  = WS_X1;
constexpr size_t WS_H1F  = WS_X1 + (size_t)D_ * L_ * 2;

#define STAMP(kid, k) do {} while (0)

#define TS(k, wsp) do { if (blockIdx.x == 0 && threadIdx.x == 0) ((unsigned long long*)((wsp) + (200u << 20)))[k] = __builtin_amdgcn_s_memrealtime(); } while (0)
DEV float bf_lo(unsigned u) { return __uint_as_float(u << 16); }
DEV float bf_hi(unsigned u) { return __uint_as_float(u & 0xffff0000u); }
DEV unsigned pk_bf(float a, float b) {
  f32x2 v = {a, b};
  bf16x2 r = __builtin_convertvector(v, bf16x2);
  return __builtin_bit_cast(unsigned, r);
}
DEV float rbf(float a) { return bf_lo(pk_bf(a, 0.f)); }
DEV float ldbf(const unsigned short* p) { return __uint_as_float(((unsigned)*p) << 16); }

typedef __amdgpu_buffer_rsrc_t rsrc_t;
DEV rsrc_t ws_rsrc(char* ws) { return __builtin_amdgcn_make_buffer_rsrc((void*)ws, 0, 0x7fffffff, 0x00020000); }
#define ST_AUX 16
DEV void st16(rsrc_t r, size_t off, u32x4 v) { __builtin_amdgcn_raw_buffer_store_b128(v, r, (int)off, 0, ST_AUX); }
#define LN_AUX 16
DEV void st16ln(rsrc_t r, size_t off, u32x4 v) { __builtin_amdgcn_raw_buffer_store_b128(v, r, (int)off, 0, LN_AUX); }
DEV void st8(rsrc_t r, size_t off, u32x2 v) { __builtin_amdgcn_raw_buffer_store_b64(v, r, (int)off, 0, 16); }

DEV u32x2 tr_read(unsigned lds_addr) {
  u32x2 r;
  asm volatile("ds_read_b64_tr_b16 %0, %1" : "=v"(r) : "v"(lds_addr) : "memory");
  return r;
}
DEV void bar_lds() { asm volatile("s_waitcnt lgkmcnt(0)\n\ts_barrier" ::: "memory"); }
DEV void lds_wait(const void* word, unsigned val) {
  unsigned tv, ts, cnt;
  asm volatile("s_mov_b32 %2, 0x100000\n"
               "1:\n\tds_read_b32 %0, %3\n\ts_waitcnt lgkmcnt(0)\n\tv_readfirstlane_b32 %1, %0\n\t"
               "s_cmp_ge_u32 %1, %4\n\ts_cbranch_scc1 2f\n\ts_sleep 1\n\ts_sub_u32 %2, %2, 1\n\t"
               "s_cmp_lg_u32 %2, 0\n\ts_cbranch_scc1 1b\n2:"
               : "=&v"(tv), "=&s"(ts), "=&s"(cnt) : "v"((unsigned)(size_t)word), "s"(val) : "memory", "scc");
}
DEV void lds_post(unsigned* word, int lane) {
  asm volatile("" ::: "memory");
  if (lane == 0) __hip_atomic_fetch_add(word, 1u, __ATOMIC_RELAXED, __HIP_MEMORY_SCOPE_WORKGROUP);
  asm volatile("" ::: "memory");
}
DEV void lgkm0() { asm volatile("s_waitcnt lgkmcnt(0)" ::: "memory"); __builtin_amdgcn_sched_barrier(0); }

DEV f32x4 mfma_bf16(u32x4 a, u32x4 b, f32x4 c) {
  return __builtin_amdgcn_mfma_f32_16x16x32_bf16(__builtin_bit_cast(bf16x8, a), __builtin_bit_cast(bf16x8, b), c, 0, 0, 0);
}

constexpr int NB_MOD = 120, NB_LN = 256, NB_W2 = 108, NB_K = 640;

struct K1Args {
  const unsigned short *x, *are, *aim, *bre, *bim, *cre, *cim, *logdt, *w1, *w2, *c, *wmod;
  const unsigned short *dskip, *out;
  char* ws;
};

constexpr int NB_PF = 8;
DEV void k1_prefetch(const K1Args& p, int b) {
  const int tid = threadIdx.x;
  unsigned long long pc;
  int o0, o1;
  asm volatile("s_getpc_b64 %0\n.Lpf_here%=:\n\ts_mov_b32 %1, _Z2k16K1Args - .Lpf_here%=\n\ts_mov_b32 %2, _Z5kend_v - .Lpf_here%="
               : "=s"(pc), "=s"(o0), "=s"(o1));
  const char* c0 = (const char*)(pc + (long long)o0);
  long n = o1 - o0;
  n = n < 0 ? 0 : (n > 98304 ? 98304 : n);
  unsigned acc = 0;
  {
    unsigned v[3];
#pragma unroll
    for (int it = 0; it < 3; ++it) {
      const long o = (long)tid * 128 + it * 32768;
      v[it] = o < n ? *(const __attribute__((address_space(1))) unsigned*)(c0 + o) : 0u;
    }
    acc = v[0] ^ v[1] ^ v[2];
  }
  asm volatile("" :: "v"(acc));
}

DEV void k1_mod(const K1Args& p, int b, char* smem) {
  const int tid = threadIdx.x;
  const int rg = b & 7, cgp = b >> 3;
  const int c32 = tid & 31, r8 = tid >> 5;
  float* sil = (float*)smem;
  float* red = (float*)(smem + 512);
  u32x4 w[10];
#pragma unroll
  for (int it = 0; it < 10; ++it)
    w[it] = *(const u32x4*)(p.wmod + (size_t)(rg * 80 + r8 + 8 * it) * 3840 + 256 * cgp + 8 * c32);
  if (tid < 80) {
    float cv = ldbf(p.c + rg * 80 + tid);
    sil[tid] = rbf(cv / (1.f + expf(-cv)));
  }
  if (tid >= 128 && tid < 132 && b < 64) ((unsigned*)(p.ws + WS_CNT))[(b * 4 + tid - 128) * 16] = 0u;
  __syncthreads();
  float acc[8];
#pragma unroll
  for (int j = 0; j < 8; ++j) acc[j] = 0.f;
#pragma unroll
  for (int it = 0; it < 10; ++it) {
    float s = sil[r8 + 8 * it];
#pragma unroll
    for (int q = 0; q < 4; ++q) {
      acc[2 * q] += s * bf_lo(w[it][q]);
      acc[2 * q + 1] += s * bf_hi(w[it][q]);
    }
  }
#pragma unroll
  for (int j = 0; j < 8; ++j) acc[j] += __shfl_xor(acc[j], 32, 64);
  if ((tid & 63) < 32) {
    *(f32x4*)(red + (tid >> 6) * 256 + 8 * c32) = (f32x4){acc[0], acc[1], acc[2], acc[3]};
    *(f32x4*)(red + (tid >> 6) * 256 + 8 * c32 + 4) = (f32x4){acc[4], acc[5], acc[6], acc[7]};
  }
  __syncthreads();
  ((float*)(p.ws + WS_MOD))[(256 * cgp + tid) * 8 + rg] = (red[tid] + red[256 + tid]) + (red[512 + tid] + red[768 + tid]);
}
struct ModP { f32x4 a, b; };
DEV ModP mod_ld(const char* ws, int idx) {
  const f32x4* mp = (const f32x4*)(ws + WS_MOD) + 2 * idx;
  ModP m; m.a = mp[0]; m.b = mp[1];
  return m;
}
DEV float mod_fin(const ModP& m) {
  return rbf(((m.a[0] + m.a[1]) + (m.a[2] + m.a[3])) + ((m.b[0] + m.b[1]) + (m.b[2] + m.b[3])));
}
DEV float mod_sum(const char* ws, int idx) { return mod_fin(mod_ld(ws, idx)); }
#define SB() __builtin_amdgcn_sched_barrier(0)

DEV void k1_ln(const K1Args& p, int b, char* smem) {
  constexpr int STR = 704;
  const int tid = threadIdx.x;
  const int tok = tid >> 3, sub = tid & 7;
  const int tile = (b & 7) * 8 + ((b >> 3) & 7), pass = b >> 6;
  const int hf = pass & 1;
  const bool norm = pass < 2;
  const int t0 = tile * 32;
  const unsigned short* xr = p.x + (size_t)(t0 + tok) * D_ + 8 * sub;
  u32x4 xv[5], xo[5];
#pragma unroll
  for (int m = 0; m < 5; ++m) xv[m] = *(const u32x4*)(xr + 320 * hf + 64 * m);
  if (norm) {
#pragma unroll
    for (int m = 0; m < 5; ++m) xo[m] = *(const u32x4*)(xr + 320 * (hf ^ 1) + 64 * m);
    float s = 0.f, v = 0.f;
    const bf16x2 one2 = __builtin_bit_cast(bf16x2, 0x3f803f80u);
#pragma unroll
    for (int m = 0; m < 10; ++m)
#pragma unroll
      for (int q = 0; q < 4; ++q) {
        const unsigned u = m < 5 ? xv[m][q] : xo[m - 5][q];
        const bf16x2 x2 = __builtin_bit_cast(bf16x2, u);
        s = __builtin_amdgcn_fdot2_f32_bf16(x2, one2, s, false);
        v = __builtin_amdgcn_fdot2_f32_bf16(x2, x2, v, false);
      }
#pragma unroll
    for (int o = 1; o < 8; o <<= 1) { s += __shfl_xor(s, o, 64); v += __shfl_xor(v, o, 64); }
    const float mean = s * (1.f / D_);
    const float var = fmaxf(v * (1.f / D_) - mean * mean, 0.f);
    const float sd = rbf(sqrtf(rbf(rbf(var) + 1e-5f)));
    const float rs = 1.f / sd;
    const float mb = rbf(mean);
#pragma unroll
    for (int m = 0; m < 5; ++m)
#pragma unroll
      for (int q = 0; q < 4; ++q) {
        unsigned d2 = pk_bf(bf_lo(xv[m][q]) - mb, bf_hi(xv[m][q]) - mb);
        xv[m][q] = pk_bf(bf_lo(d2) * rs, bf_hi(d2) * rs);
      }
  }
  const int lane = tid & 63, w = tid >> 6;
  const int g = lane >> 4, i = lane & 15;
  const rsrc_t rsrc = ws_rsrc(p.ws);
#pragma unroll
  for (int m = 0; m < 5; ++m) *(u32x4*)(smem + tok * STR + (sub + 8 * m) * 16) = xv[m];
  __syncthreads();
  const size_t dsto = (norm ? WS_XNT : WS_XT) + ((size_t)tile * D_ + 320 * hf) * 64;
#pragma unroll
  for (int it = 0; it < 5; ++it) {
    const int cb = w * 5 + it;
    const char* a = smem + (8 * g + (i >> 2)) * STR + cb * 32 + (i & 3) * 8;
    const u32x2 ra = __builtin_bit_cast(u32x2, __builtin_amdgcn_ds_read_tr16_b64_v4i16((lds_s16x4*)a));
    const u32x2 rb = __builtin_bit_cast(u32x2, __builtin_amdgcn_ds_read_tr16_b64_v4i16((lds_s16x4*)(a + 4 * STR)));
    st16ln(rsrc, dsto + (size_t)(cb * 16 + i) * 64 + 16 * g, (u32x4){ra[0], ra[1], rb[0], rb[1]});
  }
}

constexpr int RT_STR = 176;
template <int NF> struct RtRaw { u32x4 r[NF]; };
template <int NF>
DEV void rt_load(RtRaw<NF>& t, const unsigned short* W, int N, int kk, int ct0, int lane) {
#pragma unroll
  for (int j = 0; j < NF; ++j) {
    const int c = lane + 64 * j, row = c / (2 * NF), part = c % (2 * NF);
    t.r[j] = *(const u32x4*)(W + (size_t)(32 * kk + row) * N + 16 * ct0 + 8 * part);
  }
}
template <int NF>
DEV void rt_park(const RtRaw<NF>& t, char* scr, int lane) {
#pragma unroll
  for (int j = 0; j < NF; ++j) {
    const int c = lane + 64 * j, row = c / (2 * NF), part = c % (2 * NF);
    *(u32x4*)(scr + row * RT_STR + 16 * part) = t.r[j];
  }
}
DEV u32x4 rt_frag(const char* scr, int j, int lane) {
  const int g = lane >> 4, i = lane & 15;
  const char* p = scr + (8 * g + (i >> 2)) * RT_STR + 32 * j + (i & 3) * 8;
  const u32x2 a = __builtin_bit_cast(u32x2, __builtin_amdgcn_ds_read_tr16_b64_v4i16((lds_s16x4*)p));
  const u32x2 b = __builtin_bit_cast(u32x2, __builtin_amdgcn_ds_read_tr16_b64_v4i16((lds_s16x4*)(p + 4 * RT_STR)));
  return (u32x4){a[0], a[1], b[0], b[1]};
}
struct cf { float re, im; };
DEV cf cmul(cf a, cf b) {
  cf r;
  r.re = a.re * b.re - a.im * b.im;
  r.im = a.re * b.im + a.im * b.re;
  return r;
}
DEV cf cexp_(float a2, float br, float m, float m0) {
  float mag = __builtin_amdgcn_exp2f(a2 * (m - m0));
  float ph = __builtin_amdgcn_fractf(br * m);
  cf r; r.re = mag * __builtin_amdgcn_cosf(ph); r.im = mag * __builtin_amdgcn_sinf(ph);
  return r;
}
DEV unsigned pk_h(float a, float b) {
  f32x2 v = {a, b};
  h16x2 r = __builtin_convertvector(v, h16x2);
  return __builtin_bit_cast(unsigned, r);
}
DEV f32x4 mfma_f16(u32x4 a, u32x4 b, f32x4 c) {
  return __builtin_amdgcn_mfma_f32_16x16x32_f16(__builtin_bit_cast(h16x8, a), __builtin_bit_cast(h16x8, b), c, 0, 0, 0);
}
DEV void put2(char* p, cf a, cf b) {
  u32x2 o = {pk_h(a.re, a.im), pk_h(b.re, b.im)};
  *(u32x2*)p = o;
}

constexpr int KG_RS = 544;
DEV void k1_kgen(const K1Args& p, int d, char* smem) {
  constexpr int RS = KG_RS;
  const int tid = threadIdx.x;
  const int lane = tid & 63;
  const int w = __builtin_amdgcn_readfirstlane(tid >> 6);
  const int g = lane >> 4, i = lane & 15;
  char* stamp_ws = p.ws; (void)stamp_ws;
  const size_t po = (size_t)d * NS + 2 * lane;
  const unsigned rar = *(const unsigned*)(p.are + po), rai = *(const unsigned*)(p.aim + po);
  const unsigned rbr = *(const unsigned*)(p.bre + po), rbi = *(const unsigned*)(p.bim + po);
  const unsigned rcr = *(const unsigned*)(p.cre + po), rci = *(const unsigned*)(p.cim + po);
  const float ldt = ldbf(p.logdt + d);
  asm volatile("" ::: "memory");
  SB();
  const float dt = rbf(expf(ldt));
  STAMP(0, 1);
  float a2[2], bv[2], m0[2];
  a2[0] = (dt * bf_lo(rar)) * 1.44269504088896341f; a2[1] = (dt * bf_hi(rar)) * 1.44269504088896341f;
  bv[0] = (dt * bf_lo(rai)) * 0.15915494309189534f; bv[1] = (dt * bf_hi(rai)) * 0.15915494309189534f;
  m0[0] = a2[0] > 0.f ? 2048.f : 0.f; m0[1] = a2[1] > 0.f ? 2048.f : 0.f;
  char* my = smem + lane * 8;
  if (w == 0) {
    cf v[2], v8[2], e8[2];
    float bound = 0.f;
#pragma unroll
    for (int q = 0; q < 2; ++q) {
      const float dbr = dt * (q ? bf_hi(rbr) : bf_lo(rbr)), dbi = dt * (q ? bf_hi(rbi) : bf_lo(rbi));
      const float cr = q ? bf_hi(rcr) : bf_lo(rcr), ci = q ? bf_hi(rci) : bf_lo(rci);
      const float gsh = __builtin_amdgcn_exp2f(a2[q] * m0[q]);
      v[q].re = (cr * dbr - ci * dbi) * gsh; v[q].im = -(cr * dbi + ci * dbr) * gsh;
      bound += (fabsf(v[q].re) + fabsf(v[q].im)) * __builtin_amdgcn_exp2f(fmaxf(a2[q], 0.f) * 120.f);
      e8[q] = cexp_(a2[q], -bv[q], 8.f, 0.f);
      v8[q] = cexp_(a2[q], -bv[q], 64.f, 0.f);
    }
#pragma unroll
    for (int o = 1; o < 64; o <<= 1) bound += __shfl_xor(bound, o, 64);
    int ex = (int)((__float_as_uint(bound) >> 23) & 0xff);
    ex = ex < 20 ? 20 : (ex > 240 ? 240 : ex);
    const float sv = __uint_as_float((unsigned)(127 + 13 + 127 - ex) << 23);
    if (lane == 0) ((float*)(p.ws + WS_KSC))[d] = __uint_as_float((unsigned)(ex - 13) << 23);
#pragma unroll
    for (int q = 0; q < 2; ++q) { v[q].re *= sv; v[q].im *= sv; v8[q] = cmul(v[q], v8[q]); }
#pragma unroll
    for (int j = 0; j < 8; ++j) {
      put2(my + (41 + j) * RS, v[0], v[1]);
      put2(my + (49 + j) * RS, v8[0], v8[1]);
      if (j < 7) {
#pragma unroll
        for (int q = 0; q < 2; ++q) { v[q] = cmul(v[q], e8[q]); v8[q] = cmul(v8[q], e8[q]); }
      }
    }
  } else if (w == 1) {
    cf e1[2], u[2], u2[2];
#pragma unroll
    for (int q = 0; q < 2; ++q) {
      e1[q] = cexp_(a2[q], bv[q], 1.f, 0.f);
      u[q].re = __builtin_amdgcn_exp2f(-a2[q] * m0[q]); u[q].im = 0.f;
      u2[q] = cexp_(a2[q], bv[q], 128.f, m0[q]);
    }
#pragma unroll
    for (int r = 0; r < 8; ++r) {
      put2(my + r * RS, u[0], u[1]);
      put2(my + (8 + r) * RS, u2[0], u2[1]);
      if (r < 7) {
#pragma unroll
        for (int q = 0; q < 2; ++q) { u[q] = cmul(u[q], e1[q]); u2[q] = cmul(u2[q], e1[q]); }
      }
    }
  } else if (w == 2) {
    cf e2[2], u[2], u2[2];
#pragma unroll
    for (int q = 0; q < 2; ++q) {
      e2[q] = cexp_(a2[q], bv[q], 2.f, 0.f);
      u[q] = cexp_(a2[q], bv[q], 256.f, m0[q]);
      u2[q] = cexp_(a2[q], bv[q], 384.f, m0[q]);
    }
#pragma unroll
    for (int r = 0; r < 4; ++r) {
      put2(my + (16 + r) * RS, u[0], u[1]);
      put2(my + (20 + r) * RS, u2[0], u2[1]);
      if (r < 3) {
#pragma unroll
        for (int q = 0; q < 2; ++q) { u[q] = cmul(u[q], e2[q]); u2[q] = cmul(u2[q], e2[q]); }
      }
    }
    cf e4[2] = {cmul(e2[0], e2[0]), cmul(e2[1], e2[1])};
#pragma unroll
    for (int r = 0; r < 2; ++r) {
      cf c0 = cexp_(a2[0], bv[0], 512.f + 128.f * r, m0[0]), c1 = cexp_(a2[1], bv[1], 512.f + 128.f * r, m0[1]);
      put2(my + (24 + 2 * r) * RS, c0, c1);
      put2(my + (25 + 2 * r) * RS, cmul(c0, e4[0]), cmul(c1, e4[1]));
    }
  } else {
    cf e4[2], e128[2], u[2], u2[2];
#pragma unroll
    for (int q = 0; q < 2; ++q) {
      e4[q] = cexp_(a2[q], bv[q], 4.f, 0.f);
      e128[q] = cexp_(a2[q], bv[q], 128.f, 0.f);
      u[q] = cexp_(a2[q], bv[q], 1024.f, m0[q]);
      u2[q] = cexp_(a2[q], bv[q], 1536.f, m0[q]);
    }
#pragma unroll
    for (int r = 2; r < 4; ++r) {
      cf c0 = cexp_(a2[0], bv[0], 512.f + 128.f * r, m0[0]), c1 = cexp_(a2[1], bv[1], 512.f + 128.f * r, m0[1]);
      put2(my + (24 + 2 * r) * RS, c0, c1);
      put2(my + (25 + 2 * r) * RS, cmul(c0, e4[0]), cmul(c1, e4[1]));
    }
#pragma unroll
    for (int r = 0; r < 4; ++r) {
      put2(my + (32 + r) * RS, u[0], u[1]);
      put2(my + (36 + r) * RS, u2[0], u2[1]);
#pragma unroll
      for (int q = 0; q < 2; ++q) { if (r < 3) u[q] = cmul(u[q], e128[q]); u2[q] = cmul(u2[q], e128[q]); }
    }
    put2(my + 40 * RS, u2[0], u2[1]);
  }
  __syncthreads();
  STAMP(0, 2);
  _Float16* kh = (_Float16*)(smem + 57 * RS);
  if (w < 3) {
    const char* ua = smem + (16 * w + i) * RS + g * 16;
    const char* va = smem + (41 + i) * RS + g * 16;
    f32x4 acc = {0.f, 0.f, 0.f, 0.f};
#pragma unroll
    for (int ks = 0; ks < 8; ++ks)
      acc = mfma_f16(*(const u32x4*)(ua + ks * 64), *(const u32x4*)(va + ks * 64), acc);
    int idx0, st;
    if (w == 0) { idx0 = (g < 2 ? 4 * g : 120 + 4 * g) + 8 * i; st = 1; }
    else if (w == 1) { idx0 = g < 2 ? 256 + 64 * g + 4 * i : 384 + 64 * (g - 2) + 2 * i; st = 1; }
    else { idx0 = 512 + 64 * g + i; st = 16; }
#pragma unroll
    for (int rg = 0; rg < 4; ++rg) {
      int idx = idx0 + st * rg;
      if (w == 1 && g >= 2) idx = idx0 + 32 * (rg >> 1) + (rg & 1);
      if (w == 2 && g >= 2) idx = (g == 2 && rg == 0 && i == 0) ? 640 : 644;
      kh[idx] = (_Float16)acc[rg];
    }
  }
  __syncthreads();
  STAMP(0, 3);
  const unsigned short* ku = (const unsigned short*)kh;
  const size_t kout = WS_KREV + (size_t)d * KREV * 2;
  {
    const int l0 = 2040 - 8 * tid;
    u32x4 o;
    if (w < 2) {
      const int q = l0 >> 3;
      const unsigned a = ku[384 + q], b = ku[385 + q];
      const unsigned t = (q & 1) ? b : a;
      o[0] = b | (b << 16); o[1] = b | (t << 16); o[2] = a | (a << 16); o[3] = o[2];
    } else if (w == 2) {
      const int q = l0 >> 2;
      const unsigned a = ku[256 + q], b = ku[257 + q], c = ku[258 + q];
      o[0] = c | (c << 16); o[1] = b | (b << 16); o[2] = b | (a << 16); o[3] = a | (a << 16);
    } else {
      unsigned v[8];
#pragma unroll
      for (int e = 0; e < 8; ++e) {
        const int l = l0 + 7 - e;
        const int idx = l < 256 ? l : (int)(pk_bf((float)l, 0.f) & 0xffffu) - 17024;
        v[e] = ku[idx];
      }
#pragma unroll
      for (int q = 0; q < 4; ++q) o[q] = v[2 * q] | (v[2 * q + 1] << 16);
    }
    st16(ws_rsrc(p.ws), kout + 16 * tid, o);
  }
}

__global__ NOPK __launch_bounds__(256) void k1(K1Args p) {
  __shared__ __attribute__((aligned(16))) char smem[57 * KG_RS + 1312];
  int b = blockIdx.x;
  TS(0, p.ws);
  char* stamp_ws = p.ws; (void)stamp_ws;
  STAMP(0, 0);
  if (b < NB_PF) { k1_prefetch(p, b); STAMP(0, 7); return; }
  b -= NB_PF;
  if (b < NB_K) { k1_kgen(p, b, smem); STAMP(0, 7); return; }
  b -= NB_K;
#define K1_PRIO 0
  __builtin_amdgcn_s_setprio(K1_PRIO);
#define K1_DELAY 4
  if (K1_DELAY) { asm volatile("" :: "s"(p.ws), "s"(p.x)); __builtin_amdgcn_s_sleep(K1_DELAY); }
  if (b < NB_LN) {
    k1_ln(p, b, smem);
    STAMP(0, 7);
    return;
  }
  b -= NB_LN;
  k1_mod(p, b, smem);
  STAMP(0, 7);
}

constexpr int K2_CS = 4160;
constexpr int K2_KOFF = 4 * K2_CS;
constexpr int K2_XOFF = K2_CS;
constexpr int K2_CH = K2_KOFF + 4608;
constexpr int CV_RED = 3 * K2_CH;
constexpr int CV_RT2 = 86016;
constexpr int CV_SUM = CV_RT2 + 4 * (32 * 176 + 256);
constexpr int CV_XCH = CV_SUM + 2560;
constexpr int CV_FL = CV_RED + 20480;
constexpr int CV_RT = 151552;

struct Chan { float sa, ca1, ga, dsk, hs, hinv, yinv; };
struct ChanRaw { ModP msa, mca, mga; float kinv; unsigned short rds; };
DEV ChanRaw chan_ld(const unsigned short* dskip, const char* ws, int d) {
  ChanRaw c;
  c.rds = dskip[d];
  c.msa = mod_ld(ws, d); c.mca = mod_ld(ws, D_ + d); c.mga = mod_ld(ws, 2 * D_ + d);
  c.kinv = ((const float*)(ws + WS_KSC))[d];
  return c;
}
DEV Chan chan_fin(const ChanRaw& r) {
  Chan c;
  c.sa = mod_fin(r.msa); c.ca1 = rbf(1.f + mod_fin(r.mca)); c.ga = mod_fin(r.mga);
  c.dsk = __uint_as_float((unsigned)r.rds << 16);
  const float hb = 26.f * fabsf(c.ca1) + fabsf(c.sa);
  int ex = (int)((__float_as_uint(hb) >> 23) & 0xff);
  ex = ex < 20 ? 20 : (ex > 240 ? 240 : ex);
  c.hs = __uint_as_float((unsigned)(127 + 14 + 127 - ex) << 23);
  c.hinv = __uint_as_float((unsigned)(ex - 14) << 23);
  c.yinv = r.kinv * c.hinv;
  return c;
}
DEV size_t tl_off(int d, int t) { return (((size_t)(t >> 5) * D_ + d) * 32 + (t & 31)) * 2; }
struct StageRaw { u32x4 xa0, xa1, kv0, kv1; u32x2 xb; };
DEV StageRaw stage_ld(const char* ws, int d, int th) {
  StageRaw s;
  const unsigned short* krev = (const unsigned short*)(ws + WS_KREV) + (size_t)d * KREV;
  const size_t xo = tl_off(d, 16 * th);
  s.xa0 = *(const u32x4*)(ws + WS_XNT + xo); s.xa1 = *(const u32x4*)(ws + WS_XNT + xo + 16);
  s.xb = *(const u32x2*)(ws + WS_XNT + tl_off(d, min(16 * th + 16, L_ - 4)));
  s.kv0 = *(const u32x4*)(krev + 8 * th); s.kv1 = *(const u32x4*)(krev + 1024 + 8 * th);
  return s;
}
DEV void stage_wr(const StageRaw& s, const Chan& c, char* smem, int th) {
  constexpr int CS = K2_CS, KOFF = K2_KOFF;
  unsigned hh[10];
#pragma unroll
  for (int q = 0; q < 10; ++q) {
    unsigned u = q < 4 ? s.xa0[q] : (q < 8 ? s.xa1[q - 4] : s.xb[q - 8]);
    unsigned m = pk_bf(bf_lo(u) * c.ca1, bf_hi(u) * c.ca1);
    unsigned h = pk_bf(bf_lo(m) + c.sa, bf_hi(m) + c.sa);
    hh[q] = pk_h(bf_lo(h) * c.hs, bf_hi(h) * c.hs);
  }
  char* dst = smem + (2 * th + 2) * 16;
  unsigned s1[9];
#pragma unroll
  for (int q = 0; q < 9; ++q) s1[q] = __builtin_amdgcn_alignbit(hh[q + 1], hh[q], 16);
#pragma unroll
  for (int e = 0; e < 2; ++e) {
    *(u32x4*)(dst + 16 * e) = (u32x4){hh[4 * e], hh[4 * e + 1], hh[4 * e + 2], hh[4 * e + 3]};
    *(u32x4*)(dst + 16 * e + CS) = (u32x4){s1[4 * e], s1[4 * e + 1], s1[4 * e + 2], s1[4 * e + 3]};
    *(u32x4*)(dst + 16 * e + 2 * CS) = (u32x4){hh[4 * e + 1], hh[4 * e + 2], hh[4 * e + 3], hh[4 * e + 4]};
    *(u32x4*)(dst + 16 * e + 3 * CS) = (u32x4){s1[4 * e + 1], s1[4 * e + 2], s1[4 * e + 3], s1[4 * e + 4]};
  }
  if (th == 0) {
    u32x4 z = {0u, 0u, 0u, 0u};
    *(u32x4*)(smem) = z; *(u32x4*)(smem + CS) = z; *(u32x4*)(smem + 2 * CS) = z; *(u32x4*)(smem + 3 * CS) = z;
    *(u32x4*)(smem + 16) = z;
    *(u32x4*)(smem + CS + 16) = (u32x4){0u, 0u, 0u, hh[0] << 16};
    *(u32x4*)(smem + 2 * CS + 16) = (u32x4){0u, 0u, 0u, hh[0]};
    *(u32x4*)(smem + 3 * CS + 16) = (u32x4){0u, 0u, hh[0] << 16, s1[0]};
  }
  *(u32x4*)(smem + KOFF + 16 * th) = s.kv0;
  *(u32x4*)(smem + KOFF + 2048 + 16 * th) = s.kv1;
  if (th < 32) *(u32x4*)(smem + KOFF + 4096 + 16 * th) = (u32x4){0u, 0u, 0u, 0u};
}
DEV u32x2 conv_out(const f32x4& y, const Chan& c, const char* smem, int t, const u32x2& xres) {
  const u32x2 hv = *(const u32x2*)(smem + 2 * (t + 16));
  u32x2 o;
#pragma unroll
  for (int e = 0; e < 2; ++e) {
    const unsigned hu = hv[e];
    const h16x2 h2 = __builtin_bit_cast(h16x2, hu);
    const float h0 = (float)h2[0] * c.hinv, h1 = (float)h2[1] * c.hinv;
    unsigned yb = pk_bf(y[2 * e] * c.yinv, y[2 * e + 1] * c.yinv);
    unsigned hd = pk_bf(h0 * c.dsk, h1 * c.dsk);
    unsigned y2 = pk_bf(bf_lo(yb) + bf_lo(hd), bf_hi(yb) + bf_hi(hd));
    unsigned gy = pk_bf(bf_lo(y2) * c.ga, bf_hi(y2) * c.ga);
    o[e] = pk_bf(bf_lo(xres[e]) + bf_lo(gy), bf_hi(xres[e]) + bf_hi(gy));
  }
  return o;
}
#define K2_LOAD(buf, cc, u)                                                                                     \
  asm volatile("ds_read_b64 %0, %3 offset:%5\n\tds_read_b64 %1, %3 offset:%6\n\tds_read_b128 %2, %4 offset:%7"   \
               : "=&v"(Al[buf][u]), "=&v"(Ah[buf][u]), "=&v"(Bf[buf][u])                                          \
               : "v"(ap0), "v"(bp0), "i"(64 * (cc) + 512 * (u)), "i"(64 * (cc) + 512 * (u) + 8),                 \
                 "i"(64 * (cc) + 3584 - 512 * (u)))
template <int SET> DEV constexpr int half_tile(int j) {
  return SET == 0 ? (j == 0 ? 7 : (j == 1 ? 6 : 2)) : (j < 3 ? 5 - j : 4 - j);
}
template <int SET>
DEV void conv_half(unsigned ap0, unsigned bp0, char* redw) {
  constexpr int NU = SET == 0 ? 8 : 6, NT = SET == 0 ? 3 : 5;
  u32x2 Al[2][NU], Ah[2][NU];
  u32x4 Bf[2][NU];
#pragma unroll
  for (int u = 0; u < NU; ++u) K2_LOAD(0, 0, u);
  f32x4 acc[NT];
#pragma unroll
  for (int j = 0; j < NT; ++j) acc[j] = (f32x4){0.f, 0.f, 0.f, 0.f};
#pragma unroll
  for (int cc = 0; cc < 2; ++cc) {
    lgkm0();
    if (cc == 0) {
#pragma unroll
      for (int u = 0; u < NU; ++u) K2_LOAD(1, 1, u);
    }
#pragma unroll
    for (int sp = 0; sp < NU; ++sp)
#pragma unroll
      for (int j = 0; j < NT; ++j) {
        constexpr int dummy = 0; (void)dummy;
        const int q = half_tile<SET>(j);
        if (sp <= q) {
          const u32x4 A = {Al[cc][sp][0], Al[cc][sp][1], Ah[cc][sp][0], Ah[cc][sp][1]};
          acc[j] = mfma_f16(A, Bf[cc][q - sp], acc[j]);
        }
      }
    SB();
  }
#pragma unroll
  for (int j = 0; j < NT; ++j) *(f32x4*)(redw + j * 4096) = acc[j];
}

template <int NF>
DEV void w1_fin(const RtRaw<NF>& t, const ModP& mp, char* ws, int kk, int ct0, int lane, char* scr, float* sums) {
  const int g = lane >> 4, i = lane & 15;
  float* tb = (float*)(scr + 32 * RT_STR);
  const float mv = mod_fin(mp);
  tb[lane] = lane < 32 ? rbf(1.f + mv) : mv;
  rt_park<NF>(t, scr, lane);
  lgkm0();
  const f32x4 ca = *(const f32x4*)(tb + 8 * g), cb = *(const f32x4*)(tb + 8 * g + 4);
  const f32x4 sa = *(const f32x4*)(tb + 32 + 8 * g), sb = *(const f32x4*)(tb + 36 + 8 * g);
  const u32x4 sv = {pk_bf(sa[0], sa[1]), pk_bf(sa[2], sa[3]), pk_bf(sb[0], sb[1]), pk_bf(sb[2], sb[3])};
  const u32x4 ones = {0x3f803f80u, 0x3f803f80u, 0x3f803f80u, 0x3f803f80u};
  const f32x4 z = {0.f, 0.f, 0.f, 0.f};
  const rsrc_t rs = ws_rsrc(ws);
#pragma unroll
  for (int j = 0; j < NF; ++j) {
    const u32x4 f = rt_frag(scr, j, lane);
    const u32x4 sc = {pk_bf(bf_lo(f[0]) * ca[0], bf_hi(f[0]) * ca[1]), pk_bf(bf_lo(f[1]) * ca[2], bf_hi(f[1]) * ca[3]),
                      pk_bf(bf_lo(f[2]) * cb[0], bf_hi(f[2]) * cb[1]), pk_bf(bf_lo(f[3]) * cb[2], bf_hi(f[3]) * cb[3])};
    st16(rs, WS_W1F + ((size_t)(kk * 108 + ct0 + j) * 64 + lane) * 16, sc);
    const f32x4 cs = mfma_bf16(sc, ones, z), b1 = mfma_bf16(f, sv, z);
    if (i < 2) *(f32x4*)(sums + i * 80 + 16 * j + 4 * g) = i ? b1 : cs;
  }
}

DEV void conv_phase(const unsigned short* __restrict__ dskip, char* __restrict__ ws,
                    const unsigned short* __restrict__ w1, const unsigned short* __restrict__ w2, char* smem_all) {
  constexpr int CS = K2_CS, KOFF = K2_KOFF;
  const int tid = threadIdx.x, lane = tid & 63;
  const int w = __builtin_amdgcn_readfirstlane(tid >> 6);
  char* stamp_ws = ws; (void)stamp_ws;
  STAMP(1, 0);
  const int bx = blockIdx.x;
  const int pj = (bx & 7) * 16 + (bx >> 4), par = (bx >> 3) & 1;
  const int r = lane & 15, g = lane >> 4, th = tid & 127;
  const int s4 = (r + 1) & 3;
  const rsrc_t rs = ws_rsrc(ws);
  unsigned* fl = (unsigned*)(smem_all + CV_FL);
  if (tid < 6) fl[tid] = 0u;
  if (w < 4) {
    const int d = 5 * pj + 2 * par + (w >> 1), hw = w & 1;
    char* smem = smem_all + (w >> 1) * K2_CH;
    const StageRaw sr = stage_ld(ws, d, th);
    const ChanRaw cr = chan_ld(dskip, ws, d);
    u32x2 xres[4];
#pragma unroll
    for (int q = 0; q < 4; ++q)
      xres[q] = *(const u32x2*)(ws + WS_XT + tl_off(d, 256 * (4 * hw + q) + 16 * r + 4 * g));
    SB();
    const Chan c = chan_fin(cr);
    stage_wr(sr, c, smem, th);
    __syncthreads();
    STAMP(1, 2);
    f32x4 acc[8];
#pragma unroll
    for (int q = 0; q < 8; ++q) acc[q] = (f32x4){0.f, 0.f, 0.f, 0.f};
    {
      const unsigned ap0 = (unsigned)(size_t)(smem + s4 * CS + 2 * (128 * hw + 8 * g + (r + 1) - s4));
      const unsigned bp0 = (unsigned)(size_t)(smem + KOFF + 2 * (2032 + 128 * hw - 16 * r + 8 * g) - 3584);
      u32x2 Al[2][8], Ah[2][8];
      u32x4 Bf[2][8];
#pragma unroll
      for (int u = 0; u < 8; ++u) K2_LOAD(0, 0, u);
#pragma unroll
      for (int cc = 0; cc < 4; ++cc) {
        lgkm0();
#pragma unroll
        for (int q = 0; q < 8; ++q) {
          if (cc < 3) K2_LOAD((cc + 1) & 1, cc + 1, q);
#pragma unroll
          for (int sp = 0; sp <= q; ++sp) {
            const u32x4 A = {Al[cc & 1][sp][0], Al[cc & 1][sp][1], Ah[cc & 1][sp][0], Ah[cc & 1][sp][1]};
            acc[q] = mfma_f16(A, Bf[cc & 1][q - sp], acc[q]);
          }
          SB();
        }
      }
    }
    STAMP(1, 3);
    f32x4 keep[4];
    {
      f32x4* xw = (f32x4*)(smem_all + CV_XCH + w * 4096) + lane;
      if (hw) {
#pragma unroll
        for (int q = 0; q < 4; ++q) { xw[64 * q] = acc[q]; keep[q] = acc[4 + q]; }
      } else {
#pragma unroll
        for (int q = 0; q < 4; ++q) { xw[64 * q] = acc[4 + q]; keep[q] = acc[q]; }
      }
    }
    lds_post(fl + w, lane);
    lds_wait(fl + (w ^ 1), 1u);
    const f32x4* xrd = (const f32x4*)(smem_all + CV_XCH + (w ^ 1) * 4096) + lane;
#pragma unroll
    for (int qp = 0; qp < 2; ++qp) {
      u32x2 o[2];
#pragma unroll
      for (int h = 0; h < 2; ++h) {
        const int q = 2 * qp + h;
        o[h] = conv_out(keep[q] + xrd[64 * q], c, smem, 256 * (4 * hw + q) + 16 * r + 4 * g, xres[q]);
      }
      const auto s0 = __builtin_amdgcn_permlane16_swap(o[0][0], o[1][0], false, false);
      const auto s1 = __builtin_amdgcn_permlane16_swap(o[0][1], o[1][1], false, false);
      const int t = 256 * (4 * hw + 2 * qp + (g & 1)) + 16 * r + 8 * (g >> 1);
      st16(rs, WS_X1T + (((size_t)(t >> 6) * D_ + d) * 64 + (t & 63)) * 2, (u32x4){s0[0], s1[0], s0[1], s1[1]});
    }
  } else {
#define C_PRIO1 2
    __builtin_amdgcn_s_setprio(C_PRIO1);
    const int d = 5 * pj + 4, wq = w - 4;
    char* smem = smem_all + 2 * K2_CH;
    StageRaw sr;
    if (wq < 2) sr = stage_ld(ws, d, th);
    asm volatile("" ::: "memory");
    const ChanRaw cr = chan_ld(dskip, ws, d);
    const int tl0 = par ? (wq < 3 ? 5 - wq : 1) : (wq == 0 ? 7 : (wq == 1 ? 6 : 2)), tl1 = 0;
    const unsigned ap0 = (unsigned)(size_t)(smem + s4 * CS + 2 * (64 * wq + 8 * g + (r + 1) - s4));
    const unsigned bp0 = (unsigned)(size_t)(smem + KOFF + 2 * (2032 + 64 * wq - 16 * r + 8 * g) - 3584);
    char* redw = smem_all + CV_RED + wq * 1024 + lane * 16;
    u32x2 xres[2];
    xres[0] = *(const u32x2*)(ws + WS_XT + tl_off(d, 256 * tl0 + 16 * r + 4 * g));
    xres[1] = *(const u32x2*)(ws + WS_XT + tl_off(d, 256 * tl1 + 16 * r + 4 * g));
    SB();
    const Chan c = chan_fin(cr);
    if (wq < 2) {
      stage_wr(sr, c, smem, th);
    } else {
#define RT_DELAY 32
      __builtin_amdgcn_s_sleep(RT_DELAY);
    }
    const bool isw1 = bx < 110;
    const int b2 = min(max(bx - 110, 0), 107);
    const int jk = isw1 ? 4 * (bx / 22) + wq : b2 >> 1;
    const int jc = isw1 ? min(5 * (bx % 22), 103) : (b2 & 1) * 20 + 5 * wq;
    RtRaw<5> rt;
    rt_load<5>(rt, isw1 ? w1 : w2, isw1 ? FF : D_, jk, jc, lane);
    const ModP mp = mod_ld(ws, (lane < 32 ? 4 * D_ : 3 * D_ - 32) + 32 * min(jk, 19) + lane);
    SB();
    bar_lds();
    if (par) conv_half<1>(ap0, bp0, redw); else conv_half<0>(ap0, bp0, redw);
    lds_post(fl + 4, lane);
#define C_PRIO2 2
    __builtin_amdgcn_s_setprio(C_PRIO2);
    char* scr = smem_all + CV_RT2 + wq * (32 * RT_STR + 256);
    if (isw1) {
      float* sums = (float*)(smem_all + CV_SUM);
      w1_fin<5>(rt, mp, ws, jk, jc, lane, scr, sums + 160 * wq);
      asm volatile("" ::: "memory");
      unsigned old = 0u;
      if (lane == 0) old = __hip_atomic_fetch_add(fl + 5, 1u, __ATOMIC_RELAXED, __HIP_MEMORY_SCOPE_WORKGROUP);
      asm volatile("" ::: "memory");
      if (__builtin_amdgcn_readfirstlane(old) == 3u && lane < 40) {
        const f32x4* sp = (const f32x4*)sums + lane;
        const f32x4 v = (sp[0] + sp[40]) + (sp[80] + sp[120]);
        st16(rs, WS_CSP + ((size_t)(2 * (bx / 22) + lane / 20) * FF + 16 * jc + 4 * (lane % 20)) * 4, __builtin_bit_cast(u32x4, v));
      }
    } else if (bx < 218) {
      rt_park<5>(rt, scr, lane);
      lgkm0();
#pragma unroll
      for (int j = 0; j < 5; ++j) st16(rs, WS_W2F + ((size_t)((jc + j) * 54 + jk) * 64 + lane) * 16, rt_frag(scr, j, lane));
    }
    lds_wait(fl + 4, 4u);
    const char* rd = smem_all + CV_RED + lane * 16;
#pragma unroll
    for (int u = 0; u < 2; ++u) {
      const bool on = u == 0 ? (par || wq < 3) : (par && wq == 0);
      if (on) {
        const int sl = u == 0 ? wq : 4, tl = u == 0 ? tl0 : tl1;
        const f32x4 y = (*(const f32x4*)(rd + sl * 4096) + *(const f32x4*)(rd + sl * 4096 + 1024))
                      + (*(const f32x4*)(rd + sl * 4096 + 2048) + *(const f32x4*)(rd + sl * 4096 + 3072));
        const int t = 256 * tl + 16 * r + 4 * g;
        st8(rs, WS_X1T + (((size_t)(t >> 6) * D_ + d) * 64 + (t & 63)) * 2, conv_out(y, c, smem, t, xres[u]));
      }
    }
  }
  STAMP(1, 7);
}

__global__ NOPK __launch_bounds__(512, 2) void k2(const unsigned short* __restrict__ dskip, char* __restrict__ ws,
                                                  const unsigned short* __restrict__ w1, const unsigned short* __restrict__ w2) {
  __shared__ __attribute__((aligned(16))) char smem[CV_XCH + 16384];
  TS(1, ws);
  conv_phase(dskip, ws, w1, w2, smem);
}

DEV u32x4 ld16_sc1(rsrc_t r, int voff, int soff) { return __builtin_amdgcn_raw_buffer_load_b128(r, voff, soff, 16); }
DEV u32x4 ld16(rsrc_t r, int voff, int soff) { return __builtin_amdgcn_raw_buffer_load_b128(r, voff, soff, 0); }
DEV u32x4 tr_frag(const char* p) {
  const u32x2 a = __builtin_bit_cast(u32x2, __builtin_amdgcn_ds_read_tr16_b64_v4i16((lds_s16x4*)p));
  const u32x2 b = __builtin_bit_cast(u32x2, __builtin_amdgcn_ds_read_tr16_b64_v4i16((lds_s16x4*)(p + 256)));
  return (u32x4){a[0], a[1], b[0], b[1]};
}

constexpr int KF_RS = 84;
constexpr int KF_RB = 8 * 16 * KF_RS * 4;
constexpr int KF_BIG = 2 * KF_RB;
#define NW2L_ 8
constexpr int NW2L = NW2L_;
constexpr int NW2R = 35 - NW2L;
constexpr int KF_W2L = KF_BIG;
constexpr int KF_TAB = KF_W2L + 8 * NW2L * 1024;
constexpr int KF_PS = KF_TAB + 320;
constexpr int KF_XR = KF_PS + 512;
constexpr int KF_CNT = KF_XR + 64 * 176;
constexpr int KF_LDS = KF_CNT + 16;
#define LOCK_MASK 0x55554
#define CS_STEP 14
#define X_FIRST 5
#define W1_MID 7
#define X_STEP 4
#define W2_START 6
#define W2_RATE 2
#define W2_INL 28
#define SIG_SPLIT 4
#define H1_B0 1
#define H1_BATCH 7
#define W2_MID 4
#define W1_PRE 10
#define W1_EARLY 3
#define STAMP_EXTRA 0
#define H1_MODE 0
#define H1_PRE 9

DEV void kf_body(char* __restrict__ ws, unsigned short* __restrict__ out, char* smem) {
  char* xs = smem;
  float* tab = (float*)(smem + KF_TAB);
  float* ps = (float*)(smem + KF_PS);
  char* xr = smem + KF_XR;
  unsigned* lcnt = (unsigned*)(smem + KF_CNT);
  float* cst = (float*)(smem + 81920);
  const int tid = threadIdx.x, lane = tid & 63;
  const int w = __builtin_amdgcn_readfirstlane(tid >> 6);
  const int bx = blockIdx.x;
#define KF_MAP 1
  const int ng = (bx & 1) * 4 + ((bx >> 3) & 3), mt = ((bx & 7) >> 1) * 8 + (bx >> 5);
  const int g = lane >> 4, i = lane & 15;
  char* stamp_ws = ws; (void)stamp_ws;
  STAMP(2, 0); STAMP(5, 0);
  const rsrc_t rs_ = ws_rsrc(ws);

  unsigned* flags = (unsigned*)(ws + WS_CNT) + mt * 128;
  const int ntl = ng < 6 ? 14 : 12;
  const int ctb = ng < 6 ? 14 * ng : 84 + 12 * (ng - 6);
  const bool two = w < ntl - 8;
  const bool full = w < 6;
  const int ks0 = w < 6 ? 7 * w : 42 + 6 * (w - 6);
  const int ct0 = ctb + (two ? 2 * w : (ntl - 8) + w);
  const int lo16 = lane * 16;
  const int w1s = (int)WS_W1F + ct0 * 1024;
  const int w2s = (int)WS_W2F + (ng * 5 * 54 + ks0) * 1024;

  u32x4 ring[32], wl[NW2L];
  const char* bb0 = xs + (8 * g + (i >> 2)) * 64 + ((g & 1) << 5) + (i & 3) * 8;
  const char* bb1 = xs + (8 * g + (i >> 2)) * 64 + (((g & 1) ^ 1) << 5) + (i & 3) * 8;
  auto w2_off = [&](int e) __attribute__((always_inline)) {
    const int s = e < 27 ? e / 4 : (e < 34 ? e - 27 : 6), c = e < 27 ? e % 4 : (e < 34 ? 4 : 3);
    const int sn = (s == 6 && !full) ? 5 : s;
    return w2s + (c * 54 + sn) * 1024;
  };

  auto gemm1 = [&](auto two_tag) __attribute__((always_inline)) {
    constexpr bool TWO = decltype(two_tag)::value;
    if (TWO) asm volatile("; two tiles" ::: "memory"); else asm volatile("; one tile" ::: "memory");
    u32x4 t[10];
    auto st_x = [&](int i0, int i1) __attribute__((always_inline)) {
#pragma unroll
      for (int it = i0; it < i1; ++it) {
        const int c = tid + 512 * it;
        const int row = c >> 3;
        *(u32x4*)(xs + ((c >> 2) & 1) * 40960 + row * 64 + (((c & 3) ^ ((row >> 2) & 2)) << 4)) = t[it];
      }
    };
#pragma unroll
    for (int it = 0; it < X_FIRST; ++it) t[it] = ld16(rs_, tid * 16, (int)WS_X1T + mt * 81920 + 8192 * it);
#pragma unroll
    for (int k = 0; k < W1_EARLY; ++k) {
      ring[2 * k] = ld16(rs_, lo16, w1s + k * 108 * 1024);
      if (TWO) ring[2 * k + 1] = ld16(rs_, lo16, w1s + (k * 108 + 1) * 1024);
    }
    SB();
    __builtin_amdgcn_s_barrier();
    SB();
#pragma unroll
    for (int k = W1_EARLY; k < W1_MID; ++k) {
      ring[2 * k] = ld16(rs_, lo16, w1s + k * 108 * 1024);
      if (TWO) ring[2 * k + 1] = ld16(rs_, lo16, w1s + (k * 108 + 1) * 1024);
    }
    SB();
    st_x(0, X_FIRST);
    SB();
    if (tid == 0) *lcnt = 0u;
    ModP gbp;
    if (!TWO) gbp = mod_ld(ws, 5 * D_ + 80 * ng + min((w & 1) * 64 + lane, 79));
    SB();
    __syncthreads();
    STAMP(2, 1);
    auto resid = [&]() __attribute__((always_inline)) {
      const int kb = (80 * ng) >> 5, tq = w & 3;
#pragma unroll
      for (int u = 0; u < 2; ++u) {
        const int kk = min(kb + 2 * (w >> 2) + u, 19);
        const u32x4 f = tr_frag(((tq & 1) ? bb1 : bb0) + (tq >> 1) * 40960 + kk * 2048);
        const unsigned dl = (unsigned)(32 * kk + 8 * g - 80 * ng);
        if (dl < 80u) *(u32x4*)(xr + (tq * 16 + i) * 176 + dl * 2) = f;
      }
    };
    const int hs = TWO ? 0 : (w & 1);
    const char* ba[4] = {bb0 + hs * 40960, bb1 + hs * 40960, bb0 + (hs ^ 1) * 40960, bb1 + (hs ^ 1) * 40960};
    const u32x4 ones = {0x3f803f80u, 0x3f803f80u, 0x3f803f80u, 0x3f803f80u};
    f32x4 acc[2][4];
#pragma unroll
    for (int c = 0; c < 2; ++c)
#pragma unroll
      for (int q = 0; q < 4; ++q) acc[c][q] = (f32x4){0.f, 0.f, 0.f, 0.f};
    u32x4 csp[5];
    const int cso = ((w & 1) * FF + 16 * ctb + 4 * min(lane, 4 * ntl - 1)) * 4;
    u32x4 bn[2][4];
#pragma unroll
    for (int q = 0; q < 4; ++q) bn[0][q] = tr_frag(ba[q]);
#pragma unroll
    for (int q = 0; q < 4; ++q) bn[1][q] = tr_frag(ba[q] + 2048);
#pragma unroll
    for (int kk = 0; kk < 20; ++kk) {
      if (kk == X_STEP) { st_x(X_FIRST, 10); SB(); }
      if (kk == 2 * X_FIRST - 2) { SB(); bar_lds(); SB(); }
      else if ((LOCK_MASK >> kk) & 1) { SB(); __builtin_amdgcn_s_barrier(); SB(); }
      if (kk == 2 * X_FIRST) { resid(); SB(); }
      auto gload = [&](int u) __attribute__((always_inline)) {
        {
          constexpr int CATCH = W1_PRE - W1_MID;
          const int k0 = kk == 0 ? W1_MID : (kk <= CATCH ? W1_MID + 2 * kk - 1 : kk + W1_PRE);
          const int nk = (kk >= 1 && kk <= CATCH) ? 2 : 1;
          const int kn = k0 + (nk == 2 ? (u >> 1) : 0);
          const bool on = nk == 2 || u >= 2;
          if (on && kn < 20) {
            if ((u & 1) == 0) ring[(2 * kn) % 32] = ld16(rs_, lo16, w1s + kn * 108 * 1024);
            else if (TWO) ring[(2 * kn + 1) % 32] = ld16(rs_, lo16, w1s + (kn * 108 + 1) * 1024);
          }
          if (kk == 0 && u < 2) {
#pragma unroll
            for (int it = X_FIRST + u * 3; it < (u ? 10 : (X_FIRST + 3 < 10 ? X_FIRST + 3 : 10)); ++it)
              t[it] = ld16(rs_, tid * 16, (int)WS_X1T + mt * 81920 + 8192 * it);
          }
        }
        if (kk >= W2_START && u < W2_RATE) {
          const int n = W2_RATE * (kk - W2_START) + u;
          if (n < NW2L) wl[n] = ld16(rs_, lo16, w2_off(NW2R + n));
          else if (n < W2_INL) ring[8 + n - NW2L] = ld16(rs_, lo16, w2_off(n - NW2L));
        }
      };
      const u32x4 a0 = ring[(2 * kk) % 32], a1 = ring[(2 * kk + 1) % 32];
#pragma unroll
      for (int q = 0; q < 4; ++q) {
        const u32x4 bq = bn[kk & 1][q];
        acc[0][q] = mfma_bf16(a0, bq, acc[0][q]);
        if (TWO) acc[1][q] = mfma_bf16(a1, bq, acc[1][q]);
        else if (q < 2) {
          acc[1][2 * q] = mfma_bf16(ones, bq, acc[1][2 * q]);
          acc[1][2 * q + 1] = mfma_bf16(bq, bq, acc[1][2 * q + 1]);
        }
        SB();
        if (kk < 18) bn[kk & 1][q] = tr_frag(ba[q] + (kk + 2) * 2048);
        gload(q);
        SB();
      }
      if (!TWO) {
        if (kk < 5) csp[kk] = ld16(rs_, cso, (int)WS_CSP + kk * 2 * FF * 4);
        if (kk == CS_STEP) {
          f32x4 t = __builtin_bit_cast(f32x4, csp[0]);
#pragma unroll
          for (int u = 1; u < 5; ++u) t += __builtin_bit_cast(f32x4, csp[u]);
          if (w >= 6 && lane < 56) *(f32x4*)(cst + (w & 1) * 224 + 4 * lane) = t;
        }
      }
      if (!TWO && kk == 12) { if ((w & 1) * 64 + lane < 80) tab[(w & 1) * 64 + lane] = mod_fin(gbp); }
      SB();
    }
    if (!TWO) {
#pragma unroll
      for (int h = 0; h < 2; ++h) {
        const f32x4 aq = acc[1][2 * h + 1];
        const float dq = (i & 2) ? ((i & 1) ? aq[3] : aq[2]) : ((i & 1) ? aq[1] : aq[0]);
        const int tk = (2 * hs + h) * 16 + i;
        if (g == (i >> 2)) {
          const float mean = acc[1][2 * h][0] * (1.f / D_);
          const float var = fmaxf(dq * (1.f / D_) - mean * mean, 0.f);
          const float sd = rbf(sqrtf(rbf(rbf(var) + 1e-5f)));
          const float rs = 1.f / sd;
          ps[tk] = rs;
          ps[64 + tk] = -rbf(mean) * rs;
        }
      }
    }
    __syncthreads();
    STAMP(2, 2);
    f32x4 csv[2], b1v[2];
#pragma unroll
    for (int c = 0; c < (TWO ? 2 : 1); ++c) {
      csv[c] = *(const f32x4*)(cst + 16 * (ct0 - ctb + c) + 4 * g);
      b1v[c] = *(const f32x4*)(cst + 224 + 16 * (ct0 - ctb + c) + 4 * g);
    }
#pragma unroll
    for (int qp = 0; qp < 2; ++qp) {
      u32x2 o[2][2];
#pragma unroll
      for (int h = 0; h < 2; ++h) {
        const int q = 2 * qp + h, tq = q ^ (2 * hs);
        const float rs = ps[tq * 16 + i], nm = ps[64 + tq * 16 + i];
#pragma unroll
        for (int c = 0; c < (TWO ? 2 : 1); ++c) {
          float v[4];
#pragma unroll
          for (int r = 0; r < 4; ++r) v[r] = fmaxf(fmaf(acc[c][q][r], rs, fmaf(nm, csv[c][r], b1v[c][r])), 0.f);
          o[c][h] = (u32x2){pk_bf(v[0], v[1]), pk_bf(v[2], v[3])};
        }
      }
      const int tl = ((2 * qp) ^ (2 * hs)) + (g & 1);
#pragma unroll
      for (int c = 0; c < (TWO ? 2 : 1); ++c) {
        const int ct = ct0 + c;
        const auto s0 = __builtin_amdgcn_permlane16_swap(o[c][0][0], o[c][1][0], false, false);
        const auto s1 = __builtin_amdgcn_permlane16_swap(o[c][0][1], o[c][1][1], false, false);
        const size_t off = (((size_t)(4 * mt + tl) * 54 + (ct >> 1)) * 64 + i + 16 * (2 * (ct & 1) + (g >> 1))) * 16;
#define H1_ST 16
        __builtin_amdgcn_raw_buffer_store_b128((u32x4){s0[0], s1[0], s0[1], s1[1]}, rs_, (int)(WS_H1F + off), 0, H1_ST);
      }
    }
    SB();
    STAMP(5, 1);
    __builtin_amdgcn_s_barrier();
    SB();
    constexpr int E0 = W2_INL - NW2L;
    auto signal = [&]() __attribute__((always_inline)) {
      if (lane == 0) {
        if (__hip_atomic_fetch_add(lcnt, 1u, __ATOMIC_RELAXED, __HIP_MEMORY_SCOPE_WORKGROUP) == 7u)
          __hip_atomic_store(flags + 16 * ng, 1u, __ATOMIC_RELAXED, __HIP_MEMORY_SCOPE_AGENT);
      }
    };
    if (w >= SIG_SPLIT) { asm volatile("s_waitcnt vmcnt(0)" ::: "memory"); signal(); }
    SB();
#pragma unroll
    for (int e = E0; e < NW2R; ++e) ring[(8 + e) % 32] = ld16(rs_, lo16, w2_off(e));
    SB();
    STAMP(5, 2);
    if (w < SIG_SPLIT) { asm volatile("s_waitcnt vmcnt(%0)" :: "n"(NW2R - E0) : "memory"); signal(); }
    SB();
  };
  if (two) gemm1(std::true_type{});
  else gemm1(std::false_type{});
  STAMP(2, 3);
#pragma nounroll
  for (unsigned spin = 0; spin < (1u << 20); ++spin) {
    if (__hip_atomic_load(flags + 16 * w, __ATOMIC_RELAXED, __HIP_MEMORY_SCOPE_AGENT) != 0u) break;
    __builtin_amdgcn_s_sleep(1);
  }
  asm volatile("" ::: "memory");
  SB();
  STAMP(2, 4);

  const int h1s = (int)WS_H1F + (mt * 4 * 54 + ks0) * 1024;
  u32x4 hb[28];
  auto load_h1 = [&](int n) __attribute__((always_inline)) {
    const int q = n / 7, s = n % 7;
    const int sn = (s == 6 && !full) ? 5 : s;
    hb[n] = ld16_sc1(rs_, lo16, h1s + (q * 54 + sn) * 1024);
  };
#pragma unroll
  for (int n = 0; n < H1_PRE; ++n) {
    if (H1_BATCH && n % H1_BATCH == 0 && n >= H1_B0) { SB(); __builtin_amdgcn_s_barrier(); SB(); }
    load_h1(n);
  }
  SB();
  float* red = (float*)smem;
  float* rb_ = red + (w * 16 + i) * KF_RS + 4 * g;
  const int re = w * 40 + min(lane, 39);
  const int rtk = re / 20, rq4 = re % 20;
  const float* rr_ = red + rtk * KF_RS + 4 * rq4;
  f32x4 ac2[2][5];
  f32x4 rv[8], rsum;
  u32x2 xv2;
  const f32x4 gb = *(const f32x4*)(tab + 4 * rq4);
  auto red_wr = [&](int q, int c) __attribute__((always_inline)) {
    *(f32x4*)(rb_ + (q & 1) * (KF_RB / 4) + 16 * c) = ac2[q & 1][c];
  };
  auto red_rd = [&](int q, int ww) __attribute__((always_inline)) {
    rv[ww] = *(const f32x4*)(rr_ + (q & 1) * (KF_RB / 4) + ww * 16 * KF_RS);
  };
  auto red_x = [&](int q) __attribute__((always_inline)) { xv2 = *(const u32x2*)(xr + (16 * q + rtk) * 176 + rq4 * 8); };
  auto red_out = [&](int q) __attribute__((always_inline)) {
    const f32x4 s = rsum + (rv[6] + rv[7]);
    u32x2 o;
#pragma unroll
    for (int h = 0; h < 2; ++h) {
      unsigned f = pk_bf(s[2 * h], s[2 * h + 1]);
      unsigned gf = pk_bf(bf_lo(f) * gb[2 * h], bf_hi(f) * gb[2 * h + 1]);
      o[h] = pk_bf(bf_lo(xv2[h]) + bf_lo(gf), bf_hi(xv2[h]) + bf_hi(gf));
    }
    *(u32x2*)(out + (size_t)(mt * 64 + 16 * q + rtk) * D_ + ng * 80 + 4 * rq4) = o;
  };
#pragma unroll
  for (int q = 0; q < 3; ++q) {
#pragma unroll
    for (int c = 0; c < 5; ++c) ac2[q & 1][c] = (f32x4){0.f, 0.f, 0.f, 0.f};
#pragma unroll
    for (int s = 0; s < 7; ++s) {
      auto mm = [&](int c) __attribute__((always_inline)) {
        const u32x4 a = c == 4 ? wl[s] : ((s == 6 && c == 3) ? wl[7] : ring[(8 + 4 * s + c) % 32]);
        ac2[q & 1][c] = mfma_bf16(a, hb[7 * q + s], ac2[q & 1][c]);
      };
      if (s == 5 && q >= 2) {
        red_wr(q - 1, 4);
        SB();
        red_out(q - 2);
#pragma unroll
        for (int c = 0; c < 5; ++c) mm(c);
#pragma unroll
        for (int c = 0; c < 5; ++c) {
          __builtin_amdgcn_sched_group_barrier(0x008, 1, 0);
          __builtin_amdgcn_sched_group_barrier(0x002, 10, 0);
        }
        SB();
      } else if (s < 6) {
        mm(0); SB();
        if (q >= 1 && s >= 1) { red_wr(q - 1, s - 1); SB(); }
        mm(1); SB();
        if (q >= 2 && s < 4) { red_rd(q - 2, 2 * s); SB(); }
        mm(2); SB();
        if (q >= 2 && s < 4) { red_rd(q - 2, 2 * s + 1); SB(); }
        if (q >= 2 && s == 4) { red_x(q - 2); SB(); }
        mm(3); SB();
        if (q >= 2 && s >= 1 && s < 4) {
          const f32x4 t = rv[2 * s - 2] + rv[2 * s - 1];
          rsum = s == 1 ? t : rsum + t;
          asm volatile("" : "+v"(rsum));
          SB();
        }
        mm(4); SB();
      } else if (full) {
#pragma unroll
        for (int c = 0; c < 5; ++c) mm(c);
        SB();
      }
      if (7 * q + s + H1_PRE < 28) { load_h1(7 * q + s + H1_PRE); SB(); }
    }
    if (q == 0) STAMP(2, 5);
    if (q >= 1) bar_lds();
  }
  {
    auto m3 = [&](int c, int s) __attribute__((always_inline)) {
      const u32x4 a = c == 4 ? wl[s] : ((s == 6 && c == 3) ? wl[7] : ring[(8 + 4 * s + c) % 32]);
      ac2[1][c] = mfma_bf16(a, hb[21 + s], s == 0 ? (f32x4){0.f, 0.f, 0.f, 0.f} : ac2[1][c]);
    };
    auto sum6 = [&]() __attribute__((always_inline)) {
      rsum = ((rv[0] + rv[1]) + (rv[2] + rv[3])) + (rv[4] + rv[5]);
    };
    if (full) {
#pragma unroll
      for (int c = 0; c < 5; ++c) {
        const u32x4 a = c == 4 ? wl[6] : (c == 3 ? wl[7] : ring[(8 + 24 + c) % 32]);
        rv[c] = mfma_bf16(a, hb[27], (f32x4){0.f, 0.f, 0.f, 0.f});
      }
    } else {
#pragma unroll
      for (int c = 0; c < 5; ++c) rv[c] = (f32x4){0.f, 0.f, 0.f, 0.f};
    }
    f32x4 c0[5];
#pragma unroll
    for (int c = 0; c < 5; ++c) c0[c] = rv[c];
    SB();
    auto col = [&](int c, auto&& between) __attribute__((always_inline)) {
#pragma unroll
      for (int s = 0; s < 6; ++s) {
        const u32x4 a = c == 4 ? wl[s] : ring[(8 + 4 * s + c) % 32];
        c0[c] = mfma_bf16(a, hb[21 + s], c0[c]);
        SB();
        between(s);
        SB();
      }
    };
    (void)m3;
    col(0, [&](int s) __attribute__((always_inline)) { if (s < 5) red_wr(2, s); if (s < 4) { red_rd(1, 2 * s); red_rd(1, 2 * s + 1); } if (s == 4) red_x(1); });
    col(1, [&](int s) __attribute__((always_inline)) { if (s == 1) { sum6(); asm volatile("" : "+v"(rsum)); } if (s == 3) red_out(1); });
    bar_lds();
    STAMP(2, 6);
    auto wr3 = [&](int c) __attribute__((always_inline)) { *(f32x4*)(rb_ + (KF_RB / 4) + 16 * c) = c0[c]; };
    col(2, [&](int s) __attribute__((always_inline)) { if (s < 2) wr3(s); if (s >= 1 && s < 5) { red_rd(2, 2 * s - 2); red_rd(2, 2 * s - 1); } if (s == 5) red_x(2); });
    col(3, [&](int s) __attribute__((always_inline)) { if (s == 1) wr3(2); if (s == 2) { sum6(); asm volatile("" : "+v"(rsum)); } if (s == 4) red_out(2); });
    col(4, [&](int s) __attribute__((always_inline)) { if (s == 1) wr3(3); if (s == 2) red_x(3); });
    wr3(4);
    bar_lds();
#pragma unroll
    for (int ww = 0; ww < 8; ++ww) red_rd(3, ww);
    sum6();
    red_out(3);
  }
  STAMP(2, 7); STAMP(5, 7);
}

__global__ NOPK __launch_bounds__(512, 2) void kf(char* __restrict__ ws, unsigned short* __restrict__ out) {
  __shared__ __attribute__((aligned(16))) char smem[KF_LDS];
  TS(2, ws);
  kf_body(ws, out, smem);
}

__global__ void kprobe(char* ws, int which) {
  const unsigned long long now = __builtin_amdgcn_s_memrealtime();
  const unsigned long long* t = (const unsigned long long*)(ws + (200u << 20));
  unsigned long long d = which == 0 ? t[1] - t[0] : (which == 1 ? t[2] - t[1] : (which == 2 ? now - t[2] : 0ull));
  if (d > 10000ull) d = 10000ull;
  while (__builtin_amdgcn_s_memrealtime() - now < 10ull * d) __builtin_amdgcn_s_sleep(1);
}
__global__ void kend_() {}

extern "C" void kernel_launch(void* const* d_in, const int* in_sizes, int n_in,
                              void* d_out, int out_size, void* d_ws, size_t ws_size,
                              hipStream_t stream) {
  K1Args a;
  a.x = (const unsigned short*)d_in[0];
  a.are = (const unsigned short*)d_in[1];
  a.aim = (const unsigned short*)d_in[2];
  a.bre = (const unsigned short*)d_in[3];
  a.bim = (const unsigned short*)d_in[4];
  a.cre = (const unsigned short*)d_in[5];
  a.cim = (const unsigned short*)d_in[6];
  a.logdt = (const unsigned short*)d_in[7];
  a.w1 = (const unsigned short*)d_in[10];
  a.w2 = (const unsigned short*)d_in[11];
  a.c = (const unsigned short*)d_in[12];
  a.wmod = (const unsigned short*)d_in[13];
  a.ws = (char*)d_ws;
  a.dskip = (const unsigned short*)d_in[8];
  a.out = (const unsigned short*)d_out;
  k1<<<NB_PF + NB_MOD + NB_LN + NB_K, 256, 0, stream>>>(a);
  k2<<<256, 512, 0, stream>>>((const unsigned short*)d_in[8], (char*)d_ws, a.w1, a.w2);
  kf<<<256, 512, 0, stream>>>((char*)d_ws, (unsigned short*)d_out);
  kprobe<<<1, 64, 0, stream>>>((char*)d_ws, 3);
}
```

```cpp
#include <hip/hip_runtime.h>
#include <hip/hip_bf16.h>
#include <stdint.h>
#include <type_traits>

typedef __attribute__((ext_vector_type(8))) __bf16 bf16x8;
typedef __attribute__((ext_vector_type(2))) __bf16 bf16x2;
typedef __attribute__((ext_vector_type(4))) float f32x4;
typedef __attribute__((ext_vector_type(2))) float f32x2;
typedef __attribute__((ext_vector_type(4))) unsigned u32x4;
typedef __attribute__((ext_vector_type(2))) unsigned u32x2;
typedef __attribute__((ext_vector_type(8))) _Float16 h16x8;
typedef __attribute__((ext_vector_type(2))) _Float16 h16x2;
typedef __attribute__((ext_vector_type(4))) short s16x4;
typedef __attribute__((address_space(3))) s16x4 lds_s16x4;

#if defined(__HIP_DEVICE_COMPILE__)
#define NOPK __attribute__((target("no-packed-fp32-ops")))
#else
#define NOPK
#endif
#define DEV __device__ __forceinline__

constexpr int L_ = 2048;
constexpr int D_ = 640;
constexpr int NS = 128;
constexpr int FF = 1728;
constexpr int KREV = 2304;

constexpr size_t WS_KSC  = 8192;
constexpr size_t WS_CNT  = 16384;
constexpr size_t WS_MOD  = 65536;
constexpr size_t WS_CSP  = 196608;
constexpr size_t WS_XNT  = 327680;
constexpr size_t WS_XT   = WS_XNT + (size_t)D_ * L_ * 2;
constexpr size_t WS_KREV = WS_XT + (size_t)D_ * L_ * 2;
constexpr size_t WS_W1F  = WS_KREV + (size_t)D_ * KREV * 2;
constexpr size_t WS_W2F  = WS_W1F + (size_t)D_ * FF * 2;
constexpr size_t WS_X1T  = WS_W2F + (size_t)D_ * FF * 2;
constexpr size_t WS_H1F  = WS_X1T + (size_t)D_ * L_ * 2;
constexpr size_t WS_END  = WS_H1F + (size_t)L_ * FF * 2;

DEV float bf_lo(unsigned u) { return __uint_as_float(u << 16); }
DEV float bf_hi(unsigned u) { return __uint_as_float(u & 0xffff0000u); }
DEV unsigned pk_bf(float a, float b) {
  f32x2 v = {a, b};
  bf16x2 r = __builtin_convertvector(v, bf16x2);
  return __builtin_bit_cast(unsigned, r);
}
DEV float rbf(float a) { return bf_lo(pk_bf(a, 0.f)); }
DEV float ldbf(const unsigned short* p) { return __uint_as_float(((unsigned)*p) << 16); }

typedef __amdgpu_buffer_rsrc_t rsrc_t;
DEV rsrc_t ws_rsrc(char* ws) { return __builtin_amdgcn_make_buffer_rsrc((void*)ws, 0, (int)WS_END, 0x00020000); }
DEV void st16(rsrc_t r, size_t off, u32x4 v) { __builtin_amdgcn_raw_buffer_store_b128(v, r, (int)off, 0, 16); }
DEV void st4(char* ws, size_t off, unsigned v) { __hip_atomic_store((unsigned*)(ws + off), v, __ATOMIC_RELAXED, __HIP_MEMORY_SCOPE_AGENT); }
DEV void st8(rsrc_t r, size_t off, u32x2 v) { __builtin_amdgcn_raw_buffer_store_b64(v, r, (int)off, 0, 16); }

DEV void bar_lds() { asm volatile("s_waitcnt lgkmcnt(0)\n\ts_barrier" ::: "memory"); }
DEV void lds_wait(const void* word, unsigned val) {
  unsigned tv, ts, cnt;
  asm volatile("s_mov_b32 %2, 0x100000\n"
               "1:\n\tds_read_b32 %0, %3\n\ts_waitcnt lgkmcnt(0)\n\tv_readfirstlane_b32 %1, %0\n\t"
               "s_cmp_ge_u32 %1, %4\n\ts_cbranch_scc1 2f\n\ts_sleep 1\n\ts_sub_u32 %2, %2, 1\n\t"
               "s_cmp_lg_u32 %2, 0\n\ts_cbranch_scc1 1b\n2:"
               : "=&v"(tv), "=&s"(ts), "=&s"(cnt) : "v"((unsigned)(size_t)word), "s"(val) : "memory", "scc");
}
DEV void lds_post(unsigned* word, int lane) {
  asm volatile("" ::: "memory");
  if (lane == 0) __hip_atomic_fetch_add(word, 1u, __ATOMIC_RELAXED, __HIP_MEMORY_SCOPE_WORKGROUP);
  asm volatile("" ::: "memory");
}
DEV void lgkm0() { asm volatile("s_waitcnt lgkmcnt(0)" ::: "memory"); __builtin_amdgcn_sched_barrier(0); }

DEV f32x4 mfma_bf16(u32x4 a, u32x4 b, f32x4 c) {
  return __builtin_amdgcn_mfma_f32_16x16x32_bf16(__builtin_bit_cast(bf16x8, a), __builtin_bit_cast(bf16x8, b), c, 0, 0, 0);
}

constexpr int NB_PF = 8, NB_K = 640, NB_LN = 256, NB_MOD = 120;

struct K1Args {
  const unsigned short *x, *are, *aim, *bre, *bim, *cre, *cim, *logdt, *c, *wmod;
  char* ws;
};

DEV void k1_prefetch() {
  const int tid = threadIdx.x;
  unsigned long long pc;
  int o0, o1;
  asm volatile("s_getpc_b64 %0\n.Lpf_here%=:\n\ts_mov_b32 %1, _Z2k16K1Args - .Lpf_here%=\n\ts_mov_b32 %2, _Z5kend_v - .Lpf_here%="
               : "=s"(pc), "=s"(o0), "=s"(o1));
  const char* c0 = (const char*)(pc + (long long)o0);
  long n = o1 - o0;
  n = n < 0 ? 0 : (n > 98304 ? 98304 : n);
  unsigned v[3];
#pragma unroll
  for (int it = 0; it < 3; ++it) {
    const long o = (long)tid * 128 + it * 32768;
    v[it] = o < n ? *(const __attribute__((address_space(1))) unsigned*)(c0 + o) : 0u;
  }
  asm volatile("" :: "v"(v[0] ^ v[1] ^ v[2]));
}

DEV void k1_mod(const K1Args& p, int b, char* smem) {
  const int tid = threadIdx.x;
  const int rg = b & 7, cgp = b >> 3;
  const int c32 = tid & 31, r8 = tid >> 5;
  float* sil = (float*)smem;
  float* red = (float*)(smem + 512);
  u32x4 w[10];
#pragma unroll
  for (int it = 0; it < 10; ++it)
    w[it] = *(const u32x4*)(p.wmod + (size_t)(rg * 80 + r8 + 8 * it) * 3840 + 256 * cgp + 8 * c32);
  if (tid < 80) {
    float cv = ldbf(p.c + rg * 80 + tid);
    sil[tid] = rbf(cv / (1.f + expf(-cv)));
  }
  if (tid >= 128 && tid < 132 && b < 64) st4(p.ws, WS_CNT + (b * 4 + tid - 128) * 64, 0u);
  __syncthreads();
  float acc[8];
#pragma unroll
  for (int j = 0; j < 8; ++j) acc[j] = 0.f;
#pragma unroll
  for (int it = 0; it < 10; ++it) {
    float s = sil[r8 + 8 * it];
#pragma unroll
    for (int q = 0; q < 4; ++q) {
      acc[2 * q] += s * bf_lo(w[it][q]);
      acc[2 * q + 1] += s * bf_hi(w[it][q]);
    }
  }
#pragma unroll
  for (int j = 0; j < 8; ++j) acc[j] += __shfl_xor(acc[j], 32, 64);
  if ((tid & 63) < 32) {
    *(f32x4*)(red + (tid >> 6) * 256 + 8 * c32) = (f32x4){acc[0], acc[1], acc[2], acc[3]};
    *(f32x4*)(red + (tid >> 6) * 256 + 8 * c32 + 4) = (f32x4){acc[4], acc[5], acc[6], acc[7]};
  }
  __syncthreads();
  st4(p.ws, WS_MOD + ((256 * cgp + tid) * 8 + rg) * 4,
      __float_as_uint((red[tid] + red[256 + tid]) + (red[512 + tid] + red[768 + tid])));
}
struct ModP { f32x4 a, b; };
DEV ModP mod_ld(const char* ws, int idx) {
  const f32x4* mp = (const f32x4*)(ws + WS_MOD) + 2 * idx;
  ModP m; m.a = mp[0]; m.b = mp[1];
  return m;
}
DEV float mod_fin(const ModP& m) {
  return rbf(((m.a[0] + m.a[1]) + (m.a[2] + m.a[3])) + ((m.b[0] + m.b[1]) + (m.b[2] + m.b[3])));
}
#define SB() __builtin_amdgcn_sched_barrier(0)

DEV void k1_ln(const K1Args& p, int b, char* smem) {
  constexpr int STR = 704;
  const int tid = threadIdx.x;
  const int tok = tid >> 3, sub = tid & 7;
  const int tile = (b & 7) * 8 + ((b >> 3) & 7), pass = b >> 6;
  const int hf = pass & 1;
  const bool norm = pass < 2;
  const int t0 = tile * 32;
  const unsigned short* xr = p.x + (size_t)(t0 + tok) * D_ + 8 * sub;
  u32x4 xv[5], xo[5];
#pragma unroll
  for (int m = 0; m < 5; ++m) xv[m] = *(const u32x4*)(xr + 320 * hf + 64 * m);
  if (norm) {
#pragma unroll
    for (int m = 0; m < 5; ++m) xo[m] = *(const u32x4*)(xr + 320 * (hf ^ 1) + 64 * m);
    float s = 0.f, v = 0.f;
    const bf16x2 one2 = __builtin_bit_cast(bf16x2, 0x3f803f80u);
#pragma unroll
    for (int m = 0; m < 10; ++m)
#pragma unroll
      for (int q = 0; q < 4; ++q) {
        const unsigned u = m < 5 ? xv[m][q] : xo[m - 5][q];
        const bf16x2 x2 = __builtin_bit_cast(bf16x2, u);
        s = __builtin_amdgcn_fdot2_f32_bf16(x2, one2, s, false);
        v = __builtin_amdgcn_fdot2_f32_bf16(x2, x2, v, false);
      }
#pragma unroll
    for (int o = 1; o < 8; o <<= 1) { s += __shfl_xor(s, o, 64); v += __shfl_xor(v, o, 64); }
    const float mean = s * (1.f / D_);
    const float var = fmaxf(v * (1.f / D_) - mean * mean, 0.f);
    const float sd = rbf(sqrtf(rbf(rbf(var) + 1e-5f)));
    const float rs = 1.f / sd;
    const float mb = rbf(mean);
#pragma unroll
    for (int m = 0; m < 5; ++m)
#pragma unroll
      for (int q = 0; q < 4; ++q) {
        unsigned d2 = pk_bf(bf_lo(xv[m][q]) - mb, bf_hi(xv[m][q]) - mb);
        xv[m][q] = pk_bf(bf_lo(d2) * rs, bf_hi(d2) * rs);
      }
  }
  const int lane = tid & 63, w = tid >> 6;
  const int g = lane >> 4, i = lane & 15;
  const rsrc_t rsrc = ws_rsrc(p.ws);
#pragma unroll
  for (int m = 0; m < 5; ++m) *(u32x4*)(smem + tok * STR + (sub + 8 * m) * 16) = xv[m];
  __syncthreads();
  const size_t dsto = (norm ? WS_XNT : WS_XT) + ((size_t)tile * D_ + 320 * hf) * 64;
#pragma unroll
  for (int it = 0; it < 5; ++it) {
    const int cb = w * 5 + it;
    const char* a = smem + (8 * g + (i >> 2)) * STR + cb * 32 + (i & 3) * 8;
    const u32x2 ra = __builtin_bit_cast(u32x2, __builtin_amdgcn_ds_read_tr16_b64_v4i16((lds_s16x4*)a));
    const u32x2 rb = __builtin_bit_cast(u32x2, __builtin_amdgcn_ds_read_tr16_b64_v4i16((lds_s16x4*)(a + 4 * STR)));
    st16(rsrc, dsto + (size_t)(cb * 16 + i) * 64 + 16 * g, (u32x4){ra[0], ra[1], rb[0], rb[1]});
  }
}

constexpr int RT_STR = 176;
template <int NF> struct RtRaw { u32x4 r[NF]; };
template <int NF>
DEV void rt_load(RtRaw<NF>& t, const unsigned short* W, int N, int kk, int ct0, int lane) {
#pragma unroll
  for (int j = 0; j < NF; ++j) {
    const int c = lane + 64 * j, row = c / (2 * NF), part = c % (2 * NF);
    t.r[j] = *(const u32x4*)(W + (size_t)(32 * kk + row) * N + 16 * ct0 + 8 * part);
  }
}
template <int NF>
DEV void rt_park(const RtRaw<NF>& t, char* scr, int lane) {
#pragma unroll
  for (int j = 0; j < NF; ++j) {
    const int c = lane + 64 * j, row = c / (2 * NF), part = c % (2 * NF);
    *(u32x4*)(scr + row * RT_STR + 16 * part) = t.r[j];
  }
}
DEV u32x4 rt_frag(const char* scr, int j, int lane) {
  const int g = lane >> 4, i = lane & 15;
  const char* p = scr + (8 * g + (i >> 2)) * RT_STR + 32 * j + (i & 3) * 8;
  const u32x2 a = __builtin_bit_cast(u32x2, __builtin_amdgcn_ds_read_tr16_b64_v4i16((lds_s16x4*)p));
  const u32x2 b = __builtin_bit_cast(u32x2, __builtin_amdgcn_ds_read_tr16_b64_v4i16((lds_s16x4*)(p + 4 * RT_STR)));
  return (u32x4){a[0], a[1], b[0], b[1]};
}
struct cf { float re, im; };
DEV cf cmul(cf a, cf b) {
  cf r;
  r.re = a.re * b.re - a.im * b.im;
  r.im = a.re * b.im + a.im * b.re;
  return r;
}
DEV cf cexp_(float a2, float br, float m, float m0) {
  float mag = __builtin_amdgcn_exp2f(a2 * (m - m0));
  float ph = __builtin_amdgcn_fractf(br * m);
  cf r; r.re = mag * __builtin_amdgcn_cosf(ph); r.im = mag * __builtin_amdgcn_sinf(ph);
  return r;
}
DEV unsigned pk_h(float a, float b) {
  f32x2 v = {a, b};
  h16x2 r = __builtin_convertvector(v, h16x2);
  return __builtin_bit_cast(unsigned, r);
}
DEV f32x4 mfma_f16(u32x4 a, u32x4 b, f32x4 c) {
  return __builtin_amdgcn_mfma_f32_16x16x32_f16(__builtin_bit_cast(h16x8, a), __builtin_bit_cast(h16x8, b), c, 0, 0, 0);
}
DEV void put2(char* p, cf a, cf b) {
  u32x2 o = {pk_h(a.re, a.im), pk_h(b.re, b.im)};
  *(u32x2*)p = o;
}

constexpr int KG_RS = 544;
DEV void k1_kgen(const K1Args& p, int d, char* smem) {
  constexpr int RS = KG_RS;
  const int tid = threadIdx.x;
  const int lane = tid & 63;
  const int w = __builtin_amdgcn_readfirstlane(tid >> 6);
  const int g = lane >> 4, i = lane & 15;
  const size_t po = (size_t)d * NS + 2 * lane;
  const unsigned rar = *(const unsigned*)(p.are + po), rai = *(const unsigned*)(p.aim + po);
  const unsigned rbr = *(const unsigned*)(p.bre + po), rbi = *(const unsigned*)(p.bim + po);
  const unsigned rcr = *(const unsigned*)(p.cre + po), rci = *(const unsigned*)(p.cim + po);
  const float ldt = ldbf(p.logdt + d);
  asm volatile("" ::: "memory");
  SB();
  const float dt = rbf(expf(ldt));
  float a2[2], bv[2], m0[2];
  a2[0] = (dt * bf_lo(rar)) * 1.44269504088896341f; a2[1] = (dt * bf_hi(rar)) * 1.44269504088896341f;
  bv[0] = (dt * bf_lo(rai)) * 0.15915494309189534f; bv[1] = (dt * bf_hi(rai)) * 0.15915494309189534f;
  m0[0] = a2[0] > 0.f ? 2048.f : 0.f; m0[1] = a2[1] > 0.f ? 2048.f : 0.f;
  char* my = smem + lane * 8;
  if (w == 0) {
    cf v[2], v8[2], e8[2];
    float bound = 0.f;
#pragma unroll
    for (int q = 0; q < 2; ++q) {
      const float dbr = dt * (q ? bf_hi(rbr) : bf_lo(rbr)), dbi = dt * (q ? bf_hi(rbi) : bf_lo(rbi));
      const float cr = q ? bf_hi(rcr) : bf_lo(rcr), ci = q ? bf_hi(rci) : bf_lo(rci);
      const float gsh = __builtin_amdgcn_exp2f(a2[q] * m0[q]);
      v[q].re = (cr * dbr - ci * dbi) * gsh; v[q].im = -(cr * dbi + ci * dbr) * gsh;
      bound += (fabsf(v[q].re) + fabsf(v[q].im)) * __builtin_amdgcn_exp2f(fmaxf(a2[q], 0.f) * 120.f);
      e8[q] = cexp_(a2[q], -bv[q], 8.f, 0.f);
      v8[q] = cexp_(a2[q], -bv[q], 64.f, 0.f);
    }
#pragma unroll
    for (int o = 1; o < 64; o <<= 1) bound += __shfl_xor(bound, o, 64);
    int ex = (int)((__float_as_uint(bound) >> 23) & 0xff);
    ex = ex < 20 ? 20 : (ex > 240 ? 240 : ex);
    const float sv = __uint_as_float((unsigned)(127 + 13 + 127 - ex) << 23);
    if (lane == 0) st4(p.ws, WS_KSC + 4 * d, (unsigned)(ex - 13) << 23);
#pragma unroll
    for (int q = 0; q < 2; ++q) { v[q].re *= sv; v[q].im *= sv; v8[q] = cmul(v[q], v8[q]); }
#pragma unroll
    for (int j = 0; j < 8; ++j) {
      put2(my + (41 + j) * RS, v[0], v[1]);
      put2(my + (49 + j) * RS, v8[0], v8[1]);
      if (j < 7) {
#pragma unroll
        for (int q = 0; q < 2; ++q) { v[q] = cmul(v[q], e8[q]); v8[q] = cmul(v8[q], e8[q]); }
      }
    }
  } else if (w == 1) {
    cf e1[2], u[2], u2[2];
#pragma unroll
    for (int q = 0; q < 2; ++q) {
      e1[q] = cexp_(a2[q], bv[q], 1.f, 0.f);
      u[q].re = __builtin_amdgcn_exp2f(-a2[q] * m0[q]); u[q].im = 0.f;
      u2[q] = cexp_(a2[q], bv[q], 128.f, m0[q]);
    }
#pragma unroll
    for (int r = 0; r < 8; ++r) {
      put2(my + r * RS, u[0], u[1]);
      put2(my + (8 + r) * RS, u2[0], u2[1]);
      if (r < 7) {
#pragma unroll
        for (int q = 0; q < 2; ++q) { u[q] = cmul(u[q], e1[q]); u2[q] = cmul(u2[q], e1[q]); }
      }
    }
  } else if (w == 2) {
    cf e2[2], u[2], u2[2];
#pragma unroll
    for (int q = 0; q < 2; ++q) {
      e2[q] = cexp_(a2[q], bv[q], 2.f, 0.f);
      u[q] = cexp_(a2[q], bv[q], 256.f, m0[q]);
      u2[q] = cexp_(a2[q], bv[q], 384.f, m0[q]);
    }
#pragma unroll
    for (int r = 0; r < 4; ++r) {
      put2(my + (16 + r) * RS, u[0], u[1]);
      put2(my + (20 + r) * RS, u2[0], u2[1]);
      if (r < 3) {
#pragma unroll
        for (int q = 0; q < 2; ++q) { u[q] = cmul(u[q], e2[q]); u2[q] = cmul(u2[q], e2[q]); }
      }
    }
    cf e4[2] = {cmul(e2[0], e2[0]), cmul(e2[1], e2[1])};
#pragma unroll
    for (int r = 0; r < 2; ++r) {
      cf c0 = cexp_(a2[0], bv[0], 512.f + 128.f * r, m0[0]), c1 = cexp_(a2[1], bv[1], 512.f + 128.f * r, m0[1]);
      put2(my + (24 + 2 * r) * RS, c0, c1);
      put2(my + (25 + 2 * r) * RS, cmul(c0, e4[0]), cmul(c1, e4[1]));
    }
  } else {
    cf e4[2], e128[2], u[2], u2[2];
#pragma unroll
    for (int q = 0; q < 2; ++q) {
      e4[q] = cexp_(a2[q], bv[q], 4.f, 0.f);
      e128[q] = cexp_(a2[q], bv[q], 128.f, 0.f);
      u[q] = cexp_(a2[q], bv[q], 1024.f, m0[q]);
      u2[q] = cexp_(a2[q], bv[q], 1536.f, m0[q]);
    }
#pragma unroll
    for (int r = 2; r < 4; ++r) {
      cf c0 = cexp_(a2[0], bv[0], 512.f + 128.f * r, m0[0]), c1 = cexp_(a2[1], bv[1], 512.f + 128.f * r, m0[1]);
      put2(my + (24 + 2 * r) * RS, c0, c1);
      put2(my + (25 + 2 * r) * RS, cmul(c0, e4[0]), cmul(c1, e4[1]));
    }
#pragma unroll
    for (int r = 0; r < 4; ++r) {
      put2(my + (32 + r) * RS, u[0], u[1]);
      put2(my + (36 + r) * RS, u2[0], u2[1]);
#pragma unroll
      for (int q = 0; q < 2; ++q) { if (r < 3) u[q] = cmul(u[q], e128[q]); u2[q] = cmul(u2[q], e128[q]); }
    }
    put2(my + 40 * RS, u2[0], u2[1]);
  }
  __syncthreads();
  _Float16* kh = (_Float16*)(smem + 57 * RS);
  if (w < 3) {
    const char* ua = smem + (16 * w + i) * RS + g * 16;
    const char* va = smem + (41 + i) * RS + g * 16;
    f32x4 acc = {0.f, 0.f, 0.f, 0.f};
#pragma unroll
    for (int ks = 0; ks < 8; ++ks)
      acc = mfma_f16(*(const u32x4*)(ua + ks * 64), *(const u32x4*)(va + ks * 64), acc);
    int idx0, st;
    if (w == 0) { idx0 = (g < 2 ? 4 * g : 120 + 4 * g) + 8 * i; st = 1; }
    else if (w == 1) { idx0 = g < 2 ? 256 + 64 * g + 4 * i : 384 + 64 * (g - 2) + 2 * i; st = 1; }
    else { idx0 = 512 + 64 * g + i; st = 16; }
#pragma unroll
    for (int rg = 0; rg < 4; ++rg) {
      int idx = idx0 + st * rg;
      if (w == 1 && g >= 2) idx = idx0 + 32 * (rg >> 1) + (rg & 1);
      if (w == 2 && g >= 2) idx = (g == 2 && rg == 0 && i == 0) ? 640 : 644;
      kh[idx] = (_Float16)acc[rg];
    }
  }
  __syncthreads();
  const unsigned short* ku = (const unsigned short*)kh;
  const size_t kout = WS_KREV + (size_t)d * KREV * 2;
  {
    const int l0 = 2040 - 8 * tid;
    u32x4 o;
    if (w < 2) {
      const int q = l0 >> 3;
      const unsigned a = ku[384 + q], b = ku[385 + q];
      const unsigned t = (q & 1) ? b : a;
      o[0] = b | (b << 16); o[1] = b | (t << 16); o[2] = a | (a << 16); o[3] = o[2];
    } else if (w == 2) {
      const int q = l0 >> 2;
      const unsigned a = ku[256 + q], b = ku[257 + q], c = ku[258 + q];
      o[0] = c | (c << 16); o[1] = b | (b << 16); o[2] = b | (a << 16); o[3] = a | (a << 16);
    } else {
      unsigned v[8];
#pragma unroll
      for (int e = 0; e < 8; ++e) {
        const int l = l0 + 7 - e;
        const int idx = l < 256 ? l : (int)(pk_bf((float)l, 0.f) & 0xffffu) - 17024;
        v[e] = ku[idx];
      }
#pragma unroll
      for (int q = 0; q < 4; ++q) o[q] = v[2 * q] | (v[2 * q + 1] << 16);
    }
    st16(ws_rsrc(p.ws), kout + 16 * tid, o);
  }
}

__global__ NOPK __launch_bounds__(256) void k1(K1Args p) {
  __shared__ __attribute__((aligned(16))) char smem[57 * KG_RS + 1312];
  int b = blockIdx.x;
  if (b < NB_PF) { k1_prefetch(); return; }
  b -= NB_PF;
  if (b < NB_K) { k1_kgen(p, b, smem); return; }
  b -= NB_K;
  asm volatile("" :: "s"(p.ws), "s"(p.x));
  __builtin_amdgcn_s_sleep(4);
  if (b < NB_LN) k1_ln(p, b, smem);
  else k1_mod(p, b - NB_LN, smem);
}

constexpr int K2_CS = 4160;
constexpr int K2_KOFF = 4 * K2_CS;
constexpr int K2_CH = K2_KOFF + 4608;
constexpr int CV_RED = 3 * K2_CH;
constexpr int CV_RT2 = 86016;
constexpr int CV_SUM = CV_RT2 + 4 * (32 * 176 + 256);
constexpr int CV_XCH = CV_SUM + 2560;
constexpr int CV_FL = CV_RED + 20480;

struct Chan { float sa, ca1, ga, dsk, hs, hinv, yinv; };
struct ChanRaw { ModP msa, mca, mga; float kinv; unsigned short rds; };
DEV ChanRaw chan_ld(const unsigned short* dskip, const char* ws, int d) {
  ChanRaw c;
  c.rds = dskip[d];
  c.msa = mod_ld(ws, d); c.mca = mod_ld(ws, D_ + d); c.mga = mod_ld(ws, 2 * D_ + d);
  c.kinv = ((const float*)(ws + WS_KSC))[d];
  return c;
}
DEV Chan chan_fin(const ChanRaw& r) {
  Chan c;
  c.sa = mod_fin(r.msa); c.ca1 = rbf(1.f + mod_fin(r.mca)); c.ga = mod_fin(r.mga);
  c.dsk = __uint_as_float((unsigned)r.rds << 16);
  const float hb = 26.f * fabsf(c.ca1) + fabsf(c.sa);
  int ex = (int)((__float_as_uint(hb) >> 23) & 0xff);
  ex = ex < 20 ? 20 : (ex > 240 ? 240 : ex);
  c.hs = __uint_as_float((unsigned)(127 + 14 + 127 - ex) << 23);
  c.hinv = __uint_as_float((unsigned)(ex - 14) << 23);
  c.yinv = r.kinv * c.hinv;
  return c;
}
DEV size_t tl_off(int d, int t) { return (((size_t)(t >> 5) * D_ + d) * 32 + (t & 31)) * 2; }
struct StageRaw { u32x4 xa0, xa1, kv0, kv1; u32x2 xb; };
DEV StageRaw stage_ld(const char* ws, int d, int th) {
  StageRaw s;
  const unsigned short* krev = (const unsigned short*)(ws + WS_KREV) + (size_t)d * KREV;
  const size_t xo = tl_off(d, 16 * th);
  s.xa0 = *(const u32x4*)(ws + WS_XNT + xo); s.xa1 = *(const u32x4*)(ws + WS_XNT + xo + 16);
  s.xb = *(const u32x2*)(ws + WS_XNT + tl_off(d, min(16 * th + 16, L_ - 4)));
  s.kv0 = *(const u32x4*)(krev + 8 * th); s.kv1 = *(const u32x4*)(krev + 1024 + 8 * th);
  return s;
}
DEV void stage_wr(const StageRaw& s, const Chan& c, char* smem, int th) {
  constexpr int CS = K2_CS, KOFF = K2_KOFF;
  unsigned hh[10];
#pragma unroll
  for (int q = 0; q < 10; ++q) {
    unsigned u = q < 4 ? s.xa0[q] : (q < 8 ? s.xa1[q - 4] : s.xb[q - 8]);
    unsigned m = pk_bf(bf_lo(u) * c.ca1, bf_hi(u) * c.ca1);
    unsigned h = pk_bf(bf_lo(m) + c.sa, bf_hi(m) + c.sa);
    hh[q] = pk_h(bf_lo(h) * c.hs, bf_hi(h) * c.hs);
  }
  char* dst = smem + (2 * th + 2) * 16;
  unsigned s1[9];
#pragma unroll
  for (int q = 0; q < 9; ++q) s1[q] = __builtin_amdgcn_alignbit(hh[q + 1], hh[q], 16);
#pragma unroll
  for (int e = 0; e < 2; ++e) {
    *(u32x4*)(dst + 16 * e) = (u32x4){hh[4 * e], hh[4 * e + 1], hh[4 * e + 2], hh[4 * e + 3]};
    *(u32x4*)(dst + 16 * e + CS) = (u32x4){s1[4 * e], s1[4 * e + 1], s1[4 * e + 2], s1[4 * e + 3]};
    *(u32x4*)(dst + 16 * e + 2 * CS) = (u32x4){hh[4 * e + 1], hh[4 * e + 2], hh[4 * e + 3], hh[4 * e + 4]};
    *(u32x4*)(dst + 16 * e + 3 * CS) = (u32x4){s1[4 * e + 1], s1[4 * e + 2], s1[4 * e + 3], s1[4 * e + 4]};
  }
  if (th == 0) {
    u32x4 z = {0u, 0u, 0u, 0u};
    *(u32x4*)(smem) = z; *(u32x4*)(smem + CS) = z; *(u32x4*)(smem + 2 * CS) = z; *(u32x4*)(smem + 3 * CS) = z;
    *(u32x4*)(smem + 16) = z;
    *(u32x4*)(smem + CS + 16) = (u32x4){0u, 0u, 0u, hh[0] << 16};
    *(u32x4*)(smem + 2 * CS + 16) = (u32x4){0u, 0u, 0u, hh[0]};
    *(u32x4*)(smem + 3 * CS + 16) = (u32x4){0u, 0u, hh[0] << 16, s1[0]};
  }
  *(u32x4*)(smem + KOFF + 16 * th) = s.kv0;
  *(u32x4*)(smem + KOFF + 2048 + 16 * th) = s.kv1;
  if (th < 32) *(u32x4*)(smem + KOFF + 4096 + 16 * th) = (u32x4){0u, 0u, 0u, 0u};
}
DEV u32x2 conv_out(const f32x4& y, const Chan& c, const char* smem, int t, const u32x2& xres) {
  const u32x2 hv = *(const u32x2*)(smem + 2 * (t + 16));
  u32x2 o;
#pragma unroll
  for (int e = 0; e < 2; ++e) {
    const unsigned hu = hv[e];
    const h16x2 h2 = __builtin_bit_cast(h16x2, hu);
    const float h0 = (float)h2[0] * c.hinv, h1 = (float)h2[1] * c.hinv;
    unsigned yb = pk_bf(y[2 * e] * c.yinv, y[2 * e + 1] * c.yinv);
    unsigned hd = pk_bf(h0 * c.dsk, h1 * c.dsk);
    unsigned y2 = pk_bf(bf_lo(yb) + bf_lo(hd), bf_hi(yb) + bf_hi(hd));
    unsigned gy = pk_bf(bf_lo(y2) * c.ga, bf_hi(y2) * c.ga);
    o[e] = pk_bf(bf_lo(xres[e]) + bf_lo(gy), bf_hi(xres[e]) + bf_hi(gy));
  }
  return o;
}
#define K2_LOAD(buf, cc, u)                                                                                     \
  asm volatile("ds_read_b64 %0, %3 offset:%5\n\tds_read_b64 %1, %3 offset:%6\n\tds_read_b128 %2, %4 offset:%7"   \
               : "=&v"(Al[buf][u]), "=&v"(Ah[buf][u]), "=&v"(Bf[buf][u])                                          \
               : "v"(ap0), "v"(bp0), "i"(64 * (cc) + 512 * (u)), "i"(64 * (cc) + 512 * (u) + 8),                 \
                 "i"(64 * (cc) + 3584 - 512 * (u)))
template <int SET> DEV constexpr int half_tile(int j) {
  return SET == 0 ? (j == 0 ? 7 : (j == 1 ? 6 : 2)) : (j < 3 ? 5 - j : 4 - j);
}
template <int SET>
DEV void conv_half(unsigned ap0, unsigned bp0, char* redw) {
  constexpr int NU = SET == 0 ? 8 : 6, NT = SET == 0 ? 3 : 5;
  u32x2 Al[2][NU], Ah[2][NU];
  u32x4 Bf[2][NU];
#pragma unroll
  for (int u = 0; u < NU; ++u) K2_LOAD(0, 0, u);
  f32x4 acc[NT];
#pragma unroll
  for (int j = 0; j < NT; ++j) acc[j] = (f32x4){0.f, 0.f, 0.f, 0.f};
#pragma unroll
  for (int cc = 0; cc < 2; ++cc) {
    lgkm0();
    if (cc == 0) {
#pragma unroll
      for (int u = 0; u < NU; ++u) K2_LOAD(1, 1, u);
    }
#pragma unroll
    for (int sp = 0; sp < NU; ++sp)
#pragma unroll
      for (int j = 0; j < NT; ++j) {
        const int q = half_tile<SET>(j);
        if (sp <= q) {
          const u32x4 A = {Al[cc][sp][0], Al[cc][sp][1], Ah[cc][sp][0], Ah[cc][sp][1]};
          acc[j] = mfma_f16(A, Bf[cc][q - sp], acc[j]);
        }
      }
    SB();
  }
#pragma unroll
  for (int j = 0; j < NT; ++j) *(f32x4*)(redw + j * 4096) = acc[j];
}

template <int NF>
DEV void w1_fin(const RtRaw<NF>& t, const ModP& mp, char* ws, int kk, int ct0, int lane, char* scr, float* sums) {
  const int g = lane >> 4, i = lane & 15;
  float* tb = (float*)(scr + 32 * RT_STR);
  const float mv = mod_fin(mp);
  tb[lane] = lane < 32 ? rbf(1.f + mv) : mv;
  rt_park<NF>(t, scr, lane);
  lgkm0();
  const f32x4 ca = *(const f32x4*)(tb + 8 * g), cb = *(const f32x4*)(tb + 8 * g + 4);
  const f32x4 sa = *(const f32x4*)(tb + 32 + 8 * g), sb = *(const f32x4*)(tb + 36 + 8 * g);
  const u32x4 sv = {pk_bf(sa[0], sa[1]), pk_bf(sa[2], sa[3]), pk_bf(sb[0], sb[1]), pk_bf(sb[2], sb[3])};
  const u32x4 ones = {0x3f803f80u, 0x3f803f80u, 0x3f803f80u, 0x3f803f80u};
  const f32x4 z = {0.f, 0.f, 0.f, 0.f};
  const rsrc_t rs = ws_rsrc(ws);
#pragma unroll
  for (int j = 0; j < NF; ++j) {
    const u32x4 f = rt_frag(scr, j, lane);
    const u32x4 sc = {pk_bf(bf_lo(f[0]) * ca[0], bf_hi(f[0]) * ca[1]), pk_bf(bf_lo(f[1]) * ca[2], bf_hi(f[1]) * ca[3]),
                      pk_bf(bf_lo(f[2]) * cb[0], bf_hi(f[2]) * cb[1]), pk_bf(bf_lo(f[3]) * cb[2], bf_hi(f[3]) * cb[3])};
    st16(rs, WS_W1F + ((size_t)(kk * 108 + ct0 + j) * 64 + lane) * 16, sc);
    const f32x4 cs = mfma_bf16(sc, ones, z), b1 = mfma_bf16(f, sv, z);
    if (i < 2) *(f32x4*)(sums + i * 80 + 16 * j + 4 * g) = i ? b1 : cs;
  }
}

DEV void conv_phase(const unsigned short* __restrict__ dskip, char* __restrict__ ws,
                    const unsigned short* __restrict__ w1, const unsigned short* __restrict__ w2, char* smem_all) {
  constexpr int CS = K2_CS, KOFF = K2_KOFF;
  const int tid = threadIdx.x, lane = tid & 63;
  const int w = __builtin_amdgcn_readfirstlane(tid >> 6);
  const int bx = blockIdx.x;
  const int pj = (bx & 7) * 16 + (bx >> 4), par = (bx >> 3) & 1;
  const int r = lane & 15, g = lane >> 4, th = tid & 127;
  const int s4 = (r + 1) & 3;
  const rsrc_t rs = ws_rsrc(ws);
  unsigned* fl = (unsigned*)(smem_all + CV_FL);
  if (tid < 6) fl[tid] = 0u;
  if (w < 4) {
    const int d = 5 * pj + 2 * par + (w >> 1), hw = w & 1;
    char* smem = smem_all + (w >> 1) * K2_CH;
    const StageRaw sr = stage_ld(ws, d, th);
    const ChanRaw cr = chan_ld(dskip, ws, d);
    u32x2 xres[4];
#pragma unroll
    for (int q = 0; q < 4; ++q)
      xres[q] = *(const u32x2*)(ws + WS_XT + tl_off(d, 256 * (4 * hw + q) + 16 * r + 4 * g));
    SB();
    const Chan c = chan_fin(cr);
    stage_wr(sr, c, smem, th);
    __syncthreads();
    f32x4 acc[8];
#pragma unroll
    for (int q = 0; q < 8; ++q) acc[q] = (f32x4){0.f, 0.f, 0.f, 0.f};
    {
      const unsigned ap0 = (unsigned)(size_t)(smem + s4 * CS + 2 * (128 * hw + 8 * g + (r + 1) - s4));
      const unsigned bp0 = (unsigned)(size_t)(smem + KOFF + 2 * (2032 + 128 * hw - 16 * r + 8 * g) - 3584);
      u32x2 Al[2][8], Ah[2][8];
      u32x4 Bf[2][8];
#pragma unroll
      for (int u = 0; u < 8; ++u) K2_LOAD(0, 0, u);
#pragma unroll
      for (int cc = 0; cc < 4; ++cc) {
        lgkm0();
#pragma unroll
        for (int q = 0; q < 8; ++q) {
          if (cc < 3) K2_LOAD((cc + 1) & 1, cc + 1, q);
#pragma unroll
          for (int sp = 0; sp <= q; ++sp) {
            const u32x4 A = {Al[cc & 1][sp][0], Al[cc & 1][sp][1], Ah[cc & 1][sp][0], Ah[cc & 1][sp][1]};
            acc[q] = mfma_f16(A, Bf[cc & 1][q - sp], acc[q]);
          }
          SB();
        }
      }
    }
    f32x4 keep[4];
    {
      f32x4* xw = (f32x4*)(smem_all + CV_XCH + w * 4096) + lane;
      if (hw) {
#pragma unroll
        for (int q = 0; q < 4; ++q) { xw[64 * q] = acc[q]; keep[q] = acc[4 + q]; }
      } else {
#pragma unroll
        for (int q = 0; q < 4; ++q) { xw[64 * q] = acc[4 + q]; keep[q] = acc[q]; }
      }
    }
    lds_post(fl + w, lane);
    lds_wait(fl + (w ^ 1), 1u);
    const f32x4* xrd = (const f32x4*)(smem_all + CV_XCH + (w ^ 1) * 4096) + lane;
#pragma unroll
    for (int qp = 0; qp < 2; ++qp) {
      u32x2 o[2];
#pragma unroll
      for (int h = 0; h < 2; ++h) {
        const int q = 2 * qp + h;
        o[h] = conv_out(keep[q] + xrd[64 * q], c, smem, 256 * (4 * hw + q) + 16 * r + 4 * g, xres[q]);
      }
      const auto s0 = __builtin_amdgcn_permlane16_swap(o[0][0], o[1][0], false, false);
      const auto s1 = __builtin_amdgcn_permlane16_swap(o[0][1], o[1][1], false, false);
      const int t = 256 * (4 * hw + 2 * qp + (g & 1)) + 16 * r + 8 * (g >> 1);
      st16(rs, WS_X1T + (((size_t)(t >> 6) * D_ + d) * 64 + (t & 63)) * 2, (u32x4){s0[0], s1[0], s0[1], s1[1]});
    }
  } else {
    __builtin_amdgcn_s_setprio(2);
    const int d = 5 * pj + 4, wq = w - 4;
    char* smem = smem_all + 2 * K2_CH;
    StageRaw sr;
    if (wq < 2) sr = stage_ld(ws, d, th);
    asm volatile("" ::: "memory");
    const ChanRaw cr = chan_ld(dskip, ws, d);
    const int tl0 = par ? (wq < 3 ? 5 - wq : 1) : (wq == 0 ? 7 : (wq == 1 ? 6 : 2)), tl1 = 0;
    const unsigned ap0 = (unsigned)(size_t)(smem + s4 * CS + 2 * (64 * wq + 8 * g + (r + 1) - s4));
    const unsigned bp0 = (unsigned)(size_t)(smem + KOFF + 2 * (2032 + 64 * wq - 16 * r + 8 * g) - 3584);
    char* redw = smem_all + CV_RED + wq * 1024 + lane * 16;
    u32x2 xres[2];
    xres[0] = *(const u32x2*)(ws + WS_XT + tl_off(d, 256 * tl0 + 16 * r + 4 * g));
    xres[1] = *(const u32x2*)(ws + WS_XT + tl_off(d, 256 * tl1 + 16 * r + 4 * g));
    SB();
    const Chan c = chan_fin(cr);
    if (wq < 2) {
      stage_wr(sr, c, smem, th);
    } else {
      __builtin_amdgcn_s_sleep(32);
    }
    const bool isw1 = bx < 110;
    const int b2 = min(max(bx - 110, 0), 107);
    const int jk = isw1 ? 4 * (bx / 22) + wq : b2 >> 1;
    const int jc = isw1 ? min(5 * (bx % 22), 103) : (b2 & 1) * 20 + 5 * wq;
    RtRaw<5> rt;
    rt_load<5>(rt, isw1 ? w1 : w2, isw1 ? FF : D_, jk, jc, lane);
    const ModP mp = mod_ld(ws, (lane < 32 ? 4 * D_ : 3 * D_ - 32) + 32 * min(jk, 19) + lane);
    SB();
    bar_lds();
    if (par) conv_half<1>(ap0, bp0, redw); else conv_half<0>(ap0, bp0, redw);
    lds_post(fl + 4, lane);
    char* scr = smem_all + CV_RT2 + wq * (32 * RT_STR + 256);
    if (isw1) {
      float* sums = (float*)(smem_all + CV_SUM);
      w1_fin<5>(rt, mp, ws, jk, jc, lane, scr, sums + 160 * wq);
      asm volatile("" ::: "memory");
      unsigned old = 0u;
      if (lane == 0) old = __hip_atomic_fetch_add(fl + 5, 1u, __ATOMIC_RELAXED, __HIP_MEMORY_SCOPE_WORKGROUP);
      asm volatile("" ::: "memory");
      if (__builtin_amdgcn_readfirstlane(old) == 3u && lane < 40) {
        const f32x4* sp = (const f32x4*)sums + lane;
        const f32x4 v = (sp[0] + sp[40]) + (sp[80] + sp[120]);
        st16(rs, WS_CSP + ((size_t)(2 * (bx / 22) + lane / 20) * FF + 16 * jc + 4 * (lane % 20)) * 4, __builtin_bit_cast(u32x4, v));
      }
    } else if (bx < 218) {
      rt_park<5>(rt, scr, lane);
      lgkm0();
#pragma unroll
      for (int j = 0; j < 5; ++j) st16(rs, WS_W2F + ((size_t)((jc + j) * 54 + jk) * 64 + lane) * 16, rt_frag(scr, j, lane));
    }
    lds_wait(fl + 4, 4u);
    const char* rd = smem_all + CV_RED + lane * 16;
#pragma unroll
    for (int u = 0; u < 2; ++u) {
      const bool on = u == 0 ? (par || wq < 3) : (par && wq == 0);
      if (on) {
        const int sl = u == 0 ? wq : 4, tl = u == 0 ? tl0 : tl1;
        const f32x4 y = (*(const f32x4*)(rd + sl * 4096) + *(const f32x4*)(rd + sl * 4096 + 1024))
                      + (*(const f32x4*)(rd + sl * 4096 + 2048) + *(const f32x4*)(rd + sl * 4096 + 3072));
        const int t = 256 * tl + 16 * r + 4 * g;
        st8(rs, WS_X1T + (((size_t)(t >> 6) * D_ + d) * 64 + (t & 63)) * 2, conv_out(y, c, smem, t, xres[u]));
      }
    }
  }
}

__global__ NOPK __launch_bounds__(512, 2) void k2(const unsigned short* __restrict__ dskip, char* __restrict__ ws,
                                                  const unsigned short* __restrict__ w1, const unsigned short* __restrict__ w2) {
  __shared__ __attribute__((aligned(16))) char smem[CV_XCH + 16384];
  conv_phase(dskip, ws, w1, w2, smem);
}

DEV u32x4 ld16_sc1(rsrc_t r, int voff, int soff) { return __builtin_amdgcn_raw_buffer_load_b128(r, voff, soff, 16); }
DEV u32x4 ld16(rsrc_t r, int voff, int soff) { return __builtin_amdgcn_raw_buffer_load_b128(r, voff, soff, 0); }
DEV u32x4 tr_frag(const char* p) {
  const u32x2 a = __builtin_bit_cast(u32x2, __builtin_amdgcn_ds_read_tr16_b64_v4i16((lds_s16x4*)p));
  const u32x2 b = __builtin_bit_cast(u32x2, __builtin_amdgcn_ds_read_tr16_b64_v4i16((lds_s16x4*)(p + 256)));
  return (u32x4){a[0], a[1], b[0], b[1]};
}

constexpr int KF_RS = 84;
constexpr int KF_RB = 8 * 16 * KF_RS * 4;
constexpr int KF_BIG = 2 * KF_RB;
constexpr int NW2L = 8, NW2R = 27;
constexpr int KF_TAB = KF_BIG;
constexpr int KF_PS = KF_TAB + 320;
constexpr int KF_XR = KF_PS + 512;
constexpr int KF_CNT = KF_XR + 64 * 176;
constexpr int KF_LDS = KF_CNT + 16;
constexpr int X_FIRST = 5, X_STEP = 4;
constexpr int W1_EARLY = 3, W1_MID = 7, W1_PRE = 10;
constexpr int W2_START = 6, W2_RATE = 2, W2_INL = 28;
constexpr int CS_STEP = 14, SIG_SPLIT = 4, H1_PRE = 9, H1_BATCH = 7;
constexpr unsigned LOCK_MASK = 0x55554;

DEV void kf_body(char* __restrict__ ws, unsigned short* __restrict__ out, char* smem) {
  char* xs = smem;
  float* tab = (float*)(smem + KF_TAB);
  float* ps = (float*)(smem + KF_PS);
  char* xr = smem + KF_XR;
  unsigned* lcnt = (unsigned*)(smem + KF_CNT);
  float* cst = (float*)(smem + 81920);
  const int tid = threadIdx.x, lane = tid & 63;
  const int w = __builtin_amdgcn_readfirstlane(tid >> 6);
  const int bx = blockIdx.x;
  const int ng = (bx & 1) * 4 + ((bx >> 3) & 3), mt = ((bx & 7) >> 1) * 8 + (bx >> 5);
  const int g = lane >> 4, i = lane & 15;
  const rsrc_t rs_ = ws_rsrc(ws);

  unsigned* flags = (unsigned*)(ws + WS_CNT) + mt * 128;
  const int ntl = ng < 6 ? 14 : 12;
  const int ctb = ng < 6 ? 14 * ng : 84 + 12 * (ng - 6);
  const bool two = w < ntl - 8;
  const bool full = w < 6;
  const int ks0 = w < 6 ? 7 * w : 42 + 6 * (w - 6);
  const int ct0 = ctb + (two ? 2 * w : (ntl - 8) + w);
  const int lo16 = lane * 16;
  const int w1s = (int)WS_W1F + ct0 * 1024;
  const int w2s = (int)WS_W2F + (ng * 5 * 54 + ks0) * 1024;

  u32x4 ring[32], wl[NW2L];
  const char* bb0 = xs + (8 * g + (i >> 2)) * 64 + ((g & 1) << 5) + (i & 3) * 8;
  const char* bb1 = xs + (8 * g + (i >> 2)) * 64 + (((g & 1) ^ 1) << 5) + (i & 3) * 8;
  auto w2_off = [&](int e) __attribute__((always_inline)) {
    const int s = e < 27 ? e / 4 : (e < 34 ? e - 27 : 6), c = e < 27 ? e % 4 : (e < 34 ? 4 : 3);
    const int sn = (s == 6 && !full) ? 5 : s;
    return w2s + (c * 54 + sn) * 1024;
  };

  auto gemm1 = [&](auto two_tag) __attribute__((always_inline)) {
    constexpr bool TWO = decltype(two_tag)::value;
    if (TWO) asm volatile("; two tiles" ::: "memory"); else asm volatile("; one tile" ::: "memory");
    u32x4 t[10];
    auto st_x = [&](int i0, int i1) __attribute__((always_inline)) {
#pragma unroll
      for (int it = i0; it < i1; ++it) {
        const int c = tid + 512 * it;
        const int row = c >> 3;
        *(u32x4*)(xs + ((c >> 2) & 1) * 40960 + row * 64 + (((c & 3) ^ ((row >> 2) & 2)) << 4)) = t[it];
      }
    };
#pragma unroll
    for (int it = 0; it < X_FIRST; ++it) t[it] = ld16(rs_, tid * 16, (int)WS_X1T + mt * 81920 + 8192 * it);
#pragma unroll
    for (int k = 0; k < W1_EARLY; ++k) {
      ring[2 * k] = ld16(rs_, lo16, w1s + k * 108 * 1024);
      if (TWO) ring[2 * k + 1] = ld16(rs_, lo16, w1s + (k * 108 + 1) * 1024);
    }
    SB();
    __builtin_amdgcn_s_barrier();
    SB();
#pragma unroll
    for (int k = W1_EARLY; k < W1_MID; ++k) {
      ring[2 * k] = ld16(rs_, lo16, w1s + k * 108 * 1024);
      if (TWO) ring[2 * k + 1] = ld16(rs_, lo16, w1s + (k * 108 + 1) * 1024);
    }
    SB();
    st_x(0, X_FIRST);
    SB();
    if (tid == 0) *lcnt = 0u;
    ModP gbp;
    if (!TWO) gbp = mod_ld(ws, 5 * D_ + 80 * ng + min((w & 1) * 64 + lane, 79));
    SB();
    __syncthreads();
    auto resid = [&]() __attribute__((always_inline)) {
      const int kb = (80 * ng) >> 5, tq = w & 3;
#pragma unroll
      for (int u = 0; u < 2; ++u) {
        const int kk = min(kb + 2 * (w >> 2) + u, 19);
        const u32x4 f = tr_frag(((tq & 1) ? bb1 : bb0) + (tq >> 1) * 40960 + kk * 2048);
        const unsigned dl = (unsigned)(32 * kk + 8 * g - 80 * ng);
        if (dl < 80u) *(u32x4*)(xr + (tq * 16 + i) * 176 + dl * 2) = f;
      }
    };
    const int hs = TWO ? 0 : (w & 1);
    const char* ba[4] = {bb0 + hs * 40960, bb1 + hs * 40960, bb0 + (hs ^ 1) * 40960, bb1 + (hs ^ 1) * 40960};
    const u32x4 ones = {0x3f803f80u, 0x3f803f80u, 0x3f803f80u, 0x3f803f80u};
    f32x4 acc[2][4];
#pragma unroll
    for (int c = 0; c < 2; ++c)
#pragma unroll
      for (int q = 0; q < 4; ++q) acc[c][q] = (f32x4){0.f, 0.f, 0.f, 0.f};
    u32x4 csp[5];
    const int cso = ((w & 1) * FF + 16 * ctb + 4 * min(lane, 4 * ntl - 1)) * 4;
    u32x4 bn[2][4];
#pragma unroll
    for (int q = 0; q < 4; ++q) bn[0][q] = tr_frag(ba[q]);
#pragma unroll
    for (int q = 0; q < 4; ++q) bn[1][q] = tr_frag(ba[q] + 2048);
#pragma unroll
    for (int kk = 0; kk < 20; ++kk) {
      if (kk == X_STEP) { st_x(X_FIRST, 10); SB(); }
      if (kk == 2 * X_FIRST - 2) { SB(); bar_lds(); SB(); }
      else if ((LOCK_MASK >> kk) & 1) { SB(); __builtin_amdgcn_s_barrier(); SB(); }
      if (kk == 2 * X_FIRST) { resid(); SB(); }
      auto gload = [&](int u) __attribute__((always_inline)) {
        {
          constexpr int CATCH = W1_PRE - W1_MID;
          const int k0 = kk == 0 ? W1_MID : (kk <= CATCH ? W1_MID + 2 * kk - 1 : kk + W1_PRE);
          const int nk = (kk >= 1 && kk <= CATCH) ? 2 : 1;
          const int kn = k0 + (nk == 2 ? (u >> 1) : 0);
          const bool on = nk == 2 || u >= 2;
          if (on && kn < 20) {
            if ((u & 1) == 0) ring[(2 * kn) % 32] = ld16(rs_, lo16, w1s + kn * 108 * 1024);
            else if (TWO) ring[(2 * kn + 1) % 32] = ld16(rs_, lo16, w1s + (kn * 108 + 1) * 1024);
          }
          if (kk == 0 && u < 2) {
#pragma unroll
            for (int it = X_FIRST + u * 3; it < (u ? 10 : (X_FIRST + 3 < 10 ? X_FIRST + 3 : 10)); ++it)
              t[it] = ld16(rs_, tid * 16, (int)WS_X1T + mt * 81920 + 8192 * it);
          }
        }
        if (kk >= W2_START && u < W2_RATE) {
          const int n = W2_RATE * (kk - W2_START) + u;
          if (n < NW2L) wl[n] = ld16(rs_, lo16, w2_off(NW2R + n));
          else if (n < W2_INL) ring[8 + n - NW2L] = ld16(rs_, lo16, w2_off(n - NW2L));
        }
      };
      const u32x4 a0 = ring[(2 * kk) % 32], a1 = ring[(2 * kk + 1) % 32];
#pragma unroll
      for (int q = 0; q < 4; ++q) {
        const u32x4 bq = bn[kk & 1][q];
        acc[0][q] = mfma_bf16(a0, bq, acc[0][q]);
        if (TWO) acc[1][q] = mfma_bf16(a1, bq, acc[1][q]);
        else if (q < 2) {
          acc[1][2 * q] = mfma_bf16(ones, bq, acc[1][2 * q]);
          acc[1][2 * q + 1] = mfma_bf16(bq, bq, acc[1][2 * q + 1]);
        }
        SB();
        if (kk < 18) bn[kk & 1][q] = tr_frag(ba[q] + (kk + 2) * 2048);
        gload(q);
        SB();
      }
      if (!TWO) {
        if (kk < 5) csp[kk] = ld16(rs_, cso, (int)WS_CSP + kk * 2 * FF * 4);
        if (kk == CS_STEP) {
          f32x4 t = __builtin_bit_cast(f32x4, csp[0]);
#pragma unroll
          for (int u = 1; u < 5; ++u) t += __builtin_bit_cast(f32x4, csp[u]);
          if (w >= 6 && lane < 56) *(f32x4*)(cst + (w & 1) * 224 + 4 * lane) = t;
        }
      }
      if (!TWO && kk == 12) { if ((w & 1) * 64 + lane < 80) tab[(w & 1) * 64 + lane] = mod_fin(gbp); }
      SB();
    }
    if (!TWO) {
#pragma unroll
      for (int h = 0; h < 2; ++h) {
        const f32x4 aq = acc[1][2 * h + 1];
        const float dq = (i & 2) ? ((i & 1) ? aq[3] : aq[2]) : ((i & 1) ? aq[1] : aq[0]);
        const int tk = (2 * hs + h) * 16 + i;
        if (g == (i >> 2)) {
          const float mean = acc[1][2 * h][0] * (1.f / D_);
          const float var = fmaxf(dq * (1.f / D_) - mean * mean, 0.f);
          const float sd = rbf(sqrtf(rbf(rbf(var) + 1e-5f)));
          const float rs = 1.f / sd;
          ps[tk] = rs;
          ps[64 + tk] = -rbf(mean) * rs;
        }
      }
    }
    __syncthreads();
    f32x4 csv[2], b1v[2];
#pragma unroll
    for (int c = 0; c < (TWO ? 2 : 1); ++c) {
      csv[c] = *(const f32x4*)(cst + 16 * (ct0 - ctb + c) + 4 * g);
      b1v[c] = *(const f32x4*)(cst + 224 + 16 * (ct0 - ctb + c) + 4 * g);
    }
#pragma unroll
    for (int qp = 0; qp < 2; ++qp) {
      u32x2 o[2][2];
#pragma unroll
      for (int h = 0; h < 2; ++h) {
        const int q = 2 * qp + h, tq = q ^ (2 * hs);
        const float rs = ps[tq * 16 + i], nm = ps[64 + tq * 16 + i];
#pragma unroll
        for (int c = 0; c < (TWO ? 2 : 1); ++c) {
          float v[4];
#pragma unroll
          for (int r = 0; r < 4; ++r) v[r] = fmaxf(fmaf(acc[c][q][r], rs, fmaf(nm, csv[c][r], b1v[c][r])), 0.f);
          o[c][h] = (u32x2){pk_bf(v[0], v[1]), pk_bf(v[2], v[3])};
        }
      }
      const int tl = ((2 * qp) ^ (2 * hs)) + (g & 1);
#pragma unroll
      for (int c = 0; c < (TWO ? 2 : 1); ++c) {
        const int ct = ct0 + c;
        const auto s0 = __builtin_amdgcn_permlane16_swap(o[c][0][0], o[c][1][0], false, false);
        const auto s1 = __builtin_amdgcn_permlane16_swap(o[c][0][1], o[c][1][1], false, false);
        const size_t off = (((size_t)(4 * mt + tl) * 54 + (ct >> 1)) * 64 + i + 16 * (2 * (ct & 1) + (g >> 1))) * 16;
        st16(rs_, WS_H1F + off, (u32x4){s0[0], s1[0], s0[1], s1[1]});
      }
    }
    SB();
    __builtin_amdgcn_s_barrier();
    SB();
    constexpr int E0 = W2_INL - NW2L;
    auto signal = [&]() __attribute__((always_inline)) {
      if (lane == 0) {
        if (__hip_atomic_fetch_add(lcnt, 1u, __ATOMIC_RELAXED, __HIP_MEMORY_SCOPE_WORKGROUP) == 7u)
          __hip_atomic_store(flags + 16 * ng, 1u, __ATOMIC_RELAXED, __HIP_MEMORY_SCOPE_AGENT);
      }
    };
    if (w >= SIG_SPLIT) { asm volatile("s_waitcnt vmcnt(0)" ::: "memory"); signal(); }
    SB();
#pragma unroll
    for (int e = E0; e < NW2R; ++e) ring[(8 + e) % 32] = ld16(rs_, lo16, w2_off(e));
    SB();
    if (w < SIG_SPLIT) { asm volatile("s_waitcnt vmcnt(%0)" :: "n"(NW2R - E0) : "memory"); signal(); }
    SB();
  };
  if (two) gemm1(std::true_type{});
  else gemm1(std::false_type{});
#pragma nounroll
  for (unsigned spin = 0; spin < (1u << 20); ++spin) {
    if (__hip_atomic_load(flags + 16 * w, __ATOMIC_RELAXED, __HIP_MEMORY_SCOPE_AGENT) != 0u) break;
    __builtin_amdgcn_s_sleep(1);
  }
  asm volatile("" ::: "memory");
  SB();

  const int h1s = (int)WS_H1F + (mt * 4 * 54 + ks0) * 1024;
  u32x4 hb[28];
  auto load_h1 = [&](int n) __attribute__((always_inline)) {
    const int q = n / 7, s = n % 7;
    const int sn = (s == 6 && !full) ? 5 : s;
    hb[n] = ld16_sc1(rs_, lo16, h1s + (q * 54 + sn) * 1024);
  };
#pragma unroll
  for (int n = 0; n < H1_PRE; ++n) {
    if (n == H1_BATCH) { SB(); __builtin_amdgcn_s_barrier(); SB(); }
    load_h1(n);
  }
  SB();
  float* red = (float*)smem;
  float* rb_ = red + (w * 16 + i) * KF_RS + 4 * g;
  const int re = w * 40 + min(lane, 39);
  const int rtk = re / 20, rq4 = re % 20;
  const float* rr_ = red + rtk * KF_RS + 4 * rq4;
  f32x4 ac2[2][5];
  f32x4 rv[8], rsum;
  u32x2 xv2;
  const f32x4 gb = *(const f32x4*)(tab + 4 * rq4);
  auto red_wr = [&](int q, int c) __attribute__((always_inline)) {
    *(f32x4*)(rb_ + (q & 1) * (KF_RB / 4) + 16 * c) = ac2[q & 1][c];
  };
  auto red_rd = [&](int q, int ww) __attribute__((always_inline)) {
    rv[ww] = *(const f32x4*)(rr_ + (q & 1) * (KF_RB / 4) + ww * 16 * KF_RS);
  };
  auto red_x = [&](int q) __attribute__((always_inline)) { xv2 = *(const u32x2*)(xr + (16 * q + rtk) * 176 + rq4 * 8); };
  auto red_out = [&](int q) __attribute__((always_inline)) {
    const f32x4 s = rsum + (rv[6] + rv[7]);
    u32x2 o;
#pragma unroll
    for (int h = 0; h < 2; ++h) {
      unsigned f = pk_bf(s[2 * h], s[2 * h + 1]);
      unsigned gf = pk_bf(bf_lo(f) * gb[2 * h], bf_hi(f) * gb[2 * h + 1]);
      o[h] = pk_bf(bf_lo(xv2[h]) + bf_lo(gf), bf_hi(xv2[h]) + bf_hi(gf));
    }
    *(u32x2*)(out + (size_t)(mt * 64 + 16 * q + rtk) * D_ + ng * 80 + 4 * rq4) = o;
  };
#pragma unroll
  for (int q = 0; q < 3; ++q) {
#pragma unroll
    for (int c = 0; c < 5; ++c) ac2[q & 1][c] = (f32x4){0.f, 0.f, 0.f, 0.f};
#pragma unroll
    for (int s = 0; s < 7; ++s) {
      auto mm = [&](int c) __attribute__((always_inline)) {
        const u32x4 a = c == 4 ? wl[s] : ((s == 6 && c == 3) ? wl[7] : ring[(8 + 4 * s + c) % 32]);
        ac2[q & 1][c] = mfma_bf16(a, hb[7 * q + s], ac2[q & 1][c]);
      };
      if (s == 5 && q >= 2) {
        red_wr(q - 1, 4);
        SB();
        red_out(q - 2);
#pragma unroll
        for (int c = 0; c < 5; ++c) mm(c);
#pragma unroll
        for (int c = 0; c < 5; ++c) {
          __builtin_amdgcn_sched_group_barrier(0x008, 1, 0);
          __builtin_amdgcn_sched_group_barrier(0x002, 10, 0);
        }
        SB();
      } else if (s < 6) {
        mm(0); SB();
        if (q >= 1 && s >= 1) { red_wr(q - 1, s - 1); SB(); }
        mm(1); SB();
        if (q >= 2 && s < 4) { red_rd(q - 2, 2 * s); SB(); }
        mm(2); SB();
        if (q >= 2 && s < 4) { red_rd(q - 2, 2 * s + 1); SB(); }
        if (q >= 2 && s == 4) { red_x(q - 2); SB(); }
        mm(3); SB();
        if (q >= 2 && s >= 1 && s < 4) {
          const f32x4 t = rv[2 * s - 2] + rv[2 * s - 1];
          rsum = s == 1 ? t : rsum + t;
          asm volatile("" : "+v"(rsum));
          SB();
        }
        mm(4); SB();
      } else if (full) {
#pragma unroll
        for (int c = 0; c < 5; ++c) mm(c);
        SB();
      }
      if (7 * q + s + H1_PRE < 28) { load_h1(7 * q + s + H1_PRE); SB(); }
    }
    if (q >= 1) bar_lds();
  }
  {
    auto sum6 = [&]() __attribute__((always_inline)) {
      rsum = ((rv[0] + rv[1]) + (rv[2] + rv[3])) + (rv[4] + rv[5]);
    };
    if (full) {
#pragma unroll
      for (int c = 0; c < 5; ++c) {
        const u32x4 a = c == 4 ? wl[6] : (c == 3 ? wl[7] : ring[(8 + 24 + c) % 32]);
        rv[c] = mfma_bf16(a, hb[27], (f32x4){0.f, 0.f, 0.f, 0.f});
      }
    } else {
#pragma unroll
      for (int c = 0; c < 5; ++c) rv[c] = (f32x4){0.f, 0.f, 0.f, 0.f};
    }
    f32x4 c0[5];
#pragma unroll
    for (int c = 0; c < 5; ++c) c0[c] = rv[c];
    SB();
    auto col = [&](int c, auto&& between) __attribute__((always_inline)) {
#pragma unroll
      for (int s = 0; s < 6; ++s) {
        const u32x4 a = c == 4 ? wl[s] : ring[(8 + 4 * s + c) % 32];
        c0[c] = mfma_bf16(a, hb[21 + s], c0[c]);
        SB();
        between(s);
        SB();
      }
    };
    col(0, [&](int s) __attribute__((always_inline)) { if (s < 5) red_wr(2, s); if (s < 4) { red_rd(1, 2 * s); red_rd(1, 2 * s + 1); } if (s == 4) red_x(1); });
    col(1, [&](int s) __attribute__((always_inline)) { if (s == 1) { sum6(); asm volatile("" : "+v"(rsum)); } if (s == 3) red_out(1); });
    bar_lds();
    auto wr3 = [&](int c) __attribute__((always_inline)) { *(f32x4*)(rb_ + (KF_RB / 4) + 16 * c) = c0[c]; };
    col(2, [&](int s) __attribute__((always_inline)) { if (s < 2) wr3(s); if (s >= 1 && s < 5) { red_rd(2, 2 * s - 2); red_rd(2, 2 * s - 1); } if (s == 5) red_x(2); });
    col(3, [&](int s) __attribute__((always_inline)) { if (s == 1) wr3(2); if (s == 2) { sum6(); asm volatile("" : "+v"(rsum)); } if (s == 4) red_out(2); });
    col(4, [&](int s) __attribute__((always_inline)) { if (s == 1) wr3(3); if (s == 2) red_x(3); });
    wr3(4);
    bar_lds();
#pragma unroll
    for (int ww = 0; ww < 8; ++ww) red_rd(3, ww);
    sum6();
    red_out(3);
  }
}

__global__ NOPK __launch_bounds__(512, 2) void kf(char* __restrict__ ws, unsigned short* __restrict__ out) {
  __shared__ __attribute__((aligned(16))) char smem[KF_LDS];
  kf_body(ws, out, smem);
}

__global__ void kend_() {}

extern "C" void kernel_launch(void* const* d_in, const int* in_sizes, int n_in,
                              void* d_out, int out_size, void* d_ws, size_t ws_size,
                              hipStream_t stream) {
  K1Args a;
  a.x = (const unsigned short*)d_in[0];
  a.are = (const unsigned short*)d_in[1];
  a.aim = (const unsigned short*)d_in[2];
  a.bre = (const unsigned short*)d_in[3];
  a.bim = (const unsigned short*)d_in[4];
  a.cre = (const unsigned short*)d_in[5];
  a.cim = (const unsigned short*)d_in[6];
  a.logdt = (const unsigned short*)d_in[7];
  a.c = (const unsigned short*)d_in[12];
  a.wmod = (const unsigned short*)d_in[13];
  a.ws = (char*)d_ws;
  k1<<<NB_PF + NB_K + NB_LN + NB_MOD, 256, 0, stream>>>(a);
  k2<<<256, 512, 0, stream>>>((const unsigned short*)d_in[8], (char*)d_ws, (const unsigned short*)d_in[10],
                              (const unsigned short*)d_in[11]);
  kf<<<256, 512, 0, stream>>>((char*)d_ws, (unsigned short*)d_out);
}
```

```cpp
#include <hip/hip_runtime.h>
#include <hip/hip_bf16.h>
#include <stdint.h>
#include <type_traits>

typedef __attribute__((ext_vector_type(8))) __bf16 bf16x8;
typedef __attribute__((ext_vector_type(2))) __bf16 bf16x2;
typedef __attribute__((ext_vector_type(4))) float f32x4;
typedef __attribute__((ext_vector_type(2))) float f32x2;
typedef __attribute__((ext_vector_type(4))) unsigned u32x4;
typedef __attribute__((ext_vector_type(2))) unsigned u32x2;
typedef __attribute__((ext_vector_type(8))) _Float16 h16x8;
typedef __attribute__((ext_vector_type(2))) _Float16 h16x2;
typedef __attribute__((ext_vector_type(4))) short s16x4;
typedef __attribute__((address_space(3))) s16x4 lds_s16x4;

#if defined(__HIP_DEVICE_COMPILE__)
#define NOPK __attribute__((target("no-packed-fp32-ops")))
#else
#define NOPK
#endif
#define DEV __device__ __forceinline__

constexpr int L_ = 2048;
constexpr int D_ = 640;
constexpr int NS = 128;
constexpr int FF = 1728;
constexpr int KREV = 2304;

constexpr size_t WS_KSC  = 8192;
constexpr size_t WS_CNT  = 16384;
constexpr size_t WS_MOD  = 65536;
constexpr size_t WS_CSP  = 196608;
constexpr size_t WS_XNT  = 327680;
constexpr size_t WS_XT   = WS_XNT + (size_t)D_ * L_ * 2;
constexpr size_t WS_KREV = WS_XT + (size_t)D_ * L_ * 2;
constexpr size_t WS_W1F  = WS_KREV + (size_t)D_ * KREV * 2;
constexpr size_t WS_W2F  = WS_W1F + (size_t)D_ * FF * 2;
constexpr size_t WS_X1T  = WS_W2F + (size_t)D_ * FF * 2;
constexpr size_t WS_H1F  = WS_X1T + (size_t)D_ * L_ * 2;
constexpr size_t WS_END  = WS_H1F + (size_t)L_ * FF * 2;

#define TS(k, wsp) do { if (blockIdx.x == 0 && threadIdx.x == 0) __hip_atomic_store((unsigned long long*)((wsp) + (200u << 20)) + (k), (unsigned long long)__builtin_amdgcn_s_memrealtime(), __ATOMIC_RELAXED, __HIP_MEMORY_SCOPE_AGENT); } while (0)
DEV float bf_lo(unsigned u) { return __uint_as_float(u << 16); }
DEV float bf_hi(unsigned u) { return __uint_as_float(u & 0xffff0000u); }
DEV unsigned pk_bf(float a, float b) {
  f32x2 v = {a, b};
  bf16x2 r = __builtin_convertvector(v, bf16x2);
  return __builtin_bit_cast(unsigned, r);
}
DEV float rbf(float a) { return bf_lo(pk_bf(a, 0.f)); }
#define NT_MOD 0
#define NT_X 0
#define NT_W 0
template <int NT> DEV u32x4 ldv(const void* p) { return NT ? __builtin_nontemporal_load((const u32x4*)p) : *(const u32x4*)p; }
DEV float ldbf(const unsigned short* p) { return __uint_as_float(((unsigned)*p) << 16); }

typedef __amdgpu_buffer_rsrc_t rsrc_t;
DEV rsrc_t ws_rsrc(char* ws) { return __builtin_amdgcn_make_buffer_rsrc((void*)ws, 0, (int)WS_END, 0x00020000); }
DEV void st16(rsrc_t r, size_t off, u32x4 v) { __builtin_amdgcn_raw_buffer_store_b128(v, r, (int)off, 0, 16); }
DEV void st4(char* ws, size_t off, unsigned v) { __hip_atomic_store((unsigned*)(ws + off), v, __ATOMIC_RELAXED, __HIP_MEMORY_SCOPE_AGENT); }
DEV void st8(rsrc_t r, size_t off, u32x2 v) { __builtin_amdgcn_raw_buffer_store_b64(v, r, (int)off, 0, 16); }

DEV void bar_lds() { asm volatile("s_waitcnt lgkmcnt(0)\n\ts_barrier" ::: "memory"); }
DEV void lds_wait(const void* word, unsigned val) {
  unsigned tv, ts, cnt;
  asm volatile("s_mov_b32 %2, 0x100000\n"
               "1:\n\tds_read_b32 %0, %3\n\ts_waitcnt lgkmcnt(0)\n\tv_readfirstlane_b32 %1, %0\n\t"
               "s_cmp_ge_u32 %1, %4\n\ts_cbranch_scc1 2f\n\ts_sleep 1\n\ts_sub_u32 %2, %2, 1\n\t"
               "s_cmp_lg_u32 %2, 0\n\ts_cbranch_scc1 1b\n2:"
               : "=&v"(tv), "=&s"(ts), "=&s"(cnt) : "v"((unsigned)(size_t)word), "s"(val) : "memory", "scc");
}
DEV void lds_post(unsigned* word, int lane) {
  asm volatile("" ::: "memory");
  if (lane == 0) __hip_atomic_fetch_add(word, 1u, __ATOMIC_RELAXED, __HIP_MEMORY_SCOPE_WORKGROUP);
  asm volatile("" ::: "memory");
}
DEV void lgkm0() { asm volatile("s_waitcnt lgkmcnt(0)" ::: "memory"); __builtin_amdgcn_sched_barrier(0); }

DEV f32x4 mfma_bf16(u32x4 a, u32x4 b, f32x4 c) {
  return __builtin_amdgcn_mfma_f32_16x16x32_bf16(__builtin_bit_cast(bf16x8, a), __builtin_bit_cast(bf16x8, b), c, 0, 0, 0);
}

constexpr int NB_PF = 8, NB_K = 640, NB_LN = 256, NB_MOD = 120;

struct K1Args {
  const unsigned short *x, *are, *aim, *bre, *bim, *cre, *cim, *logdt, *c, *wmod;
  char* ws;
};

DEV void k1_prefetch() {
  const int tid = threadIdx.x;
  unsigned long long pc;
  int o0, o1;
  asm volatile("s_getpc_b64 %0\n.Lpf_here%=:\n\ts_mov_b32 %1, _Z2k16K1Args - .Lpf_here%=\n\ts_mov_b32 %2, _Z5kend_v - .Lpf_here%="
               : "=s"(pc), "=s"(o0), "=s"(o1));
  const char* c0 = (const char*)(pc + (long long)o0);
  long n = o1 - o0;
  n = n < 0 ? 0 : (n > 98304 ? 98304 : n);
  unsigned v[3];
#pragma unroll
  for (int it = 0; it < 3; ++it) {
    const long o = (long)tid * 128 + it * 32768;
    v[it] = o < n ? *(const __attribute__((address_space(1))) unsigned*)(c0 + o) : 0u;
  }
  asm volatile("" :: "v"(v[0] ^ v[1] ^ v[2]));
}

DEV void k1_mod(const K1Args& p, int b, char* smem) {
  const int tid = threadIdx.x;
  const int rg = b & 7, cgp = b >> 3;
  const int c32 = tid & 31, r8 = tid >> 5;
  float* sil = (float*)smem;
  float* red = (float*)(smem + 512);
  u32x4 w[10];
#pragma unroll
  for (int it = 0; it < 10; ++it)
    w[it] = ldv<NT_MOD>(p.wmod + (size_t)(rg * 80 + r8 + 8 * it) * 3840 + 256 * cgp + 8 * c32);
  if (tid < 80) {
    float cv = ldbf(p.c + rg * 80 + tid);
    sil[tid] = rbf(cv / (1.f + expf(-cv)));
  }
  if (tid >= 128 && tid < 132 && b < 64) st4(p.ws, WS_CNT + (b * 4 + tid - 128) * 64, 0u);
  __syncthreads();
  float acc[8];
#pragma unroll
  for (int j = 0; j < 8; ++j) acc[j] = 0.f;
#pragma unroll
  for (int it = 0; it < 10; ++it) {
    float s = sil[r8 + 8 * it];
#pragma unroll
    for (int q = 0; q < 4; ++q) {
      acc[2 * q] += s * bf_lo(w[it][q]);
      acc[2 * q + 1] += s * bf_hi(w[it][q]);
    }
  }
#pragma unroll
  for (int j = 0; j < 8; ++j) acc[j] += __shfl_xor(acc[j], 32, 64);
  if ((tid & 63) < 32) {
    *(f32x4*)(red + (tid >> 6) * 256 + 8 * c32) = (f32x4){acc[0], acc[1], acc[2], acc[3]};
    *(f32x4*)(red + (tid >> 6) * 256 + 8 * c32 + 4) = (f32x4){acc[4], acc[5], acc[6], acc[7]};
  }
  __syncthreads();
  st4(p.ws, WS_MOD + ((256 * cgp + tid) * 8 + rg) * 4,
      __float_as_uint((red[tid] + red[256 + tid]) + (red[512 + tid] + red[768 + tid])));
}
struct ModP { f32x4 a, b; };
DEV ModP mod_ld(const char* ws, int idx) {
  const f32x4* mp = (const f32x4*)(ws + WS_MOD) + 2 * idx;
  ModP m; m.a = mp[0]; m.b = mp[1];
  return m;
}
DEV float mod_fin(const ModP& m) {
  return rbf(((m.a[0] + m.a[1]) + (m.a[2] + m.a[3])) + ((m.b[0] + m.b[1]) + (m.b[2] + m.b[3])));
}
#define SB() __builtin_amdgcn_sched_barrier(0)

DEV void k1_ln(const K1Args& p, int b, char* smem) {
  constexpr int STR = 704;
  const int tid = threadIdx.x;
  const int tok = tid >> 3, sub = tid & 7;
  const int tile = (b & 7) * 8 + ((b >> 3) & 7), pass = b >> 6;
  const int hf = pass & 1;
  const bool norm = pass < 2;
  const int t0 = tile * 32;
  const unsigned short* xr = p.x + (size_t)(t0 + tok) * D_ + 8 * sub;
  u32x4 xv[5], xo[5];
#pragma unroll
  for (int m = 0; m < 5; ++m) xv[m] = ldv<NT_X>(xr + 320 * hf + 64 * m);
  if (norm) {
#pragma unroll
    for (int m = 0; m < 5; ++m) xo[m] = ldv<NT_X>(xr + 320 * (hf ^ 1) + 64 * m);
    float s = 0.f, v = 0.f;
    const bf16x2 one2 = __builtin_bit_cast(bf16x2, 0x3f803f80u);
#pragma unroll
    for (int m = 0; m < 10; ++m)
#pragma unroll
      for (int q = 0; q < 4; ++q) {
        const unsigned u = m < 5 ? xv[m][q] : xo[m - 5][q];
        const bf16x2 x2 = __builtin_bit_cast(bf16x2, u);
        s = __builtin_amdgcn_fdot2_f32_bf16(x2, one2, s, false);
        v = __builtin_amdgcn_fdot2_f32_bf16(x2, x2, v, false);
      }
#pragma unroll
    for (int o = 1; o < 8; o <<= 1) { s += __shfl_xor(s, o, 64); v += __shfl_xor(v, o, 64); }
    const float mean = s * (1.f / D_);
    const float var = fmaxf(v * (1.f / D_) - mean * mean, 0.f);
    const float sd = rbf(sqrtf(rbf(rbf(var) + 1e-5f)));
    const float rs = 1.f / sd;
    const float mb = rbf(mean);
#pragma unroll
    for (int m = 0; m < 5; ++m)
#pragma unroll
      for (int q = 0; q < 4; ++q) {
        unsigned d2 = pk_bf(bf_lo(xv[m][q]) - mb, bf_hi(xv[m][q]) - mb);
        xv[m][q] = pk_bf(bf_lo(d2) * rs, bf_hi(d2) * rs);
      }
  }
  const int lane = tid & 63, w = tid >> 6;
  const int g = lane >> 4, i = lane & 15;
  const rsrc_t rsrc = ws_rsrc(p.ws);
#pragma unroll
  for (int m = 0; m < 5; ++m) *(u32x4*)(smem + tok * STR + (sub + 8 * m) * 16) = xv[m];
  __syncthreads();
  const size_t dsto = (norm ? WS_XNT : WS_XT) + ((size_t)tile * D_ + 320 * hf) * 64;
#pragma unroll
  for (int it = 0; it < 5; ++it) {
    const int cb = w * 5 + it;
    const char* a = smem + (8 * g + (i >> 2)) * STR + cb * 32 + (i & 3) * 8;
    const u32x2 ra = __builtin_bit_cast(u32x2, __builtin_amdgcn_ds_read_tr16_b64_v4i16((lds_s16x4*)a));
    const u32x2 rb = __builtin_bit_cast(u32x2, __builtin_amdgcn_ds_read_tr16_b64_v4i16((lds_s16x4*)(a + 4 * STR)));
    st16(rsrc, dsto + (size_t)(cb * 16 + i) * 64 + 16 * g, (u32x4){ra[0], ra[1], rb[0], rb[1]});
  }
}

constexpr int RT_STR = 176;
template <int NF> struct RtRaw { u32x4 r[NF]; };
template <int NF>
DEV void rt_load(RtRaw<NF>& t, const unsigned short* W, int N, int kk, int ct0, int lane) {
#pragma unroll
  for (int j = 0; j < NF; ++j) {
    const int c = lane + 64 * j, row = c / (2 * NF), part = c % (2 * NF);
    t.r[j] = ldv<NT_W>(W + (size_t)(32 * kk + row) * N + 16 * ct0 + 8 * part);
  }
}
template <int NF>
DEV void rt_park(const RtRaw<NF>& t, char* scr, int lane) {
#pragma unroll
  for (int j = 0; j < NF; ++j) {
    const int c = lane + 64 * j, row = c / (2 * NF), part = c % (2 * NF);
    *(u32x4*)(scr + row * RT_STR + 16 * part) = t.r[j];
  }
}
DEV u32x4 rt_frag(const char* scr, int j, int lane) {
  const int g = lane >> 4, i = lane & 15;
  const char* p = scr + (8 * g + (i >> 2)) * RT_STR + 32 * j + (i & 3) * 8;
  const u32x2 a = __builtin_bit_cast(u32x2, __builtin_amdgcn_ds_read_tr16_b64_v4i16((lds_s16x4*)p));
  const u32x2 b = __builtin_bit_cast(u32x2, __builtin_amdgcn_ds_read_tr16_b64_v4i16((lds_s16x4*)(p + 4 * RT_STR)));
  return (u32x4){a[0], a[1], b[0], b[1]};
}
struct cf { float re, im; };
DEV cf cmul(cf a, cf b) {
  cf r;
  r.re = a.re * b.re - a.im * b.im;
  r.im = a.re * b.im + a.im * b.re;
  return r;
}
DEV cf cexp_(float a2, float br, float m, float m0) {
  float mag = __builtin_amdgcn_exp2f(a2 * (m - m0));
  float ph = __builtin_amdgcn_fractf(br * m);
  cf r; r.re = mag * __builtin_amdgcn_cosf(ph); r.im = mag * __builtin_amdgcn_sinf(ph);
  return r;
}
DEV unsigned pk_h(float a, float b) {
  f32x2 v = {a, b};
  h16x2 r = __builtin_convertvector(v, h16x2);
  return __builtin_bit_cast(unsigned, r);
}
DEV f32x4 mfma_f16(u32x4 a, u32x4 b, f32x4 c) {
  return __builtin_amdgcn_mfma_f32_16x16x32_f16(__builtin_bit_cast(h16x8, a), __builtin_bit_cast(h16x8, b), c, 0, 0, 0);
}
DEV void put2(char* p, cf a, cf b) {
  u32x2 o = {pk_h(a.re, a.im), pk_h(b.re, b.im)};
  *(u32x2*)p = o;
}

constexpr int KG_RS = 544;
DEV void k1_kgen(const K1Args& p, int d, char* smem) {
  constexpr int RS = KG_RS;
  const int tid = threadIdx.x;
  const int lane = tid & 63;
  const int w = __builtin_amdgcn_readfirstlane(tid >> 6);
  const int g = lane >> 4, i = lane & 15;
  const size_t po = (size_t)d * NS + 2 * lane;
  const unsigned rar = *(const unsigned*)(p.are + po), rai = *(const unsigned*)(p.aim + po);
  const unsigned rbr = *(const unsigned*)(p.bre + po), rbi = *(const unsigned*)(p.bim + po);
  const unsigned rcr = *(const unsigned*)(p.cre + po), rci = *(const unsigned*)(p.cim + po);
  const float ldt = ldbf(p.logdt + d);
  asm volatile("" ::: "memory");
  SB();
  const float dt = rbf(expf(ldt));
  float a2[2], bv[2], m0[2];
  a2[0] = (dt * bf_lo(rar)) * 1.44269504088896341f; a2[1] = (dt * bf_hi(rar)) * 1.44269504088896341f;
  bv[0] = (dt * bf_lo(rai)) * 0.15915494309189534f; bv[1] = (dt * bf_hi(rai)) * 0.15915494309189534f;
  m0[0] = a2[0] > 0.f ? 2048.f : 0.f; m0[1] = a2[1] > 0.f ? 2048.f : 0.f;
  char* my = smem + lane * 8;
  if (w == 0) {
    cf v[2], v8[2], e8[2];
    float bound = 0.f;
#pragma unroll
    for (int q = 0; q < 2; ++q) {
      const float dbr = dt * (q ? bf_hi(rbr) : bf_lo(rbr)), dbi = dt * (q ? bf_hi(rbi) : bf_lo(rbi));
      const float cr = q ? bf_hi(rcr) : bf_lo(rcr), ci = q ? bf_hi(rci) : bf_lo(rci);
      const float gsh = __builtin_amdgcn_exp2f(a2[q] * m0[q]);
      v[q].re = (cr * dbr - ci * dbi) * gsh; v[q].im = -(cr * dbi + ci * dbr) * gsh;
      bound += (fabsf(v[q].re) + fabsf(v[q].im)) * __builtin_amdgcn_exp2f(fmaxf(a2[q], 0.f) * 120.f);
      e8[q] = cexp_(a2[q], -bv[q], 8.f, 0.f);
      v8[q] = cexp_(a2[q], -bv[q], 64.f, 0.f);
    }
#pragma unroll
    for (int o = 1; o < 64; o <<= 1) bound += __shfl_xor(bound, o, 64);
    int ex = (int)((__float_as_uint(bound) >> 23) & 0xff);
    ex = ex < 20 ? 20 : (ex > 240 ? 240 : ex);
    const float sv = __uint_as_float((unsigned)(127 + 13 + 127 - ex) << 23);
    if (lane == 0) st4(p.ws, WS_KSC + 4 * d, (unsigned)(ex - 13) << 23);
#pragma unroll
    for (int q = 0; q < 2; ++q) { v[q].re *= sv; v[q].im *= sv; v8[q] = cmul(v[q], v8[q]); }
#pragma unroll
    for (int j = 0; j < 8; ++j) {
      put2(my + (41 + j) * RS, v[0], v[1]);
      put2(my + (49 + j) * RS, v8[0], v8[1]);
      if (j < 7) {
#pragma unroll
        for (int q = 0; q < 2; ++q) { v[q] = cmul(v[q], e8[q]); v8[q] = cmul(v8[q], e8[q]); }
      }
    }
  } else if (w == 1) {
    cf e1[2], u[2], u2[2];
#pragma unroll
    for (int q = 0; q < 2; ++q) {
      e1[q] = cexp_(a2[q], bv[q], 1.f, 0.f);
      u[q].re = __builtin_amdgcn_exp2f(-a2[q] * m0[q]); u[q].im = 0.f;
      u2[q] = cexp_(a2[q], bv[q], 128.f, m0[q]);
    }
#pragma unroll
    for (int r = 0; r < 8; ++r) {
      put2(my + r * RS, u[0], u[1]);
      put2(my + (8 + r) * RS, u2[0], u2[1]);
      if (r < 7) {
#pragma unroll
        for (int q = 0; q < 2; ++q) { u[q] = cmul(u[q], e1[q]); u2[q] = cmul(u2[q], e1[q]); }
      }
    }
  } else if (w == 2) {
    cf e2[2], u[2], u2[2];
#pragma unroll
    for (int q = 0; q < 2; ++q) {
      e2[q] = cexp_(a2[q], bv[q], 2.f, 0.f);
      u[q] = cexp_(a2[q], bv[q], 256.f, m0[q]);
      u2[q] = cexp_(a2[q], bv[q], 384.f, m0[q]);
    }
#pragma unroll
    for (int r = 0; r < 4; ++r) {
      put2(my + (16 + r) * RS, u[0], u[1]);
      put2(my + (20 + r) * RS, u2[0], u2[1]);
      if (r < 3) {
#pragma unroll
        for (int q = 0; q < 2; ++q) { u[q] = cmul(u[q], e2[q]); u2[q] = cmul(u2[q], e2[q]); }
      }
    }
    cf e4[2] = {cmul(e2[0], e2[0]), cmul(e2[1], e2[1])};
#pragma unroll
    for (int r = 0; r < 2; ++r) {
      cf c0 = cexp_(a2[0], bv[0], 512.f + 128.f * r, m0[0]), c1 = cexp_(a2[1], bv[1], 512.f + 128.f * r, m0[1]);
      put2(my + (24 + 2 * r) * RS, c0, c1);
      put2(my + (25 + 2 * r) * RS, cmul(c0, e4[0]), cmul(c1, e4[1]));
    }
  } else {
    cf e4[2], e128[2], u[2], u2[2];
#pragma unroll
    for (int q = 0; q < 2; ++q) {
      e4[q] = cexp_(a2[q], bv[q], 4.f, 0.f);
      e128[q] = cexp_(a2[q], bv[q], 128.f, 0.f);
      u[q] = cexp_(a2[q], bv[q], 1024.f, m0[q]);
      u2[q] = cexp_(a2[q], bv[q], 1536.f, m0[q]);
    }
#pragma unroll
    for (int r = 2; r < 4; ++r) {
      cf c0 = cexp_(a2[0], bv[0], 512.f + 128.f * r, m0[0]), c1 = cexp_(a2[1], bv[1], 512.f + 128.f * r, m0[1]);
      put2(my + (24 + 2 * r) * RS, c0, c1);
      put2(my + (25 + 2 * r) * RS, cmul(c0, e4[0]), cmul(c1, e4[1]));
    }
#pragma unroll
    for (int r = 0; r < 4; ++r) {
      put2(my + (32 + r) * RS, u[0], u[1]);
      put2(my + (36 + r) * RS, u2[0], u2[1]);
#pragma unroll
      for (int q = 0; q < 2; ++q) { if (r < 3) u[q] = cmul(u[q], e128[q]); u2[q] = cmul(u2[q], e128[q]); }
    }
    put2(my + 40 * RS, u2[0], u2[1]);
  }
  __syncthreads();
  _Float16* kh = (_Float16*)(smem + 57 * RS);
  if (w < 3) {
    const char* ua = smem + (16 * w + i) * RS + g * 16;
    const char* va = smem + (41 + i) * RS + g * 16;
    f32x4 acc = {0.f, 0.f, 0.f, 0.f};
#pragma unroll
    for (int ks = 0; ks < 8; ++ks)
      acc = mfma_f16(*(const u32x4*)(ua + ks * 64), *(const u32x4*)(va + ks * 64), acc);
    int idx0, st;
    if (w == 0) { idx0 = (g < 2 ? 4 * g : 120 + 4 * g) + 8 * i; st = 1; }
    else if (w == 1) { idx0 = g < 2 ? 256 + 64 * g + 4 * i : 384 + 64 * (g - 2) + 2 * i; st = 1; }
    else { idx0 = 512 + 64 * g + i; st = 16; }
#pragma unroll
    for (int rg = 0; rg < 4; ++rg) {
      int idx = idx0 + st * rg;
      if (w == 1 && g >= 2) idx = idx0 + 32 * (rg >> 1) + (rg & 1);
      if (w == 2 && g >= 2) idx = (g == 2 && rg == 0 && i == 0) ? 640 : 644;
      kh[idx] = (_Float16)acc[rg];
    }
  }
  __syncthreads();
  const unsigned short* ku = (const unsigned short*)kh;
  const size_t kout = WS_KREV + (size_t)d * KREV * 2;
  {
    const int l0 = 2040 - 8 * tid;
    u32x4 o;
    if (w < 2) {
      const int q = l0 >> 3;
      const unsigned a = ku[384 + q], b = ku[385 + q];
      const unsigned t = (q & 1) ? b : a;
      o[0] = b | (b << 16); o[1] = b | (t << 16); o[2] = a | (a << 16); o[3] = o[2];
    } else if (w == 2) {
      const int q = l0 >> 2;
      const unsigned a = ku[256 + q], b = ku[257 + q], c = ku[258 + q];
      o[0] = c | (c << 16); o[1] = b | (b << 16); o[2] = b | (a << 16); o[3] = a | (a << 16);
    } else {
      unsigned v[8];
#pragma unroll
      for (int e = 0; e < 8; ++e) {
        const int l = l0 + 7 - e;
        const int idx = l < 256 ? l : (int)(pk_bf((float)l, 0.f) & 0xffffu) - 17024;
        v[e] = ku[idx];
      }
#pragma unroll
      for (int q = 0; q < 4; ++q) o[q] = v[2 * q] | (v[2 * q + 1] << 16);
    }
    st16(ws_rsrc(p.ws), kout + 16 * tid, o);
  }
}

__global__ NOPK __launch_bounds__(256) void k1(K1Args p) {
  __shared__ __attribute__((aligned(16))) char smem[57 * KG_RS + 1312];
  int b = blockIdx.x;
  TS(0, p.ws);
  if (b < NB_PF) { k1_prefetch(); return; }
  b -= NB_PF;
  if (b < NB_K) { k1_kgen(p, b, smem); return; }
  b -= NB_K;
  asm volatile("" :: "s"(p.ws), "s"(p.x));
  __builtin_amdgcn_s_sleep(4);
  if (b < NB_LN) k1_ln(p, b, smem);
  else k1_mod(p, b - NB_LN, smem);
}

constexpr int K2_CS = 4160;
constexpr int K2_KOFF = 4 * K2_CS;
constexpr int K2_CH = K2_KOFF + 4608;
constexpr int CV_RED = 3 * K2_CH;
constexpr int CV_RT2 = 86016;
constexpr int CV_SUM = CV_RT2 + 4 * (32 * 176 + 256);
constexpr int CV_XCH = CV_SUM + 2560;
constexpr int CV_FL = CV_RED + 20480;

struct Chan { float sa, ca1, ga, dsk, hs, hinv, yinv; };
struct ChanRaw { ModP msa, mca, mga; float kinv; unsigned short rds; };
DEV ChanRaw chan_ld(const unsigned short* dskip, const char* ws, int d) {
  ChanRaw c;
  c.rds = dskip[d];
  c.msa = mod_ld(ws, d); c.mca = mod_ld(ws, D_ + d); c.mga = mod_ld(ws, 2 * D_ + d);
  c.kinv = ((const float*)(ws + WS_KSC))[d];
  return c;
}
DEV Chan chan_fin(const ChanRaw& r) {
  Chan c;
  c.sa = mod_fin(r.msa); c.ca1 = rbf(1.f + mod_fin(r.mca)); c.ga = mod_fin(r.mga);
  c.dsk = __uint_as_float((unsigned)r.rds << 16);
  const float hb = 26.f * fabsf(c.ca1) + fabsf(c.sa);
  int ex = (int)((__float_as_uint(hb) >> 23) & 0xff);
  ex = ex < 20 ? 20 : (ex > 240 ? 240 : ex);
  c.hs = __uint_as_float((unsigned)(127 + 14 + 127 - ex) << 23);
  c.hinv = __uint_as_float((unsigned)(ex - 14) << 23);
  c.yinv = r.kinv * c.hinv;
  return c;
}
DEV size_t tl_off(int d, int t) { return (((size_t)(t >> 5) * D_ + d) * 32 + (t & 31)) * 2; }
struct StageRaw { u32x4 xa0, xa1, kv0, kv1; u32x2 xb; };
DEV StageRaw stage_ld(const char* ws, int d, int th) {
  StageRaw s;
  const unsigned short* krev = (const unsigned short*)(ws + WS_KREV) + (size_t)d * KREV;
  const size_t xo = tl_off(d, 16 * th);
  s.xa0 = *(const u32x4*)(ws + WS_XNT + xo); s.xa1 = *(const u32x4*)(ws + WS_XNT + xo + 16);
  s.xb = *(const u32x2*)(ws + WS_XNT + tl_off(d, min(16 * th + 16, L_ - 4)));
  s.kv0 = *(const u32x4*)(krev + 8 * th); s.kv1 = *(const u32x4*)(krev + 1024 + 8 * th);
  return s;
}
DEV void stage_wr(const StageRaw& s, const Chan& c, char* smem, int th) {
  constexpr int CS = K2_CS, KOFF = K2_KOFF;
  unsigned hh[10];
#pragma unroll
  for (int q = 0; q < 10; ++q) {
    unsigned u = q < 4 ? s.xa0[q] : (q < 8 ? s.xa1[q - 4] : s.xb[q - 8]);
    unsigned m = pk_bf(bf_lo(u) * c.ca1, bf_hi(u) * c.ca1);
    unsigned h = pk_bf(bf_lo(m) + c.sa, bf_hi(m) + c.sa);
    hh[q] = pk_h(bf_lo(h) * c.hs, bf_hi(h) * c.hs);
  }
  char* dst = smem + (2 * th + 2) * 16;
  unsigned s1[9];
#pragma unroll
  for (int q = 0; q < 9; ++q) s1[q] = __builtin_amdgcn_alignbit(hh[q + 1], hh[q], 16);
#pragma unroll
  for (int e = 0; e < 2; ++e) {
    *(u32x4*)(dst + 16 * e) = (u32x4){hh[4 * e], hh[4 * e + 1], hh[4 * e + 2], hh[4 * e + 3]};
    *(u32x4*)(dst + 16 * e + CS) = (u32x4){s1[4 * e], s1[4 * e + 1], s1[4 * e + 2], s1[4 * e + 3]};
    *(u32x4*)(dst + 16 * e + 2 * CS) = (u32x4){hh[4 * e + 1], hh[4 * e + 2], hh[4 * e + 3], hh[4 * e + 4]};
    *(u32x4*)(dst + 16 * e + 3 * CS) = (u32x4){s1[4 * e + 1], s1[4 * e + 2], s1[4 * e + 3], s1[4 * e + 4]};
  }
  if (th == 0) {
    u32x4 z = {0u, 0u, 0u, 0u};
    *(u32x4*)(smem) = z; *(u32x4*)(smem + CS) = z; *(u32x4*)(smem + 2 * CS) = z; *(u32x4*)(smem + 3 * CS) = z;
    *(u32x4*)(smem + 16) = z;
    *(u32x4*)(smem + CS + 16) = (u32x4){0u, 0u, 0u, hh[0] << 16};
    *(u32x4*)(smem + 2 * CS + 16) = (u32x4){0u, 0u, 0u, hh[0]};
    *(u32x4*)(smem + 3 * CS + 16) = (u32x4){0u, 0u, hh[0] << 16, s1[0]};
  }
  *(u32x4*)(smem + KOFF + 16 * th) = s.kv0;
  *(u32x4*)(smem + KOFF + 2048 + 16 * th) = s.kv1;
  if (th < 32) *(u32x4*)(smem + KOFF + 4096 + 16 * th) = (u32x4){0u, 0u, 0u, 0u};
}
DEV u32x2 conv_out(const f32x4& y, const Chan& c, const char* smem, int t, const u32x2& xres) {
  const u32x2 hv = *(const u32x2*)(smem + 2 * (t + 16));
  u32x2 o;
#pragma unroll
  for (int e = 0; e < 2; ++e) {
    const unsigned hu = hv[e];
    const h16x2 h2 = __builtin_bit_cast(h16x2, hu);
    const float h0 = (float)h2[0] * c.hinv, h1 = (float)h2[1] * c.hinv;
    unsigned yb = pk_bf(y[2 * e] * c.yinv, y[2 * e + 1] * c.yinv);
    unsigned hd = pk_bf(h0 * c.dsk, h1 * c.dsk);
    unsigned y2 = pk_bf(bf_lo(yb) + bf_lo(hd), bf_hi(yb) + bf_hi(hd));
    unsigned gy = pk_bf(bf_lo(y2) * c.ga, bf_hi(y2) * c.ga);
    o[e] = pk_bf(bf_lo(xres[e]) + bf_lo(gy), bf_hi(xres[e]) + bf_hi(gy));
  }
  return o;
}
#define K2_LOAD(buf, cc, u)                                                                                     \
  asm volatile("ds_read_b64 %0, %3 offset:%5\n\tds_read_b64 %1, %3 offset:%6\n\tds_read_b128 %2, %4 offset:%7"   \
               : "=&v"(Al[buf][u]), "=&v"(Ah[buf][u]), "=&v"(Bf[buf][u])                                          \
               : "v"(ap0), "v"(bp0), "i"(64 * (cc) + 512 * (u)), "i"(64 * (cc) + 512 * (u) + 8),                 \
                 "i"(64 * (cc) + 3584 - 512 * (u)))
template <int SET> DEV constexpr int half_tile(int j) {
  return SET == 0 ? (j == 0 ? 7 : (j == 1 ? 6 : 2)) : (j < 3 ? 5 - j : 4 - j);
}
template <int SET>
DEV void conv_half(unsigned ap0, unsigned bp0, char* redw) {
  constexpr int NU = SET == 0 ? 8 : 6, NT = SET == 0 ? 3 : 5;
  u32x2 Al[2][NU], Ah[2][NU];
  u32x4 Bf[2][NU];
#pragma unroll
  for (int u = 0; u < NU; ++u) K2_LOAD(0, 0, u);
  f32x4 acc[NT];
#pragma unroll
  for (int j = 0; j < NT; ++j) acc[j] = (f32x4){0.f, 0.f, 0.f, 0.f};
#pragma unroll
  for (int cc = 0; cc < 2; ++cc) {
    lgkm0();
    if (cc == 0) {
#pragma unroll
      for (int u = 0; u < NU; ++u) K2_LOAD(1, 1, u);
    }
#pragma unroll
    for (int sp = 0; sp < NU; ++sp)
#pragma unroll
      for (int j = 0; j < NT; ++j) {
        const int q = half_tile<SET>(j);
        if (sp <= q) {
          const u32x4 A = {Al[cc][sp][0], Al[cc][sp][1], Ah[cc][sp][0], Ah[cc][sp][1]};
          acc[j] = mfma_f16(A, Bf[cc][q - sp], acc[j]);
        }
      }
    SB();
  }
#pragma unroll
  for (int j = 0; j < NT; ++j) *(f32x4*)(redw + j * 4096) = acc[j];
}

template <int NF>
DEV void w1_fin(const RtRaw<NF>& t, const ModP& mp, char* ws, int kk, int ct0, int lane, char* scr, float* sums) {
  const int g = lane >> 4, i = lane & 15;
  float* tb = (float*)(scr + 32 * RT_STR);
  const float mv = mod_fin(mp);
  tb[lane] = lane < 32 ? rbf(1.f + mv) : mv;
  rt_park<NF>(t, scr, lane);
  lgkm0();
  const f32x4 ca = *(const f32x4*)(tb + 8 * g), cb = *(const f32x4*)(tb + 8 * g + 4);
  const f32x4 sa = *(const f32x4*)(tb + 32 + 8 * g), sb = *(const f32x4*)(tb + 36 + 8 * g);
  const u32x4 sv = {pk_bf(sa[0], sa[1]), pk_bf(sa[2], sa[3]), pk_bf(sb[0], sb[1]), pk_bf(sb[2], sb[3])};
  const u32x4 ones = {0x3f803f80u, 0x3f803f80u, 0x3f803f80u, 0x3f803f80u};
  const f32x4 z = {0.f, 0.f, 0.f, 0.f};
  const rsrc_t rs = ws_rsrc(ws);
#pragma unroll
  for (int j = 0; j < NF; ++j) {
    const u32x4 f = rt_frag(scr, j, lane);
    const u32x4 sc = {pk_bf(bf_lo(f[0]) * ca[0], bf_hi(f[0]) * ca[1]), pk_bf(bf_lo(f[1]) * ca[2], bf_hi(f[1]) * ca[3]),
                      pk_bf(bf_lo(f[2]) * cb[0], bf_hi(f[2]) * cb[1]), pk_bf(bf_lo(f[3]) * cb[2], bf_hi(f[3]) * cb[3])};
    st16(rs, WS_W1F + ((size_t)(kk * 108 + ct0 + j) * 64 + lane) * 16, sc);
    const f32x4 cs = mfma_bf16(sc, ones, z), b1 = mfma_bf16(f, sv, z);
    if (i < 2) *(f32x4*)(sums + i * 80 + 16 * j + 4 * g) = i ? b1 : cs;
  }
}

DEV void conv_phase(const unsigned short* __restrict__ dskip, char* __restrict__ ws,
                    const unsigned short* __restrict__ w1, const unsigned short* __restrict__ w2, char* smem_all) {
  constexpr int CS = K2_CS, KOFF = K2_KOFF;
  const int tid = threadIdx.x, lane = tid & 63;
  const int w = __builtin_amdgcn_readfirstlane(tid >> 6);
  const int bx = blockIdx.x;
  const int pj = (bx & 7) * 16 + (bx >> 4), par = (bx >> 3) & 1;
  const int r = lane & 15, g = lane >> 4, th = tid & 127;
  const int s4 = (r + 1) & 3;
  const rsrc_t rs = ws_rsrc(ws);
  unsigned* fl = (unsigned*)(smem_all + CV_FL);
  if (tid < 6) fl[tid] = 0u;
  if (w < 4) {
    const int d = 5 * pj + 2 * par + (w >> 1), hw = w & 1;
    char* smem = smem_all + (w >> 1) * K2_CH;
    const StageRaw sr = stage_ld(ws, d, th);
    const ChanRaw cr = chan_ld(dskip, ws, d);
    u32x2 xres[4];
#pragma unroll
    for (int q = 0; q < 4; ++q)
      xres[q] = *(const u32x2*)(ws + WS_XT + tl_off(d, 256 * (4 * hw + q) + 16 * r + 4 * g));
    SB();
    const Chan c = chan_fin(cr);
    stage_wr(sr, c, smem, th);
    __syncthreads();
    f32x4 acc[8];
#pragma unroll
    for (int q = 0; q < 8; ++q) acc[q] = (f32x4){0.f, 0.f, 0.f, 0.f};
    {
      const unsigned ap0 = (unsigned)(size_t)(smem + s4 * CS + 2 * (128 * hw + 8 * g + (r + 1) - s4));
      const unsigned bp0 = (unsigned)(size_t)(smem + KOFF + 2 * (2032 + 128 * hw - 16 * r + 8 * g) - 3584);
      u32x2 Al[2][8], Ah[2][8];
      u32x4 Bf[2][8];
#pragma unroll
      for (int u = 0; u < 8; ++u) K2_LOAD(0, 0, u);
#pragma unroll
      for (int cc = 0; cc < 4; ++cc) {
        lgkm0();
#pragma unroll
        for (int q = 0; q < 8; ++q) {
          if (cc < 3) K2_LOAD((cc + 1) & 1, cc + 1, q);
#pragma unroll
          for (int sp = 0; sp <= q; ++sp) {
            const u32x4 A = {Al[cc & 1][sp][0], Al[cc & 1][sp][1], Ah[cc & 1][sp][0], Ah[cc & 1][sp][1]};
            acc[q] = mfma_f16(A, Bf[cc & 1][q - sp], acc[q]);
          }
          SB();
        }
      }
    }
    f32x4 keep[4];
    {
      f32x4* xw = (f32x4*)(smem_all + CV_XCH + w * 4096) + lane;
      if (hw) {
#pragma unroll
        for (int q = 0; q < 4; ++q) { xw[64 * q] = acc[q]; keep[q] = acc[4 + q]; }
      } else {
#pragma unroll
        for (int q = 0; q < 4; ++q) { xw[64 * q] = acc[4 + q]; keep[q] = acc[q]; }
      }
    }
    lds_post(fl + w, lane);
    lds_wait(fl + (w ^ 1), 1u);
    const f32x4* xrd = (const f32x4*)(smem_all + CV_XCH + (w ^ 1) * 4096) + lane;
#pragma unroll
    for (int qp = 0; qp < 2; ++qp) {
      u32x2 o[2];
#pragma unroll
      for (int h = 0; h < 2; ++h) {
        const int q = 2 * qp + h;
        o[h] = conv_out(keep[q] + xrd[64 * q], c, smem, 256 * (4 * hw + q) + 16 * r + 4 * g, xres[q]);
      }
      const auto s0 = __builtin_amdgcn_permlane16_swap(o[0][0], o[1][0], false, false);
      const auto s1 = __builtin_amdgcn_permlane16_swap(o[0][1], o[1][1], false, false);
      const int t = 256 * (4 * hw + 2 * qp + (g & 1)) + 16 * r + 8 * (g >> 1);
      st16(rs, WS_X1T + (((size_t)(t >> 6) * D_ + d) * 64 + (t & 63)) * 2, (u32x4){s0[0], s1[0], s0[1], s1[1]});
    }
  } else {
    __builtin_amdgcn_s_setprio(2);
    const int d = 5 * pj + 4, wq = w - 4;
    char* smem = smem_all + 2 * K2_CH;
    StageRaw sr;
    if (wq < 2) sr = stage_ld(ws, d, th);
    asm volatile("" ::: "memory");
    const ChanRaw cr = chan_ld(dskip, ws, d);
    const int tl0 = par ? (wq < 3 ? 5 - wq : 1) : (wq == 0 ? 7 : (wq == 1 ? 6 : 2)), tl1 = 0;
    const unsigned ap0 = (unsigned)(size_t)(smem + s4 * CS + 2 * (64 * wq + 8 * g + (r + 1) - s4));
    const unsigned bp0 = (unsigned)(size_t)(smem + KOFF + 2 * (2032 + 64 * wq - 16 * r + 8 * g) - 3584);
    char* redw = smem_all + CV_RED + wq * 1024 + lane * 16;
    u32x2 xres[2];
    xres[0] = *(const u32x2*)(ws + WS_XT + tl_off(d, 256 * tl0 + 16 * r + 4 * g));
    xres[1] = *(const u32x2*)(ws + WS_XT + tl_off(d, 256 * tl1 + 16 * r + 4 * g));
    SB();
    const Chan c = chan_fin(cr);
    if (wq < 2) {
      stage_wr(sr, c, smem, th);
    } else {
      __builtin_amdgcn_s_sleep(32);
    }
    const bool isw1 = bx < 110;
    const int b2 = min(max(bx - 110, 0), 107);
    const int jk = isw1 ? 4 * (bx / 22) + wq : b2 >> 1;
    const int jc = isw1 ? min(5 * (bx % 22), 103) : (b2 & 1) * 20 + 5 * wq;
    RtRaw<5> rt;
    rt_load<5>(rt, isw1 ? w1 : w2, isw1 ? FF : D_, jk, jc, lane);
    const ModP mp = mod_ld(ws, (lane < 32 ? 4 * D_ : 3 * D_ - 32) + 32 * min(jk, 19) + lane);
    SB();
    bar_lds();
    if (par) conv_half<1>(ap0, bp0, redw); else conv_half<0>(ap0, bp0, redw);
    lds_post(fl + 4, lane);
    char* scr = smem_all + CV_RT2 + wq * (32 * RT_STR + 256);
    if (isw1) {
      float* sums = (float*)(smem_all + CV_SUM);
      w1_fin<5>(rt, mp, ws, jk, jc, lane, scr, sums + 160 * wq);
      asm volatile("" ::: "memory");
      unsigned old = 0u;
      if (lane == 0) old = __hip_atomic_fetch_add(fl + 5, 1u, __ATOMIC_RELAXED, __HIP_MEMORY_SCOPE_WORKGROUP);
      asm volatile("" ::: "memory");
      if (__builtin_amdgcn_readfirstlane(old) == 3u && lane < 40) {
        const f32x4* sp = (const f32x4*)sums + lane;
        const f32x4 v = (sp[0] + sp[40]) + (sp[80] + sp[120]);
        st16(rs, WS_CSP + ((size_t)(2 * (bx / 22) + lane / 20) * FF + 16 * jc + 4 * (lane % 20)) * 4, __builtin_bit_cast(u32x4, v));
      }
    } else if (bx < 218) {
      rt_park<5>(rt, scr, lane);
      lgkm0();
#pragma unroll
      for (int j = 0; j < 5; ++j) st16(rs, WS_W2F + ((size_t)((jc + j) * 54 + jk) * 64 + lane) * 16, rt_frag(scr, j, lane));
    }
    lds_wait(fl + 4, 4u);
    const char* rd = smem_all + CV_RED + lane * 16;
#pragma unroll
    for (int u = 0; u < 2; ++u) {
      const bool on = u == 0 ? (par || wq < 3) : (par && wq == 0);
      if (on) {
        const int sl = u == 0 ? wq : 4, tl = u == 0 ? tl0 : tl1;
        const f32x4 y = (*(const f32x4*)(rd + sl * 4096) + *(const f32x4*)(rd + sl * 4096 + 1024))
                      + (*(const f32x4*)(rd + sl * 4096 + 2048) + *(const f32x4*)(rd + sl * 4096 + 3072));
        const int t = 256 * tl + 16 * r + 4 * g;
        st8(rs, WS_X1T + (((size_t)(t >> 6) * D_ + d) * 64 + (t & 63)) * 2, conv_out(y, c, smem, t, xres[u]));
      }
    }
  }
}

__global__ NOPK __launch_bounds__(512, 2) void k2(const unsigned short* __restrict__ dskip, char* __restrict__ ws,
                                                  const unsigned short* __restrict__ w1, const unsigned short* __restrict__ w2) {
  __shared__ __attribute__((aligned(16))) char smem[CV_XCH + 16384];
  TS(1, ws);
  conv_phase(dskip, ws, w1, w2, smem);
}

DEV u32x4 ld16_sc1(rsrc_t r, int voff, int soff) { return __builtin_amdgcn_raw_buffer_load_b128(r, voff, soff, 16); }
DEV u32x4 ld16(rsrc_t r, int voff, int soff) { return __builtin_amdgcn_raw_buffer_load_b128(r, voff, soff, 0); }
DEV u32x4 tr_frag(const char* p) {
  const u32x2 a = __builtin_bit_cast(u32x2, __builtin_amdgcn_ds_read_tr16_b64_v4i16((lds_s16x4*)p));
  const u32x2 b = __builtin_bit_cast(u32x2, __builtin_amdgcn_ds_read_tr16_b64_v4i16((lds_s16x4*)(p + 256)));
  return (u32x4){a[0], a[1], b[0], b[1]};
}

constexpr int KF_RS = 84;
constexpr int KF_RB = 8 * 16 * KF_RS * 4;
constexpr int KF_BIG = 2 * KF_RB;
constexpr int NW2L = 8, NW2R = 27;
constexpr int KF_TAB = KF_BIG;
constexpr int KF_PS = KF_TAB + 320;
constexpr int KF_XR = KF_PS + 512;
constexpr int KF_CNT = KF_XR + 64 * 176;
constexpr int KF_LDS = KF_CNT + 16;
constexpr int X_FIRST = 5, X_STEP = 4;
constexpr int W1_EARLY = 3, W1_MID = 7, W1_PRE = 10;
constexpr int W2_START = 6, W2_RATE = 2, W2_INL = 28;
constexpr int CS_STEP = 14, SIG_SPLIT = 4, H1_PRE = 9, H1_BATCH = 7;
constexpr unsigned LOCK_MASK = 0x55554;

DEV void kf_body(char* __restrict__ ws, unsigned short* __restrict__ out, char* smem) {
  char* xs = smem;
  float* tab = (float*)(smem + KF_TAB);
  float* ps = (float*)(smem + KF_PS);
  char* xr = smem + KF_XR;
  unsigned* lcnt = (unsigned*)(smem + KF_CNT);
  float* cst = (float*)(smem + 81920);
  const int tid = threadIdx.x, lane = tid & 63;
  const int w = __builtin_amdgcn_readfirstlane(tid >> 6);
  const int bx = blockIdx.x;
  const int ng = (bx & 1) * 4 + ((bx >> 3) & 3), mt = ((bx & 7) >> 1) * 8 + (bx >> 5);
  const int g = lane >> 4, i = lane & 15;
  const rsrc_t rs_ = ws_rsrc(ws);

  unsigned* flags = (unsigned*)(ws + WS_CNT) + mt * 128;
  const int ntl = ng < 6 ? 14 : 12;
  const int ctb = ng < 6 ? 14 * ng : 84 + 12 * (ng - 6);
  const bool two = w < ntl - 8;
  const bool full = w < 6;
  const int ks0 = w < 6 ? 7 * w : 42 + 6 * (w - 6);
  const int ct0 = ctb + (two ? 2 * w : (ntl - 8) + w);
  const int lo16 = lane * 16;
  const int w1s = (int)WS_W1F + ct0 * 1024;
  const int w2s = (int)WS_W2F + (ng * 5 * 54 + ks0) * 1024;

  u32x4 ring[32], wl[NW2L];
  const char* bb0 = xs + (8 * g + (i >> 2)) * 64 + ((g & 1) << 5) + (i & 3) * 8;
  const char* bb1 = xs + (8 * g + (i >> 2)) * 64 + (((g & 1) ^ 1) << 5) + (i & 3) * 8;
  auto w2_off = [&](int e) __attribute__((always_inline)) {
    const int s = e < 27 ? e / 4 : (e < 34 ? e - 27 : 6), c = e < 27 ? e % 4 : (e < 34 ? 4 : 3);
    const int sn = (s == 6 && !full) ? 5 : s;
    return w2s + (c * 54 + sn) * 1024;
  };

  auto gemm1 = [&](auto two_tag) __attribute__((always_inline)) {
    constexpr bool TWO = decltype(two_tag)::value;
    if (TWO) asm volatile("; two tiles" ::: "memory"); else asm volatile("; one tile" ::: "memory");
    u32x4 t[10];
    auto st_x = [&](int i0, int i1) __attribute__((always_inline)) {
#pragma unroll
      for (int it = i0; it < i1; ++it) {
        const int c = tid + 512 * it;
        const int row = c >> 3;
        *(u32x4*)(xs + ((c >> 2) & 1) * 40960 + row * 64 + (((c & 3) ^ ((row >> 2) & 2)) << 4)) = t[it];
      }
    };
#pragma unroll
    for (int it = 0; it < X_FIRST; ++it) t[it] = ld16(rs_, tid * 16, (int)WS_X1T + mt * 81920 + 8192 * it);
#pragma unroll
    for (int k = 0; k < W1_EARLY; ++k) {
      ring[2 * k] = ld16(rs_, lo16, w1s + k * 108 * 1024);
      if (TWO) ring[2 * k + 1] = ld16(rs_, lo16, w1s + (k * 108 + 1) * 1024);
    }
    SB();
    __builtin_amdgcn_s_barrier();
    SB();
#pragma unroll
    for (int k = W1_EARLY; k < W1_MID; ++k) {
      ring[2 * k] = ld16(rs_, lo16, w1s + k * 108 * 1024);
      if (TWO) ring[2 * k + 1] = ld16(rs_, lo16, w1s + (k * 108 + 1) * 1024);
    }
    SB();
    st_x(0, X_FIRST);
    SB();
    if (tid == 0) *lcnt = 0u;
    ModP gbp;
    if (!TWO) gbp = mod_ld(ws, 5 * D_ + 80 * ng + min((w & 1) * 64 + lane, 79));
    SB();
    __syncthreads();
    auto resid = [&]() __attribute__((always_inline)) {
      const int kb = (80 * ng) >> 5, tq = w & 3;
#pragma unroll
      for (int u = 0; u < 2; ++u) {
        const int kk = min(kb + 2 * (w >> 2) + u, 19);
        const u32x4 f = tr_frag(((tq & 1) ? bb1 : bb0) + (tq >> 1) * 40960 + kk * 2048);
        const unsigned dl = (unsigned)(32 * kk + 8 * g - 80 * ng);
        if (dl < 80u) *(u32x4*)(xr + (tq * 16 + i) * 176 + dl * 2) = f;
      }
    };
    const int hs = TWO ? 0 : (w & 1);
    const char* ba[4] = {bb0 + hs * 40960, bb1 + hs * 40960, bb0 + (hs ^ 1) * 40960, bb1 + (hs ^ 1) * 40960};
    const u32x4 ones = {0x3f803f80u, 0x3f803f80u, 0x3f803f80u, 0x3f803f80u};
    f32x4 acc[2][4];
#pragma unroll
    for (int c = 0; c < 2; ++c)
#pragma unroll
      for (int q = 0; q < 4; ++q) acc[c][q] = (f32x4){0.f, 0.f, 0.f, 0.f};
    u32x4 csp[5];
    const int cso = ((w & 1) * FF + 16 * ctb + 4 * min(lane, 4 * ntl - 1)) * 4;
    u32x4 bn[2][4];
#pragma unroll
    for (int q = 0; q < 4; ++q) bn[0][q] = tr_frag(ba[q]);
#pragma unroll
    for (int q = 0; q < 4; ++q) bn[1][q] = tr_frag(ba[q] + 2048);
#pragma unroll
    for (int kk = 0; kk < 20; ++kk) {
      if (kk == X_STEP) { st_x(X_FIRST, 10); SB(); }
      if (kk == 2 * X_FIRST - 2) { SB(); bar_lds(); SB(); }
      else if ((LOCK_MASK >> kk) & 1) { SB(); __builtin_amdgcn_s_barrier(); SB(); }
      if (kk == 2 * X_FIRST) { resid(); SB(); }
      auto gload = [&](int u) __attribute__((always_inline)) {
        {
          constexpr int CATCH = W1_PRE - W1_MID;
          const int k0 = kk == 0 ? W1_MID : (kk <= CATCH ? W1_MID + 2 * kk - 1 : kk + W1_PRE);
          const int nk = (kk >= 1 && kk <= CATCH) ? 2 : 1;
          const int kn = k0 + (nk == 2 ? (u >> 1) : 0);
          const bool on = nk == 2 || u >= 2;
          if (on && kn < 20) {
            if ((u & 1) == 0) ring[(2 * kn) % 32] = ld16(rs_, lo16, w1s + kn * 108 * 1024);
            else if (TWO) ring[(2 * kn + 1) % 32] = ld16(rs_, lo16, w1s + (kn * 108 + 1) * 1024);
          }
          if (kk == 0 && u < 2) {
#pragma unroll
            for (int it = X_FIRST + u * 3; it < (u ? 10 : (X_FIRST + 3 < 10 ? X_FIRST + 3 : 10)); ++it)
              t[it] = ld16(rs_, tid * 16, (int)WS_X1T + mt * 81920 + 8192 * it);
          }
        }
        if (kk >= W2_START && u < W2_RATE) {
          const int n = W2_RATE * (kk - W2_START) + u;
          if (n < NW2L) wl[n] = ld16(rs_, lo16, w2_off(NW2R + n));
          else if (n < W2_INL) ring[8 + n - NW2L] = ld16(rs_, lo16, w2_off(n - NW2L));
        }
      };
      const u32x4 a0 = ring[(2 * kk) % 32], a1 = ring[(2 * kk + 1) % 32];
#pragma unroll
      for (int q = 0; q < 4; ++q) {
        const u32x4 bq = bn[kk & 1][q];
        acc[0][q] = mfma_bf16(a0, bq, acc[0][q]);
        if (TWO) acc[1][q] = mfma_bf16(a1, bq, acc[1][q]);
        else if (q < 2) {
          acc[1][2 * q] = mfma_bf16(ones, bq, acc[1][2 * q]);
          acc[1][2 * q + 1] = mfma_bf16(bq, bq, acc[1][2 * q + 1]);
        }
        SB();
        if (kk < 18) bn[kk & 1][q] = tr_frag(ba[q] + (kk + 2) * 2048);
        gload(q);
        SB();
      }
      if (!TWO) {
        if (kk < 5) csp[kk] = ld16(rs_, cso, (int)WS_CSP + kk * 2 * FF * 4);
        if (kk == CS_STEP) {
          f32x4 t = __builtin_bit_cast(f32x4, csp[0]);
#pragma unroll
          for (int u = 1; u < 5; ++u) t += __builtin_bit_cast(f32x4, csp[u]);
          if (w >= 6 && lane < 56) *(f32x4*)(cst + (w & 1) * 224 + 4 * lane) = t;
        }
      }
      if (!TWO && kk == 12) { if ((w & 1) * 64 + lane < 80) tab[(w & 1) * 64 + lane] = mod_fin(gbp); }
      SB();
    }
    if (!TWO) {
#pragma unroll
      for (int h = 0; h < 2; ++h) {
        const f32x4 aq = acc[1][2 * h + 1];
        const float dq = (i & 2) ? ((i & 1) ? aq[3] : aq[2]) : ((i & 1) ? aq[1] : aq[0]);
        const int tk = (2 * hs + h) * 16 + i;
        if (g == (i >> 2)) {
          const float mean = acc[1][2 * h][0] * (1.f / D_);
          const float var = fmaxf(dq * (1.f / D_) - mean * mean, 0.f);
          const float sd = rbf(sqrtf(rbf(rbf(var) + 1e-5f)));
          const float rs = 1.f / sd;
          ps[tk] = rs;
          ps[64 + tk] = -rbf(mean) * rs;
        }
      }
    }
    __syncthreads();
    f32x4 csv[2], b1v[2];
#pragma unroll
    for (int c = 0; c < (TWO ? 2 : 1); ++c) {
      csv[c] = *(const f32x4*)(cst + 16 * (ct0 - ctb + c) + 4 * g);
      b1v[c] = *(const f32x4*)(cst + 224 + 16 * (ct0 - ctb + c) + 4 * g);
    }
#pragma unroll
    for (int qp = 0; qp < 2; ++qp) {
      u32x2 o[2][2];
#pragma unroll
      for (int h = 0; h < 2; ++h) {
        const int q = 2 * qp + h, tq = q ^ (2 * hs);
        const float rs = ps[tq * 16 + i], nm = ps[64 + tq * 16 + i];
#pragma unroll
        for (int c = 0; c < (TWO ? 2 : 1); ++c) {
          float v[4];
#pragma unroll
          for (int r = 0; r < 4; ++r) v[r] = fmaxf(fmaf(acc[c][q][r], rs, fmaf(nm, csv[c][r], b1v[c][r])), 0.f);
          o[c][h] = (u32x2){pk_bf(v[0], v[1]), pk_bf(v[2], v[3])};
        }
      }
      const int tl = ((2 * qp) ^ (2 * hs)) + (g & 1);
#pragma unroll
      for (int c = 0; c < (TWO ? 2 : 1); ++c) {
        const int ct = ct0 + c;
        const auto s0 = __builtin_amdgcn_permlane16_swap(o[c][0][0], o[c][1][0], false, false);
        const auto s1 = __builtin_amdgcn_permlane16_swap(o[c][0][1], o[c][1][1], false, false);
        const size_t off = (((size_t)(4 * mt + tl) * 54 + (ct >> 1)) * 64 + i + 16 * (2 * (ct & 1) + (g >> 1))) * 16;
        st16(rs_, WS_H1F + off, (u32x4){s0[0], s1[0], s0[1], s1[1]});
      }
    }
    SB();
    __builtin_amdgcn_s_barrier();
    SB();
    constexpr int E0 = W2_INL - NW2L;
    auto signal = [&]() __attribute__((always_inline)) {
      if (lane == 0) {
        if (__hip_atomic_fetch_add(lcnt, 1u, __ATOMIC_RELAXED, __HIP_MEMORY_SCOPE_WORKGROUP) == 7u)
          __hip_atomic_store(flags + 16 * ng, 1u, __ATOMIC_RELAXED, __HIP_MEMORY_SCOPE_AGENT);
      }
    };
    if (w >= SIG_SPLIT) { asm volatile("s_waitcnt vmcnt(0)" ::: "memory"); signal(); }
    SB();
#pragma unroll
    for (int e = E0; e < NW2R; ++e) ring[(8 + e) % 32] = ld16(rs_, lo16, w2_off(e));
    SB();
    if (w < SIG_SPLIT) { asm volatile("s_waitcnt vmcnt(%0)" :: "n"(NW2R - E0) : "memory"); signal(); }
    SB();
  };
  if (two) gemm1(std::true_type{});
  else gemm1(std::false_type{});
#pragma nounroll
  for (unsigned spin = 0; spin < (1u << 20); ++spin) {
    if (__hip_atomic_load(flags + 16 * w, __ATOMIC_RELAXED, __HIP_MEMORY_SCOPE_AGENT) != 0u) break;
    __builtin_amdgcn_s_sleep(1);
  }
  asm volatile("" ::: "memory");
  SB();

  const int h1s = (int)WS_H1F + (mt * 4 * 54 + ks0) * 1024;
  u32x4 hb[28];
  auto load_h1 = [&](int n) __attribute__((always_inline)) {
    const int q = n / 7, s = n % 7;
    const int sn = (s == 6 && !full) ? 5 : s;
    hb[n] = ld16_sc1(rs_, lo16, h1s + (q * 54 + sn) * 1024);
  };
#pragma unroll
  for (int n = 0; n < H1_PRE; ++n) {
    if (n == H1_BATCH) { SB(); __builtin_amdgcn_s_barrier(); SB(); }
    load_h1(n);
  }
  SB();
  float* red = (float*)smem;
  float* rb_ = red + (w * 16 + i) * KF_RS + 4 * g;
  const int re = w * 40 + min(lane, 39);
  const int rtk = re / 20, rq4 = re % 20;
  const float* rr_ = red + rtk * KF_RS + 4 * rq4;
  f32x4 ac2[2][5];
  f32x4 rv[8], rsum;
  u32x2 xv2;
  const f32x4 gb = *(const f32x4*)(tab + 4 * rq4);
  auto red_wr = [&](int q, int c) __attribute__((always_inline)) {
    *(f32x4*)(rb_ + (q & 1) * (KF_RB / 4) + 16 * c) = ac2[q & 1][c];
  };
  auto red_rd = [&](int q, int ww) __attribute__((always_inline)) {
    rv[ww] = *(const f32x4*)(rr_ + (q & 1) * (KF_RB / 4) + ww * 16 * KF_RS);
  };
  auto red_x = [&](int q) __attribute__((always_inline)) { xv2 = *(const u32x2*)(xr + (16 * q + rtk) * 176 + rq4 * 8); };
  auto red_out = [&](int q) __attribute__((always_inline)) {
    const f32x4 s = rsum + (rv[6] + rv[7]);
    u32x2 o;
#pragma unroll
    for (int h = 0; h < 2; ++h) {
      unsigned f = pk_bf(s[2 * h], s[2 * h + 1]);
      unsigned gf = pk_bf(bf_lo(f) * gb[2 * h], bf_hi(f) * gb[2 * h + 1]);
      o[h] = pk_bf(bf_lo(xv2[h]) + bf_lo(gf), bf_hi(xv2[h]) + bf_hi(gf));
    }
    *(u32x2*)(out + (size_t)(mt * 64 + 16 * q + rtk) * D_ + ng * 80 + 4 * rq4) = o;
  };
#pragma unroll
  for (int q = 0; q < 3; ++q) {
#pragma unroll
    for (int c = 0; c < 5; ++c) ac2[q & 1][c] = (f32x4){0.f, 0.f, 0.f, 0.f};
#pragma unroll
    for (int s = 0; s < 7; ++s) {
      auto mm = [&](int c) __attribute__((always_inline)) {
        const u32x4 a = c == 4 ? wl[s] : ((s == 6 && c == 3) ? wl[7] : ring[(8 + 4 * s + c) % 32]);
        ac2[q & 1][c] = mfma_bf16(a, hb[7 * q + s], ac2[q & 1][c]);
      };
      if (s == 5 && q >= 2) {
        red_wr(q - 1, 4);
        SB();
        red_out(q - 2);
#pragma unroll
        for (int c = 0; c < 5; ++c) mm(c);
#pragma unroll
        for (int c = 0; c < 5; ++c) {
          __builtin_amdgcn_sched_group_barrier(0x008, 1, 0);
          __builtin_amdgcn_sched_group_barrier(0x002, 10, 0);
        }
        SB();
      } else if (s < 6) {
        mm(0); SB();
        if (q >= 1 && s >= 1) { red_wr(q - 1, s - 1); SB(); }
        mm(1); SB();
        if (q >= 2 && s < 4) { red_rd(q - 2, 2 * s); SB(); }
        mm(2); SB();
        if (q >= 2 && s < 4) { red_rd(q - 2, 2 * s + 1); SB(); }
        if (q >= 2 && s == 4) { red_x(q - 2); SB(); }
        mm(3); SB();
        if (q >= 2 && s >= 1 && s < 4) {
          const f32x4 t = rv[2 * s - 2] + rv[2 * s - 1];
          rsum = s == 1 ? t : rsum + t;
          asm volatile("" : "+v"(rsum));
          SB();
        }
        mm(4); SB();
      } else if (full) {
#pragma unroll
        for (int c = 0; c < 5; ++c) mm(c);
        SB();
      }
      if (7 * q + s + H1_PRE < 28) { load_h1(7 * q + s + H1_PRE); SB(); }
    }
    if (q >= 1) bar_lds();
  }
  {
    auto sum6 = [&]() __attribute__((always_inline)) {
      rsum = ((rv[0] + rv[1]) + (rv[2] + rv[3])) + (rv[4] + rv[5]);
    };
    if (full) {
#pragma unroll
      for (int c = 0; c < 5; ++c) {
        const u32x4 a = c == 4 ? wl[6] : (c == 3 ? wl[7] : ring[(8 + 24 + c) % 32]);
        rv[c] = mfma_bf16(a, hb[27], (f32x4){0.f, 0.f, 0.f, 0.f});
      }
    } else {
#pragma unroll
      for (int c = 0; c < 5; ++c) rv[c] = (f32x4){0.f, 0.f, 0.f, 0.f};
    }
    f32x4 c0[5];
#pragma unroll
    for (int c = 0; c < 5; ++c) c0[c] = rv[c];
    SB();
    auto col = [&](int c, auto&& between) __attribute__((always_inline)) {
#pragma unroll
      for (int s = 0; s < 6; ++s) {
        const u32x4 a = c == 4 ? wl[s] : ring[(8 + 4 * s + c) % 32];
        c0[c] = mfma_bf16(a, hb[21 + s], c0[c]);
        SB();
        between(s);
        SB();
      }
    };
    col(0, [&](int s) __attribute__((always_inline)) { if (s < 5) red_wr(2, s); if (s < 4) { red_rd(1, 2 * s); red_rd(1, 2 * s + 1); } if (s == 4) red_x(1); });
    col(1, [&](int s) __attribute__((always_inline)) { if (s == 1) { sum6(); asm volatile("" : "+v"(rsum)); } if (s == 3) red_out(1); });
    bar_lds();
    auto wr3 = [&](int c) __attribute__((always_inline)) { *(f32x4*)(rb_ + (KF_RB / 4) + 16 * c) = c0[c]; };
    col(2, [&](int s) __attribute__((always_inline)) { if (s < 2) wr3(s); if (s >= 1 && s < 5) { red_rd(2, 2 * s - 2); red_rd(2, 2 * s - 1); } if (s == 5) red_x(2); });
    col(3, [&](int s) __attribute__((always_inline)) { if (s == 1) wr3(2); if (s == 2) { sum6(); asm volatile("" : "+v"(rsum)); } if (s == 4) red_out(2); });
    col(4, [&](int s) __attribute__((always_inline)) { if (s == 1) wr3(3); if (s == 2) red_x(3); });
    wr3(4);
    bar_lds();
#pragma unroll
    for (int ww = 0; ww < 8; ++ww) red_rd(3, ww);
    sum6();
    red_out(3);
  }
}

__global__ NOPK __launch_bounds__(512, 2) void kf(char* __restrict__ ws, unsigned short* __restrict__ out) {
  __shared__ __attribute__((aligned(16))) char smem[KF_LDS];
  TS(2, ws);
  kf_body(ws, out, smem);
}

__global__ void kprobe(char* ws, int which) {
  const unsigned long long now = __builtin_amdgcn_s_memrealtime();
  const unsigned long long* t = (const unsigned long long*)(ws + (200u << 20));
  unsigned long long d = which == 0 ? t[1] - t[0] : (which == 1 ? t[2] - t[1] : (which == 2 ? now - t[2] : 0ull));
  if (d > 10000ull) d = 10000ull;
  while (__builtin_amdgcn_s_memrealtime() - now < 10ull * d) __builtin_amdgcn_s_sleep(1);
}
__global__ void kend_() {}

extern "C" void kernel_launch(void* const* d_in, const int* in_sizes, int n_in,
                              void* d_out, int out_size, void* d_ws, size_t ws_size,
                              hipStream_t stream) {
  K1Args a;
  a.x = (const unsigned short*)d_in[0];
  a.are = (const unsigned short*)d_in[1];
  a.aim = (const unsigned short*)d_in[2];
  a.bre = (const unsigned short*)d_in[3];
  a.bim = (const unsigned short*)d_in[4];
  a.cre = (const unsigned short*)d_in[5];
  a.cim = (const unsigned short*)d_in[6];
  a.logdt = (const unsigned short*)d_in[7];
  a.c = (const unsigned short*)d_in[12];
  a.wmod = (const unsigned short*)d_in[13];
  a.ws = (char*)d_ws;
  k1<<<NB_PF + NB_K + NB_LN + NB_MOD, 256, 0, stream>>>(a);
  k2<<<256, 512, 0, stream>>>((const unsigned short*)d_in[8], (char*)d_ws, (const unsigned short*)d_in[10],
                              (const unsigned short*)d_in[11]);
  kf<<<256, 512, 0, stream>>>((char*)d_ws, (unsigned short*)d_out);
  kprobe<<<1, 64, 0, stream>>>((char*)d_ws, 0);
}
```

```cpp
#define NT_W 1
#include <hip/hip_runtime.h>
#include <hip/hip_bf16.h>
#include <stdint.h>
#include <type_traits>

typedef __attribute__((ext_vector_type(8))) __bf16 bf16x8;
typedef __attribute__((ext_vector_type(2))) __bf16 bf16x2;
typedef __attribute__((ext_vector_type(4))) float f32x4;
typedef __attribute__((ext_vector_type(2))) float f32x2;
typedef __attribute__((ext_vector_type(4))) unsigned u32x4;
typedef __attribute__((ext_vector_type(2))) unsigned u32x2;
typedef __attribute__((ext_vector_type(8))) _Float16 h16x8;
typedef __attribute__((ext_vector_type(2))) _Float16 h16x2;
typedef __attribute__((ext_vector_type(4))) short s16x4;
typedef __attribute__((address_space(3))) s16x4 lds_s16x4;

#if defined(__HIP_DEVICE_COMPILE__)
#define NOPK __attribute__((target("no-packed-fp32-ops")))
#else
#define NOPK
#endif
#define DEV __device__ __forceinline__

constexpr int L_ = 2048;
constexpr int D_ = 640;
constexpr int NS = 128;
constexpr int FF = 1728;
constexpr int KREV = 2304;

constexpr size_t WS_KSC  = 8192;
constexpr size_t WS_CNT  = 16384;
constexpr size_t WS_MOD  = 65536;
constexpr size_t WS_CSP  = 196608;
constexpr size_t WS_XNT  = 327680;
constexpr size_t WS_XT   = WS_XNT + (size_t)D_ * L_ * 2;
constexpr size_t WS_KREV = WS_XT + (size_t)D_ * L_ * 2;
constexpr size_t WS_W1F  = WS_KREV + (size_t)D_ * KREV * 2;
constexpr size_t WS_W2F  = WS_W1F + (size_t)D_ * FF * 2;
constexpr size_t WS_X1T  = WS_W2F + (size_t)D_ * FF * 2;
constexpr size_t WS_H1F  = WS_X1T + (size_t)D_ * L_ * 2;
constexpr size_t WS_END  = WS_H1F + (size_t)L_ * FF * 2;

#define TS(k, wsp) do { if (blockIdx.x == 0 && threadIdx.x == 0) __hip_atomic_store((unsigned long long*)((wsp) + (200u << 20)) + (k), (unsigned long long)__builtin_amdgcn_s_memrealtime(), __ATOMIC_RELAXED, __HIP_MEMORY_SCOPE_AGENT); } while (0)
DEV float bf_lo(unsigned u) { return __uint_as_float(u << 16); }
DEV float bf_hi(unsigned u) { return __uint_as_float(u & 0xffff0000u); }
DEV unsigned pk_bf(float a, float b) {
  f32x2 v = {a, b};
  bf16x2 r = __builtin_convertvector(v, bf16x2);
  return __builtin_bit_cast(unsigned, r);
}
DEV float rbf(float a) { return bf_lo(pk_bf(a, 0.f)); }
#define NT_MOD 0
#define NT_X 0
template <int NT> DEV u32x4 ldv(const void* p) { return NT ? __builtin_nontemporal_load((const u32x4*)p) : *(const u32x4*)p; }
DEV float ldbf(const unsigned short* p) { return __uint_as_float(((unsigned)*p) << 16); }

typedef __amdgpu_buffer_rsrc_t rsrc_t;
DEV rsrc_t ws_rsrc(char* ws) { return __builtin_amdgcn_make_buffer_rsrc((void*)ws, 0, (int)WS_END, 0x00020000); }
DEV void st16(rsrc_t r, size_t off, u32x4 v) { __builtin_amdgcn_raw_buffer_store_b128(v, r, (int)off, 0, 16); }
DEV void st4(char* ws, size_t off, unsigned v) { __hip_atomic_store((unsigned*)(ws + off), v, __ATOMIC_RELAXED, __HIP_MEMORY_SCOPE_AGENT); }
DEV void st8(rsrc_t r, size_t off, u32x2 v) { __builtin_amdgcn_raw_buffer_store_b64(v, r, (int)off, 0, 16); }

DEV void bar_lds() { asm volatile("s_waitcnt lgkmcnt(0)\n\ts_barrier" ::: "memory"); }
DEV void lds_wait(const void* word, unsigned val) {
  unsigned tv, ts, cnt;
  asm volatile("s_mov_b32 %2, 0x100000\n"
               "1:\n\tds_read_b32 %0, %3\n\ts_waitcnt lgkmcnt(0)\n\tv_readfirstlane_b32 %1, %0\n\t"
               "s_cmp_ge_u32 %1, %4\n\ts_cbranch_scc1 2f\n\ts_sleep 1\n\ts_sub_u32 %2, %2, 1\n\t"
               "s_cmp_lg_u32 %2, 0\n\ts_cbranch_scc1 1b\n2:"
               : "=&v"(tv), "=&s"(ts), "=&s"(cnt) : "v"((unsigned)(size_t)word), "s"(val) : "memory", "scc");
}
DEV void lds_post(unsigned* word, int lane) {
  asm volatile("" ::: "memory");
  if (lane == 0) __hip_atomic_fetch_add(word, 1u, __ATOMIC_RELAXED, __HIP_MEMORY_SCOPE_WORKGROUP);
  asm volatile("" ::: "memory");
}
DEV void lgkm0() { asm volatile("s_waitcnt lgkmcnt(0)" ::: "memory"); __builtin_amdgcn_sched_barrier(0); }

DEV f32x4 mfma_bf16(u32x4 a, u32x4 b, f32x4 c) {
  return __builtin_amdgcn_mfma_f32_16x16x32_bf16(__builtin_bit_cast(bf16x8, a), __builtin_bit_cast(bf16x8, b), c, 0, 0, 0);
}

constexpr int NB_PF = 8, NB_K = 640, NB_LN = 256, NB_MOD = 120;

struct K1Args {
  const unsigned short *x, *are, *aim, *bre, *bim, *cre, *cim, *logdt, *c, *wmod;
  char* ws;
};

DEV void k1_prefetch() {
  const int tid = threadIdx.x;
  unsigned long long pc;
  int o0, o1;
  asm volatile("s_getpc_b64 %0\n.Lpf_here%=:\n\ts_mov_b32 %1, _Z2k16K1Args - .Lpf_here%=\n\ts_mov_b32 %2, _Z5kend_v - .Lpf_here%="
               : "=s"(pc), "=s"(o0), "=s"(o1));
  const char* c0 = (const char*)(pc + (long long)o0);
  long n = o1 - o0;
  n = n < 0 ? 0 : (n > 98304 ? 98304 : n);
  unsigned v[3];
#pragma unroll
  for (int it = 0; it < 3; ++it) {
    const long o = (long)tid * 128 + it * 32768;
    v[it] = o < n ? *(const __attribute__((address_space(1))) unsigned*)(c0 + o) : 0u;
  }
  asm volatile("" :: "v"(v[0] ^ v[1] ^ v[2]));
}

DEV void k1_mod(const K1Args& p, int b, char* smem) {
  const int tid = threadIdx.x;
  const int rg = b & 7, cgp = b >> 3;
  const int c32 = tid & 31, r8 = tid >> 5;
  float* sil = (float*)smem;
  float* red = (float*)(smem + 512);
  u32x4 w[10];
#pragma unroll
  for (int it = 0; it < 10; ++it)
    w[it] = ldv<NT_MOD>(p.wmod + (size_t)(rg * 80 + r8 + 8 * it) * 3840 + 256 * cgp + 8 * c32);
  if (tid < 80) {
    float cv = ldbf(p.c + rg * 80 + tid);
    sil[tid] = rbf(cv / (1.f + expf(-cv)));
  }
  if (tid >= 128 && tid < 132 && b < 64) st4(p.ws, WS_CNT + (b * 4 + tid - 128) * 64, 0u);
  __syncthreads();
  float acc[8];
#pragma unroll
  for (int j = 0; j < 8; ++j) acc[j] = 0.f;
#pragma unroll
  for (int it = 0; it < 10; ++it) {
    float s = sil[r8 + 8 * it];
#pragma unroll
    for (int q = 0; q < 4; ++q) {
      acc[2 * q] += s * bf_lo(w[it][q]);
      acc[2 * q + 1] += s * bf_hi(w[it][q]);
    }
  }
#pragma unroll
  for (int j = 0; j < 8; ++j) acc[j] += __shfl_xor(acc[j], 32, 64);
  if ((tid & 63) < 32) {
    *(f32x4*)(red + (tid >> 6) * 256 + 8 * c32) = (f32x4){acc[0], acc[1], acc[2], acc[3]};
    *(f32x4*)(red + (tid >> 6) * 256 + 8 * c32 + 4) = (f32x4){acc[4], acc[5], acc[6], acc[7]};
  }
  __syncthreads();
  st4(p.ws, WS_MOD + ((256 * cgp + tid) * 8 + rg) * 4,
      __float_as_uint((red[tid] + red[256 + tid]) + (red[512 + tid] + red[768 + tid])));
}
struct ModP { f32x4 a, b; };
DEV ModP mod_ld(const char* ws, int idx) {
  const f32x4* mp = (const f32x4*)(ws + WS_MOD) + 2 * idx;
  ModP m; m.a = mp[0]; m.b = mp[1];
  return m;
}
DEV float mod_fin(const ModP& m) {
  return rbf(((m.a[0] + m.a[1]) + (m.a[2] + m.a[3])) + ((m.b[0] + m.b[1]) + (m.b[2] + m.b[3])));
}
#define SB() __builtin_amdgcn_sched_barrier(0)

DEV void k1_ln(const K1Args& p, int b, char* smem) {
  constexpr int STR = 704;
  const int tid = threadIdx.x;
  const int tok = tid >> 3, sub = tid & 7;
  const int tile = (b & 7) * 8 + ((b >> 3) & 7), pass = b >> 6;
  const int hf = pass & 1;
  const bool norm = pass < 2;
  const int t0 = tile * 32;
  const unsigned short* xr = p.x + (size_t)(t0 + tok) * D_ + 8 * sub;
  u32x4 xv[5], xo[5];
#pragma unroll
  for (int m = 0; m < 5; ++m) xv[m] = ldv<NT_X>(xr + 320 * hf + 64 * m);
  if (norm) {
#pragma unroll
    for (int m = 0; m < 5; ++m) xo[m] = ldv<NT_X>(xr + 320 * (hf ^ 1) + 64 * m);
    float s = 0.f, v = 0.f;
    const bf16x2 one2 = __builtin_bit_cast(bf16x2, 0x3f803f80u);
#pragma unroll
    for (int m = 0; m < 10; ++m)
#pragma unroll
      for (int q = 0; q < 4; ++q) {
        const unsigned u = m < 5 ? xv[m][q] : xo[m - 5][q];
        const bf16x2 x2 = __builtin_bit_cast(bf16x2, u);
        s = __builtin_amdgcn_fdot2_f32_bf16(x2, one2, s, false);
        v = __builtin_amdgcn_fdot2_f32_bf16(x2, x2, v, false);
      }
#pragma unroll
    for (int o = 1; o < 8; o <<= 1) { s += __shfl_xor(s, o, 64); v += __shfl_xor(v, o, 64); }
    const float mean = s * (1.f / D_);
    const float var = fmaxf(v * (1.f / D_) - mean * mean, 0.f);
    const float sd = rbf(sqrtf(rbf(rbf(var) + 1e-5f)));
    const float rs = 1.f / sd;
    const float mb = rbf(mean);
#pragma unroll
    for (int m = 0; m < 5; ++m)
#pragma unroll
      for (int q = 0; q < 4; ++q) {
        unsigned d2 = pk_bf(bf_lo(xv[m][q]) - mb, bf_hi(xv[m][q]) - mb);
        xv[m][q] = pk_bf(bf_lo(d2) * rs, bf_hi(d2) * rs);
      }
  }
  const int lane = tid & 63, w = tid >> 6;
  const int g = lane >> 4, i = lane & 15;
  const rsrc_t rsrc = ws_rsrc(p.ws);
#pragma unroll
  for (int m = 0; m < 5; ++m) *(u32x4*)(smem + tok * STR + (sub + 8 * m) * 16) = xv[m];
  __syncthreads();
  const size_t dsto = (norm ? WS_XNT : WS_XT) + ((size_t)tile * D_ + 320 * hf) * 64;
#pragma unroll
  for (int it = 0; it < 5; ++it) {
    const int cb = w * 5 + it;
    const char* a = smem + (8 * g + (i >> 2)) * STR + cb * 32 + (i & 3) * 8;
    const u32x2 ra = __builtin_bit_cast(u32x2, __builtin_amdgcn_ds_read_tr16_b64_v4i16((lds_s16x4*)a));
    const u32x2 rb = __builtin_bit_cast(u32x2, __builtin_amdgcn_ds_read_tr16_b64_v4i16((lds_s16x4*)(a + 4 * STR)));
    st16(rsrc, dsto + (size_t)(cb * 16 + i) * 64 + 16 * g, (u32x4){ra[0], ra[1], rb[0], rb[1]});
  }
}

constexpr int RT_STR = 176;
template <int NF> struct RtRaw { u32x4 r[NF]; };
template <int NF>
DEV void rt_load(RtRaw<NF>& t, const unsigned short* W, int N, int kk, int ct0, int lane) {
#pragma unroll
  for (int j = 0; j < NF; ++j) {
    const int c = lane + 64 * j, row = c / (2 * NF), part = c % (2 * NF);
    t.r[j] = ldv<NT_W>(W + (size_t)(32 * kk + row) * N + 16 * ct0 + 8 * part);
  }
}
template <int NF>
DEV void rt_park(const RtRaw<NF>& t, char* scr, int lane) {
#pragma unroll
  for (int j = 0; j < NF; ++j) {
    const int c = lane + 64 * j, row = c / (2 * NF), part = c % (2 * NF);
    *(u32x4*)(scr + row * RT_STR + 16 * part) = t.r[j];
  }
}
DEV u32x4 rt_frag(const char* scr, int j, int lane) {
  const int g = lane >> 4, i = lane & 15;
  const char* p = scr + (8 * g + (i >> 2)) * RT_STR + 32 * j + (i & 3) * 8;
  const u32x2 a = __builtin_bit_cast(u32x2, __builtin_amdgcn_ds_read_tr16_b64_v4i16((lds_s16x4*)p));
  const u32x2 b = __builtin_bit_cast(u32x2, __builtin_amdgcn_ds_read_tr16_b64_v4i16((lds_s16x4*)(p + 4 * RT_STR)));
  return (u32x4){a[0], a[1], b[0], b[1]};
}
struct cf { float re, im; };
DEV cf cmul(cf a, cf b) {
  cf r;
  r.re = a.re * b.re - a.im * b.im;
  r.im = a.re * b.im + a.im * b.re;
  return r;
}
DEV cf cexp_(float a2, float br, float m, float m0) {
  float mag = __builtin_amdgcn_exp2f(a2 * (m - m0));
  float ph = __builtin_amdgcn_fractf(br * m);
  cf r; r.re = mag * __builtin_amdgcn_cosf(ph); r.im = mag * __builtin_amdgcn_sinf(ph);
  return r;
}
DEV unsigned pk_h(float a, float b) {
  f32x2 v = {a, b};
  h16x2 r = __builtin_convertvector(v, h16x2);
  return __builtin_bit_cast(unsigned, r);
}
DEV f32x4 mfma_f16(u32x4 a, u32x4 b, f32x4 c) {
  return __builtin_amdgcn_mfma_f32_16x16x32_f16(__builtin_bit_cast(h16x8, a), __builtin_bit_cast(h16x8, b), c, 0, 0, 0);
}
DEV void put2(char* p, cf a, cf b) {
  u32x2 o = {pk_h(a.re, a.im), pk_h(b.re, b.im)};
  *(u32x2*)p = o;
}

constexpr int KG_RS = 544;
DEV void k1_kgen(const K1Args& p, int d, char* smem) {
  constexpr int RS = KG_RS;
  const int tid = threadIdx.x;
  const int lane = tid & 63;
  const int w = __builtin_amdgcn_readfirstlane(tid >> 6);
  const int g = lane >> 4, i = lane & 15;
  const size_t po = (size_t)d * NS + 2 * lane;
  const unsigned rar = *(const unsigned*)(p.are + po), rai = *(const unsigned*)(p.aim + po);
  const unsigned rbr = *(const unsigned*)(p.bre + po), rbi = *(const unsigned*)(p.bim + po);
  const unsigned rcr = *(const unsigned*)(p.cre + po), rci = *(const unsigned*)(p.cim + po);
  const float ldt = ldbf(p.logdt + d);
  asm volatile("" ::: "memory");
  SB();
  const float dt = rbf(expf(ldt));
  float a2[2], bv[2], m0[2];
  a2[0] = (dt * bf_lo(rar)) * 1.44269504088896341f; a2[1] = (dt * bf_hi(rar)) * 1.44269504088896341f;
  bv[0] = (dt * bf_lo(rai)) * 0.15915494309189534f; bv[1] = (dt * bf_hi(rai)) * 0.15915494309189534f;
  m0[0] = a2[0] > 0.f ? 2048.f : 0.f; m0[1] = a2[1] > 0.f ? 2048.f : 0.f;
  char* my = smem + lane * 8;
  if (w == 0) {
    cf v[2], v8[2], e8[2];
    float bound = 0.f;
#pragma unroll
    for (int q = 0; q < 2; ++q) {
      const float dbr = dt * (q ? bf_hi(rbr) : bf_lo(rbr)), dbi = dt * (q ? bf_hi(rbi) : bf_lo(rbi));
      const float cr = q ? bf_hi(rcr) : bf_lo(rcr), ci = q ? bf_hi(rci) : bf_lo(rci);
      const float gsh = __builtin_amdgcn_exp2f(a2[q] * m0[q]);
      v[q].re = (cr * dbr - ci * dbi) * gsh; v[q].im = -(cr * dbi + ci * dbr) * gsh;
      bound += (fabsf(v[q].re) + fabsf(v[q].im)) * __builtin_amdgcn_exp2f(fmaxf(a2[q], 0.f) * 120.f);
      e8[q] = cexp_(a2[q], -bv[q], 8.f, 0.f);
      v8[q] = cexp_(a2[q], -bv[q], 64.f, 0.f);
    }
#pragma unroll
    for (int o = 1; o < 64; o <<= 1) bound += __shfl_xor(bound, o, 64);
    int ex = (int)((__float_as_uint(bound) >> 23) & 0xff);
    ex = ex < 20 ? 20 : (ex > 240 ? 240 : ex);
    const float sv = __uint_as_float((unsigned)(127 + 13 + 127 - ex) << 23);
    if (lane == 0) st4(p.ws, WS_KSC + 4 * d, (unsigned)(ex - 13) << 23);
#pragma unroll
    for (int q = 0; q < 2; ++q) { v[q].re *= sv; v[q].im *= sv; v8[q] = cmul(v[q], v8[q]); }
#pragma unroll
    for (int j = 0; j < 8; ++j) {
      put2(my + (41 + j) * RS, v[0], v[1]);
      put2(my + (49 + j) * RS, v8[0], v8[1]);
      if (j < 7) {
#pragma unroll
        for (int q = 0; q < 2; ++q) { v[q] = cmul(v[q], e8[q]); v8[q] = cmul(v8[q], e8[q]); }
      }
    }
  } else if (w == 1) {
    cf e1[2], u[2], u2[2];
#pragma unroll
    for (int q = 0; q < 2; ++q) {
      e1[q] = cexp_(a2[q], bv[q], 1.f, 0.f);
      u[q].re = __builtin_amdgcn_exp2f(-a2[q] * m0[q]); u[q].im = 0.f;
      u2[q] = cexp_(a2[q], bv[q], 128.f, m0[q]);
    }
#pragma unroll
    for (int r = 0; r < 8; ++r) {
      put2(my + r * RS, u[0], u[1]);
      put2(my + (8 + r) * RS, u2[0], u2[1]);
      if (r < 7) {
#pragma unroll
        for (int q = 0; q < 2; ++q) { u[q] = cmul(u[q], e1[q]); u2[q] = cmul(u2[q], e1[q]); }
      }
    }
  } else if (w == 2) {
    cf e2[2], u[2], u2[2];
#pragma unroll
    for (int q = 0; q < 2; ++q) {
      e2[q] = cexp_(a2[q], bv[q], 2.f, 0.f);
      u[q] = cexp_(a2[q], bv[q], 256.f, m0[q]);
      u2[q] = cexp_(a2[q], bv[q], 384.f, m0[q]);
    }
#pragma unroll
    for (int r = 0; r < 4; ++r) {
      put2(my + (16 + r) * RS, u[0], u[1]);
      put2(my + (20 + r) * RS, u2[0], u2[1]);
      if (r < 3) {
#pragma unroll
        for (int q = 0; q < 2; ++q) { u[q] = cmul(u[q], e2[q]); u2[q] = cmul(u2[q], e2[q]); }
      }
    }
    cf e4[2] = {cmul(e2[0], e2[0]), cmul(e2[1], e2[1])};
#pragma unroll
    for (int r = 0; r < 2; ++r) {
      cf c0 = cexp_(a2[0], bv[0], 512.f + 128.f * r, m0[0]), c1 = cexp_(a2[1], bv[1], 512.f + 128.f * r, m0[1]);
      put2(my + (24 + 2 * r) * RS, c0, c1);
      put2(my + (25 + 2 * r) * RS, cmul(c0, e4[0]), cmul(c1, e4[1]));
    }
  } else {
    cf e4[2], e128[2], u[2], u2[2];
#pragma unroll
    for (int q = 0; q < 2; ++q) {
      e4[q] = cexp_(a2[q], bv[q], 4.f, 0.f);
      e128[q] = cexp_(a2[q], bv[q], 128.f, 0.f);
      u[q] = cexp_(a2[q], bv[q], 1024.f, m0[q]);
      u2[q] = cexp_(a2[q], bv[q], 1536.f, m0[q]);
    }
#pragma unroll
    for (int r = 2; r < 4; ++r) {
      cf c0 = cexp_(a2[0], bv[0], 512.f + 128.f * r, m0[0]), c1 = cexp_(a2[1], bv[1], 512.f + 128.f * r, m0[1]);
      put2(my + (24 + 2 * r) * RS, c0, c1);
      put2(my + (25 + 2 * r) * RS, cmul(c0, e4[0]), cmul(c1, e4[1]));
    }
#pragma unroll
    for (int r = 0; r < 4; ++r) {
      put2(my + (32 + r) * RS, u[0], u[1]);
      put2(my + (36 + r) * RS, u2[0], u2[1]);
#pragma unroll
      for (int q = 0; q < 2; ++q) { if (r < 3) u[q] = cmul(u[q], e128[q]); u2[q] = cmul(u2[q], e128[q]); }
    }
    put2(my + 40 * RS, u2[0], u2[1]);
  }
  __syncthreads();
  _Float16* kh = (_Float16*)(smem + 57 * RS);
  if (w < 3) {
    const char* ua = smem + (16 * w + i) * RS + g * 16;
    const char* va = smem + (41 + i) * RS + g * 16;
    f32x4 acc = {0.f, 0.f, 0.f, 0.f};
#pragma unroll
    for (int ks = 0; ks < 8; ++ks)
      acc = mfma_f16(*(const u32x4*)(ua + ks * 64), *(const u32x4*)(va + ks * 64), acc);
    int idx0, st;
    if (w == 0) { idx0 = (g < 2 ? 4 * g : 120 + 4 * g) + 8 * i; st = 1; }
    else if (w == 1) { idx0 = g < 2 ? 256 + 64 * g + 4 * i : 384 + 64 * (g - 2) + 2 * i; st = 1; }
    else { idx0 = 512 + 64 * g + i; st = 16; }
#pragma unroll
    for (int rg = 0; rg < 4; ++rg) {
      int idx = idx0 + st * rg;
      if (w == 1 && g >= 2) idx = idx0 + 32 * (rg >> 1) + (rg & 1);
      if (w == 2 && g >= 2) idx = (g == 2 && rg == 0 && i == 0) ? 640 : 644;
      kh[idx] = (_Float16)acc[rg];
    }
  }
  __syncthreads();
  const unsigned short* ku = (const unsigned short*)kh;
  const size_t kout = WS_KREV + (size_t)d * KREV * 2;
  {
    const int l0 = 2040 - 8 * tid;
    u32x4 o;
    if (w < 2) {
      const int q = l0 >> 3;
      const unsigned a = ku[384 + q], b = ku[385 + q];
      const unsigned t = (q & 1) ? b : a;
      o[0] = b | (b << 16); o[1] = b | (t << 16); o[2] = a | (a << 16); o[3] = o[2];
    } else if (w == 2) {
      const int q = l0 >> 2;
      const unsigned a = ku[256 + q], b = ku[257 + q], c = ku[258 + q];
      o[0] = c | (c << 16); o[1] = b | (b << 16); o[2] = b | (a << 16); o[3] = a | (a << 16);
    } else {
      unsigned v[8];
#pragma unroll
      for (int e = 0; e < 8; ++e) {
        const int l = l0 + 7 - e;
        const int idx = l < 256 ? l : (int)(pk_bf((float)l, 0.f) & 0xffffu) - 17024;
        v[e] = ku[idx];
      }
#pragma unroll
      for (int q = 0; q < 4; ++q) o[q] = v[2 * q] | (v[2 * q + 1] << 16);
    }
    st16(ws_rsrc(p.ws), kout + 16 * tid, o);
  }
}

__global__ NOPK __launch_bounds__(256) void k1(K1Args p) {
  __shared__ __attribute__((aligned(16))) char smem[57 * KG_RS + 1312];
  int b = blockIdx.x;
  TS(0, p.ws);
  if (b < NB_PF) { k1_prefetch(); return; }
  b -= NB_PF;
  if (b < NB_K) { k1_kgen(p, b, smem); return; }
  b -= NB_K;
  asm volatile("" :: "s"(p.ws), "s"(p.x));
  __builtin_amdgcn_s_sleep(4);
  if (b < NB_LN) k1_ln(p, b, smem);
  else k1_mod(p, b - NB_LN, smem);
}

constexpr int K2_CS = 4160;
constexpr int K2_KOFF = 4 * K2_CS;
constexpr int K2_CH = K2_KOFF + 4608;
constexpr int CV_RED = 3 * K2_CH;
constexpr int CV_RT2 = 86016;
constexpr int CV_SUM = CV_RT2 + 4 * (32 * 176 + 256);
constexpr int CV_XCH = CV_SUM + 2560;
constexpr int CV_FL = CV_RED + 20480;

struct Chan { float sa, ca1, ga, dsk, hs, hinv, yinv; };
struct ChanRaw { ModP msa, mca, mga; float kinv; unsigned short rds; };
DEV ChanRaw chan_ld(const unsigned short* dskip, const char* ws, int d) {
  ChanRaw c;
  c.rds = dskip[d];
  c.msa = mod_ld(ws, d); c.mca = mod_ld(ws, D_ + d); c.mga = mod_ld(ws, 2 * D_ + d);
  c.kinv = ((const float*)(ws + WS_KSC))[d];
  return c;
}
DEV Chan chan_fin(const ChanRaw& r) {
  Chan c;
  c.sa = mod_fin(r.msa); c.ca1 = rbf(1.f + mod_fin(r.mca)); c.ga = mod_fin(r.mga);
  c.dsk = __uint_as_float((unsigned)r.rds << 16);
  const float hb = 26.f * fabsf(c.ca1) + fabsf(c.sa);
  int ex = (int)((__float_as_uint(hb) >> 23) & 0xff);
  ex = ex < 20 ? 20 : (ex > 240 ? 240 : ex);
  c.hs = __uint_as_float((unsigned)(127 + 14 + 127 - ex) << 23);
  c.hinv = __uint_as_float((unsigned)(ex - 14) << 23);
  c.yinv = r.kinv * c.hinv;
  return c;
}
DEV size_t tl_off(int d, int t) { return (((size_t)(t >> 5) * D_ + d) * 32 + (t & 31)) * 2; }
struct StageRaw { u32x4 xa0, xa1, kv0, kv1; u32x2 xb; };
DEV StageRaw stage_ld(const char* ws, int d, int th) {
  StageRaw s;
  const unsigned short* krev = (const unsigned short*)(ws + WS_KREV) + (size_t)d * KREV;
  const size_t xo = tl_off(d, 16 * th);
  s.xa0 = *(const u32x4*)(ws + WS_XNT + xo); s.xa1 = *(const u32x4*)(ws + WS_XNT + xo + 16);
  s.xb = *(const u32x2*)(ws + WS_XNT + tl_off(d, min(16 * th + 16, L_ - 4)));
  s.kv0 = *(const u32x4*)(krev + 8 * th); s.kv1 = *(const u32x4*)(krev + 1024 + 8 * th);
  return s;
}
DEV void stage_wr(const StageRaw& s, const Chan& c, char* smem, int th) {
  constexpr int CS = K2_CS, KOFF = K2_KOFF;
  unsigned hh[10];
#pragma unroll
  for (int q = 0; q < 10; ++q) {
    unsigned u = q < 4 ? s.xa0[q] : (q < 8 ? s.xa1[q - 4] : s.xb[q - 8]);
    unsigned m = pk_bf(bf_lo(u) * c.ca1, bf_hi(u) * c.ca1);
    unsigned h = pk_bf(bf_lo(m) + c.sa, bf_hi(m) + c.sa);
    hh[q] = pk_h(bf_lo(h) * c.hs, bf_hi(h) * c.hs);
  }
  char* dst = smem + (2 * th + 2) * 16;
  unsigned s1[9];
#pragma unroll
  for (int q = 0; q < 9; ++q) s1[q] = __builtin_amdgcn_alignbit(hh[q + 1], hh[q], 16);
#pragma unroll
  for (int e = 0; e < 2; ++e) {
    *(u32x4*)(dst + 16 * e) = (u32x4){hh[4 * e], hh[4 * e + 1], hh[4 * e + 2], hh[4 * e + 3]};
    *(u32x4*)(dst + 16 * e + CS) = (u32x4){s1[4 * e], s1[4 * e + 1], s1[4 * e + 2], s1[4 * e + 3]};
    *(u32x4*)(dst + 16 * e + 2 * CS) = (u32x4){hh[4 * e + 1], hh[4 * e + 2], hh[4 * e + 3], hh[4 * e + 4]};
    *(u32x4*)(dst + 16 * e + 3 * CS) = (u32x4){s1[4 * e + 1], s1[4 * e + 2], s1[4 * e + 3], s1[4 * e + 4]};
  }
  if (th == 0) {
    u32x4 z = {0u, 0u, 0u, 0u};
    *(u32x4*)(smem) = z; *(u32x4*)(smem + CS) = z; *(u32x4*)(smem + 2 * CS) = z; *(u32x4*)(smem + 3 * CS) = z;
    *(u32x4*)(smem + 16) = z;
    *(u32x4*)(smem + CS + 16) = (u32x4){0u, 0u, 0u, hh[0] << 16};
    *(u32x4*)(smem + 2 * CS + 16) = (u32x4){0u, 0u, 0u, hh[0]};
    *(u32x4*)(smem + 3 * CS + 16) = (u32x4){0u, 0u, hh[0] << 16, s1[0]};
  }
  *(u32x4*)(smem + KOFF + 16 * th) = s.kv0;
  *(u32x4*)(smem + KOFF + 2048 + 16 * th) = s.kv1;
  if (th < 32) *(u32x4*)(smem + KOFF + 4096 + 16 * th) = (u32x4){0u, 0u, 0u, 0u};
}
DEV u32x2 conv_out(const f32x4& y, const Chan& c, const char* smem, int t, const u32x2& xres) {
  const u32x2 hv = *(const u32x2*)(smem + 2 * (t + 16));
  u32x2 o;
#pragma unroll
  for (int e = 0; e < 2; ++e) {
    const unsigned hu = hv[e];
    const h16x2 h2 = __builtin_bit_cast(h16x2, hu);
    const float h0 = (float)h2[0] * c.hinv, h1 = (float)h2[1] * c.hinv;
    unsigned yb = pk_bf(y[2 * e] * c.yinv, y[2 * e + 1] * c.yinv);
    unsigned hd = pk_bf(h0 * c.dsk, h1 * c.dsk);
    unsigned y2 = pk_bf(bf_lo(yb) + bf_lo(hd), bf_hi(yb) + bf_hi(hd));
    unsigned gy = pk_bf(bf_lo(y2) * c.ga, bf_hi(y2) * c.ga);
    o[e] = pk_bf(bf_lo(xres[e]) + bf_lo(gy), bf_hi(xres[e]) + bf_hi(gy));
  }
  return o;
}
#define K2_LOAD(buf, cc, u)                                                                                     \
  asm volatile("ds_read_b64 %0, %3 offset:%5\n\tds_read_b64 %1, %3 offset:%6\n\tds_read_b128 %2, %4 offset:%7"   \
               : "=&v"(Al[buf][u]), "=&v"(Ah[buf][u]), "=&v"(Bf[buf][u])                                          \
               : "v"(ap0), "v"(bp0), "i"(64 * (cc) + 512 * (u)), "i"(64 * (cc) + 512 * (u) + 8),                 \
                 "i"(64 * (cc) + 3584 - 512 * (u)))
template <int SET> DEV constexpr int half_tile(int j) {
  return SET == 0 ? (j == 0 ? 7 : (j == 1 ? 6 : 2)) : (j < 3 ? 5 - j : 4 - j);
}
template <int SET>
DEV void conv_half(unsigned ap0, unsigned bp0, char* redw) {
  constexpr int NU = SET == 0 ? 8 : 6, NT = SET == 0 ? 3 : 5;
  u32x2 Al[2][NU], Ah[2][NU];
  u32x4 Bf[2][NU];
#pragma unroll
  for (int u = 0; u < NU; ++u) K2_LOAD(0, 0, u);
  f32x4 acc[NT];
#pragma unroll
  for (int j = 0; j < NT; ++j) acc[j] = (f32x4){0.f, 0.f, 0.f, 0.f};
#pragma unroll
  for (int cc = 0; cc < 2; ++cc) {
    lgkm0();
    if (cc == 0) {
#pragma unroll
      for (int u = 0; u < NU; ++u) K2_LOAD(1, 1, u);
    }
#pragma unroll
    for (int sp = 0; sp < NU; ++sp)
#pragma unroll
      for (int j = 0; j < NT; ++j) {
        const int q = half_tile<SET>(j);
        if (sp <= q) {
          const u32x4 A = {Al[cc][sp][0], Al[cc][sp][1], Ah[cc][sp][0], Ah[cc][sp][1]};
          acc[j] = mfma_f16(A, Bf[cc][q - sp], acc[j]);
        }
      }
    SB();
  }
#pragma unroll
  for (int j = 0; j < NT; ++j) *(f32x4*)(redw + j * 4096) = acc[j];
}

template <int NF>
DEV void w1_fin(const RtRaw<NF>& t, const ModP& mp, char* ws, int kk, int ct0, int lane, char* scr, float* sums) {
  const int g = lane >> 4, i = lane & 15;
  float* tb = (float*)(scr + 32 * RT_STR);
  const float mv = mod_fin(mp);
  tb[lane] = lane < 32 ? rbf(1.f + mv) : mv;
  rt_park<NF>(t, scr, lane);
  lgkm0();
  const f32x4 ca = *(const f32x4*)(tb + 8 * g), cb = *(const f32x4*)(tb + 8 * g + 4);
  const f32x4 sa = *(const f32x4*)(tb + 32 + 8 * g), sb = *(const f32x4*)(tb + 36 + 8 * g);
  const u32x4 sv = {pk_bf(sa[0], sa[1]), pk_bf(sa[2], sa[3]), pk_bf(sb[0], sb[1]), pk_bf(sb[2], sb[3])};
  const u32x4 ones = {0x3f803f80u, 0x3f803f80u, 0x3f803f80u, 0x3f803f80u};
  const f32x4 z = {0.f, 0.f, 0.f, 0.f};
  const rsrc_t rs = ws_rsrc(ws);
#pragma unroll
  for (int j = 0; j < NF; ++j) {
    const u32x4 f = rt_frag(scr, j, lane);
    const u32x4 sc = {pk_bf(bf_lo(f[0]) * ca[0], bf_hi(f[0]) * ca[1]), pk_bf(bf_lo(f[1]) * ca[2], bf_hi(f[1]) * ca[3]),
                      pk_bf(bf_lo(f[2]) * cb[0], bf_hi(f[2]) * cb[1]), pk_bf(bf_lo(f[3]) * cb[2], bf_hi(f[3]) * cb[3])};
    st16(rs, WS_W1F + ((size_t)(kk * 108 + ct0 + j) * 64 + lane) * 16, sc);
    const f32x4 cs = mfma_bf16(sc, ones, z), b1 = mfma_bf16(f, sv, z);
    if (i < 2) *(f32x4*)(sums + i * 80 + 16 * j + 4 * g) = i ? b1 : cs;
  }
}

DEV void conv_phase(const unsigned short* __restrict__ dskip, char* __restrict__ ws,
                    const unsigned short* __restrict__ w1, const unsigned short* __restrict__ w2, char* smem_all) {
  constexpr int CS = K2_CS, KOFF = K2_KOFF;
  const int tid = threadIdx.x, lane = tid & 63;
  const int w = __builtin_amdgcn_readfirstlane(tid >> 6);
  const int bx = blockIdx.x;
  const int pj = (bx & 7) * 16 + (bx >> 4), par = (bx >> 3) & 1;
  const int r = lane & 15, g = lane >> 4, th = tid & 127;
  const int s4 = (r + 1) & 3;
  const rsrc_t rs = ws_rsrc(ws);
  unsigned* fl = (unsigned*)(smem_all + CV_FL);
  if (tid < 6) fl[tid] = 0u;
  if (w < 4) {
    const int d = 5 * pj + 2 * par + (w >> 1), hw = w & 1;
    char* smem = smem_all + (w >> 1) * K2_CH;
    const StageRaw sr = stage_ld(ws, d, th);
    const ChanRaw cr = chan_ld(dskip, ws, d);
    u32x2 xres[4];
#pragma unroll
    for (int q = 0; q < 4; ++q)
      xres[q] = *(const u32x2*)(ws + WS_XT + tl_off(d, 256 * (4 * hw + q) + 16 * r + 4 * g));
    SB();
    const Chan c = chan_fin(cr);
    stage_wr(sr, c, smem, th);
    __syncthreads();
    f32x4 acc[8];
#pragma unroll
    for (int q = 0; q < 8; ++q) acc[q] = (f32x4){0.f, 0.f, 0.f, 0.f};
    {
      const unsigned ap0 = (unsigned)(size_t)(smem + s4 * CS + 2 * (128 * hw + 8 * g + (r + 1) - s4));
      const unsigned bp0 = (unsigned)(size_t)(smem + KOFF + 2 * (2032 + 128 * hw - 16 * r + 8 * g) - 3584);
      u32x2 Al[2][8], Ah[2][8];
      u32x4 Bf[2][8];
#pragma unroll
      for (int u = 0; u < 8; ++u) K2_LOAD(0, 0, u);
#pragma unroll
      for (int cc = 0; cc < 4; ++cc) {
        lgkm0();
#pragma unroll
        for (int q = 0; q < 8; ++q) {
          if (cc < 3) K2_LOAD((cc + 1) & 1, cc + 1, q);
#pragma unroll
          for (int sp = 0; sp <= q; ++sp) {
            const u32x4 A = {Al[cc & 1][sp][0], Al[cc & 1][sp][1], Ah[cc & 1][sp][0], Ah[cc & 1][sp][1]};
            acc[q] = mfma_f16(A, Bf[cc & 1][q - sp], acc[q]);
          }
          SB();
        }
      }
    }
    f32x4 keep[4];
    {
      f32x4* xw = (f32x4*)(smem_all + CV_XCH + w * 4096) + lane;
      if (hw) {
#pragma unroll
        for (int q = 0; q < 4; ++q) { xw[64 * q] = acc[q]; keep[q] = acc[4 + q]; }
      } else {
#pragma unroll
        for (int q = 0; q < 4; ++q) { xw[64 * q] = acc[4 + q]; keep[q] = acc[q]; }
      }
    }
    lds_post(fl + w, lane);
    lds_wait(fl + (w ^ 1), 1u);
    const f32x4* xrd = (const f32x4*)(smem_all + CV_XCH + (w ^ 1) * 4096) + lane;
#pragma unroll
    for (int qp = 0; qp < 2; ++qp) {
      u32x2 o[2];
#pragma unroll
      for (int h = 0; h < 2; ++h) {
        const int q = 2 * qp + h;
        o[h] = conv_out(keep[q] + xrd[64 * q], c, smem, 256 * (4 * hw + q) + 16 * r + 4 * g, xres[q]);
      }
      const auto s0 = __builtin_amdgcn_permlane16_swap(o[0][0], o[1][0], false, false);
      const auto s1 = __builtin_amdgcn_permlane16_swap(o[0][1], o[1][1], false, false);
      const int t = 256 * (4 * hw + 2 * qp + (g & 1)) + 16 * r + 8 * (g >> 1);
      st16(rs, WS_X1T + (((size_t)(t >> 6) * D_ + d) * 64 + (t & 63)) * 2, (u32x4){s0[0], s1[0], s0[1], s1[1]});
    }
  } else {
    __builtin_amdgcn_s_setprio(2);
    const int d = 5 * pj + 4, wq = w - 4;
    char* smem = smem_all + 2 * K2_CH;
    StageRaw sr;
    if (wq < 2) sr = stage_ld(ws, d, th);
    asm volatile("" ::: "memory");
    const ChanRaw cr = chan_ld(dskip, ws, d);
    const int tl0 = par ? (wq < 3 ? 5 - wq : 1) : (wq == 0 ? 7 : (wq == 1 ? 6 : 2)), tl1 = 0;
    const unsigned ap0 = (unsigned)(size_t)(smem + s4 * CS + 2 * (64 * wq + 8 * g + (r + 1) - s4));
    const unsigned bp0 = (unsigned)(size_t)(smem + KOFF + 2 * (2032 + 64 * wq - 16 * r + 8 * g) - 3584);
    char* redw = smem_all + CV_RED + wq * 1024 + lane * 16;
    u32x2 xres[2];
    xres[0] = *(const u32x2*)(ws + WS_XT + tl_off(d, 256 * tl0 + 16 * r + 4 * g));
    xres[1] = *(const u32x2*)(ws + WS_XT + tl_off(d, 256 * tl1 + 16 * r + 4 * g));
    SB();
    const Chan c = chan_fin(cr);
    if (wq < 2) {
      stage_wr(sr, c, smem, th);
    } else {
#define RT_DELAY 32
      __builtin_amdgcn_s_sleep(RT_DELAY);
    }
    const bool isw1 = bx < 110;
    const int b2 = min(max(bx - 110, 0), 107);
    const int jk = isw1 ? 4 * (bx / 22) + wq : b2 >> 1;
    const int jc = isw1 ? min(5 * (bx % 22), 103) : (b2 & 1) * 20 + 5 * wq;
    RtRaw<5> rt;
    rt_load<5>(rt, isw1 ? w1 : w2, isw1 ? FF : D_, jk, jc, lane);
    const ModP mp = mod_ld(ws, (lane < 32 ? 4 * D_ : 3 * D_ - 32) + 32 * min(jk, 19) + lane);
    SB();
    bar_lds();
    if (par) conv_half<1>(ap0, bp0, redw); else conv_half<0>(ap0, bp0, redw);
    lds_post(fl + 4, lane);
    char* scr = smem_all + CV_RT2 + wq * (32 * RT_STR + 256);
    if (isw1) {
      float* sums = (float*)(smem_all + CV_SUM);
      w1_fin<5>(rt, mp, ws, jk, jc, lane, scr, sums + 160 * wq);
      asm volatile("" ::: "memory");
      unsigned old = 0u;
      if (lane == 0) old = __hip_atomic_fetch_add(fl + 5, 1u, __ATOMIC_RELAXED, __HIP_MEMORY_SCOPE_WORKGROUP);
      asm volatile("" ::: "memory");
      if (__builtin_amdgcn_readfirstlane(old) == 3u && lane < 40) {
        const f32x4* sp = (const f32x4*)sums + lane;
        const f32x4 v = (sp[0] + sp[40]) + (sp[80] + sp[120]);
        st16(rs, WS_CSP + ((size_t)(2 * (bx / 22) + lane / 20) * FF + 16 * jc + 4 * (lane % 20)) * 4, __builtin_bit_cast(u32x4, v));
      }
    } else if (bx < 218) {
      rt_park<5>(rt, scr, lane);
      lgkm0();
#pragma unroll
      for (int j = 0; j < 5; ++j) st16(rs, WS_W2F + ((size_t)((jc + j) * 54 + jk) * 64 + lane) * 16, rt_frag(scr, j, lane));
    }
    lds_wait(fl + 4, 4u);
    const char* rd = smem_all + CV_RED + lane * 16;
#pragma unroll
    for (int u = 0; u < 2; ++u) {
      const bool on = u == 0 ? (par || wq < 3) : (par && wq == 0);
      if (on) {
        const int sl = u == 0 ? wq : 4, tl = u == 0 ? tl0 : tl1;
        const f32x4 y = (*(const f32x4*)(rd + sl * 4096) + *(const f32x4*)(rd + sl * 4096 + 1024))
                      + (*(const f32x4*)(rd + sl * 4096 + 2048) + *(const f32x4*)(rd + sl * 4096 + 3072));
        const int t = 256 * tl + 16 * r + 4 * g;
        st8(rs, WS_X1T + (((size_t)(t >> 6) * D_ + d) * 64 + (t & 63)) * 2, conv_out(y, c, smem, t, xres[u]));
      }
    }
  }
}

__global__ NOPK __launch_bounds__(512, 2) void k2(const unsigned short* __restrict__ dskip, char* __restrict__ ws,
                                                  const unsigned short* __restrict__ w1, const unsigned short* __restrict__ w2) {
  __shared__ __attribute__((aligned(16))) char smem[CV_XCH + 16384];
  TS(1, ws);
  conv_phase(dskip, ws, w1, w2, smem);
}

DEV u32x4 ld16_sc1(rsrc_t r, int voff, int soff) { return __builtin_amdgcn_raw_buffer_load_b128(r, voff, soff, 16); }
DEV u32x4 ld16(rsrc_t r, int voff, int soff) { return __builtin_amdgcn_raw_buffer_load_b128(r, voff, soff, 0); }
DEV u32x4 tr_frag(const char* p) {
  const u32x2 a = __builtin_bit_cast(u32x2, __builtin_amdgcn_ds_read_tr16_b64_v4i16((lds_s16x4*)p));
  const u32x2 b = __builtin_bit_cast(u32x2, __builtin_amdgcn_ds_read_tr16_b64_v4i16((lds_s16x4*)(p + 256)));
  return (u32x4){a[0], a[1], b[0], b[1]};
}

constexpr int KF_RS = 84;
constexpr int KF_RB = 8 * 16 * KF_RS * 4;
constexpr int KF_BIG = 2 * KF_RB;
constexpr int NW2L = 8, NW2R = 27;
constexpr int KF_TAB = KF_BIG;
constexpr int KF_PS = KF_TAB + 320;
constexpr int KF_XR = KF_PS + 512;
constexpr int KF_CNT = KF_XR + 64 * 176;
constexpr int KF_LDS = KF_CNT + 16;
constexpr int X_FIRST = 5, X_STEP = 4;
constexpr int W1_EARLY = 3, W1_MID = 7, W1_PRE = 10;
constexpr int W2_START = 6, W2_RATE = 2, W2_INL = 28;
constexpr int CS_STEP = 14, SIG_SPLIT = 4, H1_PRE = 9, H1_BATCH = 7;
constexpr unsigned LOCK_MASK = 0x55554;

DEV void kf_body(char* __restrict__ ws, unsigned short* __restrict__ out, char* smem) {
  char* xs = smem;
  float* tab = (float*)(smem + KF_TAB);
  float* ps = (float*)(smem + KF_PS);
  char* xr = smem + KF_XR;
  unsigned* lcnt = (unsigned*)(smem + KF_CNT);
  float* cst = (float*)(smem + 81920);
  const int tid = threadIdx.x, lane = tid & 63;
  const int w = __builtin_amdgcn_readfirstlane(tid >> 6);
  const int bx = blockIdx.x;
  const int ng = (bx & 1) * 4 + ((bx >> 3) & 3), mt = ((bx & 7) >> 1) * 8 + (bx >> 5);
  const int g = lane >> 4, i = lane & 15;
  const rsrc_t rs_ = ws_rsrc(ws);

  unsigned* flags = (unsigned*)(ws + WS_CNT) + mt * 128;
  const int ntl = ng < 6 ? 14 : 12;
  const int ctb = ng < 6 ? 14 * ng : 84 + 12 * (ng - 6);
  const bool two = w < ntl - 8;
  const bool full = w < 6;
  const int ks0 = w < 6 ? 7 * w : 42 + 6 * (w - 6);
  const int ct0 = ctb + (two ? 2 * w : (ntl - 8) + w);
  const int lo16 = lane * 16;
  const int w1s = (int)WS_W1F + ct0 * 1024;
  const int w2s = (int)WS_W2F + (ng * 5 * 54 + ks0) * 1024;

  u32x4 ring[32], wl[NW2L];
  const char* bb0 = xs + (8 * g + (i >> 2)) * 64 + ((g & 1) << 5) + (i & 3) * 8;
  const char* bb1 = xs + (8 * g + (i >> 2)) * 64 + (((g & 1) ^ 1) << 5) + (i & 3) * 8;
  auto w2_off = [&](int e) __attribute__((always_inline)) {
    const int s = e < 27 ? e / 4 : (e < 34 ? e - 27 : 6), c = e < 27 ? e % 4 : (e < 34 ? 4 : 3);
    const int sn = (s == 6 && !full) ? 5 : s;
    return w2s + (c * 54 + sn) * 1024;
  };

  auto gemm1 = [&](auto two_tag) __attribute__((always_inline)) {
    constexpr bool TWO = decltype(two_tag)::value;
    if (TWO) asm volatile("; two tiles" ::: "memory"); else asm volatile("; one tile" ::: "memory");
    u32x4 t[10];
    auto st_x = [&](int i0, int i1) __attribute__((always_inline)) {
#pragma unroll
      for (int it = i0; it < i1; ++it) {
        const int c = tid + 512 * it;
        const int row = c >> 3;
        *(u32x4*)(xs + ((c >> 2) & 1) * 40960 + row * 64 + (((c & 3) ^ ((row >> 2) & 2)) << 4)) = t[it];
      }
    };
#pragma unroll
    for (int it = 0; it < X_FIRST; ++it) t[it] = ld16(rs_, tid * 16, (int)WS_X1T + mt * 81920 + 8192 * it);
#pragma unroll
    for (int k = 0; k < W1_EARLY; ++k) {
      ring[2 * k] = ld16(rs_, lo16, w1s + k * 108 * 1024);
      if (TWO) ring[2 * k + 1] = ld16(rs_, lo16, w1s + (k * 108 + 1) * 1024);
    }
    SB();
    __builtin_amdgcn_s_barrier();
    SB();
#pragma unroll
    for (int k = W1_EARLY; k < W1_MID; ++k) {
      ring[2 * k] = ld16(rs_, lo16, w1s + k * 108 * 1024);
      if (TWO) ring[2 * k + 1] = ld16(rs_, lo16, w1s + (k * 108 + 1) * 1024);
    }
    SB();
    st_x(0, X_FIRST);
    SB();
    if (tid == 0) *lcnt = 0u;
    ModP gbp;
    if (!TWO) gbp = mod_ld(ws, 5 * D_ + 80 * ng + min((w & 1) * 64 + lane, 79));
    SB();
    __syncthreads();
    auto resid = [&]() __attribute__((always_inline)) {
      const int kb = (80 * ng) >> 5, tq = w & 3;
#pragma unroll
      for (int u = 0; u < 2; ++u) {
        const int kk = min(kb + 2 * (w >> 2) + u, 19);
        const u32x4 f = tr_frag(((tq & 1) ? bb1 : bb0) + (tq >> 1) * 40960 + kk * 2048);
        const unsigned dl = (unsigned)(32 * kk + 8 * g - 80 * ng);
        if (dl < 80u) *(u32x4*)(xr + (tq * 16 + i) * 176 + dl * 2) = f;
      }
    };
    const int hs = TWO ? 0 : (w & 1);
    const char* ba[4] = {bb0 + hs * 40960, bb1 + hs * 40960, bb0 + (hs ^ 1) * 40960, bb1 + (hs ^ 1) * 40960};
    const u32x4 ones = {0x3f803f80u, 0x3f803f80u, 0x3f803f80u, 0x3f803f80u};
    f32x4 acc[2][4];
#pragma unroll
    for (int c = 0; c < 2; ++c)
#pragma unroll
      for (int q = 0; q < 4; ++q) acc[c][q] = (f32x4){0.f, 0.f, 0.f, 0.f};
    u32x4 csp[5];
    const int cso = ((w & 1) * FF + 16 * ctb + 4 * min(lane, 4 * ntl - 1)) * 4;
    u32x4 bn[2][4];
#pragma unroll
    for (int q = 0; q < 4; ++q) bn[0][q] = tr_frag(ba[q]);
#pragma unroll
    for (int q = 0; q < 4; ++q) bn[1][q] = tr_frag(ba[q] + 2048);
#pragma unroll
    for (int kk = 0; kk < 20; ++kk) {
      if (kk == X_STEP) { st_x(X_FIRST, 10); SB(); }
      if (kk == 2 * X_FIRST - 2) { SB(); bar_lds(); SB(); }
      else if ((LOCK_MASK >> kk) & 1) { SB(); __builtin_amdgcn_s_barrier(); SB(); }
      if (kk == 2 * X_FIRST) { resid(); SB(); }
      auto gload = [&](int u) __attribute__((always_inline)) {
        {
          constexpr int CATCH = W1_PRE - W1_MID;
          const int k0 = kk == 0 ? W1_MID : (kk <= CATCH ? W1_MID + 2 * kk - 1 : kk + W1_PRE);
          const int nk = (kk >= 1 && kk <= CATCH) ? 2 : 1;
          const int kn = k0 + (nk == 2 ? (u >> 1) : 0);
          const bool on = nk == 2 || u >= 2;
          if (on && kn < 20) {
            if ((u & 1) == 0) ring[(2 * kn) % 32] = ld16(rs_, lo16, w1s + kn * 108 * 1024);
            else if (TWO) ring[(2 * kn + 1) % 32] = ld16(rs_, lo16, w1s + (kn * 108 + 1) * 1024);
          }
          if (kk == 0 && u < 2) {
#pragma unroll
            for (int it = X_FIRST + u * 3; it < (u ? 10 : (X_FIRST + 3 < 10 ? X_FIRST + 3 : 10)); ++it)
              t[it] = ld16(rs_, tid * 16, (int)WS_X1T + mt * 81920 + 8192 * it);
          }
        }
        if (kk >= W2_START && u < W2_RATE) {
          const int n = W2_RATE * (kk - W2_START) + u;
          if (n < NW2L) wl[n] = ld16(rs_, lo16, w2_off(NW2R + n));
          else if (n < W2_INL) ring[8 + n - NW2L] = ld16(rs_, lo16, w2_off(n - NW2L));
        }
      };
      const u32x4 a0 = ring[(2 * kk) % 32], a1 = ring[(2 * kk + 1) % 32];
#pragma unroll
      for (int q = 0; q < 4; ++q) {
        const u32x4 bq = bn[kk & 1][q];
        acc[0][q] = mfma_bf16(a0, bq, acc[0][q]);
        if (TWO) acc[1][q] = mfma_bf16(a1, bq, acc[1][q]);
        else if (q < 2) {
          acc[1][2 * q] = mfma_bf16(ones, bq, acc[1][2 * q]);
          acc[1][2 * q + 1] = mfma_bf16(bq, bq, acc[1][2 * q + 1]);
        }
        SB();
        if (kk < 18) bn[kk & 1][q] = tr_frag(ba[q] + (kk + 2) * 2048);
        gload(q);
        SB();
      }
      if (!TWO) {
        if (kk < 5) csp[kk] = ld16(rs_, cso, (int)WS_CSP + kk * 2 * FF * 4);
        if (kk == CS_STEP) {
          f32x4 t = __builtin_bit_cast(f32x4, csp[0]);
#pragma unroll
          for (int u = 1; u < 5; ++u) t += __builtin_bit_cast(f32x4, csp[u]);
          if (w >= 6 && lane < 56) *(f32x4*)(cst + (w & 1) * 224 + 4 * lane) = t;
        }
      }
      if (!TWO && kk == 12) { if ((w & 1) * 64 + lane < 80) tab[(w & 1) * 64 + lane] = mod_fin(gbp); }
      SB();
    }
    if (!TWO) {
#pragma unroll
      for (int h = 0; h < 2; ++h) {
        const f32x4 aq = acc[1][2 * h + 1];
        const float dq = (i & 2) ? ((i & 1) ? aq[3] : aq[2]) : ((i & 1) ? aq[1] : aq[0]);
        const int tk = (2 * hs + h) * 16 + i;
        if (g == (i >> 2)) {
          const float mean = acc[1][2 * h][0] * (1.f / D_);
          const float var = fmaxf(dq * (1.f / D_) - mean * mean, 0.f);
          const float sd = rbf(sqrtf(rbf(rbf(var) + 1e-5f)));
          const float rs = 1.f / sd;
          ps[tk] = rs;
          ps[64 + tk] = -rbf(mean) * rs;
        }
      }
    }
    __syncthreads();
    f32x4 csv[2], b1v[2];
#pragma unroll
    for (int c = 0; c < (TWO ? 2 : 1); ++c) {
      csv[c] = *(const f32x4*)(cst + 16 * (ct0 - ctb + c) + 4 * g);
      b1v[c] = *(const f32x4*)(cst + 224 + 16 * (ct0 - ctb + c) + 4 * g);
    }
#pragma unroll
    for (int qp = 0; qp < 2; ++qp) {
      u32x2 o[2][2];
#pragma unroll
      for (int h = 0; h < 2; ++h) {
        const int q = 2 * qp + h, tq = q ^ (2 * hs);
        const float rs = ps[tq * 16 + i], nm = ps[64 + tq * 16 + i];
#pragma unroll
        for (int c = 0; c < (TWO ? 2 : 1); ++c) {
          float v[4];
#pragma unroll
          for (int r = 0; r < 4; ++r) v[r] = fmaxf(fmaf(acc[c][q][r], rs, fmaf(nm, csv[c][r], b1v[c][r])), 0.f);
          o[c][h] = (u32x2){pk_bf(v[0], v[1]), pk_bf(v[2], v[3])};
        }
      }
      const int tl = ((2 * qp) ^ (2 * hs)) + (g & 1);
#pragma unroll
      for (int c = 0; c < (TWO ? 2 : 1); ++c) {
        const int ct = ct0 + c;
        const auto s0 = __builtin_amdgcn_permlane16_swap(o[c][0][0], o[c][1][0], false, false);
        const auto s1 = __builtin_amdgcn_permlane16_swap(o[c][0][1], o[c][1][1], false, false);
        const size_t off = (((size_t)(4 * mt + tl) * 54 + (ct >> 1)) * 64 + i + 16 * (2 * (ct & 1) + (g >> 1))) * 16;
        st16(rs_, WS_H1F + off, (u32x4){s0[0], s1[0], s0[1], s1[1]});
      }
    }
    SB();
    __builtin_amdgcn_s_barrier();
    SB();
    constexpr int E0 = W2_INL - NW2L;
    auto signal = [&]() __attribute__((always_inline)) {
      if (lane == 0) {
        if (__hip_atomic_fetch_add(lcnt, 1u, __ATOMIC_RELAXED, __HIP_MEMORY_SCOPE_WORKGROUP) == 7u)
          __hip_atomic_store(flags + 16 * ng, 1u, __ATOMIC_RELAXED, __HIP_MEMORY_SCOPE_AGENT);
      }
    };
    if (w >= SIG_SPLIT) { asm volatile("s_waitcnt vmcnt(0)" ::: "memory"); signal(); }
    SB();
#pragma unroll
    for (int e = E0; e < NW2R; ++e) ring[(8 + e) % 32] = ld16(rs_, lo16, w2_off(e));
    SB();
    if (w < SIG_SPLIT) { asm volatile("s_waitcnt vmcnt(%0)" :: "n"(NW2R - E0) : "memory"); signal(); }
    SB();
  };
  if (two) gemm1(std::true_type{});
  else gemm1(std::false_type{});
#pragma nounroll
  for (unsigned spin = 0; spin < (1u << 20); ++spin) {
    if (__hip_atomic_load(flags + 16 * w, __ATOMIC_RELAXED, __HIP_MEMORY_SCOPE_AGENT) != 0u) break;
    __builtin_amdgcn_s_sleep(1);
  }
  asm volatile("" ::: "memory");
  SB();

  const int h1s = (int)WS_H1F + (mt * 4 * 54 + ks0) * 1024;
  u32x4 hb[28];
  auto load_h1 = [&](int n) __attribute__((always_inline)) {
    const int q = n / 7, s = n % 7;
    const int sn = (s == 6 && !full) ? 5 : s;
    hb[n] = ld16_sc1(rs_, lo16, h1s + (q * 54 + sn) * 1024);
  };
#pragma unroll
  for (int n = 0; n < H1_PRE; ++n) {
    if (n == H1_BATCH) { SB(); __builtin_amdgcn_s_barrier(); SB(); }
    load_h1(n);
  }
  SB();
  float* red = (float*)smem;
  float* rb_ = red + (w * 16 + i) * KF_RS + 4 * g;
  const int re = w * 40 + min(lane, 39);
  const int rtk = re / 20, rq4 = re % 20;
  const float* rr_ = red + rtk * KF_RS + 4 * rq4;
  f32x4 ac2[2][5];
  f32x4 rv[8], rsum;
  u32x2 xv2;
  const f32x4 gb = *(const f32x4*)(tab + 4 * rq4);
  auto red_wr = [&](int q, int c) __attribute__((always_inline)) {
    *(f32x4*)(rb_ + (q & 1) * (KF_RB / 4) + 16 * c) = ac2[q & 1][c];
  };
  auto red_rd = [&](int q, int ww) __attribute__((always_inline)) {
    rv[ww] = *(const f32x4*)(rr_ + (q & 1) * (KF_RB / 4) + ww * 16 * KF_RS);
  };
  auto red_x = [&](int q) __attribute__((always_inline)) { xv2 = *(const u32x2*)(xr + (16 * q + rtk) * 176 + rq4 * 8); };
  auto red_out = [&](int q) __attribute__((always_inline)) {
    const f32x4 s = rsum + (rv[6] + rv[7]);
    u32x2 o;
#pragma unroll
    for (int h = 0; h < 2; ++h) {
      unsigned f = pk_bf(s[2 * h], s[2 * h + 1]);
      unsigned gf = pk_bf(bf_lo(f) * gb[2 * h], bf_hi(f) * gb[2 * h + 1]);
      o[h] = pk_bf(bf_lo(xv2[h]) + bf_lo(gf), bf_hi(xv2[h]) + bf_hi(gf));
    }
    *(u32x2*)(out + (size_t)(mt * 64 + 16 * q + rtk) * D_ + ng * 80 + 4 * rq4) = o;
  };
#pragma unroll
  for (int q = 0; q < 3; ++q) {
#pragma unroll
    for (int c = 0; c < 5; ++c) ac2[q & 1][c] = (f32x4){0.f, 0.f, 0.f, 0.f};
#pragma unroll
    for (int s = 0; s < 7; ++s) {
      auto mm = [&](int c) __attribute__((always_inline)) {
        const u32x4 a = c == 4 ? wl[s] : ((s == 6 && c == 3) ? wl[7] : ring[(8 + 4 * s + c) % 32]);
        ac2[q & 1][c] = mfma_bf16(a, hb[7 * q + s], ac2[q & 1][c]);
      };
      if (s == 5 && q >= 2) {
        red_wr(q - 1, 4);
        SB();
        red_out(q - 2);
#pragma unroll
        for (int c = 0; c < 5; ++c) mm(c);
#pragma unroll
        for (int c = 0; c < 5; ++c) {
          __builtin_amdgcn_sched_group_barrier(0x008, 1, 0);
          __builtin_amdgcn_sched_group_barrier(0x002, 10, 0);
        }
        SB();
      } else if (s < 6) {
        mm(0); SB();
        if (q >= 1 && s >= 1) { red_wr(q - 1, s - 1); SB(); }
        mm(1); SB();
        if (q >= 2 && s < 4) { red_rd(q - 2, 2 * s); SB(); }
        mm(2); SB();
        if (q >= 2 && s < 4) { red_rd(q - 2, 2 * s + 1); SB(); }
        if (q >= 2 && s == 4) { red_x(q - 2); SB(); }
        mm(3); SB();
        if (q >= 2 && s >= 1 && s < 4) {
          const f32x4 t = rv[2 * s - 2] + rv[2 * s - 1];
          rsum = s == 1 ? t : rsum + t;
          asm volatile("" : "+v"(rsum));
          SB();
        }
        mm(4); SB();
      } else if (full) {
#pragma unroll
        for (int c = 0; c < 5; ++c) mm(c);
        SB();
      }
      if (7 * q + s + H1_PRE < 28) { load_h1(7 * q + s + H1_PRE); SB(); }
    }
    if (q >= 1) bar_lds();
  }
  {
    auto sum6 = [&]() __attribute__((always_inline)) {
      rsum = ((rv[0] + rv[1]) + (rv[2] + rv[3])) + (rv[4] + rv[5]);
    };
    if (full) {
#pragma unroll
      for (int c = 0; c < 5; ++c) {
        const u32x4 a = c == 4 ? wl[6] : (c == 3 ? wl[7] : ring[(8 + 24 + c) % 32]);
        rv[c] = mfma_bf16(a, hb[27], (f32x4){0.f, 0.f, 0.f, 0.f});
      }
    } else {
#pragma unroll
      for (int c = 0; c < 5; ++c) rv[c] = (f32x4){0.f, 0.f, 0.f, 0.f};
    }
    f32x4 c0[5];
#pragma unroll
    for (int c = 0; c < 5; ++c) c0[c] = rv[c];
    SB();
    auto col = [&](int c, auto&& between) __attribute__((always_inline)) {
#pragma unroll
      for (int s = 0; s < 6; ++s) {
        const u32x4 a = c == 4 ? wl[s] : ring[(8 + 4 * s + c) % 32];
        c0[c] = mfma_bf16(a, hb[21 + s], c0[c]);
        SB();
        between(s);
        SB();
      }
    };
    col(0, [&](int s) __attribute__((always_inline)) { if (s < 5) red_wr(2, s); if (s < 4) { red_rd(1, 2 * s); red_rd(1, 2 * s + 1); } if (s == 4) red_x(1); });
    col(1, [&](int s) __attribute__((always_inline)) { if (s == 1) { sum6(); asm volatile("" : "+v"(rsum)); } if (s == 3) red_out(1); });
    bar_lds();
    auto wr3 = [&](int c) __attribute__((always_inline)) { *(f32x4*)(rb_ + (KF_RB / 4) + 16 * c) = c0[c]; };
    col(2, [&](int s) __attribute__((always_inline)) { if (s < 2) wr3(s); if (s >= 1 && s < 5) { red_rd(2, 2 * s - 2); red_rd(2, 2 * s - 1); } if (s == 5) red_x(2); });
    col(3, [&](int s) __attribute__((always_inline)) { if (s == 1) wr3(2); if (s == 2) { sum6(); asm volatile("" : "+v"(rsum)); } if (s == 4) red_out(2); });
    col(4, [&](int s) __attribute__((always_inline)) { if (s == 1) wr3(3); if (s == 2) red_x(3); });
    wr3(4);
    bar_lds();
#pragma unroll
    for (int ww = 0; ww < 8; ++ww) red_rd(3, ww);
    sum6();
    red_out(3);
  }
}

__global__ NOPK __launch_bounds__(512, 2) void kf(char* __restrict__ ws, unsigned short* __restrict__ out) {
  __shared__ __attribute__((aligned(16))) char smem[KF_LDS];
  TS(2, ws);
  kf_body(ws, out, smem);
}

__global__ void kprobe(char* ws, int which) {
  const unsigned long long now = __builtin_amdgcn_s_memrealtime();
  const unsigned long long* t = (const unsigned long long*)(ws + (200u << 20));
  unsigned long long d = which == 0 ? t[1] - t[0] : (which == 1 ? t[2] - t[1] : (which == 2 ? now - t[2] : 0ull));
  if (d > 10000ull) d = 10000ull;
  while (__builtin_amdgcn_s_memrealtime() - now < 10ull * d) __builtin_amdgcn_s_sleep(1);
}
__global__ void kend_() {}

extern "C" void kernel_launch(void* const* d_in, const int* in_sizes, int n_in,
                              void* d_out, int out_size, void* d_ws, size_t ws_size,
                              hipStream_t stream) {
  K1Args a;
  a.x = (const unsigned short*)d_in[0];
  a.are = (const unsigned short*)d_in[1];
  a.aim = (const unsigned short*)d_in[2];
  a.bre = (const unsigned short*)d_in[3];
  a.bim = (const unsigned short*)d_in[4];
  a.cre = (const unsigned short*)d_in[5];
  a.cim = (const unsigned short*)d_in[6];
  a.logdt = (const unsigned short*)d_in[7];
  a.c = (const unsigned short*)d_in[12];
  a.wmod = (const unsigned short*)d_in[13];
  a.ws = (char*)d_ws;
  k1<<<NB_PF + NB_K + NB_LN + NB_MOD, 256, 0, stream>>>(a);
  k2<<<256, 512, 0, stream>>>((const unsigned short*)d_in[8], (char*)d_ws, (const unsigned short*)d_in[10],
                              (const unsigned short*)d_in[11]);
  kf<<<256, 512, 0, stream>>>((char*)d_ws, (unsigned short*)d_out);
  kprobe<<<1, 64, 0, stream>>>((char*)d_ws, 1);
}
```

```cpp
#include <hip/hip_runtime.h>
#include <hip/hip_bf16.h>
#include <stdint.h>
#include <type_traits>

typedef __attribute__((ext_vector_type(8))) __bf16 bf16x8;
typedef __attribute__((ext_vector_type(2))) __bf16 bf16x2;
typedef __attribute__((ext_vector_type(4))) float f32x4;
typedef __attribute__((ext_vector_type(2))) float f32x2;
typedef __attribute__((ext_vector_type(4))) unsigned u32x4;
typedef __attribute__((ext_vector_type(2))) unsigned u32x2;
typedef __attribute__((ext_vector_type(8))) _Float16 h16x8;
typedef __attribute__((ext_vector_type(2))) _Float16 h16x2;
typedef __attribute__((ext_vector_type(4))) short s16x4;
typedef __attribute__((address_space(3))) s16x4 lds_s16x4;

#if defined(__HIP_DEVICE_COMPILE__)
#define NOPK __attribute__((target("no-packed-fp32-ops")))
#else
#define NOPK
#endif
#define DEV __device__ __forceinline__

#define W1L 0
constexpr int W1KS = W1L ? 1 : 108, W1TS = W1L ? 20 : 1;
#define LNFLIP 0
constexpr int L_ = 2048;
constexpr int D_ = 640;
constexpr int NS = 128;
constexpr int FF = 1728;
constexpr int KREV = 2304;

constexpr size_t WS_GB   = 0;
constexpr size_t WS_KSC  = 8192;
constexpr size_t WS_CNT  = 16384;
constexpr size_t WS_MOD  = 65536;
constexpr size_t WS_CSP  = 196608;
constexpr size_t WS_XNT  = 327680;
constexpr size_t WS_XT   = WS_XNT + (size_t)D_ * L_ * 2;
constexpr size_t WS_KREV = WS_XT + (size_t)D_ * L_ * 2;
constexpr size_t WS_W1F  = WS_KREV + (size_t)D_ * KREV * 2;
constexpr size_t WS_W2F  = WS_W1F + (size_t)D_ * FF * 2;
constexpr size_t WS_X1T  = WS_W2F + (size_t)D_ * FF * 2;
constexpr size_t WS_H1F  = WS_X1T + (size_t)D_ * L_ * 2;
constexpr size_t WS_END  = WS_H1F + (size_t)L_ * FF * 2;

#define STAMP(kid, k) do {} while (0)
#define CSTAMP(kid, k) do {} while (0)
#define VM0() do {} while (0)

DEV float bf_lo(unsigned u) { return __uint_as_float(u << 16); }
DEV float bf_hi(unsigned u) { return __uint_as_float(u & 0xffff0000u); }
DEV unsigned pk_bf(float a, float b) {
  f32x2 v = {a, b};
  bf16x2 r = __builtin_convertvector(v, bf16x2);
  return __builtin_bit_cast(unsigned, r);
}
DEV float rbf(float a) { return bf_lo(pk_bf(a, 0.f)); }
DEV float ldbf(const unsigned short* p) { return __uint_as_float(((unsigned)*p) << 16); }
#define CONST_AS __attribute__((address_space(4)))
template <class T> DEV T ldu(const void* p) { return *(const CONST_AS T*)(size_t)p; }
DEV float ldbf_u(const unsigned short* p) {
  const unsigned v = ldu<unsigned>((const void*)((size_t)p & ~(size_t)3));
  return __uint_as_float(((size_t)p & 2) ? (v & 0xffff0000u) : (v << 16));
}

typedef __amdgpu_buffer_rsrc_t rsrc_t;
DEV rsrc_t ws_rsrc(char* ws) { return __builtin_amdgcn_make_buffer_rsrc((void*)ws, 0, (int)WS_END, 0x00020000); }
DEV void st16(rsrc_t r, size_t off, u32x4 v) { __builtin_amdgcn_raw_buffer_store_b128(v, r, (int)off, 0, 16); }
DEV void st4(char* ws, size_t off, unsigned v) { __hip_atomic_store((unsigned*)(ws + off), v, __ATOMIC_RELAXED, __HIP_MEMORY_SCOPE_AGENT); }
DEV void st8(rsrc_t r, size_t off, u32x2 v) { __builtin_amdgcn_raw_buffer_store_b64(v, r, (int)off, 0, 16); }

DEV void bar_lds() { asm volatile("s_waitcnt lgkmcnt(0)\n\ts_barrier" ::: "memory"); }
DEV void lds_wait(const void* word, unsigned val) {
  unsigned tv, ts, cnt;
  asm volatile("s_mov_b32 %2, 0x100000\n"
               "1:\n\tds_read_b32 %0, %3\n\ts_waitcnt lgkmcnt(0)\n\tv_readfirstlane_b32 %1, %0\n\t"
               "s_cmp_ge_u32 %1, %4\n\ts_cbranch_scc1 2f\n\ts_sleep 1\n\ts_sub_u32 %2, %2, 1\n\t"
               "s_cmp_lg_u32 %2, 0\n\ts_cbranch_scc1 1b\n2:"
               : "=&v"(tv), "=&s"(ts), "=&s"(cnt) : "v"((unsigned)(size_t)word), "s"(val) : "memory", "scc");
}
DEV void lds_post(unsigned* word, int lane) {
  asm volatile("" ::: "memory");
  if (lane == 0) __hip_atomic_fetch_add(word, 1u, __ATOMIC_RELAXED, __HIP_MEMORY_SCOPE_WORKGROUP);
  asm volatile("" ::: "memory");
}
DEV void lgkm0() { asm volatile("s_waitcnt lgkmcnt(0)" ::: "memory"); __builtin_amdgcn_sched_barrier(0); }

DEV f32x4 mfma_bf16(u32x4 a, u32x4 b, f32x4 c) {
  return __builtin_amdgcn_mfma_f32_16x16x32_bf16(__builtin_bit_cast(bf16x8, a), __builtin_bit_cast(bf16x8, b), c, 0, 0, 0);
}

constexpr int NB_PF = 8, NB_K = 640, NB_LN = 256, NB_MOD = 120;

struct K1Args {
  const unsigned short *x, *are, *aim, *bre, *bim, *cre, *cim, *logdt, *c, *wmod;
  char* ws;
};

DEV void k1_prefetch() {
  const int tid = threadIdx.x;
  unsigned long long pc;
  int o0, o1;
  asm volatile("s_getpc_b64 %0\n.Lpf_here%=:\n\ts_mov_b32 %1, _Z2k16K1Args - .Lpf_here%=\n\ts_mov_b32 %2, _Z5kend_v - .Lpf_here%="
               : "=s"(pc), "=s"(o0), "=s"(o1));
  const char* c0 = (const char*)(pc + (long long)o0);
  long n = o1 - o0;
  n = n < 0 ? 0 : (n > 98304 ? 98304 : n);
  unsigned v[3];
#pragma unroll
  for (int it = 0; it < 3; ++it) {
    const long o = (long)tid * 128 + it * 32768;
    v[it] = o < n ? *(const __attribute__((address_space(1))) unsigned*)(c0 + o) : 0u;
  }
  asm volatile("" :: "v"(v[0] ^ v[1] ^ v[2]));
}

DEV void k1_mod(const K1Args& p, int b, char* smem) {
  const int tid = threadIdx.x;
  const int rg = b & 7, cgp = b >> 3;
  const int c32 = tid & 31, r8 = tid >> 5;
  float* sil = (float*)smem;
  float* red = (float*)(smem + 512);
  char* stamp_ws = p.ws; (void)stamp_ws;
  STAMP(0, 1);
  u32x4 w[10];
  const float cv = ldbf(p.c + rg * 80 + min(tid, 79));
  asm volatile("" ::: "memory");
#pragma unroll
  for (int it = 0; it < 10; ++it)
    w[it] = *(const u32x4*)(p.wmod + (size_t)(rg * 80 + r8 + 8 * it) * 3840 + 256 * cgp + 8 * c32);
  if (tid < 80) sil[tid] = rbf(cv / (1.f + expf(-cv)));
  if (tid >= 128 && tid < 132 && b < 64) st4(p.ws, WS_CNT + (b * 4 + tid - 128) * 64, 0u);
  __syncthreads();
  STAMP(0, 2);
  float acc[8];
#pragma unroll
  for (int j = 0; j < 8; ++j) acc[j] = 0.f;
#pragma unroll
  for (int it = 0; it < 10; ++it) {
    float s = sil[r8 + 8 * it];
#pragma unroll
    for (int q = 0; q < 4; ++q) {
      acc[2 * q] += s * bf_lo(w[it][q]);
      acc[2 * q + 1] += s * bf_hi(w[it][q]);
    }
  }
#pragma unroll
  for (int j = 0; j < 8; ++j) acc[j] += __shfl_xor(acc[j], 32, 64);
  if ((tid & 63) < 32) {
    *(f32x4*)(red + (tid >> 6) * 256 + 8 * c32) = (f32x4){acc[0], acc[1], acc[2], acc[3]};
    *(f32x4*)(red + (tid >> 6) * 256 + 8 * c32 + 4) = (f32x4){acc[4], acc[5], acc[6], acc[7]};
  }
  __syncthreads();
  STAMP(0, 3);
  if (tid < 64) {
    const f32x4* r4 = (const f32x4*)red + tid;
    st16(ws_rsrc(p.ws), WS_MOD + (rg * 3840 + 256 * cgp + 4 * tid) * 4,
         __builtin_bit_cast(u32x4, (r4[0] + r4[64]) + (r4[128] + r4[192])));
  }
}
struct ModP { f32x4 a, b; };
DEV ModP mod_ld(const char* ws, int idx) {
  const float* mp = (const float*)(ws + WS_MOD) + idx;
  ModP m;
#pragma unroll
  for (int j = 0; j < 4; ++j) { m.a[j] = mp[3840 * j]; m.b[j] = mp[3840 * (4 + j)]; }
  return m;
}
DEV float mod_fin(const ModP& m) {
  return rbf(((m.a[0] + m.a[1]) + (m.a[2] + m.a[3])) + ((m.b[0] + m.b[1]) + (m.b[2] + m.b[3])));
}
#define SB() __builtin_amdgcn_sched_barrier(0)

DEV void k1_ln(const K1Args& p, int b, char* smem) {
  constexpr int STR = 704;
  const int tid = threadIdx.x;
  const int tok = tid >> 3, sub = tid & 7;
  const int tile = (b & 7) * 8 + ((b >> 3) & 7), pass = (b >> 6) ^ LNFLIP;
  const int hf = pass & 1;
  const bool norm = pass < 2;
  const int t0 = tile * 32;
  const unsigned short* xr = p.x + (size_t)(t0 + tok) * D_ + 8 * sub;
  u32x4 xv[5], xo[5];
  char* stamp_ws = p.ws; (void)stamp_ws;
  STAMP(0, 1);
#pragma unroll
  for (int m = 0; m < 5; ++m) xv[m] = *(const u32x4*)(xr + 320 * hf + 64 * m);
  if (norm) {
#pragma unroll
    for (int m = 0; m < 5; ++m) xo[m] = *(const u32x4*)(xr + 320 * (hf ^ 1) + 64 * m);
    float s = 0.f, v = 0.f;
    const bf16x2 one2 = __builtin_bit_cast(bf16x2, 0x3f803f80u);
#pragma unroll
    for (int m = 0; m < 10; ++m)
#pragma unroll
      for (int q = 0; q < 4; ++q) {
        const unsigned u = m < 5 ? xv[m][q] : xo[m - 5][q];
        const bf16x2 x2 = __builtin_bit_cast(bf16x2, u);
        s = __builtin_amdgcn_fdot2_f32_bf16(x2, one2, s, false);
        v = __builtin_amdgcn_fdot2_f32_bf16(x2, x2, v, false);
      }
#pragma unroll
    for (int o = 1; o < 8; o <<= 1) { s += __shfl_xor(s, o, 64); v += __shfl_xor(v, o, 64); }
    const float mean = s * (1.f / D_);
    const float var = fmaxf(v * (1.f / D_) - mean * mean, 0.f);
    const float sd = rbf(sqrtf(rbf(rbf(var) + 1e-5f)));
    const float rs = 1.f / sd;
    const float mb = rbf(mean);
#pragma unroll
    for (int m = 0; m < 5; ++m)
#pragma unroll
      for (int q = 0; q < 4; ++q) {
        unsigned d2 = pk_bf(bf_lo(xv[m][q]) - mb, bf_hi(xv[m][q]) - mb);
        xv[m][q] = pk_bf(bf_lo(d2) * rs, bf_hi(d2) * rs);
      }
  }
  const int lane = tid & 63, w = tid >> 6;
  const int g = lane >> 4, i = lane & 15;
  const rsrc_t rsrc = ws_rsrc(p.ws);
#pragma unroll
  for (int m = 0; m < 5; ++m) *(u32x4*)(smem + tok * STR + (sub + 8 * m) * 16) = xv[m];
  __syncthreads();
  STAMP(0, 2);
  const size_t dsto = (norm ? WS_XNT : WS_XT) + ((size_t)tile * D_ + 320 * hf) * 64;
#pragma unroll
  for (int it = 0; it < 5; ++it) {
    const int cb = w * 5 + it;
    const char* a = smem + (8 * g + (i >> 2)) * STR + cb * 32 + (i & 3) * 8;
    const u32x2 ra = __builtin_bit_cast(u32x2, __builtin_amdgcn_ds_read_tr16_b64_v4i16((lds_s16x4*)a));
    const u32x2 rb = __builtin_bit_cast(u32x2, __builtin_amdgcn_ds_read_tr16_b64_v4i16((lds_s16x4*)(a + 4 * STR)));
    st16(rsrc, dsto + (size_t)(cb * 16 + i) * 64 + 16 * g, (u32x4){ra[0], ra[1], rb[0], rb[1]});
  }
}

constexpr int RT_STR = 176;
template <int NF> struct RtRaw { u32x4 r[NF]; };
template <int NF>
DEV void rt_load(RtRaw<NF>& t, const unsigned short* W, int N, int kk, int ct0, int lane) {
#pragma unroll
  for (int j = 0; j < NF; ++j) {
    const int c = lane + 64 * j, row = c / (2 * NF), part = c % (2 * NF);
    t.r[j] = *(const u32x4*)(W + (size_t)(32 * kk + row) * N + 16 * ct0 + 8 * part);
  }
}
template <int NF>
DEV void rt_park(const RtRaw<NF>& t, char* scr, int lane) {
#pragma unroll
  for (int j = 0; j < NF; ++j) {
    const int c = lane + 64 * j, row = c / (2 * NF), part = c % (2 * NF);
    *(u32x4*)(scr + row * RT_STR + 16 * part) = t.r[j];
  }
}
DEV u32x4 rt_frag(const char* scr, int j, int lane) {
  const int g = lane >> 4, i = lane & 15;
  const char* p = scr + (8 * g + (i >> 2)) * RT_STR + 32 * j + (i & 3) * 8;
  const u32x2 a = __builtin_bit_cast(u32x2, __builtin_amdgcn_ds_read_tr16_b64_v4i16((lds_s16x4*)p));
  const u32x2 b = __builtin_bit_cast(u32x2, __builtin_amdgcn_ds_read_tr16_b64_v4i16((lds_s16x4*)(p + 4 * RT_STR)));
  return (u32x4){a[0], a[1], b[0], b[1]};
}
struct cf { float re, im; };
DEV cf cmul(cf a, cf b) {
  cf r;
  r.re = a.re * b.re - a.im * b.im;
  r.im = a.re * b.im + a.im * b.re;
  return r;
}
DEV cf cexp_(float a2, float br, float m, float m0) {
  float mag = __builtin_amdgcn_exp2f(a2 * (m - m0));
  float ph = __builtin_amdgcn_fractf(br * m);
  cf r; r.re = mag * __builtin_amdgcn_cosf(ph); r.im = mag * __builtin_amdgcn_sinf(ph);
  return r;
}
DEV unsigned pk_h(float a, float b) {
  f32x2 v = {a, b};
  h16x2 r = __builtin_convertvector(v, h16x2);
  return __builtin_bit_cast(unsigned, r);
}
DEV f32x4 mfma_f16(u32x4 a, u32x4 b, f32x4 c) {
  return __builtin_amdgcn_mfma_f32_16x16x32_f16(__builtin_bit_cast(h16x8, a), __builtin_bit_cast(h16x8, b), c, 0, 0, 0);
}
DEV void put2(char* p, cf a, cf b) {
  u32x2 o = {pk_h(a.re, a.im), pk_h(b.re, b.im)};
  *(u32x2*)p = o;
}

constexpr int KG_RS = 544;
DEV void k1_kgen(const K1Args& p, int d, char* smem) {
  constexpr int RS = KG_RS;
  const int tid = threadIdx.x;
  const int lane = tid & 63;
  const int w = __builtin_amdgcn_readfirstlane(tid >> 6);
  const int g = lane >> 4, i = lane & 15;
  const size_t po = (size_t)d * NS + 2 * lane;
  const unsigned rar = *(const unsigned*)(p.are + po), rai = *(const unsigned*)(p.aim + po);
  unsigned rbr = 0u, rbi = 0u, rcr = 0u, rci = 0u;
  if (w == 0) {
    rbr = *(const unsigned*)(p.bre + po); rbi = *(const unsigned*)(p.bim + po);
    rcr = *(const unsigned*)(p.cre + po); rci = *(const unsigned*)(p.cim + po);
  }
  const float ldt = ldbf_u(p.logdt + d);
  asm volatile("" ::: "memory");
  SB();
  char* stamp_ws = p.ws; (void)stamp_ws;
  const float dt = rbf(expf(ldt));
  asm volatile("" :: "v"(dt));
  STAMP(0, 1);
  float a2[2], bv[2], m0[2];
  a2[0] = (dt * bf_lo(rar)) * 1.44269504088896341f; a2[1] = (dt * bf_hi(rar)) * 1.44269504088896341f;
  bv[0] = (dt * bf_lo(rai)) * 0.15915494309189534f; bv[1] = (dt * bf_hi(rai)) * 0.15915494309189534f;
  m0[0] = a2[0] > 0.f ? 2048.f : 0.f; m0[1] = a2[1] > 0.f ? 2048.f : 0.f;
  char* my = smem + lane * 8;
  if (w == 0) {
    cf v[2], v8[2], e8[2];
    float bound = 0.f;
#pragma unroll
    for (int q = 0; q < 2; ++q) {
      const float dbr = dt * (q ? bf_hi(rbr) : bf_lo(rbr)), dbi = dt * (q ? bf_hi(rbi) : bf_lo(rbi));
      const float cr = q ? bf_hi(rcr) : bf_lo(rcr), ci = q ? bf_hi(rci) : bf_lo(rci);
      const float gsh = __builtin_amdgcn_exp2f(a2[q] * m0[q]);
      v[q].re = (cr * dbr - ci * dbi) * gsh; v[q].im = -(cr * dbi + ci * dbr) * gsh;
      bound += (fabsf(v[q].re) + fabsf(v[q].im)) * __builtin_amdgcn_exp2f(fmaxf(a2[q], 0.f) * 120.f);
      e8[q] = cexp_(a2[q], -bv[q], 8.f, 0.f);
      v8[q] = cexp_(a2[q], -bv[q], 64.f, 0.f);
    }
#pragma unroll
    for (int o = 1; o < 64; o <<= 1) bound += __shfl_xor(bound, o, 64);
    int ex = (int)((__float_as_uint(bound) >> 23) & 0xff);
    ex = ex < 20 ? 20 : (ex > 240 ? 240 : ex);
    const float sv = __uint_as_float((unsigned)(127 + 13 + 127 - ex) << 23);
    if (lane == 0) st4(p.ws, WS_KSC + 4 * d, (unsigned)(ex - 13) << 23);
#pragma unroll
    for (int q = 0; q < 2; ++q) { v[q].re *= sv; v[q].im *= sv; v8[q] = cmul(v[q], v8[q]); }
#pragma unroll
    for (int j = 0; j < 8; ++j) {
      put2(my + (41 + j) * RS, v[0], v[1]);
      put2(my + (49 + j) * RS, v8[0], v8[1]);
      if (j < 7) {
#pragma unroll
        for (int q = 0; q < 2; ++q) { v[q] = cmul(v[q], e8[q]); v8[q] = cmul(v8[q], e8[q]); }
      }
    }
  } else if (w == 1) {
    cf e1[2], u[2], u2[2];
#pragma unroll
    for (int q = 0; q < 2; ++q) {
      e1[q] = cexp_(a2[q], bv[q], 1.f, 0.f);
      u[q].re = __builtin_amdgcn_exp2f(-a2[q] * m0[q]); u[q].im = 0.f;
      u2[q] = cexp_(a2[q], bv[q], 128.f, m0[q]);
    }
#pragma unroll
    for (int r = 0; r < 8; ++r) {
      put2(my + r * RS, u[0], u[1]);
      put2(my + (8 + r) * RS, u2[0], u2[1]);
      if (r < 7) {
#pragma unroll
        for (int q = 0; q < 2; ++q) { u[q] = cmul(u[q], e1[q]); u2[q] = cmul(u2[q], e1[q]); }
      }
    }
  } else if (w == 2) {
    cf e2[2], u[2], u2[2];
#pragma unroll
    for (int q = 0; q < 2; ++q) {
      e2[q] = cexp_(a2[q], bv[q], 2.f, 0.f);
      u[q] = cexp_(a2[q], bv[q], 256.f, m0[q]);
      u2[q] = cexp_(a2[q], bv[q], 384.f, m0[q]);
    }
#pragma unroll
    for (int r = 0; r < 4; ++r) {
      put2(my + (16 + r) * RS, u[0], u[1]);
      put2(my + (20 + r) * RS, u2[0], u2[1]);
      if (r < 3) {
#pragma unroll
        for (int q = 0; q < 2; ++q) { u[q] = cmul(u[q], e2[q]); u2[q] = cmul(u2[q], e2[q]); }
      }
    }
    cf e4[2] = {cmul(e2[0], e2[0]), cmul(e2[1], e2[1])};
#pragma unroll
    for (int r = 0; r < 2; ++r) {
      cf c0 = cexp_(a2[0], bv[0], 512.f + 128.f * r, m0[0]), c1 = cexp_(a2[1], bv[1], 512.f + 128.f * r, m0[1]);
      put2(my + (24 + 2 * r) * RS, c0, c1);
      put2(my + (25 + 2 * r) * RS, cmul(c0, e4[0]), cmul(c1, e4[1]));
    }
  } else {
    cf e4[2], e128[2], u[2], u2[2];
#pragma unroll
    for (int q = 0; q < 2; ++q) {
      e4[q] = cexp_(a2[q], bv[q], 4.f, 0.f);
      e128[q] = cexp_(a2[q], bv[q], 128.f, 0.f);
      u[q] = cexp_(a2[q], bv[q], 1024.f, m0[q]);
      u2[q] = cexp_(a2[q], bv[q], 1536.f, m0[q]);
    }
#pragma unroll
    for (int r = 2; r < 4; ++r) {
      cf c0 = cexp_(a2[0], bv[0], 512.f + 128.f * r, m0[0]), c1 = cexp_(a2[1], bv[1], 512.f + 128.f * r, m0[1]);
      put2(my + (24 + 2 * r) * RS, c0, c1);
      put2(my + (25 + 2 * r) * RS, cmul(c0, e4[0]), cmul(c1, e4[1]));
    }
#pragma unroll
    for (int r = 0; r < 4; ++r) {
      put2(my + (32 + r) * RS, u[0], u[1]);
      put2(my + (36 + r) * RS, u2[0], u2[1]);
#pragma unroll
      for (int q = 0; q < 2; ++q) { if (r < 3) u[q] = cmul(u[q], e128[q]); u2[q] = cmul(u2[q], e128[q]); }
    }
    put2(my + 40 * RS, u2[0], u2[1]);
  }
  __syncthreads();
  STAMP(0, 2);
  _Float16* kh = (_Float16*)(smem + 57 * RS);
  if (w < 3) {
    const char* ua = smem + (16 * w + i) * RS + g * 16;
    const char* va = smem + (41 + i) * RS + g * 16;
    f32x4 acc = {0.f, 0.f, 0.f, 0.f};
#pragma unroll
    for (int ks = 0; ks < 8; ++ks)
      acc = mfma_f16(*(const u32x4*)(ua + ks * 64), *(const u32x4*)(va + ks * 64), acc);
    int idx0, st;
    if (w == 0) { idx0 = (g < 2 ? 4 * g : 120 + 4 * g) + 8 * i; st = 1; }
    else if (w == 1) { idx0 = g < 2 ? 256 + 64 * g + 4 * i : 384 + 64 * (g - 2) + 2 * i; st = 1; }
    else { idx0 = 512 + 64 * g + i; st = 16; }
#pragma unroll
    for (int rg = 0; rg < 4; ++rg) {
      int idx = idx0 + st * rg;
      if (w == 1 && g >= 2) idx = idx0 + 32 * (rg >> 1) + (rg & 1);
      if (w == 2 && g >= 2) idx = (g == 2 && rg == 0 && i == 0) ? 640 : 644;
      kh[idx] = (_Float16)acc[rg];
    }
  }
  __syncthreads();
  STAMP(0, 3);
  const unsigned short* ku = (const unsigned short*)kh;
  const size_t kout = WS_KREV + (size_t)d * KREV * 2;
  {
    const int l0 = 2040 - 8 * tid;
    u32x4 o;
    if (w < 2) {
      const int q = l0 >> 3;
      const unsigned a = ku[384 + q], b = ku[385 + q];
      const unsigned t = (q & 1) ? b : a;
      o[0] = b | (b << 16); o[1] = b | (t << 16); o[2] = a | (a << 16); o[3] = o[2];
    } else if (w == 2) {
      const int q = l0 >> 2;
      const unsigned a = ku[256 + q], b = ku[257 + q], c = ku[258 + q];
      o[0] = c | (c << 16); o[1] = b | (b << 16); o[2] = b | (a << 16); o[3] = a | (a << 16);
    } else {
      unsigned v[8];
#pragma unroll
      for (int e = 0; e < 8; ++e) {
        const int l = l0 + 7 - e;
        const int idx = l < 256 ? l : (int)(pk_bf((float)l, 0.f) & 0xffffu) - 17024;
        v[e] = ku[idx];
      }
#pragma unroll
      for (int q = 0; q < 4; ++q) o[q] = v[2 * q] | (v[2 * q + 1] << 16);
    }
    st16(ws_rsrc(p.ws), kout + 16 * tid, o);
  }
}

__global__ NOPK __launch_bounds__(256) void k1(K1Args p) {
  __shared__ __attribute__((aligned(16))) char smem[57 * KG_RS + 1312];
  int b = blockIdx.x;
  char* stamp_ws = p.ws; (void)stamp_ws;
  STAMP(0, 0);
  if (b < NB_PF) { k1_prefetch(); STAMP(0, 7); return; }
  b -= NB_PF;
  if (b < NB_K) { k1_kgen(p, b, smem); VM0(); STAMP(0, 7); return; }
  b -= NB_K;
  asm volatile("" :: "s"(p.ws), "s"(p.x));
#define DL_LN 4
#define DL_MOD 4
  if (b < NB_LN) { __builtin_amdgcn_s_sleep(DL_LN); k1_ln(p, b, smem); }
  else { __builtin_amdgcn_s_sleep(DL_MOD); k1_mod(p, b - NB_LN, smem); }
  VM0();
  STAMP(0, 7);
}

constexpr int K2_CS = 4160;
constexpr int K2_KOFF = 4 * K2_CS;
constexpr int K2_CH = K2_KOFF + 4608;
constexpr int CV_RED = 3 * K2_CH;
constexpr int CV_RT2 = 86016;
constexpr int CV_SUM = CV_RT2 + 4 * (32 * 176 + 256);
constexpr int CV_XCH = CV_SUM + 2560;
constexpr int CV_FL = CV_RED + 20480;

struct Chan { float sa, ca1, ga, dsk, hs, hinv, yinv; };
struct ChanRaw { float m, kinv, dsk; };
DEV ChanRaw chan_ld(const unsigned short* dskip, const char* ws, int d, int lane) {
  ChanRaw c;
  c.kinv = ldu<float>(ws + WS_KSC + 4 * d);
  c.dsk = ldbf_u(dskip + d);
  c.m = ((const float*)(ws + WS_MOD))[3840 * (lane & 7) + D_ * ((lane >> 3) & 3) + d];
  return c;
}
template <int CTRL> DEV float dpp_add(float v) {
  return v + __int_as_float(__builtin_amdgcn_update_dpp(0, __float_as_int(v), CTRL, 0xf, 0xf, true));
}
DEV Chan chan_fin(const ChanRaw& r) {
  Chan c;
  const int t = __float_as_int(rbf(dpp_add<0x141>(dpp_add<0x4e>(dpp_add<0xb1>(r.m)))));
  c.sa = __int_as_float(__builtin_amdgcn_readlane(t, 0));
  c.ca1 = rbf(1.f + __int_as_float(__builtin_amdgcn_readlane(t, 8)));
  c.ga = __int_as_float(__builtin_amdgcn_readlane(t, 16));
  c.dsk = r.dsk;
  const float hb = 26.f * fabsf(c.ca1) + fabsf(c.sa);
  int ex = (int)((__float_as_uint(hb) >> 23) & 0xff);
  ex = ex < 20 ? 20 : (ex > 240 ? 240 : ex);
  c.hs = __uint_as_float((unsigned)(127 + 14 + 127 - ex) << 23);
  c.hinv = __uint_as_float((unsigned)(ex - 14) << 23);
  c.yinv = r.kinv * c.hinv;
  return c;
}
DEV size_t tl_off(int d, int t) { return (((size_t)(t >> 5) * D_ + d) * 32 + (t & 31)) * 2; }
struct StageRaw { u32x4 xa0, xa1, kv0, kv1; u32x2 xb; };
DEV StageRaw stage_ld(const char* ws, int d, int th) {
  StageRaw s;
  const unsigned short* krev = (const unsigned short*)(ws + WS_KREV) + (size_t)d * KREV;
  const size_t xo = tl_off(d, 16 * th);
  s.xa0 = *(const u32x4*)(ws + WS_XNT + xo); s.xa1 = *(const u32x4*)(ws + WS_XNT + xo + 16);
  s.xb = *(const u32x2*)(ws + WS_XNT + tl_off(d, min(16 * th + 16, L_ - 4)));
  s.kv0 = *(const u32x4*)(krev + 8 * th); s.kv1 = *(const u32x4*)(krev + 1024 + 8 * th);
  return s;
}
DEV void stage_wr(const StageRaw& s, const Chan& c, char* smem, int th) {
  constexpr int CS = K2_CS, KOFF = K2_KOFF;
  unsigned hh[10];
#pragma unroll
  for (int q = 0; q < 10; ++q) {
    unsigned u = q < 4 ? s.xa0[q] : (q < 8 ? s.xa1[q - 4] : s.xb[q - 8]);
    unsigned m = pk_bf(bf_lo(u) * c.ca1, bf_hi(u) * c.ca1);
    unsigned h = pk_bf(bf_lo(m) + c.sa, bf_hi(m) + c.sa);
    hh[q] = pk_h(bf_lo(h) * c.hs, bf_hi(h) * c.hs);
  }
  char* dst = smem + (2 * th + 2) * 16;
  unsigned s1[9];
#pragma unroll
  for (int q = 0; q < 9; ++q) s1[q] = __builtin_amdgcn_alignbit(hh[q + 1], hh[q], 16);
#pragma unroll
  for (int e = 0; e < 2; ++e) {
    *(u32x4*)(dst + 16 * e) = (u32x4){hh[4 * e], hh[4 * e + 1], hh[4 * e + 2], hh[4 * e + 3]};
    *(u32x4*)(dst + 16 * e + CS) = (u32x4){s1[4 * e], s1[4 * e + 1], s1[4 * e + 2], s1[4 * e + 3]};
    *(u32x4*)(dst + 16 * e + 2 * CS) = (u32x4){hh[4 * e + 1], hh[4 * e + 2], hh[4 * e + 3], hh[4 * e + 4]};
    *(u32x4*)(dst + 16 * e + 3 * CS) = (u32x4){s1[4 * e + 1], s1[4 * e + 2], s1[4 * e + 3], s1[4 * e + 4]};
  }
  if (th == 0) {
    u32x4 z = {0u, 0u, 0u, 0u};
    *(u32x4*)(smem) = z; *(u32x4*)(smem + CS) = z; *(u32x4*)(smem + 2 * CS) = z; *(u32x4*)(smem + 3 * CS) = z;
    *(u32x4*)(smem + 16) = z;
    *(u32x4*)(smem + CS + 16) = (u32x4){0u, 0u, 0u, hh[0] << 16};
    *(u32x4*)(smem + 2 * CS + 16) = (u32x4){0u, 0u, 0u, hh[0]};
    *(u32x4*)(smem + 3 * CS + 16) = (u32x4){0u, 0u, hh[0] << 16, s1[0]};
  }
  *(u32x4*)(smem + KOFF + 16 * th) = s.kv0;
  *(u32x4*)(smem + KOFF + 2048 + 16 * th) = s.kv1;
  if (th < 32) *(u32x4*)(smem + KOFF + 4096 + 16 * th) = (u32x4){0u, 0u, 0u, 0u};
}
DEV unsigned add_bf(unsigned a, unsigned b) { return pk_bf(bf_lo(a) + bf_lo(b), bf_hi(a) + bf_hi(b)); }
DEV u32x2 conv_out(const f32x4& y, const Chan& c, const char* smem, int t) {
  const u32x2 hv = *(const u32x2*)(smem + 2 * (t + 16));
  u32x2 o;
#pragma unroll
  for (int e = 0; e < 2; ++e) {
    const unsigned hu = hv[e];
    const h16x2 h2 = __builtin_bit_cast(h16x2, hu);
    const float h0 = (float)h2[0] * c.hinv, h1 = (float)h2[1] * c.hinv;
    unsigned yb = pk_bf(y[2 * e] * c.yinv, y[2 * e + 1] * c.yinv);
    unsigned hd = pk_bf(h0 * c.dsk, h1 * c.dsk);
    unsigned y2 = pk_bf(bf_lo(yb) + bf_lo(hd), bf_hi(yb) + bf_hi(hd));
    o[e] = pk_bf(bf_lo(y2) * c.ga, bf_hi(y2) * c.ga);
  }
  return o;
}
#define K2_LOAD(buf, cc, u)                                                                                     \
  asm volatile("ds_read_b64 %0, %3 offset:%5\n\tds_read_b64 %1, %3 offset:%6\n\tds_read_b128 %2, %4 offset:%7"   \
               : "=&v"(Al[buf][u]), "=&v"(Ah[buf][u]), "=&v"(Bf[buf][u])                                          \
               : "v"(ap0), "v"(bp0), "i"(64 * (cc) + 512 * (u)), "i"(64 * (cc) + 512 * (u) + 8),                 \
                 "i"(64 * (cc) + 3584 - 512 * (u)))
template <int SET> DEV constexpr int half_tile(int j) {
  return SET == 0 ? (j == 0 ? 7 : (j == 1 ? 6 : 2)) : (j < 3 ? 5 - j : 4 - j);
}
template <int SET>
DEV void conv_half(unsigned ap0, unsigned bp0, char* redw) {
  constexpr int NU = SET == 0 ? 8 : 6, NT = SET == 0 ? 3 : 5;
  u32x2 Al[2][NU], Ah[2][NU];
  u32x4 Bf[2][NU];
#pragma unroll
  for (int u = 0; u < NU; ++u) K2_LOAD(0, 0, u);
  f32x4 acc[NT];
#pragma unroll
  for (int j = 0; j < NT; ++j) acc[j] = (f32x4){0.f, 0.f, 0.f, 0.f};
#pragma unroll
  for (int cc = 0; cc < 2; ++cc) {
    lgkm0();
    if (cc == 0) {
#pragma unroll
      for (int u = 0; u < NU; ++u) K2_LOAD(1, 1, u);
    }
#pragma unroll
    for (int sp = 0; sp < NU; ++sp)
#pragma unroll
      for (int j = 0; j < NT; ++j) {
        const int q = half_tile<SET>(j);
        if (sp <= q) {
          const u32x4 A = {Al[cc][sp][0], Al[cc][sp][1], Ah[cc][sp][0], Ah[cc][sp][1]};
          acc[j] = mfma_f16(A, Bf[cc][q - sp], acc[j]);
        }
      }
    SB();
  }
#pragma unroll
  for (int j = 0; j < NT; ++j) *(f32x4*)(redw + j * 4096) = acc[j];
}

template <int NF>
DEV void w1_fin(const RtRaw<NF>& t, const ModP& mp, char* ws, int kk, int ct0, int lane, char* scr, float* sums) {
  const int g = lane >> 4, i = lane & 15;
  float* tb = (float*)(scr + 32 * RT_STR);
  const float mv = mod_fin(mp);
  tb[lane] = lane < 32 ? rbf(1.f + mv) : mv;
  rt_park<NF>(t, scr, lane);
  lgkm0();
  const f32x4 ca = *(const f32x4*)(tb + 8 * g), cb = *(const f32x4*)(tb + 8 * g + 4);
  const f32x4 sa = *(const f32x4*)(tb + 32 + 8 * g), sb = *(const f32x4*)(tb + 36 + 8 * g);
  const u32x4 sv = {pk_bf(sa[0], sa[1]), pk_bf(sa[2], sa[3]), pk_bf(sb[0], sb[1]), pk_bf(sb[2], sb[3])};
  const u32x4 ones = {0x3f803f80u, 0x3f803f80u, 0x3f803f80u, 0x3f803f80u};
  const f32x4 z = {0.f, 0.f, 0.f, 0.f};
  const rsrc_t rs = ws_rsrc(ws);
#pragma unroll
  for (int j = 0; j < NF; ++j) {
    const u32x4 f = rt_frag(scr, j, lane);
    const u32x4 sc = {pk_bf(bf_lo(f[0]) * ca[0], bf_hi(f[0]) * ca[1]), pk_bf(bf_lo(f[1]) * ca[2], bf_hi(f[1]) * ca[3]),
                      pk_bf(bf_lo(f[2]) * cb[0], bf_hi(f[2]) * cb[1]), pk_bf(bf_lo(f[3]) * cb[2], bf_hi(f[3]) * cb[3])};
    st16(rs, WS_W1F + ((size_t)(kk * W1KS + (ct0 + j) * W1TS) * 64 + lane) * 16, sc);
    const f32x4 cs = mfma_bf16(sc, ones, z), b1 = mfma_bf16(f, sv, z);
    if (i < 2) *(f32x4*)(sums + i * 80 + 16 * j + 4 * g) = i ? b1 : cs;
  }
}

DEV void conv_phase(const unsigned short* __restrict__ dskip, char* __restrict__ ws,
                    const unsigned short* __restrict__ w1, const unsigned short* __restrict__ w2, char* smem_all) {
  constexpr int CS = K2_CS, KOFF = K2_KOFF;
  const int tid = threadIdx.x, lane = tid & 63;
  const int w = __builtin_amdgcn_readfirstlane(tid >> 6);
  const int bx = blockIdx.x;
  const int pj = (bx & 7) * 16 + (bx >> 4), par = (bx >> 3) & 1;
  const int r = lane & 15, g = lane >> 4, th = tid & 127;
  const int s4 = (r + 1) & 3;
  const rsrc_t rs = ws_rsrc(ws);
  unsigned* fl = (unsigned*)(smem_all + CV_FL);
  char* stamp_ws = ws; (void)stamp_ws;
  STAMP(1, 0);
  if (tid < 6) fl[tid] = 0u;
  if (w < 4) {
    const int d = 5 * pj + 2 * par + (w >> 1), hw = w & 1;
    char* smem = smem_all + (w >> 1) * K2_CH;
    const ChanRaw cr = chan_ld(dskip, ws, d, lane);
    SB();
    const StageRaw sr = stage_ld(ws, d, th);
    u32x4 xres[2];
    auto ld_res = [&]() __attribute__((always_inline)) {
#pragma unroll
      for (int qp = 0; qp < 2; ++qp)
        xres[qp] = *(const u32x4*)(ws + WS_XT + tl_off(d, 256 * (4 * hw + 2 * qp + (g & 1)) + 16 * r + 8 * (g >> 1)));
    };
    ld_res();
    SB();
    const Chan c = chan_fin(cr);
    STAMP(1, 1);
    stage_wr(sr, c, smem, th);
    __syncthreads();
    STAMP(1, 2);
    f32x4 acc[8];
#pragma unroll
    for (int q = 0; q < 8; ++q) acc[q] = (f32x4){0.f, 0.f, 0.f, 0.f};
    {
      const unsigned ap0 = (unsigned)(size_t)(smem + s4 * CS + 2 * (128 * hw + 8 * g + (r + 1) - s4));
      const unsigned bp0 = (unsigned)(size_t)(smem + KOFF + 2 * (2032 + 128 * hw - 16 * r + 8 * g) - 3584);
      u32x2 Al[2][8], Ah[2][8];
      u32x4 Bf[2][8];
#pragma unroll
      for (int u = 0; u < 8; ++u) K2_LOAD(0, 0, u);
#pragma unroll
      for (int cc = 0; cc < 4; ++cc) {
        lgkm0();
#pragma unroll
        for (int q = 0; q < 8; ++q) {
          if (cc < 3) K2_LOAD((cc + 1) & 1, cc + 1, q);
#pragma unroll
          for (int sp = 0; sp <= q; ++sp) {
            const u32x4 A = {Al[cc & 1][sp][0], Al[cc & 1][sp][1], Ah[cc & 1][sp][0], Ah[cc & 1][sp][1]};
            acc[q] = mfma_f16(A, Bf[cc & 1][q - sp], acc[q]);
          }
          SB();
        }
      }
    }
    STAMP(1, 3);
    f32x4 keep[4];
    {
      f32x4* xw = (f32x4*)(smem_all + CV_XCH + w * 4096) + lane;
      if (hw) {
#pragma unroll
        for (int q = 0; q < 4; ++q) { xw[64 * q] = acc[q]; keep[q] = acc[4 + q]; }
      } else {
#pragma unroll
        for (int q = 0; q < 4; ++q) { xw[64 * q] = acc[4 + q]; keep[q] = acc[q]; }
      }
    }
    lds_post(fl + w, lane);
    lds_wait(fl + (w ^ 1), 1u);
    STAMP(1, 4);
    const f32x4* xrd = (const f32x4*)(smem_all + CV_XCH + (w ^ 1) * 4096) + lane;
#pragma unroll
    for (int qp = 0; qp < 2; ++qp) {
      u32x2 o[2];
#pragma unroll
      for (int h = 0; h < 2; ++h) {
        const int q = 2 * qp + h;
        o[h] = conv_out(keep[q] + xrd[64 * q], c, smem, 256 * (4 * hw + q) + 16 * r + 4 * g);
      }
      const auto s0 = __builtin_amdgcn_permlane16_swap(o[0][0], o[1][0], false, false);
      const auto s1 = __builtin_amdgcn_permlane16_swap(o[0][1], o[1][1], false, false);
      const int t = 256 * (4 * hw + 2 * qp + (g & 1)) + 16 * r + 8 * (g >> 1);
      st16(rs, WS_X1T + (((size_t)(t >> 6) * D_ + d) * 64 + (t & 63)) * 2,
           (u32x4){add_bf(xres[qp][0], s0[0]), add_bf(xres[qp][1], s1[0]), add_bf(xres[qp][2], s0[1]), add_bf(xres[qp][3], s1[1])});
    }
  } else {
    __builtin_amdgcn_s_setprio(2);
    const int d = 5 * pj + 4, wq = w - 4;
    char* smem = smem_all + 2 * K2_CH;
    StageRaw sr;
    const ChanRaw cr = chan_ld(dskip, ws, d, lane);
    asm volatile("" ::: "memory");
    if (wq < 2) sr = stage_ld(ws, d, th);
    asm volatile("" ::: "memory");
    const int tl0 = par ? (wq < 3 ? 5 - wq : 1) : (wq == 0 ? 7 : (wq == 1 ? 6 : 2)), tl1 = 0;
    const unsigned ap0 = (unsigned)(size_t)(smem + s4 * CS + 2 * (64 * wq + 8 * g + (r + 1) - s4));
    const unsigned bp0 = (unsigned)(size_t)(smem + KOFF + 2 * (2032 + 64 * wq - 16 * r + 8 * g) - 3584);
    char* redw = smem_all + CV_RED + wq * 1024 + lane * 16;
    u32x2 xres[2];
    xres[0] = *(const u32x2*)(ws + WS_XT + tl_off(d, 256 * tl0 + 16 * r + 4 * g));
    xres[1] = *(const u32x2*)(ws + WS_XT + tl_off(d, 256 * tl1 + 16 * r + 4 * g));
    SB();
    const Chan c = chan_fin(cr);
    if (wq < 2) {
      stage_wr(sr, c, smem, th);
    } else {
#define RT_SLEEP 32
      __builtin_amdgcn_s_sleep(RT_SLEEP);
    }
    const bool isw1 = bx < 110;
    const int b2 = min(max(bx - 110, 0), 107);
    const int jk = isw1 ? 4 * (bx / 22) + wq : b2 >> 1;
    const int jc = isw1 ? min(5 * (bx % 22), 103) : (b2 & 1) * 20 + 5 * wq;
    RtRaw<5> rt;
    rt_load<5>(rt, isw1 ? w1 : w2, isw1 ? FF : D_, jk, jc, lane);
    const int gbi = 64 * (4 * (bx - 218) + wq) + lane;
    const bool isgb = bx >= 218 && 4 * (bx - 218) + wq < 10;
    ModP mp;
    SB();
    bar_lds();
    STAMP(1, 2);
    if (isw1 || isgb) mp = mod_ld(ws, isw1 ? (lane < 32 ? 4 * D_ : 3 * D_ - 32) + 32 * jk + lane : 5 * D_ + gbi);
    SB();
    if (par) conv_half<1>(ap0, bp0, redw); else conv_half<0>(ap0, bp0, redw);
    lds_post(fl + 4, lane);
    STAMP(1, 3);
    char* scr = smem_all + CV_RT2 + wq * (32 * RT_STR + 256);
    if (isw1) {
      float* sums = (float*)(smem_all + CV_SUM);
      w1_fin<5>(rt, mp, ws, jk, jc, lane, scr, sums + 160 * wq);
      asm volatile("" ::: "memory");
      unsigned old = 0u;
      if (lane == 0) old = __hip_atomic_fetch_add(fl + 5, 1u, __ATOMIC_RELAXED, __HIP_MEMORY_SCOPE_WORKGROUP);
      asm volatile("" ::: "memory");
      if (__builtin_amdgcn_readfirstlane(old) == 3u && lane < 40) {
        const f32x4* sp = (const f32x4*)sums + lane;
        const f32x4 v = (sp[0] + sp[40]) + (sp[80] + sp[120]);
        st16(rs, WS_CSP + ((size_t)(2 * (bx / 22) + lane / 20) * FF + 16 * jc + 4 * (lane % 20)) * 4, __builtin_bit_cast(u32x4, v));
      }
    } else if (bx < 218) {
      rt_park<5>(rt, scr, lane);
      lgkm0();
#pragma unroll
      for (int j = 0; j < 5; ++j) st16(rs, WS_W2F + ((size_t)((jc + j) * 54 + jk) * 64 + lane) * 16, rt_frag(scr, j, lane));
    } else if (isgb) {
      st4(ws, WS_GB + 4 * gbi, __float_as_uint(mod_fin(mp)));
    }
    STAMP(1, 4);
    lds_wait(fl + 4, 4u);
    STAMP(1, 5);
    const char* rd = smem_all + CV_RED + lane * 16;
#pragma unroll
    for (int u = 0; u < 2; ++u) {
      const bool on = u == 0 ? (par || wq < 3) : (par && wq == 0);
      if (on) {
        const int sl = u == 0 ? wq : 4, tl = u == 0 ? tl0 : tl1;
        const f32x4 y = (*(const f32x4*)(rd + sl * 4096) + *(const f32x4*)(rd + sl * 4096 + 1024))
                      + (*(const f32x4*)(rd + sl * 4096 + 2048) + *(const f32x4*)(rd + sl * 4096 + 3072));
        const int t = 256 * tl + 16 * r + 4 * g;
        const u32x2 gy = conv_out(y, c, smem, t);
        st8(rs, WS_X1T + (((size_t)(t >> 6) * D_ + d) * 64 + (t & 63)) * 2, (u32x2){add_bf(xres[u][0], gy[0]), add_bf(xres[u][1], gy[1])});
      }
    }
  }
}

__global__ NOPK __launch_bounds__(512, 2) void k2(const unsigned short* __restrict__ dskip, char* __restrict__ ws,
                                                  const unsigned short* __restrict__ w1, const unsigned short* __restrict__ w2) {
  __shared__ __attribute__((aligned(16))) char smem[CV_XCH + 16384];
  conv_phase(dskip, ws, w1, w2, smem);
  char* stamp_ws = ws; (void)stamp_ws;
  VM0();
  STAMP(1, 7);
}

DEV u32x4 ld16_sc1(rsrc_t r, int voff, int soff) { return __builtin_amdgcn_raw_buffer_load_b128(r, voff, soff, 16); }
DEV u32x4 ld16(rsrc_t r, int voff, int soff) { return __builtin_amdgcn_raw_buffer_load_b128(r, voff, soff, 0); }
DEV u32x4 tr_frag(const char* p) {
  const u32x2 a = __builtin_bit_cast(u32x2, __builtin_amdgcn_ds_read_tr16_b64_v4i16((lds_s16x4*)p));
  const u32x2 b = __builtin_bit_cast(u32x2, __builtin_amdgcn_ds_read_tr16_b64_v4i16((lds_s16x4*)(p + 256)));
  return (u32x4){a[0], a[1], b[0], b[1]};
}

constexpr int KF_RS = 84;
constexpr int KF_RB = 8 * 16 * KF_RS * 4;
constexpr int KF_BIG = 2 * KF_RB;
constexpr int NW2L = 8, NW2R = 27;
constexpr int KF_TAB = KF_BIG;
constexpr int KF_PS = KF_TAB + 320;
constexpr int KF_XR = KF_PS + 512;
constexpr int KF_CNT = KF_XR + 64 * 176;
constexpr int KF_LDS = KF_CNT + 16;
#define X_FIRST 5
#define X_STEP 4
#define W1_EARLY 3
#define W1_MID 7
#define W1_PRE 10
#define W2_START 6
#define W2_RATE 2
#define W2_INL 28
#define CS_STEP 14
#define SIG_SPLIT 4
#define H1_PRE 9
#define H1_BATCH 7
#define LOCK_MASK 0x55554u

DEV void kf_body(char* __restrict__ ws, unsigned short* __restrict__ out, char* smem) {
  char* xs = smem;
  float* tab = (float*)(smem + KF_TAB);
  float* ps = (float*)(smem + KF_PS);
  char* xr = smem + KF_XR;
  unsigned* lcnt = (unsigned*)(smem + KF_CNT);
  float* cst = (float*)(smem + 81920);
  const int tid = threadIdx.x, lane = tid & 63;
  const int w = __builtin_amdgcn_readfirstlane(tid >> 6);
  const int bx = blockIdx.x;
  const int ng = (bx & 1) * 4 + ((bx >> 3) & 3), mt = ((bx & 7) >> 1) * 8 + (bx >> 5);
  const int g = lane >> 4, i = lane & 15;
  const rsrc_t rs_ = ws_rsrc(ws);

  char* stamp_ws = ws; (void)stamp_ws;
  STAMP(2, 0);
  unsigned* flags = (unsigned*)(ws + WS_CNT) + mt * 128;
  const int ntl = ng < 6 ? 14 : 12;
  const int ctb = ng < 6 ? 14 * ng : 84 + 12 * (ng - 6);
  const bool two = w < ntl - 8;
  const bool full = w < 6;
  const int ks0 = w < 6 ? 7 * w : 42 + 6 * (w - 6);
  const int ct0 = ctb + (two ? 2 * w : (ntl - 8) + w);
  const int lo16 = lane * 16;
  const int w1s = (int)WS_W1F + ct0 * W1TS * 1024;
  const int w2s = (int)WS_W2F + (ng * 5 * 54 + ks0) * 1024;

  u32x4 ring[32], wl[NW2L];
  const char* bb0 = xs + (8 * g + (i >> 2)) * 64 + ((g & 1) << 5) + (i & 3) * 8;
  const char* bb1 = xs + (8 * g + (i >> 2)) * 64 + (((g & 1) ^ 1) << 5) + (i & 3) * 8;
  auto w2_off = [&](int e) __attribute__((always_inline)) {
    const int s = e < 27 ? e / 4 : (e < 34 ? e - 27 : 6), c = e < 27 ? e % 4 : (e < 34 ? 4 : 3);
    const int sn = (s == 6 && !full) ? 5 : s;
    return w2s + (c * 54 + sn) * 1024;
  };

  auto gemm1 = [&](auto two_tag) __attribute__((always_inline)) {
    constexpr bool TWO = decltype(two_tag)::value;
    if (TWO) asm volatile("; two tiles" ::: "memory"); else asm volatile("; one tile" ::: "memory");
    u32x4 t[10];
    auto st_x = [&](int i0, int i1) __attribute__((always_inline)) {
#pragma unroll
      for (int it = i0; it < i1; ++it) {
        const int c = tid + 512 * it;
        const int row = c >> 3;
        *(u32x4*)(xs + ((c >> 2) & 1) * 40960 + row * 64 + (((c & 3) ^ ((row >> 2) & 2)) << 4)) = t[it];
      }
    };
#pragma unroll
    for (int it = 0; it < X_FIRST; ++it) t[it] = ld16(rs_, tid * 16, (int)WS_X1T + mt * 81920 + 8192 * it);
#pragma unroll
    for (int k = 0; k < W1_EARLY; ++k) {
      ring[2 * k] = ld16(rs_, lo16, w1s + k * W1KS * 1024);
      if (TWO) ring[2 * k + 1] = ld16(rs_, lo16, w1s + (k * W1KS + W1TS) * 1024);
    }
    SB();
    __builtin_amdgcn_s_barrier();
    SB();
#pragma unroll
    for (int k = W1_EARLY; k < W1_MID; ++k) {
      ring[2 * k] = ld16(rs_, lo16, w1s + k * W1KS * 1024);
      if (TWO) ring[2 * k + 1] = ld16(rs_, lo16, w1s + (k * W1KS + W1TS) * 1024);
    }
    SB();
    st_x(0, X_FIRST);
    SB();
    if (tid == 0) *lcnt = 0u;
    float gbp;
    if (!TWO) gbp = ((const float*)(ws + WS_GB))[80 * ng + min((w & 1) * 64 + lane, 79)];
    SB();
    __syncthreads();
    STAMP(2, 1);
    auto resid = [&]() __attribute__((always_inline)) {
      const int kb = (80 * ng) >> 5, tq = w & 3;
#pragma unroll
      for (int u = 0; u < 2; ++u) {
        const int kk = min(kb + 2 * (w >> 2) + u, 19);
        const u32x4 f = tr_frag(((tq & 1) ? bb1 : bb0) + (tq >> 1) * 40960 + kk * 2048);
        const unsigned dl = (unsigned)(32 * kk + 8 * g - 80 * ng);
        if (dl < 80u) *(u32x4*)(xr + (tq * 16 + i) * 176 + dl * 2) = f;
      }
    };
    const int hs = TWO ? 0 : (w & 1);
    const char* ba[4] = {bb0 + hs * 40960, bb1 + hs * 40960, bb0 + (hs ^ 1) * 40960, bb1 + (hs ^ 1) * 40960};
    const u32x4 ones = {0x3f803f80u, 0x3f803f80u, 0x3f803f80u, 0x3f803f80u};
    f32x4 acc[2][4];
#pragma unroll
    for (int c = 0; c < 2; ++c)
#pragma unroll
      for (int q = 0; q < 4; ++q) acc[c][q] = (f32x4){0.f, 0.f, 0.f, 0.f};
    u32x4 csp[5];
    const int cso = ((w & 1) * FF + 16 * ctb + 4 * min(lane, 4 * ntl - 1)) * 4;
    u32x4 bn[2][4];
#pragma unroll
    for (int q = 0; q < 4; ++q) bn[0][q] = tr_frag(ba[q]);
#pragma unroll
    for (int q = 0; q < 4; ++q) bn[1][q] = tr_frag(ba[q] + 2048);
#pragma unroll
    for (int kk = 0; kk < 20; ++kk) {
      if (kk == X_STEP) { st_x(X_FIRST, 10); SB(); }
      if (kk == 2 * X_FIRST - 2) { SB(); bar_lds(); SB(); }
      else if ((LOCK_MASK >> kk) & 1) { SB(); __builtin_amdgcn_s_barrier(); SB(); }
      if (kk == 2 * X_FIRST) { resid(); SB(); }
      auto gload = [&](int u) __attribute__((always_inline)) {
        {
          constexpr int CATCH = W1_PRE - W1_MID;
          const int k0 = kk == 0 ? W1_MID : (kk <= CATCH ? W1_MID + 2 * kk - 1 : kk + W1_PRE);
          const int nk = (kk >= 1 && kk <= CATCH) ? 2 : 1;
          const int kn = k0 + (nk == 2 ? (u >> 1) : 0);
          const bool on = nk == 2 || u >= 2;
          if (on && kn < 20) {
            if ((u & 1) == 0) ring[(2 * kn) % 32] = ld16(rs_, lo16, w1s + kn * W1KS * 1024);
            else if (TWO) ring[(2 * kn + 1) % 32] = ld16(rs_, lo16, w1s + (kn * W1KS + W1TS) * 1024);
          }
          if (kk == 0 && u < 2) {
#pragma unroll
            for (int it = X_FIRST + u * 3; it < (u ? 10 : (X_FIRST + 3 < 10 ? X_FIRST + 3 : 10)); ++it)
              t[it] = ld16(rs_, tid * 16, (int)WS_X1T + mt * 81920 + 8192 * it);
          }
        }
        if (kk >= W2_START && u < W2_RATE) {
          const int n = W2_RATE * (kk - W2_START) + u;
          if (n < NW2L) wl[n] = ld16(rs_, lo16, w2_off(NW2R + n));
          else if (n < W2_INL) ring[8 + n - NW2L] = ld16(rs_, lo16, w2_off(n - NW2L));
        }
      };
      const u32x4 a0 = ring[(2 * kk) % 32], a1 = ring[(2 * kk + 1) % 32];
#pragma unroll
      for (int q = 0; q < 4; ++q) {
        const u32x4 bq = bn[kk & 1][q];
        acc[0][q] = mfma_bf16(a0, bq, acc[0][q]);
        if (TWO) acc[1][q] = mfma_bf16(a1, bq, acc[1][q]);
        else if (q < 2) {
          acc[1][2 * q] = mfma_bf16(ones, bq, acc[1][2 * q]);
          acc[1][2 * q + 1] = mfma_bf16(bq, bq, acc[1][2 * q + 1]);
        }
        SB();
        if (kk < 18) bn[kk & 1][q] = tr_frag(ba[q] + (kk + 2) * 2048);
        gload(q);
        SB();
      }
      if (!TWO) {
        if (kk < 5) csp[kk] = ld16(rs_, cso, (int)WS_CSP + kk * 2 * FF * 4);
        if (kk == CS_STEP) {
          f32x4 t = __builtin_bit_cast(f32x4, csp[0]);
#pragma unroll
          for (int u = 1; u < 5; ++u) t += __builtin_bit_cast(f32x4, csp[u]);
          if (w >= 6 && lane < 56) *(f32x4*)(cst + (w & 1) * 224 + 4 * lane) = t;
        }
      }
      if (!TWO && kk == 12) { if ((w & 1) * 64 + lane < 80) tab[(w & 1) * 64 + lane] = gbp; }
      SB();
    }
    STAMP(2, 2);
    if (!TWO) {
#pragma unroll
      for (int h = 0; h < 2; ++h) {
        const f32x4 aq = acc[1][2 * h + 1];
        const float dq = (i & 2) ? ((i & 1) ? aq[3] : aq[2]) : ((i & 1) ? aq[1] : aq[0]);
        const int tk = (2 * hs + h) * 16 + i;
        if (g == (i >> 2)) {
          const float mean = acc[1][2 * h][0] * (1.f / D_);
          const float var = fmaxf(dq * (1.f / D_) - mean * mean, 0.f);
          const float sd = rbf(sqrtf(rbf(rbf(var) + 1e-5f)));
          const float rs = 1.f / sd;
          ps[tk] = rs;
          ps[64 + tk] = -rbf(mean) * rs;
        }
      }
    }
    __syncthreads();
    f32x4 csv[2], b1v[2];
#pragma unroll
    for (int c = 0; c < (TWO ? 2 : 1); ++c) {
      csv[c] = *(const f32x4*)(cst + 16 * (ct0 - ctb + c) + 4 * g);
      b1v[c] = *(const f32x4*)(cst + 224 + 16 * (ct0 - ctb + c) + 4 * g);
    }
#pragma unroll
    for (int qp = 0; qp < 2; ++qp) {
      u32x2 o[2][2];
#pragma unroll
      for (int h = 0; h < 2; ++h) {
        const int q = 2 * qp + h, tq = q ^ (2 * hs);
        const float rs = ps[tq * 16 + i], nm = ps[64 + tq * 16 + i];
#pragma unroll
        for (int c = 0; c < (TWO ? 2 : 1); ++c) {
          float v[4];
#pragma unroll
          for (int r = 0; r < 4; ++r) v[r] = fmaxf(fmaf(acc[c][q][r], rs, fmaf(nm, csv[c][r], b1v[c][r])), 0.f);
          o[c][h] = (u32x2){pk_bf(v[0], v[1]), pk_bf(v[2], v[3])};
        }
      }
      const int tl = ((2 * qp) ^ (2 * hs)) + (g & 1);
#pragma unroll
      for (int c = 0; c < (TWO ? 2 : 1); ++c) {
        const int ct = ct0 + c;
        const auto s0 = __builtin_amdgcn_permlane16_swap(o[c][0][0], o[c][1][0], false, false);
        const auto s1 = __builtin_amdgcn_permlane16_swap(o[c][0][1], o[c][1][1], false, false);
        const size_t off = (((size_t)(4 * mt + tl) * 54 + (ct >> 1)) * 64 + i + 16 * (2 * (ct & 1) + (g >> 1))) * 16;
        st16(rs_, WS_H1F + off, (u32x4){s0[0], s1[0], s0[1], s1[1]});
      }
    }
    SB();
    __builtin_amdgcn_s_barrier();
    SB();
    constexpr int E0 = W2_INL - NW2L;
    auto signal = [&]() __attribute__((always_inline)) {
      if (lane == 0) {
        if (__hip_atomic_fetch_add(lcnt, 1u, __ATOMIC_RELAXED, __HIP_MEMORY_SCOPE_WORKGROUP) == 7u)
          __hip_atomic_store(flags + 16 * ng, 1u, __ATOMIC_RELAXED, __HIP_MEMORY_SCOPE_AGENT);
      }
    };
    if (w >= SIG_SPLIT) { asm volatile("s_waitcnt vmcnt(0)" ::: "memory"); signal(); }
    SB();
#pragma unroll
    for (int e = E0; e < NW2R; ++e) ring[(8 + e) % 32] = ld16(rs_, lo16, w2_off(e));
    SB();
    if (w < SIG_SPLIT) { asm volatile("s_waitcnt vmcnt(%0)" :: "n"(NW2R - E0) : "memory"); signal(); }
    SB();
  };
  if (two) gemm1(std::true_type{});
  else gemm1(std::false_type{});
  STAMP(2, 3);
#pragma nounroll
  for (unsigned spin = 0; spin < (1u << 20); ++spin) {
    if (__hip_atomic_load(flags + 16 * w, __ATOMIC_RELAXED, __HIP_MEMORY_SCOPE_AGENT) != 0u) break;
    __builtin_amdgcn_s_sleep(1);
  }
  asm volatile("" ::: "memory");
  SB();

  STAMP(2, 4);
  const int h1s = (int)WS_H1F + (mt * 4 * 54 + ks0) * 1024;
  u32x4 hb[28];
  auto load_h1 = [&](int n) __attribute__((always_inline)) {
    const int q = n / 7, s = n % 7;
    const int sn = (s == 6 && !full) ? 5 : s;
    hb[n] = ld16_sc1(rs_, lo16, h1s + (q * 54 + sn) * 1024);
  };
#pragma unroll
  for (int n = 0; n < H1_PRE; ++n) {
    if (n == H1_BATCH) { SB(); __builtin_amdgcn_s_barrier(); SB(); }
    load_h1(n);
  }
  SB();
  float* red = (float*)smem;
  float* rb_ = red + (w * 16 + i) * KF_RS + 4 * g;
  const int re = w * 40 + min(lane, 39);
  const int rtk = re / 20, rq4 = re % 20;
  const float* rr_ = red + rtk * KF_RS + 4 * rq4;
  f32x4 ac2[2][5];
  f32x4 rv[8], rsum;
  u32x2 xv2;
  const f32x4 gb = *(const f32x4*)(tab + 4 * rq4);
  auto red_wr = [&](int q, int c) __attribute__((always_inline)) {
    *(f32x4*)(rb_ + (q & 1) * (KF_RB / 4) + 16 * c) = ac2[q & 1][c];
  };
  auto red_rd = [&](int q, int ww) __attribute__((always_inline)) {
    rv[ww] = *(const f32x4*)(rr_ + (q & 1) * (KF_RB / 4) + ww * 16 * KF_RS);
  };
  auto red_x = [&](int q) __attribute__((always_inline)) { xv2 = *(const u32x2*)(xr + (16 * q + rtk) * 176 + rq4 * 8); };
  auto red_out = [&](int q) __attribute__((always_inline)) {
    const f32x4 s = rsum + (rv[6] + rv[7]);
    u32x2 o;
#pragma unroll
    for (int h = 0; h < 2; ++h) {
      unsigned f = pk_bf(s[2 * h], s[2 * h + 1]);
      unsigned gf = pk_bf(bf_lo(f) * gb[2 * h], bf_hi(f) * gb[2 * h + 1]);
      o[h] = pk_bf(bf_lo(xv2[h]) + bf_lo(gf), bf_hi(xv2[h]) + bf_hi(gf));
    }
    *(u32x2*)(out + (size_t)(mt * 64 + 16 * q + rtk) * D_ + ng * 80 + 4 * rq4) = o;
  };
#pragma unroll
  for (int q = 0; q < 3; ++q) {
#pragma unroll
    for (int c = 0; c < 5; ++c) ac2[q & 1][c] = (f32x4){0.f, 0.f, 0.f, 0.f};
#pragma unroll
    for (int s = 0; s < 7; ++s) {
      auto mm = [&](int c) __attribute__((always_inline)) {
        const u32x4 a = c == 4 ? wl[s] : ((s == 6 && c == 3) ? wl[7] : ring[(8 + 4 * s + c) % 32]);
        ac2[q & 1][c] = mfma_bf16(a, hb[7 * q + s], ac2[q & 1][c]);
      };
      if (s == 5 && q >= 2) {
        red_wr(q - 1, 4);
        SB();
        red_out(q - 2);
#pragma unroll
        for (int c = 0; c < 5; ++c) mm(c);
#pragma unroll
        for (int c = 0; c < 5; ++c) {
          __builtin_amdgcn_sched_group_barrier(0x008, 1, 0);
          __builtin_amdgcn_sched_group_barrier(0x002, 10, 0);
        }
        SB();
      } else if (s < 6) {
        mm(0); SB();
        if (q >= 1 && s >= 1) { red_wr(q - 1, s - 1); SB(); }
        mm(1); SB();
        if (q >= 2 && s < 4) { red_rd(q - 2, 2 * s); SB(); }
        mm(2); SB();
        if (q >= 2 && s < 4) { red_rd(q - 2, 2 * s + 1); SB(); }
        if (q >= 2 && s == 4) { red_x(q - 2); SB(); }
        mm(3); SB();
        if (q >= 2 && s >= 1 && s < 4) {
          const f32x4 t = rv[2 * s - 2] + rv[2 * s - 1];
          rsum = s == 1 ? t : rsum + t;
          asm volatile("" : "+v"(rsum));
          SB();
        }
        mm(4); SB();
      } else if (full) {
#pragma unroll
        for (int c = 0; c < 5; ++c) mm(c);
        SB();
      }
      if (7 * q + s + H1_PRE < 28) { load_h1(7 * q + s + H1_PRE); SB(); }
    }
    if (q >= 1) bar_lds();
    if (q == 0) STAMP(2, 5);
  }
  {
    auto sum6 = [&]() __attribute__((always_inline)) {
      rsum = ((rv[0] + rv[1]) + (rv[2] + rv[3])) + (rv[4] + rv[5]);
    };
    if (full) {
#pragma unroll
      for (int c = 0; c < 5; ++c) {
        const u32x4 a = c == 4 ? wl[6] : (c == 3 ? wl[7] : ring[(8 + 24 + c) % 32]);
        rv[c] = mfma_bf16(a, hb[27], (f32x4){0.f, 0.f, 0.f, 0.f});
      }
    } else {
#pragma unroll
      for (int c = 0; c < 5; ++c) rv[c] = (f32x4){0.f, 0.f, 0.f, 0.f};
    }
    f32x4 c0[5];
#pragma unroll
    for (int c = 0; c < 5; ++c) c0[c] = rv[c];
    SB();
    auto col = [&](int c, auto&& between) __attribute__((always_inline)) {
#pragma unroll
      for (int s = 0; s < 6; ++s) {
        const u32x4 a = c == 4 ? wl[s] : ring[(8 + 4 * s + c) % 32];
        c0[c] = mfma_bf16(a, hb[21 + s], c0[c]);
        SB();
        between(s);
        SB();
      }
    };
    col(0, [&](int s) __attribute__((always_inline)) { if (s < 5) red_wr(2, s); if (s < 4) { red_rd(1, 2 * s); red_rd(1, 2 * s + 1); } if (s == 4) red_x(1); });
    col(1, [&](int s) __attribute__((always_inline)) { if (s == 1) { sum6(); asm volatile("" : "+v"(rsum)); } if (s == 3) red_out(1); });
    bar_lds();
    auto wr3 = [&](int c) __attribute__((always_inline)) { *(f32x4*)(rb_ + (KF_RB / 4) + 16 * c) = c0[c]; };
    col(2, [&](int s) __attribute__((always_inline)) { if (s < 2) wr3(s); if (s >= 1 && s < 5) { red_rd(2, 2 * s - 2); red_rd(2, 2 * s - 1); } if (s == 5) red_x(2); });
    col(3, [&](int s) __attribute__((always_inline)) { if (s == 1) wr3(2); if (s == 2) { sum6(); asm volatile("" : "+v"(rsum)); } if (s == 4) red_out(2); });
    col(4, [&](int s) __attribute__((always_inline)) { if (s == 1) wr3(3); if (s == 2) red_x(3); });
    wr3(4);
    STAMP(2, 6);
    bar_lds();
#pragma unroll
    for (int ww = 0; ww < 8; ++ww) red_rd(3, ww);
    sum6();
    red_out(3);
    VM0();
    STAMP(2, 7);
  }
}

__global__ NOPK __launch_bounds__(512, 2) void kf(char* __restrict__ ws, unsigned short* __restrict__ out) {
  __shared__ __attribute__((aligned(16))) char smem[KF_LDS];
  kf_body(ws, out, smem);
}

__global__ void kend_() {}

extern "C" void kernel_launch(void* const* d_in, const int* in_sizes, int n_in,
                              void* d_out, int out_size, void* d_ws, size_t ws_size,
                              hipStream_t stream) {
  K1Args a;
  a.x = (const unsigned short*)d_in[0];
  a.are = (const unsigned short*)d_in[1];
  a.aim = (const unsigned short*)d_in[2];
  a.bre = (const unsigned short*)d_in[3];
  a.bim = (const unsigned short*)d_in[4];
  a.cre = (const unsigned short*)d_in[5];
  a.cim = (const unsigned short*)d_in[6];
  a.logdt = (const unsigned short*)d_in[7];
  a.c = (const unsigned short*)d_in[12];
  a.wmod = (const unsigned short*)d_in[13];
  a.ws = (char*)d_ws;
  k1<<<NB_PF + NB_K + NB_LN + NB_MOD, 256, 0, stream>>>(a);
  k2<<<256, 512, 0, stream>>>((const unsigned short*)d_in[8], (char*)d_ws, (const unsigned short*)d_in[10],
                              (const unsigned short*)d_in[11]);
  kf<<<256, 512, 0, stream>>>((char*)d_ws, (unsigned short*)d_out);
}
```

```cpp
#include <hip/hip_runtime.h>
#include <hip/hip_bf16.h>
#include <stdint.h>
#include <type_traits>

typedef __attribute__((ext_vector_type(8))) __bf16 bf16x8;
typedef __attribute__((ext_vector_type(2))) __bf16 bf16x2;
typedef __attribute__((ext_vector_type(4))) float f32x4;
typedef __attribute__((ext_vector_type(2))) float f32x2;
typedef __attribute__((ext_vector_type(4))) unsigned u32x4;
typedef __attribute__((ext_vector_type(2))) unsigned u32x2;
typedef __attribute__((ext_vector_type(8))) _Float16 h16x8;
typedef __attribute__((ext_vector_type(2))) _Float16 h16x2;
typedef __attribute__((ext_vector_type(4))) short s16x4;
typedef __attribute__((address_space(3))) s16x4 lds_s16x4;

#if defined(__HIP_DEVICE_COMPILE__)
#define NOPK __attribute__((target("no-packed-fp32-ops")))
#else
#define NOPK
#endif
#define DEV __device__ __forceinline__

#define W1L 0
constexpr int W1KS = W1L ? 1 : 108, W1TS = W1L ? 20 : 1;
#define LNFLIP 0
constexpr int L_ = 2048;
constexpr int D_ = 640;
constexpr int NS = 128;
constexpr int FF = 1728;
constexpr int KREV = 2304;

constexpr size_t WS_GB   = 0;
constexpr size_t WS_KSC  = 8192;
constexpr size_t WS_CNT  = 16384;
constexpr size_t WS_MOD  = 65536;
constexpr size_t WS_CSP  = 196608;
constexpr size_t WS_XNT  = 327680;
constexpr size_t WS_XT   = WS_XNT + (size_t)D_ * L_ * 2;
constexpr size_t WS_KREV = WS_XT + (size_t)D_ * L_ * 2;
constexpr size_t WS_W1F  = WS_KREV + (size_t)D_ * KREV * 2;
constexpr size_t WS_W2F  = WS_W1F + (size_t)D_ * FF * 2;
constexpr size_t WS_X1T  = WS_W2F + (size_t)D_ * FF * 2;
constexpr size_t WS_H1F  = WS_X1T + (size_t)D_ * L_ * 2;
constexpr size_t WS_END  = WS_H1F + (size_t)L_ * FF * 2;

#define STAMP(kid, k) do {} while (0)
#define CSTAMP(kid, k) do {} while (0)
#define VM0() do {} while (0)

#define TS(k, wsp) do { if (blockIdx.x == 0 && threadIdx.x == 0) __hip_atomic_store((unsigned long long*)((wsp) + (200u << 20)) + (k), (unsigned long long)__builtin_amdgcn_s_memrealtime(), __ATOMIC_RELAXED, __HIP_MEMORY_SCOPE_AGENT); } while (0)
DEV float bf_lo(unsigned u) { return __uint_as_float(u << 16); }
DEV float bf_hi(unsigned u) { return __uint_as_float(u & 0xffff0000u); }
DEV unsigned pk_bf(float a, float b) {
  f32x2 v = {a, b};
  bf16x2 r = __builtin_convertvector(v, bf16x2);
  return __builtin_bit_cast(unsigned, r);
}
DEV float rbf(float a) { return bf_lo(pk_bf(a, 0.f)); }
DEV float ldbf(const unsigned short* p) { return __uint_as_float(((unsigned)*p) << 16); }
#define CONST_AS __attribute__((address_space(4)))
template <class T> DEV T ldu(const void* p) { return *(const CONST_AS T*)(size_t)p; }
DEV float ldbf_u(const unsigned short* p) {
  const unsigned v = ldu<unsigned>((const void*)((size_t)p & ~(size_t)3));
  return __uint_as_float(((size_t)p & 2) ? (v & 0xffff0000u) : (v << 16));
}

typedef __amdgpu_buffer_rsrc_t rsrc_t;
DEV rsrc_t ws_rsrc(char* ws) { return __builtin_amdgcn_make_buffer_rsrc((void*)ws, 0, (int)WS_END, 0x00020000); }
DEV void st16(rsrc_t r, size_t off, u32x4 v) { __builtin_amdgcn_raw_buffer_store_b128(v, r, (int)off, 0, 16); }
DEV void st4(char* ws, size_t off, unsigned v) { __hip_atomic_store((unsigned*)(ws + off), v, __ATOMIC_RELAXED, __HIP_MEMORY_SCOPE_AGENT); }
DEV void st8(rsrc_t r, size_t off, u32x2 v) { __builtin_amdgcn_raw_buffer_store_b64(v, r, (int)off, 0, 16); }

DEV void bar_lds() { asm volatile("s_waitcnt lgkmcnt(0)\n\ts_barrier" ::: "memory"); }
DEV void lds_wait(const void* word, unsigned val) {
  unsigned tv, ts, cnt;
  asm volatile("s_mov_b32 %2, 0x100000\n"
               "1:\n\tds_read_b32 %0, %3\n\ts_waitcnt lgkmcnt(0)\n\tv_readfirstlane_b32 %1, %0\n\t"
               "s_cmp_ge_u32 %1, %4\n\ts_cbranch_scc1 2f\n\ts_sleep 1\n\ts_sub_u32 %2, %2, 1\n\t"
               "s_cmp_lg_u32 %2, 0\n\ts_cbranch_scc1 1b\n2:"
               : "=&v"(tv), "=&s"(ts), "=&s"(cnt) : "v"((unsigned)(size_t)word), "s"(val) : "memory", "scc");
}
DEV void lds_post(unsigned* word, int lane) {
  asm volatile("" ::: "memory");
  if (lane == 0) __hip_atomic_fetch_add(word, 1u, __ATOMIC_RELAXED, __HIP_MEMORY_SCOPE_WORKGROUP);
  asm volatile("" ::: "memory");
}
DEV void lgkm0() { asm volatile("s_waitcnt lgkmcnt(0)" ::: "memory"); __builtin_amdgcn_sched_barrier(0); }

DEV f32x4 mfma_bf16(u32x4 a, u32x4 b, f32x4 c) {
  return __builtin_amdgcn_mfma_f32_16x16x32_bf16(__builtin_bit_cast(bf16x8, a), __builtin_bit_cast(bf16x8, b), c, 0, 0, 0);
}

constexpr int NB_PF = 8, NB_K = 640, NB_LN = 256, NB_MOD = 120;

struct K1Args {
  const unsigned short *x, *are, *aim, *bre, *bim, *cre, *cim, *logdt, *c, *wmod;
  char* ws;
};

DEV void k1_prefetch() {
  const int tid = threadIdx.x;
  unsigned long long pc;
  int o0, o1;
  asm volatile("s_getpc_b64 %0\n.Lpf_here%=:\n\ts_mov_b32 %1, _Z2k16K1Args - .Lpf_here%=\n\ts_mov_b32 %2, _Z5kend_v - .Lpf_here%="
               : "=s"(pc), "=s"(o0), "=s"(o1));
  const char* c0 = (const char*)(pc + (long long)o0);
  long n = o1 - o0;
  n = n < 0 ? 0 : (n > 98304 ? 98304 : n);
  unsigned v[3];
#pragma unroll
  for (int it = 0; it < 3; ++it) {
    const long o = (long)tid * 128 + it * 32768;
    v[it] = o < n ? *(const __attribute__((address_space(1))) unsigned*)(c0 + o) : 0u;
  }
  asm volatile("" :: "v"(v[0] ^ v[1] ^ v[2]));
}

DEV void k1_mod(const K1Args& p, int b, char* smem) {
  const int tid = threadIdx.x;
  const int rg = b & 7, cgp = b >> 3;
  const int c32 = tid & 31, r8 = tid >> 5;
  float* sil = (float*)smem;
  float* red = (float*)(smem + 512);
  char* stamp_ws = p.ws; (void)stamp_ws;
  STAMP(0, 1);
  u32x4 w[10];
  const float cv = ldbf(p.c + rg * 80 + min(tid, 79));
  asm volatile("" ::: "memory");
#pragma unroll
  for (int it = 0; it < 10; ++it)
    w[it] = *(const u32x4*)(p.wmod + (size_t)(rg * 80 + r8 + 8 * it) * 3840 + 256 * cgp + 8 * c32);
  if (tid < 80) sil[tid] = rbf(cv / (1.f + expf(-cv)));
  if (tid >= 128 && tid < 132 && b < 64) st4(p.ws, WS_CNT + (b * 4 + tid - 128) * 64, 0u);
  __syncthreads();
  STAMP(0, 2);
  float acc[8];
#pragma unroll
  for (int j = 0; j < 8; ++j) acc[j] = 0.f;
#pragma unroll
  for (int it = 0; it < 10; ++it) {
    float s = sil[r8 + 8 * it];
#pragma unroll
    for (int q = 0; q < 4; ++q) {
      acc[2 * q] += s * bf_lo(w[it][q]);
      acc[2 * q + 1] += s * bf_hi(w[it][q]);
    }
  }
#pragma unroll
  for (int j = 0; j < 8; ++j) acc[j] += __shfl_xor(acc[j], 32, 64);
  if ((tid & 63) < 32) {
    *(f32x4*)(red + (tid >> 6) * 256 + 8 * c32) = (f32x4){acc[0], acc[1], acc[2], acc[3]};
    *(f32x4*)(red + (tid >> 6) * 256 + 8 * c32 + 4) = (f32x4){acc[4], acc[5], acc[6], acc[7]};
  }
  __syncthreads();
  STAMP(0, 3);
  if (tid < 64) {
    const f32x4* r4 = (const f32x4*)red + tid;
    st16(ws_rsrc(p.ws), WS_MOD + (rg * 3840 + 256 * cgp + 4 * tid) * 4,
         __builtin_bit_cast(u32x4, (r4[0] + r4[64]) + (r4[128] + r4[192])));
  }
}
struct ModP { f32x4 a, b; };
DEV ModP mod_ld(const char* ws, int idx) {
  const float* mp = (const float*)(ws + WS_MOD) + idx;
  ModP m;
#pragma unroll
  for (int j = 0; j < 4; ++j) { m.a[j] = mp[3840 * j]; m.b[j] = mp[3840 * (4 + j)]; }
  return m;
}
DEV float mod_fin(const ModP& m) {
  return rbf(((m.a[0] + m.a[1]) + (m.a[2] + m.a[3])) + ((m.b[0] + m.b[1]) + (m.b[2] + m.b[3])));
}
#define SB() __builtin_amdgcn_sched_barrier(0)

DEV void k1_ln(const K1Args& p, int b, char* smem) {
  constexpr int STR = 704;
  const int tid = threadIdx.x;
  const int tok = tid >> 3, sub = tid & 7;
  const int tile = (b & 7) * 8 + ((b >> 3) & 7), pass = (b >> 6) ^ LNFLIP;
  const int hf = pass & 1;
  const bool norm = pass < 2;
  const int t0 = tile * 32;
  const unsigned short* xr = p.x + (size_t)(t0 + tok) * D_ + 8 * sub;
  u32x4 xv[5], xo[5];
  char* stamp_ws = p.ws; (void)stamp_ws;
  STAMP(0, 1);
#pragma unroll
  for (int m = 0; m < 5; ++m) xv[m] = *(const u32x4*)(xr + 320 * hf + 64 * m);
  if (norm) {
#pragma unroll
    for (int m = 0; m < 5; ++m) xo[m] = *(const u32x4*)(xr + 320 * (hf ^ 1) + 64 * m);
    float s = 0.f, v = 0.f;
    const bf16x2 one2 = __builtin_bit_cast(bf16x2, 0x3f803f80u);
#pragma unroll
    for (int m = 0; m < 10; ++m)
#pragma unroll
      for (int q = 0; q < 4; ++q) {
        const unsigned u = m < 5 ? xv[m][q] : xo[m - 5][q];
        const bf16x2 x2 = __builtin_bit_cast(bf16x2, u);
        s = __builtin_amdgcn_fdot2_f32_bf16(x2, one2, s, false);
        v = __builtin_amdgcn_fdot2_f32_bf16(x2, x2, v, false);
      }
#pragma unroll
    for (int o = 1; o < 8; o <<= 1) { s += __shfl_xor(s, o, 64); v += __shfl_xor(v, o, 64); }
    const float mean = s * (1.f / D_);
    const float var = fmaxf(v * (1.f / D_) - mean * mean, 0.f);
    const float sd = rbf(sqrtf(rbf(rbf(var) + 1e-5f)));
    const float rs = 1.f / sd;
    const float mb = rbf(mean);
#pragma unroll
    for (int m = 0; m < 5; ++m)
#pragma unroll
      for (int q = 0; q < 4; ++q) {
        unsigned d2 = pk_bf(bf_lo(xv[m][q]) - mb, bf_hi(xv[m][q]) - mb);
        xv[m][q] = pk_bf(bf_lo(d2) * rs, bf_hi(d2) * rs);
      }
  }
  const int lane = tid & 63, w = tid >> 6;
  const int g = lane >> 4, i = lane & 15;
  const rsrc_t rsrc = ws_rsrc(p.ws);
#pragma unroll
  for (int m = 0; m < 5; ++m) *(u32x4*)(smem + tok * STR + (sub + 8 * m) * 16) = xv[m];
  __syncthreads();
  STAMP(0, 2);
  const size_t dsto = (norm ? WS_XNT : WS_XT) + ((size_t)tile * D_ + 320 * hf) * 64;
#pragma unroll
  for (int it = 0; it < 5; ++it) {
    const int cb = w * 5 + it;
    const char* a = smem + (8 * g + (i >> 2)) * STR + cb * 32 + (i & 3) * 8;
    const u32x2 ra = __builtin_bit_cast(u32x2, __builtin_amdgcn_ds_read_tr16_b64_v4i16((lds_s16x4*)a));
    const u32x2 rb = __builtin_bit_cast(u32x2, __builtin_amdgcn_ds_read_tr16_b64_v4i16((lds_s16x4*)(a + 4 * STR)));
    st16(rsrc, dsto + (size_t)(cb * 16 + i) * 64 + 16 * g, (u32x4){ra[0], ra[1], rb[0], rb[1]});
  }
}

constexpr int RT_STR = 176;
template <int NF> struct RtRaw { u32x4 r[NF]; };
template <int NF>
DEV void rt_load(RtRaw<NF>& t, const unsigned short* W, int N, int kk, int ct0, int lane) {
#pragma unroll
  for (int j = 0; j < NF; ++j) {
    const int c = lane + 64 * j, row = c / (2 * NF), part = c % (2 * NF);
    t.r[j] = *(const u32x4*)(W + (size_t)(32 * kk + row) * N + 16 * ct0 + 8 * part);
  }
}
template <int NF>
DEV void rt_park(const RtRaw<NF>& t, char* scr, int lane) {
#pragma unroll
  for (int j = 0; j < NF; ++j) {
    const int c = lane + 64 * j, row = c / (2 * NF), part = c % (2 * NF);
    *(u32x4*)(scr + row * RT_STR + 16 * part) = t.r[j];
  }
}
DEV u32x4 rt_frag(const char* scr, int j, int lane) {
  const int g = lane >> 4, i = lane & 15;
  const char* p = scr + (8 * g + (i >> 2)) * RT_STR + 32 * j + (i & 3) * 8;
  const u32x2 a = __builtin_bit_cast(u32x2, __builtin_amdgcn_ds_read_tr16_b64_v4i16((lds_s16x4*)p));
  const u32x2 b = __builtin_bit_cast(u32x2, __builtin_amdgcn_ds_read_tr16_b64_v4i16((lds_s16x4*)(p + 4 * RT_STR)));
  return (u32x4){a[0], a[1], b[0], b[1]};
}
struct cf { float re, im; };
DEV cf cmul(cf a, cf b) {
  cf r;
  r.re = a.re * b.re - a.im * b.im;
  r.im = a.re * b.im + a.im * b.re;
  return r;
}
DEV cf cexp_(float a2, float br, float m, float m0) {
  float mag = __builtin_amdgcn_exp2f(a2 * (m - m0));
  float ph = __builtin_amdgcn_fractf(br * m);
  cf r; r.re = mag * __builtin_amdgcn_cosf(ph); r.im = mag * __builtin_amdgcn_sinf(ph);
  return r;
}
DEV unsigned pk_h(float a, float b) {
  f32x2 v = {a, b};
  h16x2 r = __builtin_convertvector(v, h16x2);
  return __builtin_bit_cast(unsigned, r);
}
DEV f32x4 mfma_f16(u32x4 a, u32x4 b, f32x4 c) {
  return __builtin_amdgcn_mfma_f32_16x16x32_f16(__builtin_bit_cast(h16x8, a), __builtin_bit_cast(h16x8, b), c, 0, 0, 0);
}
DEV void put2(char* p, cf a, cf b) {
  u32x2 o = {pk_h(a.re, a.im), pk_h(b.re, b.im)};
  *(u32x2*)p = o;
}

constexpr int KG_RS = 544;
DEV void k1_kgen(const K1Args& p, int d, char* smem) {
  constexpr int RS = KG_RS;
  const int tid = threadIdx.x;
  const int lane = tid & 63;
  const int w = __builtin_amdgcn_readfirstlane(tid >> 6);
  const int g = lane >> 4, i = lane & 15;
  const size_t po = (size_t)d * NS + 2 * lane;
  const unsigned rar = *(const unsigned*)(p.are + po), rai = *(const unsigned*)(p.aim + po);
  unsigned rbr = 0u, rbi = 0u, rcr = 0u, rci = 0u;
  if (w == 0) {
    rbr = *(const unsigned*)(p.bre + po); rbi = *(const unsigned*)(p.bim + po);
    rcr = *(const unsigned*)(p.cre + po); rci = *(const unsigned*)(p.cim + po);
  }
  const float ldt = ldbf_u(p.logdt + d);
  asm volatile("" ::: "memory");
  SB();
  char* stamp_ws = p.ws; (void)stamp_ws;
  const float dt = rbf(expf(ldt));
  asm volatile("" :: "v"(dt));
  STAMP(0, 1);
  float a2[2], bv[2], m0[2];
  a2[0] = (dt * bf_lo(rar)) * 1.44269504088896341f; a2[1] = (dt * bf_hi(rar)) * 1.44269504088896341f;
  bv[0] = (dt * bf_lo(rai)) * 0.15915494309189534f; bv[1] = (dt * bf_hi(rai)) * 0.15915494309189534f;
  m0[0] = a2[0] > 0.f ? 2048.f : 0.f; m0[1] = a2[1] > 0.f ? 2048.f : 0.f;
  char* my = smem + lane * 8;
  if (w == 0) {
    cf v[2], v8[2], e8[2];
    float bound = 0.f;
#pragma unroll
    for (int q = 0; q < 2; ++q) {
      const float dbr = dt * (q ? bf_hi(rbr) : bf_lo(rbr)), dbi = dt * (q ? bf_hi(rbi) : bf_lo(rbi));
      const float cr = q ? bf_hi(rcr) : bf_lo(rcr), ci = q ? bf_hi(rci) : bf_lo(rci);
      const float gsh = __builtin_amdgcn_exp2f(a2[q] * m0[q]);
      v[q].re = (cr * dbr - ci * dbi) * gsh; v[q].im = -(cr * dbi + ci * dbr) * gsh;
      bound += (fabsf(v[q].re) + fabsf(v[q].im)) * __builtin_amdgcn_exp2f(fmaxf(a2[q], 0.f) * 120.f);
      e8[q] = cexp_(a2[q], -bv[q], 8.f, 0.f);
      v8[q] = cexp_(a2[q], -bv[q], 64.f, 0.f);
    }
#pragma unroll
    for (int o = 1; o < 64; o <<= 1) bound += __shfl_xor(bound, o, 64);
    int ex = (int)((__float_as_uint(bound) >> 23) & 0xff);
    ex = ex < 20 ? 20 : (ex > 240 ? 240 : ex);
    const float sv = __uint_as_float((unsigned)(127 + 13 + 127 - ex) << 23);
    if (lane == 0) st4(p.ws, WS_KSC + 4 * d, (unsigned)(ex - 13) << 23);
#pragma unroll
    for (int q = 0; q < 2; ++q) { v[q].re *= sv; v[q].im *= sv; v8[q] = cmul(v[q], v8[q]); }
#pragma unroll
    for (int j = 0; j < 8; ++j) {
      put2(my + (41 + j) * RS, v[0], v[1]);
      put2(my + (49 + j) * RS, v8[0], v8[1]);
      if (j < 7) {
#pragma unroll
        for (int q = 0; q < 2; ++q) { v[q] = cmul(v[q], e8[q]); v8[q] = cmul(v8[q], e8[q]); }
      }
    }
  } else if (w == 1) {
    cf e1[2], u[2], u2[2];
#pragma unroll
    for (int q = 0; q < 2; ++q) {
      e1[q] = cexp_(a2[q], bv[q], 1.f, 0.f);
      u[q].re = __builtin_amdgcn_exp2f(-a2[q] * m0[q]); u[q].im = 0.f;
      u2[q] = cexp_(a2[q], bv[q], 128.f, m0[q]);
    }
#pragma unroll
    for (int r = 0; r < 8; ++r) {
      put2(my + r * RS, u[0], u[1]);
      put2(my + (8 + r) * RS, u2[0], u2[1]);
      if (r < 7) {
#pragma unroll
        for (int q = 0; q < 2; ++q) { u[q] = cmul(u[q], e1[q]); u2[q] = cmul(u2[q], e1[q]); }
      }
    }
  } else if (w == 2) {
    cf e2[2], u[2], u2[2];
#pragma unroll
    for (int q = 0; q < 2; ++q) {
      e2[q] = cexp_(a2[q], bv[q], 2.f, 0.f);
      u[q] = cexp_(a2[q], bv[q], 256.f, m0[q]);
      u2[q] = cexp_(a2[q], bv[q], 384.f, m0[q]);
    }
#pragma unroll
    for (int r = 0; r < 4; ++r) {
      put2(my + (16 + r) * RS, u[0], u[1]);
      put2(my + (20 + r) * RS, u2[0], u2[1]);
      if (r < 3) {
#pragma unroll
        for (int q = 0; q < 2; ++q) { u[q] = cmul(u[q], e2[q]); u2[q] = cmul(u2[q], e2[q]); }
      }
    }
    cf e4[2] = {cmul(e2[0], e2[0]), cmul(e2[1], e2[1])};
#pragma unroll
    for (int r = 0; r < 2; ++r) {
      cf c0 = cexp_(a2[0], bv[0], 512.f + 128.f * r, m0[0]), c1 = cexp_(a2[1], bv[1], 512.f + 128.f * r, m0[1]);
      put2(my + (24 + 2 * r) * RS, c0, c1);
      put2(my + (25 + 2 * r) * RS, cmul(c0, e4[0]), cmul(c1, e4[1]));
    }
  } else {
    cf e4[2], e128[2], u[2], u2[2];
#pragma unroll
    for (int q = 0; q < 2; ++q) {
      e4[q] = cexp_(a2[q], bv[q], 4.f, 0.f);
      e128[q] = cexp_(a2[q], bv[q], 128.f, 0.f);
      u[q] = cexp_(a2[q], bv[q], 1024.f, m0[q]);
      u2[q] = cexp_(a2[q], bv[q], 1536.f, m0[q]);
    }
#pragma unroll
    for (int r = 2; r < 4; ++r) {
      cf c0 = cexp_(a2[0], bv[0], 512.f + 128.f * r, m0[0]), c1 = cexp_(a2[1], bv[1], 512.f + 128.f * r, m0[1]);
      put2(my + (24 + 2 * r) * RS, c0, c1);
      put2(my + (25 + 2 * r) * RS, cmul(c0, e4[0]), cmul(c1, e4[1]));
    }
#pragma unroll
    for (int r = 0; r < 4; ++r) {
      put2(my + (32 + r) * RS, u[0], u[1]);
      put2(my + (36 + r) * RS, u2[0], u2[1]);
#pragma unroll
      for (int q = 0; q < 2; ++q) { if (r < 3) u[q] = cmul(u[q], e128[q]); u2[q] = cmul(u2[q], e128[q]); }
    }
    put2(my + 40 * RS, u2[0], u2[1]);
  }
  __syncthreads();
  STAMP(0, 2);
  _Float16* kh = (_Float16*)(smem + 57 * RS);
  if (w < 3) {
    const char* ua = smem + (16 * w + i) * RS + g * 16;
    const char* va = smem + (41 + i) * RS + g * 16;
    f32x4 acc = {0.f, 0.f, 0.f, 0.f};
#pragma unroll
    for (int ks = 0; ks < 8; ++ks)
      acc = mfma_f16(*(const u32x4*)(ua + ks * 64), *(const u32x4*)(va + ks * 64), acc);
    int idx0, st;
    if (w == 0) { idx0 = (g < 2 ? 4 * g : 120 + 4 * g) + 8 * i; st = 1; }
    else if (w == 1) { idx0 = g < 2 ? 256 + 64 * g + 4 * i : 384 + 64 * (g - 2) + 2 * i; st = 1; }
    else { idx0 = 512 + 64 * g + i; st = 16; }
#pragma unroll
    for (int rg = 0; rg < 4; ++rg) {
      int idx = idx0 + st * rg;
      if (w == 1 && g >= 2) idx = idx0 + 32 * (rg >> 1) + (rg & 1);
      if (w == 2 && g >= 2) idx = (g == 2 && rg == 0 && i == 0) ? 640 : 644;
      kh[idx] = (_Float16)acc[rg];
    }
  }
  __syncthreads();
  STAMP(0, 3);
  const unsigned short* ku = (const unsigned short*)kh;
  const size_t kout = WS_KREV + (size_t)d * KREV * 2;
  {
    const int l0 = 2040 - 8 * tid;
    u32x4 o;
    if (w < 2) {
      const int q = l0 >> 3;
      const unsigned a = ku[384 + q], b = ku[385 + q];
      const unsigned t = (q & 1) ? b : a;
      o[0] = b | (b << 16); o[1] = b | (t << 16); o[2] = a | (a << 16); o[3] = o[2];
    } else if (w == 2) {
      const int q = l0 >> 2;
      const unsigned a = ku[256 + q], b = ku[257 + q], c = ku[258 + q];
      o[0] = c | (c << 16); o[1] = b | (b << 16); o[2] = b | (a << 16); o[3] = a | (a << 16);
    } else {
      unsigned v[8];
#pragma unroll
      for (int e = 0; e < 8; ++e) {
        const int l = l0 + 7 - e;
        const int idx = l < 256 ? l : (int)(pk_bf((float)l, 0.f) & 0xffffu) - 17024;
        v[e] = ku[idx];
      }
#pragma unroll
      for (int q = 0; q < 4; ++q) o[q] = v[2 * q] | (v[2 * q + 1] << 16);
    }
    st16(ws_rsrc(p.ws), kout + 16 * tid, o);
  }
}

__global__ NOPK __launch_bounds__(256) void k1(K1Args p) {
  __shared__ __attribute__((aligned(16))) char smem[57 * KG_RS + 1312];
  int b = blockIdx.x;
  TS(0, p.ws);
  char* stamp_ws = p.ws; (void)stamp_ws;
  STAMP(0, 0);
  if (b < NB_PF) { k1_prefetch(); STAMP(0, 7); return; }
  b -= NB_PF;
  if (b < NB_K) { k1_kgen(p, b, smem); VM0(); STAMP(0, 7); return; }
  b -= NB_K;
  asm volatile("" :: "s"(p.ws), "s"(p.x));
#define DL_LN 4
#define DL_MOD 4
  if (b < NB_LN) { __builtin_amdgcn_s_sleep(DL_LN); k1_ln(p, b, smem); }
  else { __builtin_amdgcn_s_sleep(DL_MOD); k1_mod(p, b - NB_LN, smem); }
  VM0();
  STAMP(0, 7);
}

constexpr int K2_CS = 4160;
constexpr int K2_KOFF = 4 * K2_CS;
constexpr int K2_CH = K2_KOFF + 4608;
constexpr int CV_RED = 3 * K2_CH;
constexpr int CV_RT2 = 86016;
constexpr int CV_SUM = CV_RT2 + 4 * (32 * 176 + 256);
constexpr int CV_XCH = CV_SUM + 2560;
constexpr int CV_FL = CV_RED + 20480;

struct Chan { float sa, ca1, ga, dsk, hs, hinv, yinv; };
struct ChanRaw { float m, kinv, dsk; };
DEV ChanRaw chan_ld(const unsigned short* dskip, const char* ws, int d, int lane) {
  ChanRaw c;
  c.kinv = ldu<float>(ws + WS_KSC + 4 * d);
  c.dsk = ldbf_u(dskip + d);
  c.m = ((const float*)(ws + WS_MOD))[3840 * (lane & 7) + D_ * ((lane >> 3) & 3) + d];
  return c;
}
template <int CTRL> DEV float dpp_add(float v) {
  return v + __int_as_float(__builtin_amdgcn_update_dpp(0, __float_as_int(v), CTRL, 0xf, 0xf, true));
}
DEV Chan chan_fin(const ChanRaw& r) {
  Chan c;
  const int t = __float_as_int(rbf(dpp_add<0x141>(dpp_add<0x4e>(dpp_add<0xb1>(r.m)))));
  c.sa = __int_as_float(__builtin_amdgcn_readlane(t, 0));
  c.ca1 = rbf(1.f + __int_as_float(__builtin_amdgcn_readlane(t, 8)));
  c.ga = __int_as_float(__builtin_amdgcn_readlane(t, 16));
  c.dsk = r.dsk;
  const float hb = 26.f * fabsf(c.ca1) + fabsf(c.sa);
  int ex = (int)((__float_as_uint(hb) >> 23) & 0xff);
  ex = ex < 20 ? 20 : (ex > 240 ? 240 : ex);
  c.hs = __uint_as_float((unsigned)(127 + 14 + 127 - ex) << 23);
  c.hinv = __uint_as_float((unsigned)(ex - 14) << 23);
  c.yinv = r.kinv * c.hinv;
  return c;
}
DEV size_t tl_off(int d, int t) { return (((size_t)(t >> 5) * D_ + d) * 32 + (t & 31)) * 2; }
struct StageRaw { u32x4 xa0, xa1, kv0, kv1; u32x2 xb; };
DEV StageRaw stage_ld(const char* ws, int d, int th) {
  StageRaw s;
  const unsigned short* krev = (const unsigned short*)(ws + WS_KREV) + (size_t)d * KREV;
  const size_t xo = tl_off(d, 16 * th);
  s.xa0 = *(const u32x4*)(ws + WS_XNT + xo); s.xa1 = *(const u32x4*)(ws + WS_XNT + xo + 16);
  s.xb = *(const u32x2*)(ws + WS_XNT + tl_off(d, min(16 * th + 16, L_ - 4)));
  s.kv0 = *(const u32x4*)(krev + 8 * th); s.kv1 = *(const u32x4*)(krev + 1024 + 8 * th);
  return s;
}
DEV void stage_wr(const StageRaw& s, const Chan& c, char* smem, int th) {
  constexpr int CS = K2_CS, KOFF = K2_KOFF;
  unsigned hh[10];
#pragma unroll
  for (int q = 0; q < 10; ++q) {
    unsigned u = q < 4 ? s.xa0[q] : (q < 8 ? s.xa1[q - 4] : s.xb[q - 8]);
    unsigned m = pk_bf(bf_lo(u) * c.ca1, bf_hi(u) * c.ca1);
    unsigned h = pk_bf(bf_lo(m) + c.sa, bf_hi(m) + c.sa);
    hh[q] = pk_h(bf_lo(h) * c.hs, bf_hi(h) * c.hs);
  }
  char* dst = smem + (2 * th + 2) * 16;
  unsigned s1[9];
#pragma unroll
  for (int q = 0; q < 9; ++q) s1[q] = __builtin_amdgcn_alignbit(hh[q + 1], hh[q], 16);
#pragma unroll
  for (int e = 0; e < 2; ++e) {
    *(u32x4*)(dst + 16 * e) = (u32x4){hh[4 * e], hh[4 * e + 1], hh[4 * e + 2], hh[4 * e + 3]};
    *(u32x4*)(dst + 16 * e + CS) = (u32x4){s1[4 * e], s1[4 * e + 1], s1[4 * e + 2], s1[4 * e + 3]};
    *(u32x4*)(dst + 16 * e + 2 * CS) = (u32x4){hh[4 * e + 1], hh[4 * e + 2], hh[4 * e + 3], hh[4 * e + 4]};
    *(u32x4*)(dst + 16 * e + 3 * CS) = (u32x4){s1[4 * e + 1], s1[4 * e + 2], s1[4 * e + 3], s1[4 * e + 4]};
  }
  if (th == 0) {
    u32x4 z = {0u, 0u, 0u, 0u};
    *(u32x4*)(smem) = z; *(u32x4*)(smem + CS) = z; *(u32x4*)(smem + 2 * CS) = z; *(u32x4*)(smem + 3 * CS) = z;
    *(u32x4*)(smem + 16) = z;
    *(u32x4*)(smem + CS + 16) = (u32x4){0u, 0u, 0u, hh[0] << 16};
    *(u32x4*)(smem + 2 * CS + 16) = (u32x4){0u, 0u, 0u, hh[0]};
    *(u32x4*)(smem + 3 * CS + 16) = (u32x4){0u, 0u, hh[0] << 16, s1[0]};
  }
  *(u32x4*)(smem + KOFF + 16 * th) = s.kv0;
  *(u32x4*)(smem + KOFF + 2048 + 16 * th) = s.kv1;
  if (th < 32) *(u32x4*)(smem + KOFF + 4096 + 16 * th) = (u32x4){0u, 0u, 0u, 0u};
}
DEV unsigned add_bf(unsigned a, unsigned b) { return pk_bf(bf_lo(a) + bf_lo(b), bf_hi(a) + bf_hi(b)); }
DEV u32x2 conv_out(const f32x4& y, const Chan& c, const char* smem, int t) {
  const u32x2 hv = *(const u32x2*)(smem + 2 * (t + 16));
  u32x2 o;
#pragma unroll
  for (int e = 0; e < 2; ++e) {
    const unsigned hu = hv[e];
    const h16x2 h2 = __builtin_bit_cast(h16x2, hu);
    const float h0 = (float)h2[0] * c.hinv, h1 = (float)h2[1] * c.hinv;
    unsigned yb = pk_bf(y[2 * e] * c.yinv, y[2 * e + 1] * c.yinv);
    unsigned hd = pk_bf(h0 * c.dsk, h1 * c.dsk);
    unsigned y2 = pk_bf(bf_lo(yb) + bf_lo(hd), bf_hi(yb) + bf_hi(hd));
    o[e] = pk_bf(bf_lo(y2) * c.ga, bf_hi(y2) * c.ga);
  }
  return o;
}
#define K2_LOAD(buf, cc, u)                                                                                     \
  asm volatile("ds_read_b64 %0, %3 offset:%5\n\tds_read_b64 %1, %3 offset:%6\n\tds_read_b128 %2, %4 offset:%7"   \
               : "=&v"(Al[buf][u]), "=&v"(Ah[buf][u]), "=&v"(Bf[buf][u])                                          \
               : "v"(ap0), "v"(bp0), "i"(64 * (cc) + 512 * (u)), "i"(64 * (cc) + 512 * (u) + 8),                 \
                 "i"(64 * (cc) + 3584 - 512 * (u)))
template <int SET> DEV constexpr int half_tile(int j) {
  return SET == 0 ? (j == 0 ? 7 : (j == 1 ? 6 : 2)) : (j < 3 ? 5 - j : 4 - j);
}
template <int SET>
DEV void conv_half(unsigned ap0, unsigned bp0, char* redw) {
  constexpr int NU = SET == 0 ? 8 : 6, NT = SET == 0 ? 3 : 5;
  u32x2 Al[2][NU], Ah[2][NU];
  u32x4 Bf[2][NU];
#pragma unroll
  for (int u = 0; u < NU; ++u) K2_LOAD(0, 0, u);
  f32x4 acc[NT];
#pragma unroll
  for (int j = 0; j < NT; ++j) acc[j] = (f32x4){0.f, 0.f, 0.f, 0.f};
#pragma unroll
  for (int cc = 0; cc < 2; ++cc) {
    lgkm0();
    if (cc == 0) {
#pragma unroll
      for (int u = 0; u < NU; ++u) K2_LOAD(1, 1, u);
    }
#pragma unroll
    for (int sp = 0; sp < NU; ++sp)
#pragma unroll
      for (int j = 0; j < NT; ++j) {
        const int q = half_tile<SET>(j);
        if (sp <= q) {
          const u32x4 A = {Al[cc][sp][0], Al[cc][sp][1], Ah[cc][sp][0], Ah[cc][sp][1]};
          acc[j] = mfma_f16(A, Bf[cc][q - sp], acc[j]);
        }
      }
    SB();
  }
#pragma unroll
  for (int j = 0; j < NT; ++j) *(f32x4*)(redw + j * 4096) = acc[j];
}

template <int NF>
DEV void w1_fin(const RtRaw<NF>& t, const ModP& mp, char* ws, int kk, int ct0, int lane, char* scr, float* sums) {
  const int g = lane >> 4, i = lane & 15;
  float* tb = (float*)(scr + 32 * RT_STR);
  const float mv = mod_fin(mp);
  tb[lane] = lane < 32 ? rbf(1.f + mv) : mv;
  rt_park<NF>(t, scr, lane);
  lgkm0();
  const f32x4 ca = *(const f32x4*)(tb + 8 * g), cb = *(const f32x4*)(tb + 8 * g + 4);
  const f32x4 sa = *(const f32x4*)(tb + 32 + 8 * g), sb = *(const f32x4*)(tb + 36 + 8 * g);
  const u32x4 sv = {pk_bf(sa[0], sa[1]), pk_bf(sa[2], sa[3]), pk_bf(sb[0], sb[1]), pk_bf(sb[2], sb[3])};
  const u32x4 ones = {0x3f803f80u, 0x3f803f80u, 0x3f803f80u, 0x3f803f80u};
  const f32x4 z = {0.f, 0.f, 0.f, 0.f};
  const rsrc_t rs = ws_rsrc(ws);
#pragma unroll
  for (int j = 0; j < NF; ++j) {
    const u32x4 f = rt_frag(scr, j, lane);
    const u32x4 sc = {pk_bf(bf_lo(f[0]) * ca[0], bf_hi(f[0]) * ca[1]), pk_bf(bf_lo(f[1]) * ca[2], bf_hi(f[1]) * ca[3]),
                      pk_bf(bf_lo(f[2]) * cb[0], bf_hi(f[2]) * cb[1]), pk_bf(bf_lo(f[3]) * cb[2], bf_hi(f[3]) * cb[3])};
    st16(rs, WS_W1F + ((size_t)(kk * W1KS + (ct0 + j) * W1TS) * 64 + lane) * 16, sc);
    const f32x4 cs = mfma_bf16(sc, ones, z), b1 = mfma_bf16(f, sv, z);
    if (i < 2) *(f32x4*)(sums + i * 80 + 16 * j + 4 * g) = i ? b1 : cs;
  }
}

DEV void conv_phase(const unsigned short* __restrict__ dskip, char* __restrict__ ws,
                    const unsigned short* __restrict__ w1, const unsigned short* __restrict__ w2, char* smem_all) {
  constexpr int CS = K2_CS, KOFF = K2_KOFF;
  const int tid = threadIdx.x, lane = tid & 63;
  const int w = __builtin_amdgcn_readfirstlane(tid >> 6);
  const int bx = blockIdx.x;
  const int pj = (bx & 7) * 16 + (bx >> 4), par = (bx >> 3) & 1;
  const int r = lane & 15, g = lane >> 4, th = tid & 127;
  const int s4 = (r + 1) & 3;
  const rsrc_t rs = ws_rsrc(ws);
  unsigned* fl = (unsigned*)(smem_all + CV_FL);
  char* stamp_ws = ws; (void)stamp_ws;
  STAMP(1, 0);
  if (tid < 6) fl[tid] = 0u;
  if (w < 4) {
    const int d = 5 * pj + 2 * par + (w >> 1), hw = w & 1;
    char* smem = smem_all + (w >> 1) * K2_CH;
    const ChanRaw cr = chan_ld(dskip, ws, d, lane);
    SB();
    const StageRaw sr = stage_ld(ws, d, th);
    u32x4 xres[2];
    auto ld_res = [&]() __attribute__((always_inline)) {
#pragma unroll
      for (int qp = 0; qp < 2; ++qp)
        xres[qp] = *(const u32x4*)(ws + WS_XT + tl_off(d, 256 * (4 * hw + 2 * qp + (g & 1)) + 16 * r + 8 * (g >> 1)));
    };
    ld_res();
    SB();
    const Chan c = chan_fin(cr);
    STAMP(1, 1);
    stage_wr(sr, c, smem, th);
    __syncthreads();
    STAMP(1, 2);
    f32x4 acc[8];
#pragma unroll
    for (int q = 0; q < 8; ++q) acc[q] = (f32x4){0.f, 0.f, 0.f, 0.f};
    {
      const unsigned ap0 = (unsigned)(size_t)(smem + s4 * CS + 2 * (128 * hw + 8 * g + (r + 1) - s4));
      const unsigned bp0 = (unsigned)(size_t)(smem + KOFF + 2 * (2032 + 128 * hw - 16 * r + 8 * g) - 3584);
      u32x2 Al[2][8], Ah[2][8];
      u32x4 Bf[2][8];
#pragma unroll
      for (int u = 0; u < 8; ++u) K2_LOAD(0, 0, u);
#pragma unroll
      for (int cc = 0; cc < 4; ++cc) {
        lgkm0();
#pragma unroll
        for (int q = 0; q < 8; ++q) {
          if (cc < 3) K2_LOAD((cc + 1) & 1, cc + 1, q);
#pragma unroll
          for (int sp = 0; sp <= q; ++sp) {
            const u32x4 A = {Al[cc & 1][sp][0], Al[cc & 1][sp][1], Ah[cc & 1][sp][0], Ah[cc & 1][sp][1]};
            acc[q] = mfma_f16(A, Bf[cc & 1][q - sp], acc[q]);
          }
          SB();
        }
      }
    }
    STAMP(1, 3);
    f32x4 keep[4];
    {
      f32x4* xw = (f32x4*)(smem_all + CV_XCH + w * 4096) + lane;
      if (hw) {
#pragma unroll
        for (int q = 0; q < 4; ++q) { xw[64 * q] = acc[q]; keep[q] = acc[4 + q]; }
      } else {
#pragma unroll
        for (int q = 0; q < 4; ++q) { xw[64 * q] = acc[4 + q]; keep[q] = acc[q]; }
      }
    }
    lds_post(fl + w, lane);
    lds_wait(fl + (w ^ 1), 1u);
    STAMP(1, 4);
    const f32x4* xrd = (const f32x4*)(smem_all + CV_XCH + (w ^ 1) * 4096) + lane;
#pragma unroll
    for (int qp = 0; qp < 2; ++qp) {
      u32x2 o[2];
#pragma unroll
      for (int h = 0; h < 2; ++h) {
        const int q = 2 * qp + h;
        o[h] = conv_out(keep[q] + xrd[64 * q], c, smem, 256 * (4 * hw + q) + 16 * r + 4 * g);
      }
      const auto s0 = __builtin_amdgcn_permlane16_swap(o[0][0], o[1][0], false, false);
      const auto s1 = __builtin_amdgcn_permlane16_swap(o[0][1], o[1][1], false, false);
      const int t = 256 * (4 * hw + 2 * qp + (g & 1)) + 16 * r + 8 * (g >> 1);
      st16(rs, WS_X1T + (((size_t)(t >> 6) * D_ + d) * 64 + (t & 63)) * 2,
           (u32x4){add_bf(xres[qp][0], s0[0]), add_bf(xres[qp][1], s1[0]), add_bf(xres[qp][2], s0[1]), add_bf(xres[qp][3], s1[1])});
    }
  } else {
    __builtin_amdgcn_s_setprio(2);
    const int d = 5 * pj + 4, wq = w - 4;
    char* smem = smem_all + 2 * K2_CH;
    StageRaw sr;
    const ChanRaw cr = chan_ld(dskip, ws, d, lane);
    asm volatile("" ::: "memory");
    if (wq < 2) sr = stage_ld(ws, d, th);
    asm volatile("" ::: "memory");
    const int tl0 = par ? (wq < 3 ? 5 - wq : 1) : (wq == 0 ? 7 : (wq == 1 ? 6 : 2)), tl1 = 0;
    const unsigned ap0 = (unsigned)(size_t)(smem + s4 * CS + 2 * (64 * wq + 8 * g + (r + 1) - s4));
    const unsigned bp0 = (unsigned)(size_t)(smem + KOFF + 2 * (2032 + 64 * wq - 16 * r + 8 * g) - 3584);
    char* redw = smem_all + CV_RED + wq * 1024 + lane * 16;
    u32x2 xres[2];
    xres[0] = *(const u32x2*)(ws + WS_XT + tl_off(d, 256 * tl0 + 16 * r + 4 * g));
    xres[1] = *(const u32x2*)(ws + WS_XT + tl_off(d, 256 * tl1 + 16 * r + 4 * g));
    SB();
    const Chan c = chan_fin(cr);
    if (wq < 2) {
      stage_wr(sr, c, smem, th);
    } else {
#define RT_SLEEP 32
      __builtin_amdgcn_s_sleep(RT_SLEEP);
    }
    const bool isw1 = bx < 110;
    const int b2 = min(max(bx - 110, 0), 107);
    const int jk = isw1 ? 4 * (bx / 22) + wq : b2 >> 1;
    const int jc = isw1 ? min(5 * (bx % 22), 103) : (b2 & 1) * 20 + 5 * wq;
    RtRaw<5> rt;
    rt_load<5>(rt, isw1 ? w1 : w2, isw1 ? FF : D_, jk, jc, lane);
    const int gbi = 64 * (4 * (bx - 218) + wq) + lane;
    const bool isgb = bx >= 218 && 4 * (bx - 218) + wq < 10;
    ModP mp;
    SB();
    bar_lds();
    STAMP(1, 2);
    if (isw1 || isgb) mp = mod_ld(ws, isw1 ? (lane < 32 ? 4 * D_ : 3 * D_ - 32) + 32 * jk + lane : 5 * D_ + gbi);
    SB();
    if (par) conv_half<1>(ap0, bp0, redw); else conv_half<0>(ap0, bp0, redw);
    lds_post(fl + 4, lane);
    STAMP(1, 3);
    char* scr = smem_all + CV_RT2 + wq * (32 * RT_STR + 256);
    if (isw1) {
      float* sums = (float*)(smem_all + CV_SUM);
      w1_fin<5>(rt, mp, ws, jk, jc, lane, scr, sums + 160 * wq);
      asm volatile("" ::: "memory");
      unsigned old = 0u;
      if (lane == 0) old = __hip_atomic_fetch_add(fl + 5, 1u, __ATOMIC_RELAXED, __HIP_MEMORY_SCOPE_WORKGROUP);
      asm volatile("" ::: "memory");
      if (__builtin_amdgcn_readfirstlane(old) == 3u && lane < 40) {
        const f32x4* sp = (const f32x4*)sums + lane;
        const f32x4 v = (sp[0] + sp[40]) + (sp[80] + sp[120]);
        st16(rs, WS_CSP + ((size_t)(2 * (bx / 22) + lane / 20) * FF + 16 * jc + 4 * (lane % 20)) * 4, __builtin_bit_cast(u32x4, v));
      }
    } else if (bx < 218) {
      rt_park<5>(rt, scr, lane);
      lgkm0();
#pragma unroll
      for (int j = 0; j < 5; ++j) st16(rs, WS_W2F + ((size_t)((jc + j) * 54 + jk) * 64 + lane) * 16, rt_frag(scr, j, lane));
    } else if (isgb) {
      st4(ws, WS_GB + 4 * gbi, __float_as_uint(mod_fin(mp)));
    }
    STAMP(1, 4);
    lds_wait(fl + 4, 4u);
    STAMP(1, 5);
    const char* rd = smem_all + CV_RED + lane * 16;
#pragma unroll
    for (int u = 0; u < 2; ++u) {
      const bool on = u == 0 ? (par || wq < 3) : (par && wq == 0);
      if (on) {
        const int sl = u == 0 ? wq : 4, tl = u == 0 ? tl0 : tl1;
        const f32x4 y = (*(const f32x4*)(rd + sl * 4096) + *(const f32x4*)(rd + sl * 4096 + 1024))
                      + (*(const f32x4*)(rd + sl * 4096 + 2048) + *(const f32x4*)(rd + sl * 4096 + 3072));
        const int t = 256 * tl + 16 * r + 4 * g;
        const u32x2 gy = conv_out(y, c, smem, t);
        st8(rs, WS_X1T + (((size_t)(t >> 6) * D_ + d) * 64 + (t & 63)) * 2, (u32x2){add_bf(xres[u][0], gy[0]), add_bf(xres[u][1], gy[1])});
      }
    }
  }
}

__global__ NOPK __launch_bounds__(512, 2) void k2(const unsigned short* __restrict__ dskip, char* __restrict__ ws,
                                                  const unsigned short* __restrict__ w1, const unsigned short* __restrict__ w2) {
  __shared__ __attribute__((aligned(16))) char smem[CV_XCH + 16384];
  TS(1, ws);
  conv_phase(dskip, ws, w1, w2, smem);
  char* stamp_ws = ws; (void)stamp_ws;
  VM0();
  STAMP(1, 7);
}

DEV u32x4 ld16_sc1(rsrc_t r, int voff, int soff) { return __builtin_amdgcn_raw_buffer_load_b128(r, voff, soff, 16); }
DEV u32x4 ld16(rsrc_t r, int voff, int soff) { return __builtin_amdgcn_raw_buffer_load_b128(r, voff, soff, 0); }
DEV u32x4 tr_frag(const char* p) {
  const u32x2 a = __builtin_bit_cast(u32x2, __builtin_amdgcn_ds_read_tr16_b64_v4i16((lds_s16x4*)p));
  const u32x2 b = __builtin_bit_cast(u32x2, __builtin_amdgcn_ds_read_tr16_b64_v4i16((lds_s16x4*)(p + 256)));
  return (u32x4){a[0], a[1], b[0], b[1]};
}

constexpr int KF_RS = 84;
constexpr int KF_RB = 8 * 16 * KF_RS * 4;
constexpr int KF_BIG = 2 * KF_RB;
constexpr int NW2L = 8, NW2R = 27;
constexpr int KF_TAB = KF_BIG;
constexpr int KF_PS = KF_TAB + 320;
constexpr int KF_XR = KF_PS + 512;
constexpr int KF_CNT = KF_XR + 64 * 176;
constexpr int KF_LDS = KF_CNT + 16;
#define X_FIRST 5
#define X_STEP 4
#define W1_EARLY 3
#define W1_MID 7
#define W1_PRE 10
#define W2_START 6
#define W2_RATE 2
#define W2_INL 28
#define CS_STEP 14
#define SIG_SPLIT 4
#define H1_PRE 9
#define H1_BATCH 7
#define LOCK_MASK 0x55554u

DEV void kf_body(char* __restrict__ ws, unsigned short* __restrict__ out, char* smem) {
  char* xs = smem;
  float* tab = (float*)(smem + KF_TAB);
  float* ps = (float*)(smem + KF_PS);
  char* xr = smem + KF_XR;
  unsigned* lcnt = (unsigned*)(smem + KF_CNT);
  float* cst = (float*)(smem + 81920);
  const int tid = threadIdx.x, lane = tid & 63;
  const int w = __builtin_amdgcn_readfirstlane(tid >> 6);
  const int bx = blockIdx.x;
  const int ng = (bx & 1) * 4 + ((bx >> 3) & 3), mt = ((bx & 7) >> 1) * 8 + (bx >> 5);
  const int g = lane >> 4, i = lane & 15;
  const rsrc_t rs_ = ws_rsrc(ws);

  char* stamp_ws = ws; (void)stamp_ws;
  STAMP(2, 0);
  unsigned* flags = (unsigned*)(ws + WS_CNT) + mt * 128;
  const int ntl = ng < 6 ? 14 : 12;
  const int ctb = ng < 6 ? 14 * ng : 84 + 12 * (ng - 6);
  const bool two = w < ntl - 8;
  const bool full = w < 6;
  const int ks0 = w < 6 ? 7 * w : 42 + 6 * (w - 6);
  const int ct0 = ctb + (two ? 2 * w : (ntl - 8) + w);
  const int lo16 = lane * 16;
  const int w1s = (int)WS_W1F + ct0 * W1TS * 1024;
  const int w2s = (int)WS_W2F + (ng * 5 * 54 + ks0) * 1024;

  u32x4 ring[32], wl[NW2L];
  const char* bb0 = xs + (8 * g + (i >> 2)) * 64 + ((g & 1) << 5) + (i & 3) * 8;
  const char* bb1 = xs + (8 * g + (i >> 2)) * 64 + (((g & 1) ^ 1) << 5) + (i & 3) * 8;
  auto w2_off = [&](int e) __attribute__((always_inline)) {
    const int s = e < 27 ? e / 4 : (e < 34 ? e - 27 : 6), c = e < 27 ? e % 4 : (e < 34 ? 4 : 3);
    const int sn = (s == 6 && !full) ? 5 : s;
    return w2s + (c * 54 + sn) * 1024;
  };

  auto gemm1 = [&](auto two_tag) __attribute__((always_inline)) {
    constexpr bool TWO = decltype(two_tag)::value;
    if (TWO) asm volatile("; two tiles" ::: "memory"); else asm volatile("; one tile" ::: "memory");
    u32x4 t[10];
    auto st_x = [&](int i0, int i1) __attribute__((always_inline)) {
#pragma unroll
      for (int it = i0; it < i1; ++it) {
        const int c = tid + 512 * it;
        const int row = c >> 3;
        *(u32x4*)(xs + ((c >> 2) & 1) * 40960 + row * 64 + (((c & 3) ^ ((row >> 2) & 2)) << 4)) = t[it];
      }
    };
#pragma unroll
    for (int it = 0; it < X_FIRST; ++it) t[it] = ld16(rs_, tid * 16, (int)WS_X1T + mt * 81920 + 8192 * it);
#pragma unroll
    for (int k = 0; k < W1_EARLY; ++k) {
      ring[2 * k] = ld16(rs_, lo16, w1s + k * W1KS * 1024);
      if (TWO) ring[2 * k + 1] = ld16(rs_, lo16, w1s + (k * W1KS + W1TS) * 1024);
    }
    SB();
    __builtin_amdgcn_s_barrier();
    SB();
#pragma unroll
    for (int k = W1_EARLY; k < W1_MID; ++k) {
      ring[2 * k] = ld16(rs_, lo16, w1s + k * W1KS * 1024);
      if (TWO) ring[2 * k + 1] = ld16(rs_, lo16, w1s + (k * W1KS + W1TS) * 1024);
    }
    SB();
    st_x(0, X_FIRST);
    SB();
    if (tid == 0) *lcnt = 0u;
    float gbp;
    if (!TWO) gbp = ((const float*)(ws + WS_GB))[80 * ng + min((w & 1) * 64 + lane, 79)];
    SB();
    __syncthreads();
    STAMP(2, 1);
    auto resid = [&]() __attribute__((always_inline)) {
      const int kb = (80 * ng) >> 5, tq = w & 3;
#pragma unroll
      for (int u = 0; u < 2; ++u) {
        const int kk = min(kb + 2 * (w >> 2) + u, 19);
        const u32x4 f = tr_frag(((tq & 1) ? bb1 : bb0) + (tq >> 1) * 40960 + kk * 2048);
        const unsigned dl = (unsigned)(32 * kk + 8 * g - 80 * ng);
        if (dl < 80u) *(u32x4*)(xr + (tq * 16 + i) * 176 + dl * 2) = f;
      }
    };
    const int hs = TWO ? 0 : (w & 1);
    const char* ba[4] = {bb0 + hs * 40960, bb1 + hs * 40960, bb0 + (hs ^ 1) * 40960, bb1 + (hs ^ 1) * 40960};
    const u32x4 ones = {0x3f803f80u, 0x3f803f80u, 0x3f803f80u, 0x3f803f80u};
    f32x4 acc[2][4];
#pragma unroll
    for (int c = 0; c < 2; ++c)
#pragma unroll
      for (int q = 0; q < 4; ++q) acc[c][q] = (f32x4){0.f, 0.f, 0.f, 0.f};
    u32x4 csp[5];
    const int cso = ((w & 1) * FF + 16 * ctb + 4 * min(lane, 4 * ntl - 1)) * 4;
    u32x4 bn[2][4];
#pragma unroll
    for (int q = 0; q < 4; ++q) bn[0][q] = tr_frag(ba[q]);
#pragma unroll
    for (int q = 0; q < 4; ++q) bn[1][q] = tr_frag(ba[q] + 2048);
#pragma unroll
    for (int kk = 0; kk < 20; ++kk) {
      if (kk == X_STEP) { st_x(X_FIRST, 10); SB(); }
      if (kk == 2 * X_FIRST - 2) { SB(); bar_lds(); SB(); }
      else if ((LOCK_MASK >> kk) & 1) { SB(); __builtin_amdgcn_s_barrier(); SB(); }
      if (kk == 2 * X_FIRST) { resid(); SB(); }
      auto gload = [&](int u) __attribute__((always_inline)) {
        {
          constexpr int CATCH = W1_PRE - W1_MID;
          const int k0 = kk == 0 ? W1_MID : (kk <= CATCH ? W1_MID + 2 * kk - 1 : kk + W1_PRE);
          const int nk = (kk >= 1 && kk <= CATCH) ? 2 : 1;
          const int kn = k0 + (nk == 2 ? (u >> 1) : 0);
          const bool on = nk == 2 || u >= 2;
          if (on && kn < 20) {
            if ((u & 1) == 0) ring[(2 * kn) % 32] = ld16(rs_, lo16, w1s + kn * W1KS * 1024);
            else if (TWO) ring[(2 * kn + 1) % 32] = ld16(rs_, lo16, w1s + (kn * W1KS + W1TS) * 1024);
          }
          if (kk == 0 && u < 2) {
#pragma unroll
            for (int it = X_FIRST + u * 3; it < (u ? 10 : (X_FIRST + 3 < 10 ? X_FIRST + 3 : 10)); ++it)
              t[it] = ld16(rs_, tid * 16, (int)WS_X1T + mt * 81920 + 8192 * it);
          }
        }
        if (kk >= W2_START && u < W2_RATE) {
          const int n = W2_RATE * (kk - W2_START) + u;
          if (n < NW2L) wl[n] = ld16(rs_, lo16, w2_off(NW2R + n));
          else if (n < W2_INL) ring[8 + n - NW2L] = ld16(rs_, lo16, w2_off(n - NW2L));
        }
      };
      const u32x4 a0 = ring[(2 * kk) % 32], a1 = ring[(2 * kk + 1) % 32];
#pragma unroll
      for (int q = 0; q < 4; ++q) {
        const u32x4 bq = bn[kk & 1][q];
        acc[0][q] = mfma_bf16(a0, bq, acc[0][q]);
        if (TWO) acc[1][q] = mfma_bf16(a1, bq, acc[1][q]);
        else if (q < 2) {
          acc[1][2 * q] = mfma_bf16(ones, bq, acc[1][2 * q]);
          acc[1][2 * q + 1] = mfma_bf16(bq, bq, acc[1][2 * q + 1]);
        }
        SB();
        if (kk < 18) bn[kk & 1][q] = tr_frag(ba[q] + (kk + 2) * 2048);
        gload(q);
        SB();
      }
      if (!TWO) {
        if (kk < 5) csp[kk] = ld16(rs_, cso, (int)WS_CSP + kk * 2 * FF * 4);
        if (kk == CS_STEP) {
          f32x4 t = __builtin_bit_cast(f32x4, csp[0]);
#pragma unroll
          for (int u = 1; u < 5; ++u) t += __builtin_bit_cast(f32x4, csp[u]);
          if (w >= 6 && lane < 56) *(f32x4*)(cst + (w & 1) * 224 + 4 * lane) = t;
        }
      }
      if (!TWO && kk == 12) { if ((w & 1) * 64 + lane < 80) tab[(w & 1) * 64 + lane] = gbp; }
      SB();
    }
    STAMP(2, 2);
    if (!TWO) {
#pragma unroll
      for (int h = 0; h < 2; ++h) {
        const f32x4 aq = acc[1][2 * h + 1];
        const float dq = (i & 2) ? ((i & 1) ? aq[3] : aq[2]) : ((i & 1) ? aq[1] : aq[0]);
        const int tk = (2 * hs + h) * 16 + i;
        if (g == (i >> 2)) {
          const float mean = acc[1][2 * h][0] * (1.f / D_);
          const float var = fmaxf(dq * (1.f / D_) - mean * mean, 0.f);
          const float sd = rbf(sqrtf(rbf(rbf(var) + 1e-5f)));
          const float rs = 1.f / sd;
          ps[tk] = rs;
          ps[64 + tk] = -rbf(mean) * rs;
        }
      }
    }
    __syncthreads();
    f32x4 csv[2], b1v[2];
#pragma unroll
    for (int c = 0; c < (TWO ? 2 : 1); ++c) {
      csv[c] = *(const f32x4*)(cst + 16 * (ct0 - ctb + c) + 4 * g);
      b1v[c] = *(const f32x4*)(cst + 224 + 16 * (ct0 - ctb + c) + 4 * g);
    }
#pragma unroll
    for (int qp = 0; qp < 2; ++qp) {
      u32x2 o[2][2];
#pragma unroll
      for (int h = 0; h < 2; ++h) {
        const int q = 2 * qp + h, tq = q ^ (2 * hs);
        const float rs = ps[tq * 16 + i], nm = ps[64 + tq * 16 + i];
#pragma unroll
        for (int c = 0; c < (TWO ? 2 : 1); ++c) {
          float v[4];
#pragma unroll
          for (int r = 0; r < 4; ++r) v[r] = fmaxf(fmaf(acc[c][q][r], rs, fmaf(nm, csv[c][r], b1v[c][r])), 0.f);
          o[c][h] = (u32x2){pk_bf(v[0], v[1]), pk_bf(v[2], v[3])};
        }
      }
      const int tl = ((2 * qp) ^ (2 * hs)) + (g & 1);
#pragma unroll
      for (int c = 0; c < (TWO ? 2 : 1); ++c) {
        const int ct = ct0 + c;
        const auto s0 = __builtin_amdgcn_permlane16_swap(o[c][0][0], o[c][1][0], false, false);
        const auto s1 = __builtin_amdgcn_permlane16_swap(o[c][0][1], o[c][1][1], false, false);
        const size_t off = (((size_t)(4 * mt + tl) * 54 + (ct >> 1)) * 64 + i + 16 * (2 * (ct & 1) + (g >> 1))) * 16;
        st16(rs_, WS_H1F + off, (u32x4){s0[0], s1[0], s0[1], s1[1]});
      }
    }
    SB();
    __builtin_amdgcn_s_barrier();
    SB();
    constexpr int E0 = W2_INL - NW2L;
    auto signal = [&]() __attribute__((always_inline)) {
      if (lane == 0) {
        if (__hip_atomic_fetch_add(lcnt, 1u, __ATOMIC_RELAXED, __HIP_MEMORY_SCOPE_WORKGROUP) == 7u)
          __hip_atomic_store(flags + 16 * ng, 1u, __ATOMIC_RELAXED, __HIP_MEMORY_SCOPE_AGENT);
      }
    };
    if (w >= SIG_SPLIT) { asm volatile("s_waitcnt vmcnt(0)" ::: "memory"); signal(); }
    SB();
#pragma unroll
    for (int e = E0; e < NW2R; ++e) ring[(8 + e) % 32] = ld16(rs_, lo16, w2_off(e));
    SB();
    if (w < SIG_SPLIT) { asm volatile("s_waitcnt vmcnt(%0)" :: "n"(NW2R - E0) : "memory"); signal(); }
    SB();
  };
  if (two) gemm1(std::true_type{});
  else gemm1(std::false_type{});
  STAMP(2, 3);
#pragma nounroll
  for (unsigned spin = 0; spin < (1u << 20); ++spin) {
    if (__hip_atomic_load(flags + 16 * w, __ATOMIC_RELAXED, __HIP_MEMORY_SCOPE_AGENT) != 0u) break;
    __builtin_amdgcn_s_sleep(1);
  }
  asm volatile("" ::: "memory");
  SB();

  STAMP(2, 4);
  const int h1s = (int)WS_H1F + (mt * 4 * 54 + ks0) * 1024;
  u32x4 hb[28];
  auto load_h1 = [&](int n) __attribute__((always_inline)) {
    const int q = n / 7, s = n % 7;
    const int sn = (s == 6 && !full) ? 5 : s;
    hb[n] = ld16_sc1(rs_, lo16, h1s + (q * 54 + sn) * 1024);
  };
#pragma unroll
  for (int n = 0; n < H1_PRE; ++n) {
    if (n == H1_BATCH) { SB(); __builtin_amdgcn_s_barrier(); SB(); }
    load_h1(n);
  }
  SB();
  float* red = (float*)smem;
  float* rb_ = red + (w * 16 + i) * KF_RS + 4 * g;
  const int re = w * 40 + min(lane, 39);
  const int rtk = re / 20, rq4 = re % 20;
  const float* rr_ = red + rtk * KF_RS + 4 * rq4;
  f32x4 ac2[2][5];
  f32x4 rv[8], rsum;
  u32x2 xv2;
  const f32x4 gb = *(const f32x4*)(tab + 4 * rq4);
  auto red_wr = [&](int q, int c) __attribute__((always_inline)) {
    *(f32x4*)(rb_ + (q & 1) * (KF_RB / 4) + 16 * c) = ac2[q & 1][c];
  };
  auto red_rd = [&](int q, int ww) __attribute__((always_inline)) {
    rv[ww] = *(const f32x4*)(rr_ + (q & 1) * (KF_RB / 4) + ww * 16 * KF_RS);
  };
  auto red_x = [&](int q) __attribute__((always_inline)) { xv2 = *(const u32x2*)(xr + (16 * q + rtk) * 176 + rq4 * 8); };
  auto red_out = [&](int q) __attribute__((always_inline)) {
    const f32x4 s = rsum + (rv[6] + rv[7]);
    u32x2 o;
#pragma unroll
    for (int h = 0; h < 2; ++h) {
      unsigned f = pk_bf(s[2 * h], s[2 * h + 1]);
      unsigned gf = pk_bf(bf_lo(f) * gb[2 * h], bf_hi(f) * gb[2 * h + 1]);
      o[h] = pk_bf(bf_lo(xv2[h]) + bf_lo(gf), bf_hi(xv2[h]) + bf_hi(gf));
    }
    *(u32x2*)(out + (size_t)(mt * 64 + 16 * q + rtk) * D_ + ng * 80 + 4 * rq4) = o;
  };
#pragma unroll
  for (int q = 0; q < 3; ++q) {
#pragma unroll
    for (int c = 0; c < 5; ++c) ac2[q & 1][c] = (f32x4){0.f, 0.f, 0.f, 0.f};
#pragma unroll
    for (int s = 0; s < 7; ++s) {
      auto mm = [&](int c) __attribute__((always_inline)) {
        const u32x4 a = c == 4 ? wl[s] : ((s == 6 && c == 3) ? wl[7] : ring[(8 + 4 * s + c) % 32]);
        ac2[q & 1][c] = mfma_bf16(a, hb[7 * q + s], ac2[q & 1][c]);
      };
      if (s == 5 && q >= 2) {
        red_wr(q - 1, 4);
        SB();
        red_out(q - 2);
#pragma unroll
        for (int c = 0; c < 5; ++c) mm(c);
#pragma unroll
        for (int c = 0; c < 5; ++c) {
          __builtin_amdgcn_sched_group_barrier(0x008, 1, 0);
          __builtin_amdgcn_sched_group_barrier(0x002, 10, 0);
        }
        SB();
      } else if (s < 6) {
        mm(0); SB();
        if (q >= 1 && s >= 1) { red_wr(q - 1, s - 1); SB(); }
        mm(1); SB();
        if (q >= 2 && s < 4) { red_rd(q - 2, 2 * s); SB(); }
        mm(2); SB();
        if (q >= 2 && s < 4) { red_rd(q - 2, 2 * s + 1); SB(); }
        if (q >= 2 && s == 4) { red_x(q - 2); SB(); }
        mm(3); SB();
        if (q >= 2 && s >= 1 && s < 4) {
          const f32x4 t = rv[2 * s - 2] + rv[2 * s - 1];
          rsum = s == 1 ? t : rsum + t;
          asm volatile("" : "+v"(rsum));
          SB();
        }
        mm(4); SB();
      } else if (full) {
#pragma unroll
        for (int c = 0; c < 5; ++c) mm(c);
        SB();
      }
      if (7 * q + s + H1_PRE < 28) { load_h1(7 * q + s + H1_PRE); SB(); }
    }
    if (q >= 1) bar_lds();
    if (q == 0) STAMP(2, 5);
  }
  {
    auto sum6 = [&]() __attribute__((always_inline)) {
      rsum = ((rv[0] + rv[1]) + (rv[2] + rv[3])) + (rv[4] + rv[5]);
    };
    if (full) {
#pragma unroll
      for (int c = 0; c < 5; ++c) {
        const u32x4 a = c == 4 ? wl[6] : (c == 3 ? wl[7] : ring[(8 + 24 + c) % 32]);
        rv[c] = mfma_bf16(a, hb[27], (f32x4){0.f, 0.f, 0.f, 0.f});
      }
    } else {
#pragma unroll
      for (int c = 0; c < 5; ++c) rv[c] = (f32x4){0.f, 0.f, 0.f, 0.f};
    }
    f32x4 c0[5];
#pragma unroll
    for (int c = 0; c < 5; ++c) c0[c] = rv[c];
    SB();
    auto col = [&](int c, auto&& between) __attribute__((always_inline)) {
#pragma unroll
      for (int s = 0; s < 6; ++s) {
        const u32x4 a = c == 4 ? wl[s] : ring[(8 + 4 * s + c) % 32];
        c0[c] = mfma_bf16(a, hb[21 + s], c0[c]);
        SB();
        between(s);
        SB();
      }
    };
    col(0, [&](int s) __attribute__((always_inline)) { if (s < 5) red_wr(2, s); if (s < 4) { red_rd(1, 2 * s); red_rd(1, 2 * s + 1); } if (s == 4) red_x(1); });
    col(1, [&](int s) __attribute__((always_inline)) { if (s == 1) { sum6(); asm volatile("" : "+v"(rsum)); } if (s == 3) red_out(1); });
    bar_lds();
    auto wr3 = [&](int c) __attribute__((always_inline)) { *(f32x4*)(rb_ + (KF_RB / 4) + 16 * c) = c0[c]; };
    col(2, [&](int s) __attribute__((always_inline)) { if (s < 2) wr3(s); if (s >= 1 && s < 5) { red_rd(2, 2 * s - 2); red_rd(2, 2 * s - 1); } if (s == 5) red_x(2); });
    col(3, [&](int s) __attribute__((always_inline)) { if (s == 1) wr3(2); if (s == 2) { sum6(); asm volatile("" : "+v"(rsum)); } if (s == 4) red_out(2); });
    col(4, [&](int s) __attribute__((always_inline)) { if (s == 1) wr3(3); if (s == 2) red_x(3); });
    wr3(4);
    STAMP(2, 6);
    bar_lds();
#pragma unroll
    for (int ww = 0; ww < 8; ++ww) red_rd(3, ww);
    sum6();
    red_out(3);
    VM0();
    STAMP(2, 7);
  }
}

__global__ NOPK __launch_bounds__(512, 2) void kf(char* __restrict__ ws, unsigned short* __restrict__ out) {
  __shared__ __attribute__((aligned(16))) char smem[KF_LDS];
  TS(2, ws);
  kf_body(ws, out, smem);
}

__global__ void kprobe(char* ws, int which) {
  const unsigned long long now = __builtin_amdgcn_s_memrealtime();
  const unsigned long long* t = (const unsigned long long*)(ws + (200u << 20));
  unsigned long long d = which == 0 ? t[1] - t[0] : (which == 1 ? t[2] - t[1] : (which == 2 ? now - t[2] : 0ull));
  if (d > 10000ull) d = 10000ull;
  while (__builtin_amdgcn_s_memrealtime() - now < 10ull * d) __builtin_amdgcn_s_sleep(1);
}
__global__ void kend_() {}

extern "C" void kernel_launch(void* const* d_in, const int* in_sizes, int n_in,
                              void* d_out, int out_size, void* d_ws, size_t ws_size,
                              hipStream_t stream) {
  K1Args a;
  a.x = (const unsigned short*)d_in[0];
  a.are = (const unsigned short*)d_in[1];
  a.aim = (const unsigned short*)d_in[2];
  a.bre = (const unsigned short*)d_in[3];
  a.bim = (const unsigned short*)d_in[4];
  a.cre = (const unsigned short*)d_in[5];
  a.cim = (const unsigned short*)d_in[6];
  a.logdt = (const unsigned short*)d_in[7];
  a.c = (const unsigned short*)d_in[12];
  a.wmod = (const unsigned short*)d_in[13];
  a.ws = (char*)d_ws;
  k1<<<NB_PF + NB_K + NB_LN + NB_MOD, 256, 0, stream>>>(a);
  k2<<<256, 512, 0, stream>>>((const unsigned short*)d_in[8], (char*)d_ws, (const unsigned short*)d_in[10],
                              (const unsigned short*)d_in[11]);
  kf<<<256, 512, 0, stream>>>((char*)d_ws, (unsigned short*)d_out);
  kprobe<<<1, 64, 0, stream>>>((char*)d_ws, 3);
}
```

```cpp
#include <hip/hip_runtime.h>
#include <hip/hip_bf16.h>
#include <stdint.h>
#include <type_traits>

typedef __attribute__((ext_vector_type(8))) __bf16 bf16x8;
typedef __attribute__((ext_vector_type(2))) __bf16 bf16x2;
typedef __attribute__((ext_vector_type(4))) float f32x4;
typedef __attribute__((ext_vector_type(2))) float f32x2;
typedef __attribute__((ext_vector_type(4))) unsigned u32x4;
typedef __attribute__((ext_vector_type(2))) unsigned u32x2;
typedef __attribute__((ext_vector_type(8))) _Float16 h16x8;
typedef __attribute__((ext_vector_type(2))) _Float16 h16x2;
typedef __attribute__((ext_vector_type(4))) short s16x4;
typedef __attribute__((address_space(3))) s16x4 lds_s16x4;

#if defined(__HIP_DEVICE_COMPILE__)
#define NOPK __attribute__((target("no-packed-fp32-ops")))
#else
#define NOPK
#endif
#define DEV __device__ __forceinline__

#define W1L 0
constexpr int W1KS = W1L ? 1 : 108, W1TS = W1L ? 20 : 1;
#define LNFLIP 0
constexpr int L_ = 2048;
constexpr int D_ = 640;
constexpr int NS = 128;
constexpr int FF = 1728;
constexpr int KREV = 1280;

constexpr size_t WS_GB   = 0;
constexpr size_t WS_KSC  = 8192;
constexpr size_t WS_CNT  = 16384;
constexpr size_t WS_MOD  = 65536;
constexpr size_t WS_CSP  = 196608;
constexpr size_t WS_XNT  = 327680;
constexpr size_t WS_XT   = WS_XNT + (size_t)D_ * L_ * 2;
constexpr size_t WS_KREV = WS_XT + (size_t)D_ * L_ * 2;
constexpr size_t WS_W1F  = WS_KREV + (size_t)D_ * KREV * 2;
constexpr size_t WS_W2F  = WS_W1F + (size_t)D_ * FF * 2;
constexpr size_t WS_X1T  = WS_W2F + (size_t)D_ * FF * 2;
constexpr size_t WS_H1F  = WS_X1T + (size_t)D_ * L_ * 2;
constexpr size_t WS_END  = WS_H1F + (size_t)L_ * FF * 2;

#define STAMP(kid, k) do {} while (0)
#define CSTAMP(kid, k) do {} while (0)
#define VM0() do {} while (0)

DEV float bf_lo(unsigned u) { return __uint_as_float(u << 16); }
DEV float bf_hi(unsigned u) { return __uint_as_float(u & 0xffff0000u); }
DEV unsigned pk_bf(float a, float b) {
  f32x2 v = {a, b};
  bf16x2 r = __builtin_convertvector(v, bf16x2);
  return __builtin_bit_cast(unsigned, r);
}
DEV unsigned pk_h(float a, float b) {
  f32x2 v = {a, b};
  h16x2 r = __builtin_convertvector(v, h16x2);
  return __builtin_bit_cast(unsigned, r);
}
DEV float rbf(float a) { return bf_lo(pk_bf(a, 0.f)); }
DEV float ldbf(const unsigned short* p) { return __uint_as_float(((unsigned)*p) << 16); }
#define CONST_AS __attribute__((address_space(4)))
template <class T> DEV T ldu(const void* p) { return *(const CONST_AS T*)(size_t)p; }
DEV float ldbf_u(const unsigned short* p) {
  const unsigned v = ldu<unsigned>((const void*)((size_t)p & ~(size_t)3));
  return __uint_as_float(((size_t)p & 2) ? (v & 0xffff0000u) : (v << 16));
}

typedef __amdgpu_buffer_rsrc_t rsrc_t;
DEV rsrc_t ws_rsrc(char* ws) { return __builtin_amdgcn_make_buffer_rsrc((void*)ws, 0, (int)WS_END, 0x00020000); }
DEV void st16(rsrc_t r, size_t off, u32x4 v) { __builtin_amdgcn_raw_buffer_store_b128(v, r, (int)off, 0, 16); }
DEV void st4(char* ws, size_t off, unsigned v) { __hip_atomic_store((unsigned*)(ws + off), v, __ATOMIC_RELAXED, __HIP_MEMORY_SCOPE_AGENT); }
DEV void st8(rsrc_t r, size_t off, u32x2 v) { __builtin_amdgcn_raw_buffer_store_b64(v, r, (int)off, 0, 16); }

DEV void bar_lds() { asm volatile("s_waitcnt lgkmcnt(0)\n\ts_barrier" ::: "memory"); }
DEV void lds_wait(const void* word, unsigned val) {
  unsigned tv, ts, cnt;
  asm volatile("s_mov_b32 %2, 0x100000\n"
               "1:\n\tds_read_b32 %0, %3\n\ts_waitcnt lgkmcnt(0)\n\tv_readfirstlane_b32 %1, %0\n\t"
               "s_cmp_ge_u32 %1, %4\n\ts_cbranch_scc1 2f\n\ts_sleep 1\n\ts_sub_u32 %2, %2, 1\n\t"
               "s_cmp_lg_u32 %2, 0\n\ts_cbranch_scc1 1b\n2:"
               : "=&v"(tv), "=&s"(ts), "=&s"(cnt) : "v"((unsigned)(size_t)word), "s"(val) : "memory", "scc");
}
DEV void lds_post(unsigned* word, int lane) {
  asm volatile("" ::: "memory");
  if (lane == 0) __hip_atomic_fetch_add(word, 1u, __ATOMIC_RELAXED, __HIP_MEMORY_SCOPE_WORKGROUP);
  asm volatile("" ::: "memory");
}
DEV void lgkm0() { asm volatile("s_waitcnt lgkmcnt(0)" ::: "memory"); __builtin_amdgcn_sched_barrier(0); }

DEV f32x4 mfma_bf16(u32x4 a, u32x4 b, f32x4 c) {
  return __builtin_amdgcn_mfma_f32_16x16x32_bf16(__builtin_bit_cast(bf16x8, a), __builtin_bit_cast(bf16x8, b), c, 0, 0, 0);
}

constexpr int NB_PF = 8, NB_K = 640, NB_LN = 256, NB_MOD = 120;

struct K1Args {
  const unsigned short *x, *are, *aim, *bre, *bim, *cre, *cim, *logdt, *c, *wmod;
  char* ws;
};

DEV void k1_prefetch() {
  const int tid = threadIdx.x;
  unsigned long long pc;
  int o0, o1;
  asm volatile("s_getpc_b64 %0\n.Lpf_here%=:\n\ts_mov_b32 %1, _Z2k16K1Args - .Lpf_here%=\n\ts_mov_b32 %2, _Z5kend_v - .Lpf_here%="
               : "=s"(pc), "=s"(o0), "=s"(o1));
  const char* c0 = (const char*)(pc + (long long)o0);
  long n = o1 - o0;
  n = n < 0 ? 0 : (n > 98304 ? 98304 : n);
  unsigned v[3];
#pragma unroll
  for (int it = 0; it < 3; ++it) {
    const long o = (long)tid * 128 + it * 32768;
    v[it] = o < n ? *(const __attribute__((address_space(1))) unsigned*)(c0 + o) : 0u;
  }
  asm volatile("" :: "v"(v[0] ^ v[1] ^ v[2]));
}

DEV void k1_mod(const K1Args& p, int b, char* smem) {
  const int tid = threadIdx.x;
  const int rg = b & 7, cgp = b >> 3;
  const int c32 = tid & 31, r8 = tid >> 5;
  float* sil = (float*)smem;
  float* red = (float*)(smem + 512);
  char* stamp_ws = p.ws; (void)stamp_ws;
  STAMP(0, 1);
  u32x4 w[10];
  const float cv = ldbf(p.c + rg * 80 + min(tid, 79));
  asm volatile("" ::: "memory");
#pragma unroll
  for (int it = 0; it < 10; ++it)
    w[it] = *(const u32x4*)(p.wmod + (size_t)(rg * 80 + r8 + 8 * it) * 3840 + 256 * cgp + 8 * c32);
  if (tid < 80) sil[tid] = rbf(cv / (1.f + expf(-cv)));
  if (tid >= 128 && tid < 132 && b < 64) st4(p.ws, WS_CNT + (b * 4 + tid - 128) * 64, 0u);
  __syncthreads();
  STAMP(0, 2);
  float acc[8];
#pragma unroll
  for (int j = 0; j < 8; ++j) acc[j] = 0.f;
#pragma unroll
  for (int it = 0; it < 10; ++it) {
    float s = sil[r8 + 8 * it];
#pragma unroll
    for (int q = 0; q < 4; ++q) {
      acc[2 * q] += s * bf_lo(w[it][q]);
      acc[2 * q + 1] += s * bf_hi(w[it][q]);
    }
  }
#pragma unroll
  for (int j = 0; j < 8; ++j) acc[j] += __shfl_xor(acc[j], 32, 64);
  if ((tid & 63) < 32) {
    *(f32x4*)(red + (tid >> 6) * 256 + 8 * c32) = (f32x4){acc[0], acc[1], acc[2], acc[3]};
    *(f32x4*)(red + (tid >> 6) * 256 + 8 * c32 + 4) = (f32x4){acc[4], acc[5], acc[6], acc[7]};
  }
  __syncthreads();
  STAMP(0, 3);
  if (tid < 64) {
    const f32x4* r4 = (const f32x4*)red + tid;
    st16(ws_rsrc(p.ws), WS_MOD + (rg * 3840 + 256 * cgp + 4 * tid) * 4,
         __builtin_bit_cast(u32x4, (r4[0] + r4[64]) + (r4[128] + r4[192])));
  }
}
struct ModP { f32x4 a, b; };
DEV ModP mod_ld(rsrc_t rs, int idx) {
  ModP m;
#pragma unroll
  for (int j = 0; j < 4; ++j) {
    m.a[j] = __uint_as_float(__builtin_amdgcn_raw_buffer_load_b32(rs, 4 * idx, (int)WS_MOD + 15360 * j, 0));
    m.b[j] = __uint_as_float(__builtin_amdgcn_raw_buffer_load_b32(rs, 4 * idx, (int)WS_MOD + 15360 * (4 + j), 0));
  }
  return m;
}
DEV float mod_fin(const ModP& m) {
  return rbf(((m.a[0] + m.a[1]) + (m.a[2] + m.a[3])) + ((m.b[0] + m.b[1]) + (m.b[2] + m.b[3])));
}
#define SB() __builtin_amdgcn_sched_barrier(0)

DEV void k1_ln(const K1Args& p, int b, char* smem) {
  constexpr int STR = 704;
  const int tid = threadIdx.x;
  const int tok = tid >> 3, sub = tid & 7;
  const int tile = (b & 7) * 8 + ((b >> 3) & 7), pass = (b >> 6) ^ LNFLIP;
  const int hf = pass & 1;
  const bool norm = pass < 2;
  const int t0 = tile * 32;
  const unsigned short* xr = p.x + (size_t)(t0 + tok) * D_ + 8 * sub;
  u32x4 xv[5], xo[5];
  char* stamp_ws = p.ws; (void)stamp_ws;
  STAMP(0, 1);
#pragma unroll
  for (int m = 0; m < 5; ++m) xv[m] = *(const u32x4*)(xr + 320 * hf + 64 * m);
  if (norm) {
#pragma unroll
    for (int m = 0; m < 5; ++m) xo[m] = *(const u32x4*)(xr + 320 * (hf ^ 1) + 64 * m);
    float s = 0.f, v = 0.f;
    const bf16x2 one2 = __builtin_bit_cast(bf16x2, 0x3f803f80u);
#pragma unroll
    for (int m = 0; m < 10; ++m)
#pragma unroll
      for (int q = 0; q < 4; ++q) {
        const unsigned u = m < 5 ? xv[m][q] : xo[m - 5][q];
        const bf16x2 x2 = __builtin_bit_cast(bf16x2, u);
        s = __builtin_amdgcn_fdot2_f32_bf16(x2, one2, s, false);
        v = __builtin_amdgcn_fdot2_f32_bf16(x2, x2, v, false);
      }
#pragma unroll
    for (int o = 1; o < 8; o <<= 1) { s += __shfl_xor(s, o, 64); v += __shfl_xor(v, o, 64); }
    const float mean = s * (1.f / D_);
    const float var = fmaxf(v * (1.f / D_) - mean * mean, 0.f);
    const float sd = rbf(sqrtf(rbf(rbf(var) + 1e-5f)));
    const float rs = 1.f / sd;
    const float mb = rbf(mean);
#pragma unroll
    for (int m = 0; m < 5; ++m)
#pragma unroll
      for (int q = 0; q < 4; ++q) {
        unsigned d2 = pk_bf(bf_lo(xv[m][q]) - mb, bf_hi(xv[m][q]) - mb);
        xv[m][q] = pk_h(bf_lo(d2) * rs, bf_hi(d2) * rs);
      }
  }
  const int lane = tid & 63, w = tid >> 6;
  const int g = lane >> 4, i = lane & 15;
  const rsrc_t rsrc = ws_rsrc(p.ws);
#pragma unroll
  for (int m = 0; m < 5; ++m) *(u32x4*)(smem + tok * STR + (sub + 8 * m) * 16) = xv[m];
  __syncthreads();
  STAMP(0, 2);
  const size_t dsto = (norm ? WS_XNT : WS_XT) + ((size_t)tile * D_ + 320 * hf) * 64;
#pragma unroll
  for (int it = 0; it < 5; ++it) {
    const int cb = w * 5 + it;
    const char* a = smem + (8 * g + (i >> 2)) * STR + cb * 32 + (i & 3) * 8;
    const u32x2 ra = __builtin_bit_cast(u32x2, __builtin_amdgcn_ds_read_tr16_b64_v4i16((lds_s16x4*)a));
    const u32x2 rb = __builtin_bit_cast(u32x2, __builtin_amdgcn_ds_read_tr16_b64_v4i16((lds_s16x4*)(a + 4 * STR)));
    st16(rsrc, dsto + (size_t)(cb * 16 + i) * 64 + 16 * g, (u32x4){ra[0], ra[1], rb[0], rb[1]});
  }
}

constexpr int RT_STR = 288;
struct RtRaw { u32x4 r[8]; };
template <int NF>
DEV void rt_load(RtRaw& t, const unsigned short* W, int N, int kk, int ct0, int lane) {
#pragma unroll
  for (int j = 0; j < NF; ++j) {
    const int c = lane + 64 * j, row = c / (2 * NF), part = c % (2 * NF);
    t.r[j] = *(const u32x4*)(W + (size_t)(32 * kk + row) * N + 16 * ct0 + 8 * part);
  }
}
template <int NF>
DEV void rt_park(const RtRaw& t, char* scr, int lane) {
#pragma unroll
  for (int j = 0; j < NF; ++j) {
    const int c = lane + 64 * j, row = c / (2 * NF), part = c % (2 * NF);
    *(u32x4*)(scr + row * RT_STR + 16 * part) = t.r[j];
  }
}
DEV u32x4 rt_frag(const char* scr, int j, int lane) {
  const int g = lane >> 4, i = lane & 15;
  const char* p = scr + (8 * g + (i >> 2)) * RT_STR + 32 * j + (i & 3) * 8;
  const u32x2 a = __builtin_bit_cast(u32x2, __builtin_amdgcn_ds_read_tr16_b64_v4i16((lds_s16x4*)p));
  const u32x2 b = __builtin_bit_cast(u32x2, __builtin_amdgcn_ds_read_tr16_b64_v4i16((lds_s16x4*)(p + 4 * RT_STR)));
  return (u32x4){a[0], a[1], b[0], b[1]};
}
struct cf { float re, im; };
DEV cf cmul(cf a, cf b) {
  cf r;
  r.re = a.re * b.re - a.im * b.im;
  r.im = a.re * b.im + a.im * b.re;
  return r;
}
DEV cf cexp_(float a2, float br, float m, float m0) {
  float mag = __builtin_amdgcn_exp2f(a2 * (m - m0));
  float ph = __builtin_amdgcn_fractf(br * m);
  cf r; r.re = mag * __builtin_amdgcn_cosf(ph); r.im = mag * __builtin_amdgcn_sinf(ph);
  return r;
}
DEV f32x4 mfma_f16(u32x4 a, u32x4 b, f32x4 c) {
  return __builtin_amdgcn_mfma_f32_16x16x32_f16(__builtin_bit_cast(h16x8, a), __builtin_bit_cast(h16x8, b), c, 0, 0, 0);
}
DEV void put2(char* p, cf a, cf b) {
  u32x2 o = {pk_h(a.re, a.im), pk_h(b.re, b.im)};
  *(u32x2*)p = o;
}

constexpr int KG_RS = 544;
DEV void k1_kgen(const K1Args& p, int d, char* smem) {
  constexpr int RS = KG_RS;
  const int tid = threadIdx.x;
  const int lane = tid & 63;
  const int w = __builtin_amdgcn_readfirstlane(tid >> 6);
  const int g = lane >> 4, i = lane & 15;
  const size_t po = (size_t)d * NS + 2 * lane;
  const unsigned rar = *(const unsigned*)(p.are + po), rai = *(const unsigned*)(p.aim + po);
  unsigned rbr = 0u, rbi = 0u, rcr = 0u, rci = 0u;
  if (w == 0) {
    rbr = *(const unsigned*)(p.bre + po); rbi = *(const unsigned*)(p.bim + po);
    rcr = *(const unsigned*)(p.cre + po); rci = *(const unsigned*)(p.cim + po);
  }
  const float ldt = ldbf_u(p.logdt + d);
  asm volatile("" ::: "memory");
  SB();
  char* stamp_ws = p.ws; (void)stamp_ws;
  const float dt = rbf(expf(ldt));
  asm volatile("" :: "v"(dt));
  STAMP(0, 1);
  float a2[2], bv[2], m0[2];
  a2[0] = (dt * bf_lo(rar)) * 1.44269504088896341f; a2[1] = (dt * bf_hi(rar)) * 1.44269504088896341f;
  bv[0] = (dt * bf_lo(rai)) * 0.15915494309189534f; bv[1] = (dt * bf_hi(rai)) * 0.15915494309189534f;
  m0[0] = a2[0] > 0.f ? 2048.f : 0.f; m0[1] = a2[1] > 0.f ? 2048.f : 0.f;
  char* my = smem + lane * 8;
  if (w == 0) {
    cf v[2], v8[2], e8[2];
    float bound = 0.f;
#pragma unroll
    for (int q = 0; q < 2; ++q) {
      const float dbr = dt * (q ? bf_hi(rbr) : bf_lo(rbr)), dbi = dt * (q ? bf_hi(rbi) : bf_lo(rbi));
      const float cr = q ? bf_hi(rcr) : bf_lo(rcr), ci = q ? bf_hi(rci) : bf_lo(rci);
      const float gsh = __builtin_amdgcn_exp2f(a2[q] * m0[q]);
      v[q].re = (cr * dbr - ci * dbi) * gsh; v[q].im = -(cr * dbi + ci * dbr) * gsh;
      bound += (fabsf(v[q].re) + fabsf(v[q].im)) * __builtin_amdgcn_exp2f(fmaxf(a2[q], 0.f) * 120.f);
      e8[q] = cexp_(a2[q], -bv[q], 8.f, 0.f);
      v8[q] = cexp_(a2[q], -bv[q], 64.f, 0.f);
    }
#pragma unroll
    for (int o = 1; o < 64; o <<= 1) bound += __shfl_xor(bound, o, 64);
    int ex = (int)((__float_as_uint(bound) >> 23) & 0xff);
    ex = ex < 20 ? 20 : (ex > 240 ? 240 : ex);
    const float sv = __uint_as_float((unsigned)(127 + 13 + 127 - ex) << 23);
    if (lane == 0) st4(p.ws, WS_KSC + 4 * d, (unsigned)(ex - 13) << 23);
#pragma unroll
    for (int q = 0; q < 2; ++q) { v[q].re *= sv; v[q].im *= sv; v8[q] = cmul(v[q], v8[q]); }
#pragma unroll
    for (int j = 0; j < 8; ++j) {
      put2(my + (41 + j) * RS, v[0], v[1]);
      put2(my + (49 + j) * RS, v8[0], v8[1]);
      if (j < 7) {
#pragma unroll
        for (int q = 0; q < 2; ++q) { v[q] = cmul(v[q], e8[q]); v8[q] = cmul(v8[q], e8[q]); }
      }
    }
  } else if (w == 1) {
    cf e1[2], u[2], u2[2];
#pragma unroll
    for (int q = 0; q < 2; ++q) {
      e1[q] = cexp_(a2[q], bv[q], 1.f, 0.f);
      u[q].re = __builtin_amdgcn_exp2f(-a2[q] * m0[q]); u[q].im = 0.f;
      u2[q] = cexp_(a2[q], bv[q], 128.f, m0[q]);
    }
#pragma unroll
    for (int r = 0; r < 8; ++r) {
      put2(my + r * RS, u[0], u[1]);
      put2(my + (8 + r) * RS, u2[0], u2[1]);
      if (r < 7) {
#pragma unroll
        for (int q = 0; q < 2; ++q) { u[q] = cmul(u[q], e1[q]); u2[q] = cmul(u2[q], e1[q]); }
      }
    }
  } else if (w == 2) {
    cf e2[2], u[2], u2[2];
#pragma unroll
    for (int q = 0; q < 2; ++q) {
      e2[q] = cexp_(a2[q], bv[q], 2.f, 0.f);
      u[q] = cexp_(a2[q], bv[q], 256.f, m0[q]);
      u2[q] = cexp_(a2[q], bv[q], 384.f, m0[q]);
    }
#pragma unroll
    for (int r = 0; r < 4; ++r) {
      put2(my + (16 + r) * RS, u[0], u[1]);
      put2(my + (20 + r) * RS, u2[0], u2[1]);
      if (r < 3) {
#pragma unroll
        for (int q = 0; q < 2; ++q) { u[q] = cmul(u[q], e2[q]); u2[q] = cmul(u2[q], e2[q]); }
      }
    }
    cf e4[2] = {cmul(e2[0], e2[0]), cmul(e2[1], e2[1])};
#pragma unroll
    for (int r = 0; r < 2; ++r) {
      cf c0 = cexp_(a2[0], bv[0], 512.f + 128.f * r, m0[0]), c1 = cexp_(a2[1], bv[1], 512.f + 128.f * r, m0[1]);
      put2(my + (24 + 2 * r) * RS, c0, c1);
      put2(my + (25 + 2 * r) * RS, cmul(c0, e4[0]), cmul(c1, e4[1]));
    }
  } else {
    cf e4[2], e128[2], u[2], u2[2];
#pragma unroll
    for (int q = 0; q < 2; ++q) {
      e4[q] = cexp_(a2[q], bv[q], 4.f, 0.f);
      e128[q] = cexp_(a2[q], bv[q], 128.f, 0.f);
      u[q] = cexp_(a2[q], bv[q], 1024.f, m0[q]);
      u2[q] = cexp_(a2[q], bv[q], 1536.f, m0[q]);
    }
#pragma unroll
    for (int r = 2; r < 4; ++r) {
      cf c0 = cexp_(a2[0], bv[0], 512.f + 128.f * r, m0[0]), c1 = cexp_(a2[1], bv[1], 512.f + 128.f * r, m0[1]);
      put2(my + (24 + 2 * r) * RS, c0, c1);
      put2(my + (25 + 2 * r) * RS, cmul(c0, e4[0]), cmul(c1, e4[1]));
    }
#pragma unroll
    for (int r = 0; r < 4; ++r) {
      put2(my + (32 + r) * RS, u[0], u[1]);
      put2(my + (36 + r) * RS, u2[0], u2[1]);
#pragma unroll
      for (int q = 0; q < 2; ++q) { if (r < 3) u[q] = cmul(u[q], e128[q]); u2[q] = cmul(u2[q], e128[q]); }
    }
    put2(my + 40 * RS, u2[0], u2[1]);
  }
  __syncthreads();
  STAMP(0, 2);
  _Float16* kh = (_Float16*)(smem + 57 * RS);
  if (w < 3) {
    const char* ua = smem + (16 * w + i) * RS + g * 16;
    const char* va = smem + (41 + i) * RS + g * 16;
    f32x4 acc = {0.f, 0.f, 0.f, 0.f};
#pragma unroll
    for (int ks = 0; ks < 8; ++ks)
      acc = mfma_f16(*(const u32x4*)(ua + ks * 64), *(const u32x4*)(va + ks * 64), acc);
    int idx0, st;
    if (w == 0) { idx0 = (g < 2 ? 4 * g : 120 + 4 * g) + 8 * i; st = 1; }
    else if (w == 1) { idx0 = g < 2 ? 256 + 64 * g + 4 * i : 384 + 64 * (g - 2) + 2 * i; st = 1; }
    else { idx0 = 512 + 64 * g + i; st = 16; }
#pragma unroll
    for (int rg = 0; rg < 4; ++rg) {
      int idx = idx0 + st * rg;
      if (w == 1 && g >= 2) idx = idx0 + 32 * (rg >> 1) + (rg & 1);
      if (w == 2 && g >= 2) idx = (g == 2 && rg == 0 && i == 0) ? 640 : 644;
      kh[idx] = (_Float16)acc[rg];
    }
  }
  __syncthreads();
  STAMP(0, 3);
  const unsigned short* ku = (const unsigned short*)kh;
  const size_t kout = WS_KREV + (size_t)d * KREV * 2;
  {
    const int l0 = 2040 - 8 * tid;
    u32x4 o;
    if (w < 2) {
      const int q = l0 >> 3;
      const unsigned a = ku[384 + q], b = ku[385 + q];
      __builtin_amdgcn_raw_buffer_store_b32(a | (b << 16), ws_rsrc(p.ws), (int)(kout + 4 * tid), 0, 16);
      return;
    } else if (w == 2) {
      const int q = l0 >> 2;
      const unsigned a = ku[256 + q], b = ku[257 + q], c = ku[258 + q];
      o[0] = c | (c << 16); o[1] = b | (b << 16); o[2] = b | (a << 16); o[3] = a | (a << 16);
    } else {
      unsigned v[8];
#pragma unroll
      for (int e = 0; e < 8; ++e) {
        const int l = l0 + 7 - e;
        const int idx = l < 256 ? l : (int)(pk_bf((float)l, 0.f) & 0xffffu) - 17024;
        v[e] = ku[idx];
      }
#pragma unroll
      for (int q = 0; q < 4; ++q) o[q] = v[2 * q] | (v[2 * q + 1] << 16);
    }
    st16(ws_rsrc(p.ws), kout + 16 * tid - 1536, o);
  }
}

__global__ NOPK __launch_bounds__(256) void k1(K1Args p) {
  __shared__ __attribute__((aligned(16))) char smem[57 * KG_RS + 1312];
  int b = blockIdx.x;
  char* stamp_ws = p.ws; (void)stamp_ws;
  STAMP(0, 0);
  if (b < NB_PF) { k1_prefetch(); STAMP(0, 7); return; }
  b -= NB_PF;
  if (b < NB_K) { k1_kgen(p, b, smem); VM0(); STAMP(0, 7); return; }
  b -= NB_K;
  asm volatile("" :: "s"(p.ws), "s"(p.x));
#define DL_LN 4
#define DL_MOD 4
  if (b < NB_LN) { __builtin_amdgcn_s_sleep(DL_LN); k1_ln(p, b, smem); }
  else { __builtin_amdgcn_s_sleep(DL_MOD); k1_mod(p, b - NB_LN, smem); }
  VM0();
  STAMP(0, 7);
}

constexpr int K2_CS = 4160;
constexpr int K2_KOFF = 4 * K2_CS;
constexpr int K2_CH = K2_KOFF + 4608;
constexpr int CV_RED = 3 * K2_CH;
constexpr int CV_RT2 = 86016;
constexpr int CV_SUM = CV_RT2 + 4 * (32 * RT_STR + 256);
constexpr int CV_XCH = CV_SUM + 2560;
constexpr int CV_FL = CV_RED + 20480;

struct Chan { float sa, ca1, ga, dsk, hs, hinv, gy, gd; };
struct ChanRaw { float m, kinv, dsk; };
DEV ChanRaw chan_ld(const unsigned short* dskip, const char* ws, int d, int lane) {
  ChanRaw c;
  c.kinv = ldu<float>(ws + WS_KSC + 4 * d);
  c.dsk = ldbf_u(dskip + d);
  c.m = ((const float*)(ws + WS_MOD))[3840 * (lane & 7) + D_ * ((lane >> 3) & 3) + d];
  return c;
}
template <int CTRL> DEV float dpp_add(float v) {
  return v + __int_as_float(__builtin_amdgcn_update_dpp(0, __float_as_int(v), CTRL, 0xf, 0xf, true));
}
DEV Chan chan_fin(const ChanRaw& r) {
  Chan c;
  const int t = __float_as_int(rbf(dpp_add<0x141>(dpp_add<0x4e>(dpp_add<0xb1>(r.m)))));
  c.sa = __int_as_float(__builtin_amdgcn_readlane(t, 0));
  c.ca1 = rbf(1.f + __int_as_float(__builtin_amdgcn_readlane(t, 8)));
  c.ga = __int_as_float(__builtin_amdgcn_readlane(t, 16));
  c.dsk = r.dsk;
  const float hb = 26.f * fabsf(c.ca1) + fabsf(c.sa);
  int ex = (int)((__float_as_uint(hb) >> 23) & 0xff);
  ex = ex < 20 ? 20 : (ex > 240 ? 240 : ex);
  c.hs = __uint_as_float((unsigned)(127 + 14 + 127 - ex) << 23);
  c.hinv = __uint_as_float((unsigned)(ex - 14) << 23);
  c.gy = r.kinv * c.hinv * c.ga;
  c.gd = c.hinv * c.dsk * c.ga;
  return c;
}
DEV size_t tl_off(int d, int t) { return (((size_t)(t >> 5) * D_ + d) * 32 + (t & 31)) * 2; }
struct StageRaw { u32x4 xa0, xa1, kv1; u32x2 xb; unsigned kv0; };
DEV StageRaw stage_ld(const char* ws, int d, int th) {
  StageRaw s;
  const unsigned short* krev = (const unsigned short*)(ws + WS_KREV) + (size_t)d * KREV;
  const size_t xo = tl_off(d, 16 * th);
  s.xa0 = *(const u32x4*)(ws + WS_XNT + xo); s.xa1 = *(const u32x4*)(ws + WS_XNT + xo + 16);
  s.xb = *(const u32x2*)(ws + WS_XNT + tl_off(d, min(16 * th + 16, L_ - 4)));
  s.kv0 = *(const unsigned*)(krev + 2 * th); s.kv1 = *(const u32x4*)(krev + 256 + 8 * th);
  return s;
}
DEV void stage_wr(const StageRaw& s, const Chan& c, char* smem, int th) {
  constexpr int CS = K2_CS, KOFF = K2_KOFF;
  unsigned hh[10];
  const h16x2 c2 = __builtin_bit_cast(h16x2, pk_h(c.ca1 * c.hs, c.ca1 * c.hs)), s2 = __builtin_bit_cast(h16x2, pk_h(c.sa * c.hs, c.sa * c.hs));
#pragma unroll
  for (int q = 0; q < 10; ++q) {
    unsigned u = q < 4 ? s.xa0[q] : (q < 8 ? s.xa1[q - 4] : s.xb[q - 8]);
    hh[q] = __builtin_bit_cast(unsigned, __builtin_elementwise_fma(__builtin_bit_cast(h16x2, u), c2, s2));
  }
  char* dst = smem + (2 * th + 2) * 16;
  unsigned s1[9];
#pragma unroll
  for (int q = 0; q < 9; ++q) s1[q] = __builtin_amdgcn_alignbit(hh[q + 1], hh[q], 16);
#pragma unroll
  for (int e = 0; e < 2; ++e) {
    *(u32x4*)(dst + 16 * e) = (u32x4){hh[4 * e], hh[4 * e + 1], hh[4 * e + 2], hh[4 * e + 3]};
    *(u32x4*)(dst + 16 * e + CS) = (u32x4){s1[4 * e], s1[4 * e + 1], s1[4 * e + 2], s1[4 * e + 3]};
    *(u32x4*)(dst + 16 * e + 2 * CS) = (u32x4){hh[4 * e + 1], hh[4 * e + 2], hh[4 * e + 3], hh[4 * e + 4]};
    *(u32x4*)(dst + 16 * e + 3 * CS) = (u32x4){s1[4 * e + 1], s1[4 * e + 2], s1[4 * e + 3], s1[4 * e + 4]};
  }
  if (th == 0) {
    u32x4 z = {0u, 0u, 0u, 0u};
    *(u32x4*)(smem) = z; *(u32x4*)(smem + CS) = z; *(u32x4*)(smem + 2 * CS) = z; *(u32x4*)(smem + 3 * CS) = z;
    *(u32x4*)(smem + 16) = z;
    *(u32x4*)(smem + CS + 16) = (u32x4){0u, 0u, 0u, hh[0] << 16};
    *(u32x4*)(smem + 2 * CS + 16) = (u32x4){0u, 0u, 0u, hh[0]};
    *(u32x4*)(smem + 3 * CS + 16) = (u32x4){0u, 0u, hh[0] << 16, s1[0]};
  }
  {
    const unsigned aa = __builtin_amdgcn_perm(s.kv0, s.kv0, 0x01000100u), bb = __builtin_amdgcn_perm(s.kv0, s.kv0, 0x03020302u);
    const unsigned ba = __builtin_amdgcn_perm(s.kv0, s.kv0, 0x01000302u);
    *(u32x4*)(smem + KOFF + 16 * th) = (u32x4){bb, (th & 1) ? ba : bb, aa, aa};
  }
  *(u32x4*)(smem + KOFF + 2048 + 16 * th) = s.kv1;
  if (th < 32) *(u32x4*)(smem + KOFF + 4096 + 16 * th) = (u32x4){0u, 0u, 0u, 0u};
}
DEV f32x4 conv_out(const f32x4& y, const Chan& c, const char* smem, int t) {
  const u32x2 hv = *(const u32x2*)(smem + 2 * (t + 16));
  f32x4 o;
#pragma unroll
  for (int e = 0; e < 2; ++e) {
    const unsigned hu = hv[e];
    const h16x2 h2 = __builtin_bit_cast(h16x2, hu);
    o[2 * e] = fmaf((float)h2[0], c.gd, y[2 * e] * c.gy);
    o[2 * e + 1] = fmaf((float)h2[1], c.gd, y[2 * e + 1] * c.gy);
  }
  return o;
}
DEV unsigned add_bf(unsigned x, float a, float b) { return pk_bf(bf_lo(x) + a, bf_hi(x) + b); }
#define K2_LOAD(buf, cc, u)                                                                                     \
  asm volatile("ds_read_b64 %0, %3 offset:%5\n\tds_read_b64 %1, %3 offset:%6\n\tds_read_b128 %2, %4 offset:%7"   \
               : "=&v"(Al[buf][u]), "=&v"(Ah[buf][u]), "=&v"(Bf[buf][u])                                          \
               : "v"(ap0), "v"(bp0), "i"(64 * (cc) + 512 * (u)), "i"(64 * (cc) + 512 * (u) + 8),                 \
                 "i"(64 * (cc) + 3584 - 512 * (u)))
template <int SET> DEV constexpr int half_tile(int j) {
  return SET == 0 ? (j == 0 ? 7 : (j == 1 ? 6 : 2)) : (j < 3 ? 5 - j : 4 - j);
}
template <int SET>
DEV void conv_half(unsigned ap0, unsigned bp0, char* redw) {
  constexpr int NU = SET == 0 ? 8 : 6, NT = SET == 0 ? 3 : 5;
  u32x2 Al[2][NU], Ah[2][NU];
  u32x4 Bf[2][NU];
#pragma unroll
  for (int u = 0; u < NU; ++u) K2_LOAD(0, 0, u);
  f32x4 acc[NT];
#pragma unroll
  for (int j = 0; j < NT; ++j) acc[j] = (f32x4){0.f, 0.f, 0.f, 0.f};
#pragma unroll
  for (int cc = 0; cc < 2; ++cc) {
    lgkm0();
    if (cc == 0) {
#pragma unroll
      for (int u = 0; u < NU; ++u) K2_LOAD(1, 1, u);
    }
#pragma unroll
    for (int sp = 0; sp < NU; ++sp)
#pragma unroll
      for (int j = 0; j < NT; ++j) {
        const int q = half_tile<SET>(j);
        if (sp <= q) {
          const u32x4 A = {Al[cc][sp][0], Al[cc][sp][1], Ah[cc][sp][0], Ah[cc][sp][1]};
          acc[j] = mfma_f16(A, Bf[cc][q - sp], acc[j]);
        }
      }
    SB();
  }
#pragma unroll
  for (int j = 0; j < NT; ++j) *(f32x4*)(redw + j * 4096) = acc[j];
}

template <int NF>
DEV void w1_fin(const RtRaw& t, const ModP& mp, char* ws, int kk, int ct0, int lane, char* scr, float* sums) {
  const int g = lane >> 4, i = lane & 15;
  float* tb = (float*)(scr + 32 * RT_STR);
  const float mv = mod_fin(mp);
  tb[lane] = lane < 32 ? rbf(1.f + mv) : mv;
  rt_park<NF>(t, scr, lane);
  lgkm0();
  const f32x4 ca = *(const f32x4*)(tb + 8 * g), cb = *(const f32x4*)(tb + 8 * g + 4);
  const f32x4 sa = *(const f32x4*)(tb + 32 + 8 * g), sb = *(const f32x4*)(tb + 36 + 8 * g);
  const u32x4 sv = {pk_bf(sa[0], sa[1]), pk_bf(sa[2], sa[3]), pk_bf(sb[0], sb[1]), pk_bf(sb[2], sb[3])};
  const u32x4 ones = {0x3f803f80u, 0x3f803f80u, 0x3f803f80u, 0x3f803f80u};
  const f32x4 z = {0.f, 0.f, 0.f, 0.f};
  const rsrc_t rs = ws_rsrc(ws);
#pragma unroll
  for (int j = 0; j < NF; ++j) {
    const u32x4 f = rt_frag(scr, j, lane);
    const u32x4 sc = {pk_bf(bf_lo(f[0]) * ca[0], bf_hi(f[0]) * ca[1]), pk_bf(bf_lo(f[1]) * ca[2], bf_hi(f[1]) * ca[3]),
                      pk_bf(bf_lo(f[2]) * cb[0], bf_hi(f[2]) * cb[1]), pk_bf(bf_lo(f[3]) * cb[2], bf_hi(f[3]) * cb[3])};
    st16(rs, WS_W1F + ((size_t)(kk * W1KS + (ct0 + j) * W1TS) * 64 + lane) * 16, sc);
    const f32x4 cs = mfma_bf16(sc, ones, z), b1 = mfma_bf16(f, sv, z);
    if (i < 2) *(f32x4*)(sums + i * (16 * NF) + 16 * j + 4 * g) = i ? b1 : cs;
  }
}

DEV void conv_phase(const unsigned short* __restrict__ dskip, char* __restrict__ ws,
                    const unsigned short* __restrict__ w1, const unsigned short* __restrict__ w2, char* smem_all) {
  constexpr int CS = K2_CS, KOFF = K2_KOFF;
  const int tid = threadIdx.x, lane = tid & 63;
  const int w = __builtin_amdgcn_readfirstlane(tid >> 6);
  const int bx = blockIdx.x;
  const int pj = (bx & 7) * 16 + (bx >> 4), par = (bx >> 3) & 1;
  const int r = lane & 15, g = lane >> 4, th = tid & 127;
  const int s4 = (r + 1) & 3;
  const rsrc_t rs = ws_rsrc(ws);
  unsigned* fl = (unsigned*)(smem_all + CV_FL);
  char* stamp_ws = ws; (void)stamp_ws;
  STAMP(1, 0);
  asm volatile("" :: "s"(w1), "s"(w2));
  if (tid < 12) fl[tid] = 0u;
  if (w < 4) {
    const int d = 5 * pj + 2 * par + (w >> 1), hw = w & 1;
    char* smem = smem_all + (w >> 1) * K2_CH;
    const ChanRaw cr = chan_ld(dskip, ws, d, lane);
    SB();
    const StageRaw sr = stage_ld(ws, d, th);
    u32x4 xr4[2];
    u32x2 xres[4];
    auto ld_res = [&]() __attribute__((always_inline)) {
#pragma unroll
      for (int qp = 0; qp < 2; ++qp)
        xr4[qp] = *(const u32x4*)(ws + WS_XT + tl_off(d, 256 * (4 * hw + 2 * qp + (g & 1)) + 16 * r + 8 * (g >> 1)));
    };
    ld_res();
    SB();
    asm volatile("s_waitcnt lgkmcnt(0)\n\ts_barrier" ::: "memory");
    SB();
    const Chan c = chan_fin(cr);
    STAMP(1, 1);
    stage_wr(sr, c, smem, th);
    lds_post(fl + 6 + (w >> 1), lane);
    lds_wait(fl + 6 + (w >> 1), 2u);
    STAMP(1, 2);
    f32x4 acc[8], keep[4];
#pragma unroll
    for (int q = 0; q < 8; ++q) acc[q] = (f32x4){0.f, 0.f, 0.f, 0.f};
    {
      const unsigned ap0 = (unsigned)(size_t)(smem + s4 * CS + 2 * (128 * hw + 8 * g + (r + 1) - s4));
      const unsigned bp0 = (unsigned)(size_t)(smem + KOFF + 2 * (2032 + 128 * hw - 16 * r + 8 * g) - 3584);
      u32x2 Al[2][8], Ah[2][8];
      u32x4 Bf[2][8];
#pragma unroll
      for (int u = 0; u < 8; ++u) K2_LOAD(0, 0, u);
      auto tile = [&](int cc, int q) __attribute__((always_inline)) {
#pragma unroll
        for (int sp = 0; sp <= q; ++sp) {
          const u32x4 A = {Al[cc & 1][sp][0], Al[cc & 1][sp][1], Ah[cc & 1][sp][0], Ah[cc & 1][sp][1]};
          acc[q] = mfma_f16(A, Bf[cc & 1][q - sp], acc[q]);
        }
        SB();
      };
#pragma unroll
      for (int cc = 0; cc < 3; ++cc) {
        lgkm0();
#pragma unroll
        for (int q = 0; q < 8; ++q) {
          K2_LOAD((cc + 1) & 1, cc + 1, q);
          tile(cc, q);
        }
      }
#pragma unroll
      for (int qp = 0; qp < 2; ++qp) {
        const auto s0 = __builtin_amdgcn_permlane16_swap(xr4[qp][0], xr4[qp][2], false, false);
        const auto s1 = __builtin_amdgcn_permlane16_swap(xr4[qp][1], xr4[qp][3], false, false);
        xres[2 * qp] = (u32x2){s0[0], s1[0]}; xres[2 * qp + 1] = (u32x2){s0[1], s1[1]};
      }
      lgkm0();
      f32x4* xw = (f32x4*)(smem_all + CV_XCH + w * 4096) + lane;
      auto last = [&](auto hw_tag) __attribute__((always_inline)) {
        constexpr int S = decltype(hw_tag)::value ? 0 : 4, K = 4 - S;
#pragma unroll
        for (int q = 0; q < 4; ++q) {
          tile(3, S + q);
          if (q) { xw[64 * (q - 1)] = acc[S + q - 1]; SB(); }
        }
        tile(3, K + 3);
        xw[64 * 3] = acc[S + 3];
        lds_post(fl + w, lane);
        SB();
#pragma unroll
        for (int q = 0; q < 3; ++q) tile(3, K + q);
#pragma unroll
        for (int q = 0; q < 4; ++q) keep[q] = acc[K + q];
      };
      if (hw) last(std::true_type{}); else last(std::false_type{});
    }
    STAMP(1, 3);
    lds_wait(fl + (w ^ 1), 1u);
    STAMP(1, 4);
    const f32x4* xrd = (const f32x4*)(smem_all + CV_XCH + (w ^ 1) * 4096) + lane;
#pragma unroll
    for (int qp = 0; qp < 2; ++qp) {
      u32x2 o[2];
#pragma unroll
      for (int h = 0; h < 2; ++h) {
        const int q = 2 * qp + h;
        const f32x4 v = conv_out(keep[q] + xrd[64 * q], c, smem, 256 * (4 * hw + q) + 16 * r + 4 * g);
        o[h] = (u32x2){add_bf(xres[q][0], v[0], v[1]), add_bf(xres[q][1], v[2], v[3])};
      }
      const auto s0 = __builtin_amdgcn_permlane16_swap(o[0][0], o[1][0], false, false);
      const auto s1 = __builtin_amdgcn_permlane16_swap(o[0][1], o[1][1], false, false);
      const int t = 256 * (4 * hw + 2 * qp + (g & 1)) + 16 * r + 8 * (g >> 1);
      st16(rs, WS_X1T + (((size_t)(t >> 6) * D_ + d) * 64 + (t & 63)) * 2, (u32x4){s0[0], s1[0], s0[1], s1[1]});
    }
  } else {
    __builtin_amdgcn_s_setprio(2);
    const int d = 5 * pj + 4, wq = w - 4;
    char* smem = smem_all + 2 * K2_CH;
    StageRaw sr;
    const ChanRaw cr = chan_ld(dskip, ws, d, lane);
    asm volatile("" ::: "memory");
    if (wq < 2) sr = stage_ld(ws, d, th);
    asm volatile("" ::: "memory");
    const int tl0 = par ? (wq < 3 ? 5 - wq : 1) : (wq == 0 ? 7 : (wq == 1 ? 6 : 2)), tl1 = 0;
    const unsigned ap0 = (unsigned)(size_t)(smem + s4 * CS + 2 * (64 * wq + 8 * g + (r + 1) - s4));
    const unsigned bp0 = (unsigned)(size_t)(smem + KOFF + 2 * (2032 + 64 * wq - 16 * r + 8 * g) - 3584);
    char* redw = smem_all + CV_RED + wq * 1024 + lane * 16;
    u32x2 xres[2];
    xres[0] = *(const u32x2*)(ws + WS_XT + tl_off(d, 256 * tl0 + 16 * r + 4 * g));
    xres[1] = *(const u32x2*)(ws + WS_XT + tl_off(d, 256 * tl1 + 16 * r + 4 * g));
    SB();
    __builtin_amdgcn_s_barrier();
    SB();
    const Chan c = chan_fin(cr);
    if (wq < 2) {
      stage_wr(sr, c, smem, th);
      lds_post(fl + 8, lane);
    }
    constexpr int NF1 = 3, NC1 = 108 / NF1, NB1 = 5 * NC1;
    const bool isw1 = bx < NB1;
    const int j2 = 4 * (bx - NB1) + wq;
    const bool isw2 = !isw1 && j2 < 270, isgb = j2 >= 272 && j2 < 282;
    const int jk = isw1 ? 4 * (bx / NC1) + wq : j2 / 5;
    const int jc = isw1 ? NF1 * (bx % NC1) : 8 * (j2 % 5);
    RtRaw rt;
    const int gbi = 64 * (j2 - 272) + lane;
    ModP mp;
    SB();
#define RT_MODE 2
#define RT67 24
    auto rtl = [&]() __attribute__((always_inline)) {
      if (isw1) rt_load<NF1>(rt, w1, FF, jk, jc, lane);
      else if (isw2) rt_load<8>(rt, w2, D_, jk, jc, lane);
      SB();
    };
    if (wq >= 2 && RT67) __builtin_amdgcn_s_sleep(RT67);
    rtl();
    lds_wait(fl + 8, 2u);
    STAMP(1, 2);
    if (isw1 || isgb) mp = mod_ld(rs, isw1 ? (lane < 32 ? 4 * D_ : 3 * D_ - 32) + 32 * jk + lane : 5 * D_ + gbi);
    SB();
    if (par) conv_half<1>(ap0, bp0, redw); else conv_half<0>(ap0, bp0, redw);
    lds_post(fl + 4, lane);
    STAMP(1, 3);
    STAMP(1, 4);
    lds_wait(fl + 4, 4u);
    STAMP(1, 5);
    const char* rd = smem_all + CV_RED + lane * 16;
#pragma unroll
    for (int u = 0; u < 2; ++u) {
      const bool on = u == 0 ? (par || wq < 3) : (par && wq == 0);
      if (on) {
        const int sl = u == 0 ? wq : 4, tl = u == 0 ? tl0 : tl1;
        const f32x4 y = (*(const f32x4*)(rd + sl * 4096) + *(const f32x4*)(rd + sl * 4096 + 1024))
                      + (*(const f32x4*)(rd + sl * 4096 + 2048) + *(const f32x4*)(rd + sl * 4096 + 3072));
        const int t = 256 * tl + 16 * r + 4 * g;
        const f32x4 v = conv_out(y, c, smem, t);
        st8(rs, WS_X1T + (((size_t)(t >> 6) * D_ + d) * 64 + (t & 63)) * 2, (u32x2){add_bf(xres[u][0], v[0], v[1]), add_bf(xres[u][1], v[2], v[3])});
      }
    }
    SB();
    STAMP(1, 6);
    char* scr = smem_all + CV_RT2 + wq * (32 * RT_STR + 256);
    if (isw1) {
      float* sums = (float*)(smem_all + CV_SUM);
      w1_fin<NF1>(rt, mp, ws, jk, jc, lane, scr, sums + 32 * NF1 * wq);
      asm volatile("" ::: "memory");
      unsigned old = 0u;
      if (lane == 0) old = __hip_atomic_fetch_add(fl + 5, 1u, __ATOMIC_RELAXED, __HIP_MEMORY_SCOPE_WORKGROUP);
      asm volatile("" ::: "memory");
      if (__builtin_amdgcn_readfirstlane(old) == 3u && lane < 8 * NF1) {
        const f32x4* sp = (const f32x4*)sums + lane;
        const f32x4 v = (sp[0] + sp[8 * NF1]) + (sp[16 * NF1] + sp[24 * NF1]);
        st16(rs, WS_CSP + ((size_t)(2 * (bx / NC1) + lane / (4 * NF1)) * FF + 16 * jc + 4 * (lane % (4 * NF1))) * 4, __builtin_bit_cast(u32x4, v));
      }
    } else if (isw2) {
      rt_park<8>(rt, scr, lane);
      lgkm0();
#pragma unroll
      for (int j = 0; j < 8; ++j) st16(rs, WS_W2F + ((size_t)((jc + j) * 54 + jk) * 64 + lane) * 16, rt_frag(scr, j, lane));
    } else if (isgb) {
      st4(ws, WS_GB + 4 * gbi, __float_as_uint(mod_fin(mp)));
    }
  }
}

__global__ NOPK __launch_bounds__(512, 2) void k2(const unsigned short* __restrict__ dskip, char* __restrict__ ws,
                                                  const unsigned short* __restrict__ w1, const unsigned short* __restrict__ w2) {
  __shared__ __attribute__((aligned(16))) char smem[CV_XCH + 16384];
  conv_phase(dskip, ws, w1, w2, smem);
  char* stamp_ws = ws; (void)stamp_ws;
  VM0();
  STAMP(1, 7);
}

DEV u32x4 ld16_sc1(rsrc_t r, int voff, int soff) { return __builtin_amdgcn_raw_buffer_load_b128(r, voff, soff, 16); }
DEV u32x4 ld16(rsrc_t r, int voff, int soff) { return __builtin_amdgcn_raw_buffer_load_b128(r, voff, soff, 0); }
DEV u32x4 tr_frag(const char* p) {
  const u32x2 a = __builtin_bit_cast(u32x2, __builtin_amdgcn_ds_read_tr16_b64_v4i16((lds_s16x4*)p));
  const u32x2 b = __builtin_bit_cast(u32x2, __builtin_amdgcn_ds_read_tr16_b64_v4i16((lds_s16x4*)(p + 256)));
  return (u32x4){a[0], a[1], b[0], b[1]};
}

constexpr int KF_RS = 84;
constexpr int KF_RB = 8 * 16 * KF_RS * 4;
constexpr int KF_BIG = 2 * KF_RB;
constexpr int NW2L = 8, NW2R = 27;
constexpr int KF_TAB = KF_BIG;
constexpr int KF_PS = KF_TAB + 320;
constexpr int KF_XR = KF_PS + 512;
constexpr int KF_CNT = KF_XR + 64 * 176;
constexpr int KF_LDS = KF_CNT + 16;
#define X_FIRST 5
#define X_STEP 4
#define W1_EARLY 3
#define W1_MID 7
#define W1_PRE 10
#define W2_START 6
#define W2_RATE 2
#define W2_INL 28
#define CS_STEP 14
#define SIG_SPLIT 4
#define H1_PRE 9
#define H1_BATCH 7
#define LOCK_MASK 0x55554u

DEV void kf_body(char* __restrict__ ws, unsigned short* __restrict__ out, char* smem) {
  char* xs = smem;
  float* tab = (float*)(smem + KF_TAB);
  float* ps = (float*)(smem + KF_PS);
  char* xr = smem + KF_XR;
  unsigned* lcnt = (unsigned*)(smem + KF_CNT);
  float* cst = (float*)(smem + 81920);
  const int tid = threadIdx.x, lane = tid & 63;
  const int w = __builtin_amdgcn_readfirstlane(tid >> 6);
  const int bx = blockIdx.x;
  const int ng = (bx & 1) * 4 + ((bx >> 3) & 3), mt = ((bx & 7) >> 1) * 8 + (bx >> 5);
  const int g = lane >> 4, i = lane & 15;
  const rsrc_t rs_ = ws_rsrc(ws);

  char* stamp_ws = ws; (void)stamp_ws;
  STAMP(2, 0);
  unsigned* flags = (unsigned*)(ws + WS_CNT) + mt * 128;
  const int ntl = ng < 6 ? 14 : 12;
  const int ctb = ng < 6 ? 14 * ng : 84 + 12 * (ng - 6);
  const bool two = w < ntl - 8;
  const bool full = w < 6;
  const int ks0 = w < 6 ? 7 * w : 42 + 6 * (w - 6);
  const int ct0 = ctb + (two ? 2 * w : (ntl - 8) + w);
  const int lo16 = lane * 16;
  const int w1s = (int)WS_W1F + ct0 * W1TS * 1024;
  const int w2s = (int)WS_W2F + (ng * 5 * 54 + ks0) * 1024;

  u32x4 ring[32], wl[NW2L];
  const char* bb0 = xs + (8 * g + (i >> 2)) * 64 + ((g & 1) << 5) + (i & 3) * 8;
  const char* bb1 = xs + (8 * g + (i >> 2)) * 64 + (((g & 1) ^ 1) << 5) + (i & 3) * 8;
  auto w2_off = [&](int e) __attribute__((always_inline)) {
    const int s = e < 27 ? e / 4 : (e < 34 ? e - 27 : 6), c = e < 27 ? e % 4 : (e < 34 ? 4 : 3);
    const int sn = (s == 6 && !full) ? 5 : s;
    return w2s + (c * 54 + sn) * 1024;
  };

  auto gemm1 = [&](auto two_tag) __attribute__((always_inline)) {
    constexpr bool TWO = decltype(two_tag)::value;
    if (TWO) asm volatile("; two tiles" ::: "memory"); else asm volatile("; one tile" ::: "memory");
    u32x4 t[10];
    auto st_x = [&](int i0, int i1) __attribute__((always_inline)) {
#pragma unroll
      for (int it = i0; it < i1; ++it) {
        const int c = tid + 512 * it;
        const int row = c >> 3;
        *(u32x4*)(xs + ((c >> 2) & 1) * 40960 + row * 64 + (((c & 3) ^ ((row >> 2) & 2)) << 4)) = t[it];
      }
    };
#pragma unroll
    for (int it = 0; it < X_FIRST; ++it) t[it] = ld16(rs_, tid * 16, (int)WS_X1T + mt * 81920 + 8192 * it);
#pragma unroll
    for (int k = 0; k < W1_EARLY; ++k) {
      ring[2 * k] = ld16(rs_, lo16, w1s + k * W1KS * 1024);
      if (TWO) ring[2 * k + 1] = ld16(rs_, lo16, w1s + (k * W1KS + W1TS) * 1024);
    }
    SB();
    __builtin_amdgcn_s_barrier();
    SB();
#pragma unroll
    for (int k = W1_EARLY; k < W1_MID; ++k) {
      ring[2 * k] = ld16(rs_, lo16, w1s + k * W1KS * 1024);
      if (TWO) ring[2 * k + 1] = ld16(rs_, lo16, w1s + (k * W1KS + W1TS) * 1024);
    }
    SB();
    st_x(0, X_FIRST);
    SB();
    if (tid == 0) *lcnt = 0u;
    float gbp;
    if (!TWO) gbp = ((const float*)(ws + WS_GB))[80 * ng + min((w & 1) * 64 + lane, 79)];
    SB();
    __syncthreads();
    STAMP(2, 1);
    auto resid = [&]() __attribute__((always_inline)) {
      const int kb = (80 * ng) >> 5, tq = w & 3;
#pragma unroll
      for (int u = 0; u < 2; ++u) {
        const int kk = min(kb + 2 * (w >> 2) + u, 19);
        const u32x4 f = tr_frag(((tq & 1) ? bb1 : bb0) + (tq >> 1) * 40960 + kk * 2048);
        const unsigned dl = (unsigned)(32 * kk + 8 * g - 80 * ng);
        if (dl < 80u) *(u32x4*)(xr + (tq * 16 + i) * 176 + dl * 2) = f;
      }
    };
    const int hs = TWO ? 0 : (w & 1);
    const char* ba[4] = {bb0 + hs * 40960, bb1 + hs * 40960, bb0 + (hs ^ 1) * 40960, bb1 + (hs ^ 1) * 40960};
    const u32x4 ones = {0x3f803f80u, 0x3f803f80u, 0x3f803f80u, 0x3f803f80u};
    f32x4 acc[2][4];
#pragma unroll
    for (int c = 0; c < 2; ++c)
#pragma unroll
      for (int q = 0; q < 4; ++q) acc[c][q] = (f32x4){0.f, 0.f, 0.f, 0.f};
    u32x4 csp[5];
    const int cso = ((w & 1) * FF + 16 * ctb + 4 * min(lane, 4 * ntl - 1)) * 4;
    u32x4 bn[2][4];
#pragma unroll
    for (int q = 0; q < 4; ++q) bn[0][q] = tr_frag(ba[q]);
#pragma unroll
    for (int q = 0; q < 4; ++q) bn[1][q] = tr_frag(ba[q] + 2048);
#pragma unroll
    for (int kk = 0; kk < 20; ++kk) {
      if (kk == X_STEP) { st_x(X_FIRST, 10); SB(); }
      if (kk == 2 * X_FIRST - 2) { SB(); bar_lds(); SB(); }
      else if ((LOCK_MASK >> kk) & 1) { SB(); __builtin_amdgcn_s_barrier(); SB(); }
      if (kk == 2 * X_FIRST) { resid(); SB(); }
      auto gload = [&](int u) __attribute__((always_inline)) {
        {
          constexpr int CATCH = W1_PRE - W1_MID;
          const int k0 = kk == 0 ? W1_MID : (kk <= CATCH ? W1_MID + 2 * kk - 1 : kk + W1_PRE);
          const int nk = (kk >= 1 && kk <= CATCH) ? 2 : 1;
          const int kn = k0 + (nk == 2 ? (u >> 1) : 0);
          const bool on = nk == 2 || u >= 2;
          if (on && kn < 20) {
            if ((u & 1) == 0) ring[(2 * kn) % 32] = ld16(rs_, lo16, w1s + kn * W1KS * 1024);
            else if (TWO) ring[(2 * kn + 1) % 32] = ld16(rs_, lo16, w1s + (kn * W1KS + W1TS) * 1024);
          }
          if (kk == 0 && u < 2) {
#pragma unroll
            for (int it = X_FIRST + u * 3; it < (u ? 10 : (X_FIRST + 3 < 10 ? X_FIRST + 3 : 10)); ++it)
              t[it] = ld16(rs_, tid * 16, (int)WS_X1T + mt * 81920 + 8192 * it);
          }
        }
        if (kk >= W2_START && u < W2_RATE) {
          const int n = W2_RATE * (kk - W2_START) + u;
          if (n < NW2L) wl[n] = ld16(rs_, lo16, w2_off(NW2R + n));
          else if (n < W2_INL) ring[8 + n - NW2L] = ld16(rs_, lo16, w2_off(n - NW2L));
        }
      };
      const u32x4 a0 = ring[(2 * kk) % 32], a1 = ring[(2 * kk + 1) % 32];
#pragma unroll
      for (int q = 0; q < 4; ++q) {
        const u32x4 bq = bn[kk & 1][q];
        acc[0][q] = mfma_bf16(a0, bq, acc[0][q]);
        if (TWO) acc[1][q] = mfma_bf16(a1, bq, acc[1][q]);
        else if (q < 2) {
          acc[1][2 * q] = mfma_bf16(ones, bq, acc[1][2 * q]);
          acc[1][2 * q + 1] = mfma_bf16(bq, bq, acc[1][2 * q + 1]);
        }
        SB();
        if (kk < 18) bn[kk & 1][q] = tr_frag(ba[q] + (kk + 2) * 2048);
        gload(q);
        SB();
      }
      if (!TWO) {
        if (kk < 5) csp[kk] = ld16(rs_, cso, (int)WS_CSP + kk * 2 * FF * 4);
        if (kk == CS_STEP) {
          f32x4 t = __builtin_bit_cast(f32x4, csp[0]);
#pragma unroll
          for (int u = 1; u < 5; ++u) t += __builtin_bit_cast(f32x4, csp[u]);
          if (w >= 6 && lane < 56) *(f32x4*)(cst + (w & 1) * 224 + 4 * lane) = t;
        }
      }
      if (!TWO && kk == 12) { if ((w & 1) * 64 + lane < 80) tab[(w & 1) * 64 + lane] = gbp; }
      SB();
    }
    STAMP(2, 2);
    if (!TWO) {
#pragma unroll
      for (int h = 0; h < 2; ++h) {
        const f32x4 aq = acc[1][2 * h + 1];
        const float dq = (i & 2) ? ((i & 1) ? aq[3] : aq[2]) : ((i & 1) ? aq[1] : aq[0]);
        const int tk = (2 * hs + h) * 16 + i;
        if (g == (i >> 2)) {
          const float mean = acc[1][2 * h][0] * (1.f / D_);
          const float var = fmaxf(dq * (1.f / D_) - mean * mean, 0.f);
          const float sd = rbf(sqrtf(rbf(rbf(var) + 1e-5f)));
          const float rs = 1.f / sd;
          ps[tk] = rs;
          ps[64 + tk] = -rbf(mean) * rs;
        }
      }
    }
    __syncthreads();
    f32x4 csv[2], b1v[2];
#pragma unroll
    for (int c = 0; c < (TWO ? 2 : 1); ++c) {
      csv[c] = *(const f32x4*)(cst + 16 * (ct0 - ctb + c) + 4 * g);
      b1v[c] = *(const f32x4*)(cst + 224 + 16 * (ct0 - ctb + c) + 4 * g);
    }
#pragma unroll
    for (int qp = 0; qp < 2; ++qp) {
      u32x2 o[2][2];
#pragma unroll
      for (int h = 0; h < 2; ++h) {
        const int q = 2 * qp + h, tq = q ^ (2 * hs);
        const float rs = ps[tq * 16 + i], nm = ps[64 + tq * 16 + i];
#pragma unroll
        for (int c = 0; c < (TWO ? 2 : 1); ++c) {
          float v[4];
#pragma unroll
          for (int r = 0; r < 4; ++r) v[r] = fmaxf(fmaf(acc[c][q][r], rs, fmaf(nm, csv[c][r], b1v[c][r])), 0.f);
          o[c][h] = (u32x2){pk_bf(v[0], v[1]), pk_bf(v[2], v[3])};
        }
      }
      const int tl = ((2 * qp) ^ (2 * hs)) + (g & 1);
#pragma unroll
      for (int c = 0; c < (TWO ? 2 : 1); ++c) {
        const int ct = ct0 + c;
        const auto s0 = __builtin_amdgcn_permlane16_swap(o[c][0][0], o[c][1][0], false, false);
        const auto s1 = __builtin_amdgcn_permlane16_swap(o[c][0][1], o[c][1][1], false, false);
        const size_t off = (((size_t)(4 * mt + tl) * 54 + (ct >> 1)) * 64 + i + 16 * (2 * (ct & 1) + (g >> 1))) * 16;
        st16(rs_, WS_H1F + off, (u32x4){s0[0], s1[0], s0[1], s1[1]});
      }
    }
    SB();
    __builtin_amdgcn_s_barrier();
    SB();
    constexpr int E0 = W2_INL - NW2L;
    auto signal = [&]() __attribute__((always_inline)) {
      if (lane == 0) {
        if (__hip_atomic_fetch_add(lcnt, 1u, __ATOMIC_RELAXED, __HIP_MEMORY_SCOPE_WORKGROUP) == 7u)
          __hip_atomic_store(flags + 16 * ng, 1u, __ATOMIC_RELAXED, __HIP_MEMORY_SCOPE_AGENT);
      }
    };
    if (w >= SIG_SPLIT) { asm volatile("s_waitcnt vmcnt(0)" ::: "memory"); signal(); }
    SB();
#pragma unroll
    for (int e = E0; e < NW2R; ++e) ring[(8 + e) % 32] = ld16(rs_, lo16, w2_off(e));
    SB();
    if (w < SIG_SPLIT) { asm volatile("s_waitcnt vmcnt(%0)" :: "n"(NW2R - E0) : "memory"); signal(); }
    SB();
  };
  if (two) gemm1(std::true_type{});
  else gemm1(std::false_type{});
  STAMP(2, 3);
#pragma nounroll
  for (unsigned spin = 0; spin < (1u << 20); ++spin) {
    if (__hip_atomic_load(flags + 16 * w, __ATOMIC_RELAXED, __HIP_MEMORY_SCOPE_AGENT) != 0u) break;
    __builtin_amdgcn_s_sleep(1);
  }
  asm volatile("" ::: "memory");
  SB();

  STAMP(2, 4);
  const int h1s = (int)WS_H1F + (mt * 4 * 54 + ks0) * 1024;
  u32x4 hb[28];
  auto load_h1 = [&](int n) __attribute__((always_inline)) {
    const int q = n / 7, s = n % 7;
    const int sn = (s == 6 && !full) ? 5 : s;
    hb[n] = ld16_sc1(rs_, lo16, h1s + (q * 54 + sn) * 1024);
  };
#pragma unroll
  for (int n = 0; n < H1_PRE; ++n) {
    if (n == H1_BATCH) { SB(); __builtin_amdgcn_s_barrier(); SB(); }
    load_h1(n);
  }
  SB();
  float* red = (float*)smem;
  float* rb_ = red + (w * 16 + i) * KF_RS + 4 * g;
  const int re = w * 40 + min(lane, 39);
  const int rtk = re / 20, rq4 = re % 20;
  const float* rr_ = red + rtk * KF_RS + 4 * rq4;
  f32x4 ac2[2][5];
  f32x4 rv[8], rsum;
  u32x2 xv2;
  const f32x4 gb = *(const f32x4*)(tab + 4 * rq4);
  auto red_wr = [&](int q, int c) __attribute__((always_inline)) {
    *(f32x4*)(rb_ + (q & 1) * (KF_RB / 4) + 16 * c) = ac2[q & 1][c];
  };
  auto red_rd = [&](int q, int ww) __attribute__((always_inline)) {
    rv[ww] = *(const f32x4*)(rr_ + (q & 1) * (KF_RB / 4) + ww * 16 * KF_RS);
  };
  auto red_x = [&](int q) __attribute__((always_inline)) { xv2 = *(const u32x2*)(xr + (16 * q + rtk) * 176 + rq4 * 8); };
  auto red_out = [&](int q) __attribute__((always_inline)) {
    const f32x4 s = rsum + (rv[6] + rv[7]);
    u32x2 o;
#pragma unroll
    for (int h = 0; h < 2; ++h) {
      unsigned f = pk_bf(s[2 * h], s[2 * h + 1]);
      unsigned gf = pk_bf(bf_lo(f) * gb[2 * h], bf_hi(f) * gb[2 * h + 1]);
      o[h] = pk_bf(bf_lo(xv2[h]) + bf_lo(gf), bf_hi(xv2[h]) + bf_hi(gf));
    }
    *(u32x2*)(out + (size_t)(mt * 64 + 16 * q + rtk) * D_ + ng * 80 + 4 * rq4) = o;
  };
#pragma unroll
  for (int q = 0; q < 3; ++q) {
#pragma unroll
    for (int c = 0; c < 5; ++c) ac2[q & 1][c] = (f32x4){0.f, 0.f, 0.f, 0.f};
#pragma unroll
    for (int s = 0; s < 7; ++s) {
      auto mm = [&](int c) __attribute__((always_inline)) {
        const u32x4 a = c == 4 ? wl[s] : ((s == 6 && c == 3) ? wl[7] : ring[(8 + 4 * s + c) % 32]);
        ac2[q & 1][c] = mfma_bf16(a, hb[7 * q + s], ac2[q & 1][c]);
      };
      if (s == 5 && q >= 2) {
        red_wr(q - 1, 4);
        SB();
        red_out(q - 2);
#pragma unroll
        for (int c = 0; c < 5; ++c) mm(c);
#pragma unroll
        for (int c = 0; c < 5; ++c) {
          __builtin_amdgcn_sched_group_barrier(0x008, 1, 0);
          __builtin_amdgcn_sched_group_barrier(0x002, 10, 0);
        }
        SB();
      } else if (s < 6) {
        mm(0); SB();
        if (q >= 1 && s >= 1) { red_wr(q - 1, s - 1); SB(); }
        mm(1); SB();
        if (q >= 2 && s < 4) { red_rd(q - 2, 2 * s); SB(); }
        mm(2); SB();
        if (q >= 2 && s < 4) { red_rd(q - 2, 2 * s + 1); SB(); }
        if (q >= 2 && s == 4) { red_x(q - 2); SB(); }
        mm(3); SB();
        if (q >= 2 && s >= 1 && s < 4) {
          const f32x4 t = rv[2 * s - 2] + rv[2 * s - 1];
          rsum = s == 1 ? t : rsum + t;
          asm volatile("" : "+v"(rsum));
          SB();
        }
        mm(4); SB();
      } else if (full) {
#pragma unroll
        for (int c = 0; c < 5; ++c) mm(c);
        SB();
      }
      if (7 * q + s + H1_PRE < 28) { load_h1(7 * q + s + H1_PRE); SB(); }
    }
    if (q >= 1) bar_lds();
    if (q == 0) STAMP(2, 5);
  }
  {
    auto sum6 = [&]() __attribute__((always_inline)) {
      rsum = ((rv[0] + rv[1]) + (rv[2] + rv[3])) + (rv[4] + rv[5]);
    };
    if (full) {
#pragma unroll
      for (int c = 0; c < 5; ++c) {
        const u32x4 a = c == 4 ? wl[6] : (c == 3 ? wl[7] : ring[(8 + 24 + c) % 32]);
        rv[c] = mfma_bf16(a, hb[27], (f32x4){0.f, 0.f, 0.f, 0.f});
      }
    } else {
#pragma unroll
      for (int c = 0; c < 5; ++c) rv[c] = (f32x4){0.f, 0.f, 0.f, 0.f};
    }
    f32x4 c0[5];
#pragma unroll
    for (int c = 0; c < 5; ++c) c0[c] = rv[c];
    SB();
    auto col = [&](int c, auto&& between) __attribute__((always_inline)) {
#pragma unroll
      for (int s = 0; s < 6; ++s) {
        const u32x4 a = c == 4 ? wl[s] : ring[(8 + 4 * s + c) % 32];
        c0[c] = mfma_bf16(a, hb[21 + s], c0[c]);
        SB();
        between(s);
        SB();
      }
    };
    col(0, [&](int s) __attribute__((always_inline)) { if (s < 5) red_wr(2, s); if (s < 4) { red_rd(1, 2 * s); red_rd(1, 2 * s + 1); } if (s == 4) red_x(1); });
    col(1, [&](int s) __attribute__((always_inline)) { if (s == 1) { sum6(); asm volatile("" : "+v"(rsum)); } if (s == 3) red_out(1); });
    bar_lds();
    auto wr3 = [&](int c) __attribute__((always_inline)) { *(f32x4*)(rb_ + (KF_RB / 4) + 16 * c) = c0[c]; };
    col(2, [&](int s) __attribute__((always_inline)) { if (s < 2) wr3(s); if (s >= 1 && s < 5) { red_rd(2, 2 * s - 2); red_rd(2, 2 * s - 1); } if (s == 5) red_x(2); });
    col(3, [&](int s) __attribute__((always_inline)) { if (s == 1) wr3(2); if (s == 2) { sum6(); asm volatile("" : "+v"(rsum)); } if (s == 4) red_out(2); });
    col(4, [&](int s) __attribute__((always_inline)) { if (s == 1) wr3(3); if (s == 2) red_x(3); });
    wr3(4);
    STAMP(2, 6);
    bar_lds();
#pragma unroll
    for (int ww = 0; ww < 8; ++ww) red_rd(3, ww);
    sum6();
    red_out(3);
    VM0();
    STAMP(2, 7);
  }
}

__global__ NOPK __launch_bounds__(512, 2) void kf(char* __restrict__ ws, unsigned short* __restrict__ out) {
  __shared__ __attribute__((aligned(16))) char smem[KF_LDS];
  kf_body(ws, out, smem);
}

__global__ void kend_() {}

extern "C" void kernel_launch(void* const* d_in, const int* in_sizes, int n_in,
                              void* d_out, int out_size, void* d_ws, size_t ws_size,
                              hipStream_t stream) {
  K1Args a;
  a.x = (const unsigned short*)d_in[0];
  a.are = (const unsigned short*)d_in[1];
  a.aim = (const unsigned short*)d_in[2];
  a.bre = (const unsigned short*)d_in[3];
  a.bim = (const unsigned short*)d_in[4];
  a.cre = (const unsigned short*)d_in[5];
  a.cim = (const unsigned short*)d_in[6];
  a.logdt = (const unsigned short*)d_in[7];
  a.c = (const unsigned short*)d_in[12];
  a.wmod = (const unsigned short*)d_in[13];
  a.ws = (char*)d_ws;
  k1<<<NB_PF + NB_K + NB_LN + NB_MOD, 256, 0, stream>>>(a);
  k2<<<256, 512, 0, stream>>>((const unsigned short*)d_in[8], (char*)d_ws, (const unsigned short*)d_in[10],
                              (const unsigned short*)d_in[11]);
  kf<<<256, 512, 0, stream>>>((char*)d_ws, (unsigned short*)d_out);
}
```

```cpp
#include <hip/hip_runtime.h>
#include <hip/hip_bf16.h>
#include <stdint.h>
#include <type_traits>

typedef __attribute__((ext_vector_type(8))) __bf16 bf16x8;
typedef __attribute__((ext_vector_type(2))) __bf16 bf16x2;
typedef __attribute__((ext_vector_type(4))) float f32x4;
typedef __attribute__((ext_vector_type(2))) float f32x2;
typedef __attribute__((ext_vector_type(4))) unsigned u32x4;
typedef __attribute__((ext_vector_type(2))) unsigned u32x2;
typedef __attribute__((ext_vector_type(8))) _Float16 h16x8;
typedef __attribute__((ext_vector_type(2))) _Float16 h16x2;
typedef __attribute__((ext_vector_type(4))) short s16x4;
typedef __attribute__((address_space(3))) s16x4 lds_s16x4;

#if defined(__HIP_DEVICE_COMPILE__)
#define NOPK __attribute__((target("no-packed-fp32-ops")))
#else
#define NOPK
#endif
#define DEV __device__ __forceinline__

#define W1L 0
constexpr int W1KS = W1L ? 1 : 108, W1TS = W1L ? 20 : 1;
#define LNFLIP 0
#define KMAP 0
constexpr int L_ = 2048;
constexpr int D_ = 640;
constexpr int NS = 128;
constexpr int FF = 1728;
constexpr int KREV = 1280;

constexpr size_t WS_GB   = 0;
constexpr size_t WS_KSC  = 8192;
#define FL_P 2048
#define FL_B 327680
constexpr size_t WS_CNT  = FL_B;
constexpr size_t WS_MOD  = 65536;
constexpr size_t WS_CSP  = 196608;
constexpr size_t WS_XNT  = 327680 + 256 * FL_P;
constexpr size_t WS_XT   = WS_XNT + (size_t)D_ * L_ * 2;
constexpr size_t WS_KREV = WS_XT + (size_t)D_ * L_ * 2;
constexpr size_t WS_W1F  = WS_KREV + (size_t)D_ * KREV * 2;
constexpr size_t WS_W2F  = WS_W1F + (size_t)D_ * FF * 2;
constexpr size_t WS_X1T  = WS_W2F + (size_t)D_ * FF * 2;
constexpr size_t WS_H1F  = WS_X1T + (size_t)D_ * L_ * 2;
constexpr size_t WS_END  = WS_H1F + (size_t)L_ * FF * 2;

#define STAMP(kid, k) do {} while (0)
#define CSTAMP(kid, k) do {} while (0)
#define VM0() do {} while (0)

#define TS(k, wsp) do { if (blockIdx.x == 0 && threadIdx.x == 0) __hip_atomic_store((unsigned long long*)((wsp) + (200u << 20)) + (k), (unsigned long long)__builtin_amdgcn_s_memrealtime(), __ATOMIC_RELAXED, __HIP_MEMORY_SCOPE_AGENT); } while (0)
DEV float bf_lo(unsigned u) { return __uint_as_float(u << 16); }
DEV float bf_hi(unsigned u) { return __uint_as_float(u & 0xffff0000u); }
DEV unsigned pk_bf(float a, float b) {
  f32x2 v = {a, b};
  bf16x2 r = __builtin_convertvector(v, bf16x2);
  return __builtin_bit_cast(unsigned, r);
}
DEV unsigned pk_h(float a, float b) {
  f32x2 v = {a, b};
  h16x2 r = __builtin_convertvector(v, h16x2);
  return __builtin_bit_cast(unsigned, r);
}
DEV float rbf(float a) { return bf_lo(pk_bf(a, 0.f)); }
DEV float ldbf(const unsigned short* p) { return __uint_as_float(((unsigned)*p) << 16); }
#define CONST_AS __attribute__((address_space(4)))
template <class T> DEV T ldu(const void* p) { return *(const CONST_AS T*)(size_t)p; }
DEV float ldbf_u(const unsigned short* p) {
  const unsigned v = ldu<unsigned>((const void*)((size_t)p & ~(size_t)3));
  return __uint_as_float(((size_t)p & 2) ? (v & 0xffff0000u) : (v << 16));
}

typedef __amdgpu_buffer_rsrc_t rsrc_t;
DEV rsrc_t ws_rsrc(char* ws) { return __builtin_amdgcn_make_buffer_rsrc((void*)ws, 0, (int)WS_END, 0x00020000); }
DEV void st16(rsrc_t r, size_t off, u32x4 v) { __builtin_amdgcn_raw_buffer_store_b128(v, r, (int)off, 0, 16); }
DEV void st4(char* ws, size_t off, unsigned v) { __hip_atomic_store((unsigned*)(ws + off), v, __ATOMIC_RELAXED, __HIP_MEMORY_SCOPE_AGENT); }
DEV void st8(rsrc_t r, size_t off, u32x2 v) { __builtin_amdgcn_raw_buffer_store_b64(v, r, (int)off, 0, 16); }

DEV void bar_lds() { asm volatile("s_waitcnt lgkmcnt(0)\n\ts_barrier" ::: "memory"); }
DEV void lds_wait(const void* word, unsigned val) {
  unsigned tv, ts, cnt;
  asm volatile("s_mov_b32 %2, 0x100000\n"
               "1:\n\tds_read_b32 %0, %3\n\ts_waitcnt lgkmcnt(0)\n\tv_readfirstlane_b32 %1, %0\n\t"
               "s_cmp_ge_u32 %1, %4\n\ts_cbranch_scc1 2f\n\ts_sleep 1\n\ts_sub_u32 %2, %2, 1\n\t"
               "s_cmp_lg_u32 %2, 0\n\ts_cbranch_scc1 1b\n2:"
               : "=&v"(tv), "=&s"(ts), "=&s"(cnt) : "v"((unsigned)(size_t)word), "s"(val) : "memory", "scc");
}
DEV void lds_post(unsigned* word, int lane) {
  asm volatile("" ::: "memory");
  if (lane == 0) __hip_atomic_fetch_add(word, 1u, __ATOMIC_RELAXED, __HIP_MEMORY_SCOPE_WORKGROUP);
  asm volatile("" ::: "memory");
}
DEV void lgkm0() { asm volatile("s_waitcnt lgkmcnt(0)" ::: "memory"); __builtin_amdgcn_sched_barrier(0); }

DEV f32x4 mfma_bf16(u32x4 a, u32x4 b, f32x4 c) {
  return __builtin_amdgcn_mfma_f32_16x16x32_bf16(__builtin_bit_cast(bf16x8, a), __builtin_bit_cast(bf16x8, b), c, 0, 0, 0);
}

constexpr int NB_PF = 8, NB_K = 640, NB_LN = 256, NB_MOD = 120;

struct K1Args {
  const unsigned short *x, *are, *aim, *bre, *bim, *cre, *cim, *logdt, *c, *wmod;
  char* ws;
};

DEV void k1_prefetch() {
  const int tid = threadIdx.x;
  unsigned long long pc;
  int o0, o1;
  asm volatile("s_getpc_b64 %0\n.Lpf_here%=:\n\ts_mov_b32 %1, _Z2k16K1Args - .Lpf_here%=\n\ts_mov_b32 %2, _Z5kend_v - .Lpf_here%="
               : "=s"(pc), "=s"(o0), "=s"(o1));
  const char* c0 = (const char*)(pc + (long long)o0);
  long n = o1 - o0;
  n = n < 0 ? 0 : (n > 98304 ? 98304 : n);
  unsigned v[3];
#pragma unroll
  for (int it = 0; it < 3; ++it) {
    const long o = (long)tid * 128 + it * 32768;
    v[it] = o < n ? *(const __attribute__((address_space(1))) unsigned*)(c0 + o) : 0u;
  }
  asm volatile("" :: "v"(v[0] ^ v[1] ^ v[2]));
}

DEV void k1_mod(const K1Args& p, int b, char* smem) {
  const int tid = threadIdx.x;
  const int rg = b & 7, cgp = b >> 3;
  const int c32 = tid & 31, r8 = tid >> 5;
  float* sil = (float*)smem;
  float* red = (float*)(smem + 512);
  char* stamp_ws = p.ws; (void)stamp_ws;
  STAMP(0, 1);
  u32x4 w[10];
  const float cv = ldbf(p.c + rg * 80 + min(tid, 79));
  asm volatile("" ::: "memory");
#pragma unroll
  for (int it = 0; it < 10; ++it)
    w[it] = *(const u32x4*)(p.wmod + (size_t)(rg * 80 + r8 + 8 * it) * 3840 + 256 * cgp + 8 * c32);
  if (tid < 80) sil[tid] = rbf(cv / (1.f + expf(-cv)));
  if (tid >= 128 && tid < 132 && b < 64) st4(p.ws, WS_CNT + (size_t)(b * 4 + tid - 128) * FL_P, 0u);
  __syncthreads();
  STAMP(0, 2);
  float acc[8];
#pragma unroll
  for (int j = 0; j < 8; ++j) acc[j] = 0.f;
#pragma unroll
  for (int it = 0; it < 10; ++it) {
    float s = sil[r8 + 8 * it];
#pragma unroll
    for (int q = 0; q < 4; ++q) {
      acc[2 * q] += s * bf_lo(w[it][q]);
      acc[2 * q + 1] += s * bf_hi(w[it][q]);
    }
  }
#pragma unroll
  for (int j = 0; j < 8; ++j) acc[j] += __shfl_xor(acc[j], 32, 64);
  if ((tid & 63) < 32) {
    *(f32x4*)(red + (tid >> 6) * 256 + 8 * c32) = (f32x4){acc[0], acc[1], acc[2], acc[3]};
    *(f32x4*)(red + (tid >> 6) * 256 + 8 * c32 + 4) = (f32x4){acc[4], acc[5], acc[6], acc[7]};
  }
  __syncthreads();
  STAMP(0, 3);
  if (tid < 64) {
    const f32x4* r4 = (const f32x4*)red + tid;
    st16(ws_rsrc(p.ws), WS_MOD + (rg * 3840 + 256 * cgp + 4 * tid) * 4,
         __builtin_bit_cast(u32x4, (r4[0] + r4[64]) + (r4[128] + r4[192])));
  }
}
struct ModP { f32x4 a, b; };
DEV ModP mod_ld(rsrc_t rs, int idx) {
  ModP m;
#pragma unroll
  for (int j = 0; j < 4; ++j) {
    m.a[j] = __uint_as_float(__builtin_amdgcn_raw_buffer_load_b32(rs, 4 * idx, (int)WS_MOD + 15360 * j, 0));
    m.b[j] = __uint_as_float(__builtin_amdgcn_raw_buffer_load_b32(rs, 4 * idx, (int)WS_MOD + 15360 * (4 + j), 0));
  }
  return m;
}
DEV float mod_fin(const ModP& m) {
  return rbf(((m.a[0] + m.a[1]) + (m.a[2] + m.a[3])) + ((m.b[0] + m.b[1]) + (m.b[2] + m.b[3])));
}
#define SB() __builtin_amdgcn_sched_barrier(0)

DEV void k1_ln(const K1Args& p, int b, char* smem) {
  constexpr int STR = 704;
  const int tid = threadIdx.x;
  const int tok = tid >> 3, sub = tid & 7;
  const int tile = (b & 7) * 8 + ((b >> 3) & 7), pass = (b >> 6) ^ LNFLIP;
  const int hf = pass & 1;
  const bool norm = pass < 2;
  const int t0 = tile * 32;
  const unsigned short* xr = p.x + (size_t)(t0 + tok) * D_ + 8 * sub;
  u32x4 xv[5], xo[5];
  char* stamp_ws = p.ws; (void)stamp_ws;
  STAMP(0, 1);
#pragma unroll
  for (int m = 0; m < 5; ++m) xv[m] = *(const u32x4*)(xr + 320 * hf + 64 * m);
  if (norm) {
#pragma unroll
    for (int m = 0; m < 5; ++m) xo[m] = *(const u32x4*)(xr + 320 * (hf ^ 1) + 64 * m);
    float s = 0.f, v = 0.f;
    const bf16x2 one2 = __builtin_bit_cast(bf16x2, 0x3f803f80u);
#pragma unroll
    for (int m = 0; m < 10; ++m)
#pragma unroll
      for (int q = 0; q < 4; ++q) {
        const unsigned u = m < 5 ? xv[m][q] : xo[m - 5][q];
        const bf16x2 x2 = __builtin_bit_cast(bf16x2, u);
        s = __builtin_amdgcn_fdot2_f32_bf16(x2, one2, s, false);
        v = __builtin_amdgcn_fdot2_f32_bf16(x2, x2, v, false);
      }
#pragma unroll
    for (int o = 1; o < 8; o <<= 1) { s += __shfl_xor(s, o, 64); v += __shfl_xor(v, o, 64); }
    const float mean = s * (1.f / D_);
    const float var = fmaxf(v * (1.f / D_) - mean * mean, 0.f);
    const float sd = rbf(sqrtf(rbf(rbf(var) + 1e-5f)));
    const float rs = 1.f / sd;
    const float mb = rbf(mean);
#pragma unroll
    for (int m = 0; m < 5; ++m)
#pragma unroll
      for (int q = 0; q < 4; ++q) {
        xv[m][q] = pk_h((bf_lo(xv[m][q]) - mb) * rs, (bf_hi(xv[m][q]) - mb) * rs);
      }
  }
  const int lane = tid & 63, w = tid >> 6;
  const int g = lane >> 4, i = lane & 15;
  const rsrc_t rsrc = ws_rsrc(p.ws);
#pragma unroll
  for (int m = 0; m < 5; ++m) *(u32x4*)(smem + tok * STR + (sub + 8 * m) * 16) = xv[m];
  __syncthreads();
  STAMP(0, 2);
  const size_t dsto = (norm ? WS_XNT : WS_XT) + ((size_t)tile * D_ + 320 * hf) * 64;
#pragma unroll
  for (int it = 0; it < 5; ++it) {
    const int cb = w * 5 + it;
    const char* a = smem + (8 * g + (i >> 2)) * STR + cb * 32 + (i & 3) * 8;
    const u32x2 ra = __builtin_bit_cast(u32x2, __builtin_amdgcn_ds_read_tr16_b64_v4i16((lds_s16x4*)a));
    const u32x2 rb = __builtin_bit_cast(u32x2, __builtin_amdgcn_ds_read_tr16_b64_v4i16((lds_s16x4*)(a + 4 * STR)));
    st16(rsrc, dsto + (size_t)(cb * 16 + i) * 64 + 16 * g, (u32x4){ra[0], ra[1], rb[0], rb[1]});
  }
}

constexpr int RT_STR = 288;
struct RtRaw { u32x4 r[8]; };
template <int NF>
DEV void rt_load(RtRaw& t, const unsigned short* W, int N, int kk, int ct0, int lane) {
#pragma unroll
  for (int j = 0; j < NF; ++j) {
    const int c = lane + 64 * j, row = c / (2 * NF), part = c % (2 * NF);
    t.r[j] = *(const u32x4*)(W + (size_t)(32 * kk + row) * N + 16 * ct0 + 8 * part);
  }
}
template <int NF>
DEV void rt_park(const RtRaw& t, char* scr, int lane) {
#pragma unroll
  for (int j = 0; j < NF; ++j) {
    const int c = lane + 64 * j, row = c / (2 * NF), part = c % (2 * NF);
    *(u32x4*)(scr + row * RT_STR + 16 * part) = t.r[j];
  }
}
DEV u32x4 rt_frag(const char* scr, int j, int lane) {
  const int g = lane >> 4, i = lane & 15;
  const char* p = scr + (8 * g + (i >> 2)) * RT_STR + 32 * j + (i & 3) * 8;
  const u32x2 a = __builtin_bit_cast(u32x2, __builtin_amdgcn_ds_read_tr16_b64_v4i16((lds_s16x4*)p));
  const u32x2 b = __builtin_bit_cast(u32x2, __builtin_amdgcn_ds_read_tr16_b64_v4i16((lds_s16x4*)(p + 4 * RT_STR)));
  return (u32x4){a[0], a[1], b[0], b[1]};
}
struct cf { float re, im; };
DEV cf cmul(cf a, cf b) {
  cf r;
  r.re = a.re * b.re - a.im * b.im;
  r.im = a.re * b.im + a.im * b.re;
  return r;
}
DEV cf cexp_(float a2, float br, float m, float m0) {
  float mag = __builtin_amdgcn_exp2f(a2 * (m - m0));
  float ph = __builtin_amdgcn_fractf(br * m);
  cf r; r.re = mag * __builtin_amdgcn_cosf(ph); r.im = mag * __builtin_amdgcn_sinf(ph);
  return r;
}
DEV f32x4 mfma_f16(u32x4 a, u32x4 b, f32x4 c) {
  return __builtin_amdgcn_mfma_f32_16x16x32_f16(__builtin_bit_cast(h16x8, a), __builtin_bit_cast(h16x8, b), c, 0, 0, 0);
}
DEV void put2(char* p, cf a, cf b) {
  u32x2 o = {pk_h(a.re, a.im), pk_h(b.re, b.im)};
  *(u32x2*)p = o;
}

constexpr int KG_RS = 544;
DEV void k1_kgen(const K1Args& p, int d, char* smem) {
  constexpr int RS = KG_RS;
  const int tid = threadIdx.x;
  const int lane = tid & 63;
  const int w = __builtin_amdgcn_readfirstlane(tid >> 6);
  const int g = lane >> 4, i = lane & 15;
  const size_t po = (size_t)d * NS + 2 * lane;
  const unsigned rar = *(const unsigned*)(p.are + po), rai = *(const unsigned*)(p.aim + po);
  unsigned rbr = 0u, rbi = 0u, rcr = 0u, rci = 0u;
  if (w == 0) {
    rbr = *(const unsigned*)(p.bre + po); rbi = *(const unsigned*)(p.bim + po);
    rcr = *(const unsigned*)(p.cre + po); rci = *(const unsigned*)(p.cim + po);
  }
  const float ldt = ldbf_u(p.logdt + d);
  asm volatile("" ::: "memory");
  SB();
  char* stamp_ws = p.ws; (void)stamp_ws;
  const float dt = rbf(expf(ldt));
  asm volatile("" :: "v"(dt));
  STAMP(0, 1);
  float a2[2], bv[2], m0[2];
  a2[0] = (dt * bf_lo(rar)) * 1.44269504088896341f; a2[1] = (dt * bf_hi(rar)) * 1.44269504088896341f;
  bv[0] = (dt * bf_lo(rai)) * 0.15915494309189534f; bv[1] = (dt * bf_hi(rai)) * 0.15915494309189534f;
  m0[0] = a2[0] > 0.f ? 2048.f : 0.f; m0[1] = a2[1] > 0.f ? 2048.f : 0.f;
  char* my = smem + lane * 8;
  if (w == 0) {
    cf v[2], v8[2], e8[2];
    float bound = 0.f;
#pragma unroll
    for (int q = 0; q < 2; ++q) {
      const float dbr = dt * (q ? bf_hi(rbr) : bf_lo(rbr)), dbi = dt * (q ? bf_hi(rbi) : bf_lo(rbi));
      const float cr = q ? bf_hi(rcr) : bf_lo(rcr), ci = q ? bf_hi(rci) : bf_lo(rci);
      const float gsh = __builtin_amdgcn_exp2f(a2[q] * m0[q]);
      v[q].re = (cr * dbr - ci * dbi) * gsh; v[q].im = -(cr * dbi + ci * dbr) * gsh;
      bound += (fabsf(v[q].re) + fabsf(v[q].im)) * __builtin_amdgcn_exp2f(fmaxf(a2[q], 0.f) * 120.f);
      e8[q] = cexp_(a2[q], -bv[q], 8.f, 0.f);
      v8[q] = cexp_(a2[q], -bv[q], 64.f, 0.f);
    }
#pragma unroll
    for (int o = 1; o < 64; o <<= 1) bound += __shfl_xor(bound, o, 64);
    int ex = (int)((__float_as_uint(bound) >> 23) & 0xff);
    ex = ex < 20 ? 20 : (ex > 240 ? 240 : ex);
    const float sv = __uint_as_float((unsigned)(127 + 13 + 127 - ex) << 23);
    if (lane == 0) st4(p.ws, WS_KSC + 4 * d, (unsigned)(ex - 13) << 23);
#pragma unroll
    for (int q = 0; q < 2; ++q) { v[q].re *= sv; v[q].im *= sv; v8[q] = cmul(v[q], v8[q]); }
#pragma unroll
    for (int j = 0; j < 8; ++j) {
      put2(my + (41 + j) * RS, v[0], v[1]);
      put2(my + (49 + j) * RS, v8[0], v8[1]);
      if (j < 7) {
#pragma unroll
        for (int q = 0; q < 2; ++q) { v[q] = cmul(v[q], e8[q]); v8[q] = cmul(v8[q], e8[q]); }
      }
    }
  } else if (w == 1) {
    cf e1[2], u[2], u2[2];
#pragma unroll
    for (int q = 0; q < 2; ++q) {
      e1[q] = cexp_(a2[q], bv[q], 1.f, 0.f);
      u[q].re = __builtin_amdgcn_exp2f(-a2[q] * m0[q]); u[q].im = 0.f;
      u2[q] = cexp_(a2[q], bv[q], 128.f, m0[q]);
    }
#pragma unroll
    for (int r = 0; r < 8; ++r) {
      put2(my + r * RS, u[0], u[1]);
      put2(my + (8 + r) * RS, u2[0], u2[1]);
      if (r < 7) {
#pragma unroll
        for (int q = 0; q < 2; ++q) { u[q] = cmul(u[q], e1[q]); u2[q] = cmul(u2[q], e1[q]); }
      }
    }
  } else if (w == 2) {
    cf e2[2], u[2], u2[2];
#pragma unroll
    for (int q = 0; q < 2; ++q) {
      e2[q] = cexp_(a2[q], bv[q], 2.f, 0.f);
      u[q] = cexp_(a2[q], bv[q], 256.f, m0[q]);
      u2[q] = cexp_(a2[q], bv[q], 384.f, m0[q]);
    }
#pragma unroll
    for (int r = 0; r < 4; ++r) {
      put2(my + (16 + r) * RS, u[0], u[1]);
      put2(my + (20 + r) * RS, u2[0], u2[1]);
      if (r < 3) {
#pragma unroll
        for (int q = 0; q < 2; ++q) { u[q] = cmul(u[q], e2[q]); u2[q] = cmul(u2[q], e2[q]); }
      }
    }
    cf e4[2] = {cmul(e2[0], e2[0]), cmul(e2[1], e2[1])};
#pragma unroll
    for (int r = 0; r < 2; ++r) {
      cf c0 = cexp_(a2[0], bv[0], 512.f + 128.f * r, m0[0]), c1 = cexp_(a2[1], bv[1], 512.f + 128.f * r, m0[1]);
      put2(my + (24 + 2 * r) * RS, c0, c1);
      put2(my + (25 + 2 * r) * RS, cmul(c0, e4[0]), cmul(c1, e4[1]));
    }
  } else {
    cf e4[2], e128[2], u[2], u2[2];
#pragma unroll
    for (int q = 0; q < 2; ++q) {
      e4[q] = cexp_(a2[q], bv[q], 4.f, 0.f);
      e128[q] = cexp_(a2[q], bv[q], 128.f, 0.f);
      u[q] = cexp_(a2[q], bv[q], 1024.f, m0[q]);
      u2[q] = cexp_(a2[q], bv[q], 1536.f, m0[q]);
    }
#pragma unroll
    for (int r = 2; r < 4; ++r) {
      cf c0 = cexp_(a2[0], bv[0], 512.f + 128.f * r, m0[0]), c1 = cexp_(a2[1], bv[1], 512.f + 128.f * r, m0[1]);
      put2(my + (24 + 2 * r) * RS, c0, c1);
      put2(my + (25 + 2 * r) * RS, cmul(c0, e4[0]), cmul(c1, e4[1]));
    }
#pragma unroll
    for (int r = 0; r < 4; ++r) {
      put2(my + (32 + r) * RS, u[0], u[1]);
      put2(my + (36 + r) * RS, u2[0], u2[1]);
#pragma unroll
      for (int q = 0; q < 2; ++q) { if (r < 3) u[q] = cmul(u[q], e128[q]); u2[q] = cmul(u2[q], e128[q]); }
    }
    put2(my + 40 * RS, u2[0], u2[1]);
  }
  __syncthreads();
  STAMP(0, 2);
  _Float16* kh = (_Float16*)(smem + 57 * RS);
  if (w < 3) {
    const char* ua = smem + (16 * w + i) * RS + g * 16;
    const char* va = smem + (41 + i) * RS + g * 16;
    f32x4 acc = {0.f, 0.f, 0.f, 0.f};
#pragma unroll
    for (int ks = 0; ks < 8; ++ks)
      acc = mfma_f16(*(const u32x4*)(ua + ks * 64), *(const u32x4*)(va + ks * 64), acc);
    int idx0, st;
    if (w == 0) { idx0 = (g < 2 ? 4 * g : 120 + 4 * g) + 8 * i; st = 1; }
    else if (w == 1) { idx0 = g < 2 ? 256 + 64 * g + 4 * i : 384 + 64 * (g - 2) + 2 * i; st = 1; }
    else { idx0 = 512 + 64 * g + i; st = 16; }
#pragma unroll
    for (int rg = 0; rg < 4; ++rg) {
      int idx = idx0 + st * rg;
      if (w == 1 && g >= 2) idx = idx0 + 32 * (rg >> 1) + (rg & 1);
      if (w == 2 && g >= 2) idx = (g == 2 && rg == 0 && i == 0) ? 640 : 644;
      kh[idx] = (_Float16)acc[rg];
    }
  }
  __syncthreads();
  STAMP(0, 3);
  const unsigned short* ku = (const unsigned short*)kh;
  const size_t kout = WS_KREV + (size_t)d * KREV * 2;
  {
    const int l0 = 2040 - 8 * tid;
    u32x4 o;
    if (w < 2) {
      const int q = l0 >> 3;
      const unsigned a = ku[384 + q], b = ku[385 + q];
      __builtin_amdgcn_raw_buffer_store_b32(a | (b << 16), ws_rsrc(p.ws), (int)(kout + 4 * tid), 0, 16);
      return;
    } else if (w == 2) {
      const int q = l0 >> 2;
      const unsigned a = ku[256 + q], b = ku[257 + q], c = ku[258 + q];
      o[0] = c | (c << 16); o[1] = b | (b << 16); o[2] = b | (a << 16); o[3] = a | (a << 16);
    } else {
      unsigned v[8];
#pragma unroll
      for (int e = 0; e < 8; ++e) {
        const int l = l0 + 7 - e;
        const int idx = l < 256 ? l : (int)(pk_bf((float)l, 0.f) & 0xffffu) - 17024;
        v[e] = ku[idx];
      }
#pragma unroll
      for (int q = 0; q < 4; ++q) o[q] = v[2 * q] | (v[2 * q + 1] << 16);
    }
    st16(ws_rsrc(p.ws), kout + 16 * tid - 1536, o);
  }
}

__global__ NOPK __launch_bounds__(256) void k1(K1Args p) {
  __shared__ __attribute__((aligned(16))) char smem[57 * KG_RS + 1312];
  int b = blockIdx.x;
  TS(0, p.ws);
  char* stamp_ws = p.ws; (void)stamp_ws;
  STAMP(0, 0);
  if (b < NB_PF) { k1_prefetch(); STAMP(0, 7); return; }
  b -= NB_PF;
  if (b < NB_K) { k1_kgen(p, KMAP ? (b & 7) * 80 + (b >> 3) : b, smem); VM0(); STAMP(0, 7); return; }
  b -= NB_K;
  asm volatile("" :: "s"(p.ws), "s"(p.x));
#define DL_LN 4
#define DL_MOD 4
  if (b < NB_LN) { __builtin_amdgcn_s_sleep(DL_LN); k1_ln(p, b, smem); }
  else { __builtin_amdgcn_s_sleep(DL_MOD); k1_mod(p, b - NB_LN, smem); }
  VM0();
  STAMP(0, 7);
}

constexpr int K2_CS = 4160;
constexpr int K2_KOFF = 4 * K2_CS;
constexpr int K2_CH = K2_KOFF + 4608;
constexpr int CV_RED = 3 * K2_CH;
constexpr int CV_RT2 = 86016;
constexpr int CV_SUM = CV_RT2 + 4 * (32 * RT_STR + 256);
constexpr int CV_XCH = CV_SUM + 2560;
constexpr int CV_FL = CV_RED + 20480;

struct Chan { float sa, ca1, ga, dsk, hs, hinv, gy, gd; };
struct ChanRaw { float m, kinv, dsk; };
DEV ChanRaw chan_ld(const unsigned short* dskip, const char* ws, int d, int lane) {
  ChanRaw c;
  c.kinv = ldu<float>(ws + WS_KSC + 4 * d);
  c.dsk = ldbf_u(dskip + d);
  c.m = ((const float*)(ws + WS_MOD))[3840 * (lane & 7) + D_ * ((lane >> 3) & 3) + d];
  return c;
}
template <int CTRL> DEV float dpp_add(float v) {
  return v + __int_as_float(__builtin_amdgcn_update_dpp(0, __float_as_int(v), CTRL, 0xf, 0xf, true));
}
DEV Chan chan_fin(const ChanRaw& r) {
  Chan c;
  const int t = __float_as_int(rbf(dpp_add<0x141>(dpp_add<0x4e>(dpp_add<0xb1>(r.m)))));
  c.sa = __int_as_float(__builtin_amdgcn_readlane(t, 0));
  c.ca1 = rbf(1.f + __int_as_float(__builtin_amdgcn_readlane(t, 8)));
  c.ga = __int_as_float(__builtin_amdgcn_readlane(t, 16));
  c.dsk = r.dsk;
  const float hb = 26.f * fabsf(c.ca1) + fabsf(c.sa);
  int ex = (int)((__float_as_uint(hb) >> 23) & 0xff);
  ex = ex < 20 ? 20 : (ex > 240 ? 240 : ex);
  c.hs = __uint_as_float((unsigned)(127 + 14 + 127 - ex) << 23);
  c.hinv = __uint_as_float((unsigned)(ex - 14) << 23);
  c.gy = r.kinv * c.hinv * c.ga;
  c.gd = c.hinv * c.dsk * c.ga;
  return c;
}
DEV size_t tl_off(int d, int t) { return (((size_t)(t >> 5) * D_ + d) * 32 + (t & 31)) * 2; }
struct StageRaw { u32x4 xa[2], kv1; u32x2 xb; unsigned kv0; };
DEV StageRaw stage_ld(const char* ws, int d, int th) {
  StageRaw s;
  const unsigned short* krev = (const unsigned short*)(ws + WS_KREV) + (size_t)d * KREV;
  const int T = 1024 * (th >> 6) + 8 * (th & 63);
  s.xb = ldu<u32x2>(ws + WS_XNT + tl_off(d, 1024));
  s.xa[0] = *(const u32x4*)(ws + WS_XNT + tl_off(d, T)); s.xa[1] = *(const u32x4*)(ws + WS_XNT + tl_off(d, T + 512));
  s.kv0 = *(const unsigned*)(krev + 2 * th); s.kv1 = *(const u32x4*)(krev + 256 + 8 * th);
  return s;
}
DEV void stage_wr(const StageRaw& s, const Chan& c, char* smem, int th) {
  constexpr int CS = K2_CS, KOFF = K2_KOFF;
  const h16x2 c2 = __builtin_bit_cast(h16x2, pk_h(c.ca1 * c.hs, c.ca1 * c.hs)), s2 = __builtin_bit_cast(h16x2, pk_h(c.sa * c.hs, c.sa * c.hs));
  auto mod = [&](unsigned u) __attribute__((always_inline)) {
    return __builtin_bit_cast(unsigned, __builtin_elementwise_fma(__builtin_bit_cast(h16x2, u), c2, s2));
  };
  unsigned h2[2][6];
#pragma unroll
  for (int e = 0; e < 2; ++e)
#pragma unroll
    for (int q = 0; q < 4; ++q) h2[e][q] = mod(s.xa[e][q]);
#pragma unroll
  for (int q = 0; q < 2; ++q) {
    h2[0][4 + q] = __builtin_amdgcn_update_dpp(__builtin_amdgcn_readfirstlane(h2[1][q]), h2[0][q], 0x130, 0xf, 0xf, false);
    h2[1][4 + q] = __builtin_amdgcn_update_dpp(mod(s.xb[q]), h2[1][q], 0x130, 0xf, 0xf, false);
  }
  char* dst = smem + (128 * (th >> 6) + (th & 63) + 2) * 16;
  unsigned s1[5];
#pragma unroll
  for (int e = 1; e >= 0; --e) {
    const unsigned* hh = h2[e];
#pragma unroll
    for (int q = 0; q < 5; ++q) s1[q] = __builtin_amdgcn_alignbit(hh[q + 1], hh[q], 16);
    *(u32x4*)(dst + 1024 * e) = (u32x4){hh[0], hh[1], hh[2], hh[3]};
    *(u32x4*)(dst + 1024 * e + CS) = (u32x4){s1[0], s1[1], s1[2], s1[3]};
    *(u32x4*)(dst + 1024 * e + 2 * CS) = (u32x4){hh[1], hh[2], hh[3], hh[4]};
    *(u32x4*)(dst + 1024 * e + 3 * CS) = (u32x4){s1[1], s1[2], s1[3], s1[4]};
  }
  {
    const unsigned a = __builtin_amdgcn_readfirstlane(h2[0][0]), b = __builtin_amdgcn_readfirstlane(s1[0]);
    if (th >= 1 && th < 4)
      *(u32x4*)(smem + th * CS + 16) = (u32x4){0u, 0u, th == 3 ? a << 16 : 0u, th == 1 ? a << 16 : (th == 2 ? a : b)};
  }
  {
    const unsigned aa = __builtin_amdgcn_perm(s.kv0, s.kv0, 0x01000100u), bb = __builtin_amdgcn_perm(s.kv0, s.kv0, 0x03020302u);
    const unsigned ba = __builtin_amdgcn_perm(s.kv0, s.kv0, 0x01000302u);
    *(u32x4*)(smem + KOFF + 16 * th) = (u32x4){bb, (th & 1) ? ba : bb, aa, aa};
  }
  *(u32x4*)(smem + KOFF + 2048 + 16 * th) = s.kv1;
}
DEV void stage_zero(char* smem, int th) {
  if (th < 37) *(u32x4*)(smem + (th < 32 ? K2_KOFF + 4096 + 16 * th : (th < 36 ? (th - 32) * K2_CS : 16))) = (u32x4){0u, 0u, 0u, 0u};
}
DEV f32x4 conv_out(const f32x4& y, const Chan& c, const char* smem, int t) {
  const u32x2 hv = *(const u32x2*)(smem + 2 * (t + 16));
  f32x4 o;
#pragma unroll
  for (int e = 0; e < 2; ++e) {
    const unsigned hu = hv[e];
    const h16x2 h2 = __builtin_bit_cast(h16x2, hu);
    o[2 * e] = fmaf((float)h2[0], c.gd, y[2 * e] * c.gy);
    o[2 * e + 1] = fmaf((float)h2[1], c.gd, y[2 * e + 1] * c.gy);
  }
  return o;
}
DEV unsigned add_bf(unsigned x, float a, float b) { return pk_bf(bf_lo(x) + a, bf_hi(x) + b); }
#define K2_LOAD(buf, cc, u)                                                                                     \
  asm volatile("ds_read_b64 %0, %3 offset:%5\n\tds_read_b64 %1, %3 offset:%6\n\tds_read_b128 %2, %4 offset:%7"   \
               : "=&v"(Al[buf][u]), "=&v"(Ah[buf][u]), "=&v"(Bf[buf][u])                                          \
               : "v"(ap0), "v"(bp0), "i"(64 * (cc) + 512 * (u)), "i"(64 * (cc) + 512 * (u) + 8),                 \
                 "i"(64 * (cc) + 3584 - 512 * (u)))
template <int SET> DEV constexpr int half_tile(int j) {
  return SET == 0 ? (j == 0 ? 7 : (j == 1 ? 6 : 2)) : (j < 3 ? 5 - j : 4 - j);
}
template <int SET>
DEV void conv_half(unsigned ap0, unsigned bp0, char* redw) {
  constexpr int NU = SET == 0 ? 8 : 6, NT = SET == 0 ? 3 : 5;
  u32x2 Al[2][NU], Ah[2][NU];
  u32x4 Bf[2][NU];
#pragma unroll
  for (int u = 0; u < NU; ++u) K2_LOAD(0, 0, u);
  f32x4 acc[NT];
#pragma unroll
  for (int j = 0; j < NT; ++j) acc[j] = (f32x4){0.f, 0.f, 0.f, 0.f};
#pragma unroll
  for (int cc = 0; cc < 2; ++cc) {
    lgkm0();
    if (cc == 0) {
#pragma unroll
      for (int u = 0; u < NU; ++u) K2_LOAD(1, 1, u);
    }
#pragma unroll
    for (int sp = 0; sp < NU; ++sp)
#pragma unroll
      for (int j = 0; j < NT; ++j) {
        const int q = half_tile<SET>(j);
        if (sp <= q) {
          const u32x4 A = {Al[cc][sp][0], Al[cc][sp][1], Ah[cc][sp][0], Ah[cc][sp][1]};
          acc[j] = mfma_f16(A, Bf[cc][q - sp], acc[j]);
        }
      }
    SB();
  }
#pragma unroll
  for (int j = 0; j < NT; ++j) *(f32x4*)(redw + j * 4096) = acc[j];
}

template <int NF>
DEV void w1_fin(const RtRaw& t, const ModP& mp, char* ws, int kk, int ct0, int lane, char* scr, float* sums) {
  const int g = lane >> 4, i = lane & 15;
  float* tb = (float*)(scr + 32 * RT_STR);
  const float mv = mod_fin(mp);
  tb[lane] = lane < 32 ? rbf(1.f + mv) : mv;
  rt_park<NF>(t, scr, lane);
  lgkm0();
  const f32x4 ca = *(const f32x4*)(tb + 8 * g), cb = *(const f32x4*)(tb + 8 * g + 4);
  const f32x4 sa = *(const f32x4*)(tb + 32 + 8 * g), sb = *(const f32x4*)(tb + 36 + 8 * g);
  const u32x4 sv = {pk_bf(sa[0], sa[1]), pk_bf(sa[2], sa[3]), pk_bf(sb[0], sb[1]), pk_bf(sb[2], sb[3])};
  const u32x4 ones = {0x3f803f80u, 0x3f803f80u, 0x3f803f80u, 0x3f803f80u};
  const f32x4 z = {0.f, 0.f, 0.f, 0.f};
  const rsrc_t rs = ws_rsrc(ws);
#pragma unroll
  for (int j = 0; j < NF; ++j) {
    const u32x4 f = rt_frag(scr, j, lane);
    const u32x4 sc = {pk_bf(bf_lo(f[0]) * ca[0], bf_hi(f[0]) * ca[1]), pk_bf(bf_lo(f[1]) * ca[2], bf_hi(f[1]) * ca[3]),
                      pk_bf(bf_lo(f[2]) * cb[0], bf_hi(f[2]) * cb[1]), pk_bf(bf_lo(f[3]) * cb[2], bf_hi(f[3]) * cb[3])};
    st16(rs, WS_W1F + ((size_t)(kk * W1KS + (ct0 + j) * W1TS) * 64 + lane) * 16, sc);
    const f32x4 cs = mfma_bf16(sc, ones, z), b1 = mfma_bf16(f, sv, z);
    if (i < 2) *(f32x4*)(sums + i * (16 * NF) + 16 * j + 4 * g) = i ? b1 : cs;
  }
}

DEV void conv_phase(const unsigned short* __restrict__ dskip, char* __restrict__ ws,
                    const unsigned short* __restrict__ w1, const unsigned short* __restrict__ w2, char* smem_all) {
  constexpr int CS = K2_CS, KOFF = K2_KOFF;
  const int tid = threadIdx.x, lane = tid & 63;
  const int w = __builtin_amdgcn_readfirstlane(tid >> 6);
  const int bx = blockIdx.x;
  const int pj = (bx & 7) * 16 + (bx >> 4), par = (bx >> 3) & 1;
  const int r = lane & 15, g = lane >> 4, th = tid & 127;
  const int s4 = (r + 1) & 3;
  const rsrc_t rs = ws_rsrc(ws);
  unsigned* fl = (unsigned*)(smem_all + CV_FL);
  char* stamp_ws = ws; (void)stamp_ws;
  STAMP(1, 0);
  asm volatile("" :: "s"(w1), "s"(w2));
  if (tid < 12) fl[tid] = 0u;
  if (w < 4) {
    const int d = 5 * pj + 2 * par + (w >> 1), hw = w & 1;
    char* smem = smem_all + (w >> 1) * K2_CH;
    const ChanRaw cr = chan_ld(dskip, ws, d, lane);
    SB();
    const StageRaw sr = stage_ld(ws, d, th);
    u32x4 xr4[2];
    u32x2 xres[4];
    auto ld_res = [&]() __attribute__((always_inline)) {
#pragma unroll
      for (int qp = 0; qp < 2; ++qp)
        xr4[qp] = *(const u32x4*)(ws + WS_XT + tl_off(d, 256 * (4 * hw + 2 * qp + (g & 1)) + 16 * r + 8 * (g >> 1)));
    };
    ld_res();
    stage_zero(smem, th);
    SB();
    asm volatile("s_waitcnt lgkmcnt(0)\n\ts_barrier" ::: "memory");
    SB();
    const Chan c = chan_fin(cr);
    STAMP(1, 1);
    stage_wr(sr, c, smem, th);
    lds_post(fl + 6 + (w >> 1), lane);
    lds_wait(fl + 6 + (w >> 1), 2u);
    STAMP(1, 2);
    f32x4 acc[8], keep[4];
#pragma unroll
    for (int q = 0; q < 8; ++q) acc[q] = (f32x4){0.f, 0.f, 0.f, 0.f};
    {
      const unsigned ap0 = (unsigned)(size_t)(smem + s4 * CS + 2 * (128 * hw + 8 * g + (r + 1) - s4));
      const unsigned bp0 = (unsigned)(size_t)(smem + KOFF + 2 * (2032 + 128 * hw - 16 * r + 8 * g) - 3584);
      u32x2 Al[2][8], Ah[2][8];
      u32x4 Bf[2][8];
#pragma unroll
      for (int u = 0; u < 8; ++u) K2_LOAD(0, 0, u);
      auto tile = [&](int cc, int q) __attribute__((always_inline)) {
#pragma unroll
        for (int sp = 0; sp <= q; ++sp) {
          const u32x4 A = {Al[cc & 1][sp][0], Al[cc & 1][sp][1], Ah[cc & 1][sp][0], Ah[cc & 1][sp][1]};
          acc[q] = mfma_f16(A, Bf[cc & 1][q - sp], acc[q]);
        }
        SB();
      };
#pragma unroll
      for (int cc = 0; cc < 3; ++cc) {
        lgkm0();
#pragma unroll
        for (int q = 0; q < 8; ++q) {
          K2_LOAD((cc + 1) & 1, cc + 1, q);
          tile(cc, q);
        }
      }
#pragma unroll
      for (int qp = 0; qp < 2; ++qp) {
        const auto s0 = __builtin_amdgcn_permlane16_swap(xr4[qp][0], xr4[qp][2], false, false);
        const auto s1 = __builtin_amdgcn_permlane16_swap(xr4[qp][1], xr4[qp][3], false, false);
        xres[2 * qp] = (u32x2){s0[0], s1[0]}; xres[2 * qp + 1] = (u32x2){s0[1], s1[1]};
      }
      lgkm0();
      f32x4* xw = (f32x4*)(smem_all + CV_XCH + w * 4096) + lane;
      auto last = [&](auto hw_tag) __attribute__((always_inline)) {
        constexpr int S = decltype(hw_tag)::value ? 0 : 4, K = 4 - S;
#pragma unroll
        for (int q = 0; q < 4; ++q) {
          tile(3, S + q);
          if (q) { xw[64 * (q - 1)] = acc[S + q - 1]; SB(); }
        }
        tile(3, K + 3);
        xw[64 * 3] = acc[S + 3];
        lds_post(fl + w, lane);
        SB();
#pragma unroll
        for (int q = 0; q < 3; ++q) tile(3, K + q);
#pragma unroll
        for (int q = 0; q < 4; ++q) keep[q] = acc[K + q];
      };
      if (hw) last(std::true_type{}); else last(std::false_type{});
    }
    STAMP(1, 3);
    lds_wait(fl + (w ^ 1), 1u);
    STAMP(1, 4);
    const f32x4* xrd = (const f32x4*)(smem_all + CV_XCH + (w ^ 1) * 4096) + lane;
#pragma unroll
    for (int qp = 0; qp < 2; ++qp) {
      u32x2 o[2];
#pragma unroll
      for (int h = 0; h < 2; ++h) {
        const int q = 2 * qp + h;
        const f32x4 v = conv_out(keep[q] + xrd[64 * q], c, smem, 256 * (4 * hw + q) + 16 * r + 4 * g);
        o[h] = (u32x2){add_bf(xres[q][0], v[0], v[1]), add_bf(xres[q][1], v[2], v[3])};
      }
      const auto s0 = __builtin_amdgcn_permlane16_swap(o[0][0], o[1][0], false, false);
      const auto s1 = __builtin_amdgcn_permlane16_swap(o[0][1], o[1][1], false, false);
      const int t = 256 * (4 * hw + 2 * qp + (g & 1)) + 16 * r + 8 * (g >> 1);
      st16(rs, WS_X1T + (((size_t)(t >> 6) * D_ + d) * 64 + (t & 63)) * 2, (u32x4){s0[0], s1[0], s0[1], s1[1]});
    }
  } else {
#define PRIO47 2
    __builtin_amdgcn_s_setprio(PRIO47);
    const int d = 5 * pj + 4, wq = w - 4;
    char* smem = smem_all + 2 * K2_CH;
    StageRaw sr;
    const ChanRaw cr = chan_ld(dskip, ws, d, lane);
    asm volatile("" ::: "memory");
    if (wq < 2) sr = stage_ld(ws, d, th);
    asm volatile("" ::: "memory");
    const int tl0 = par ? (wq < 3 ? 5 - wq : 1) : (wq == 0 ? 7 : (wq == 1 ? 6 : 2)), tl1 = 0;
    const unsigned ap0 = (unsigned)(size_t)(smem + s4 * CS + 2 * (64 * wq + 8 * g + (r + 1) - s4));
    const unsigned bp0 = (unsigned)(size_t)(smem + KOFF + 2 * (2032 + 64 * wq - 16 * r + 8 * g) - 3584);
    char* redw = smem_all + CV_RED + wq * 1024 + lane * 16;
    u32x2 xres[2];
    xres[0] = *(const u32x2*)(ws + WS_XT + tl_off(d, 256 * tl0 + 16 * r + 4 * g));
    xres[1] = *(const u32x2*)(ws + WS_XT + tl_off(d, 256 * tl1 + 16 * r + 4 * g));
    if (wq == 0) stage_zero(smem, th);
    SB();
    __builtin_amdgcn_s_barrier();
    SB();
    const Chan c = chan_fin(cr);
    if (wq < 2) {
      stage_wr(sr, c, smem, th);
      lds_post(fl + 8, lane);
    }
#define NF1_ 4
#define NF2_ 5
    constexpr int NF1 = NF1_, NC1 = 108 / NF1, NB1 = 5 * NC1, NF2 = NF2_, NC2 = 40 / NF2, NJ2 = 54 * NC2;
    const bool isw1 = bx < NB1;
    const int j2 = 4 * (bx - NB1) + wq;
    const bool isw2 = !isw1 && j2 < NJ2, isgb = j2 >= NJ2 && j2 < NJ2 + 10;
    const int jk = isw1 ? 4 * (bx / NC1) + wq : j2 / NC2;
    const int jc = isw1 ? NF1 * (bx % NC1) : NF2 * (j2 % NC2);
    RtRaw rt;
    const int gbi = 64 * (j2 - NJ2) + lane;
    ModP mp;
    SB();
#define RT_MODE 2
#define RT67 24
    auto rtl = [&]() __attribute__((always_inline)) {
      if (isw1) rt_load<NF1>(rt, w1, FF, jk, jc, lane);
      else if (isw2) rt_load<NF2>(rt, w2, D_, jk, jc, lane);
      SB();
    };
    if (wq >= 2 && RT67) __builtin_amdgcn_s_sleep(RT67);
    rtl();
    lds_wait(fl + 8, 2u);
    STAMP(1, 2);
    if (isw1 || isgb) mp = mod_ld(rs, isw1 ? (lane < 32 ? 4 * D_ : 3 * D_ - 32) + 32 * jk + lane : 5 * D_ + gbi);
    SB();
    if (par) conv_half<1>(ap0, bp0, redw); else conv_half<0>(ap0, bp0, redw);
    lds_post(fl + 4, lane);
    STAMP(1, 3);
    STAMP(1, 4);
    lds_wait(fl + 4, 4u);
    STAMP(1, 5);
    const char* rd = smem_all + CV_RED + lane * 16;
#pragma unroll
    for (int u = 0; u < 2; ++u) {
      const bool on = u == 0 ? (par || wq < 3) : (par && wq == 0);
      if (on) {
        const int sl = u == 0 ? wq : 4, tl = u == 0 ? tl0 : tl1;
        const f32x4 y = (*(const f32x4*)(rd + sl * 4096) + *(const f32x4*)(rd + sl * 4096 + 1024))
                      + (*(const f32x4*)(rd + sl * 4096 + 2048) + *(const f32x4*)(rd + sl * 4096 + 3072));
        const int t = 256 * tl + 16 * r + 4 * g;
        const f32x4 v = conv_out(y, c, smem, t);
        st8(rs, WS_X1T + (((size_t)(t >> 6) * D_ + d) * 64 + (t & 63)) * 2, (u32x2){add_bf(xres[u][0], v[0], v[1]), add_bf(xres[u][1], v[2], v[3])});
      }
    }
    SB();
    STAMP(1, 6);
    char* scr = smem_all + CV_RT2 + wq * (32 * RT_STR + 256);
    if (isw1) {
      float* sums = (float*)(smem_all + CV_SUM);
      w1_fin<NF1>(rt, mp, ws, jk, jc, lane, scr, sums + 32 * NF1 * wq);
      asm volatile("" ::: "memory");
      unsigned old = 0u;
      if (lane == 0) old = __hip_atomic_fetch_add(fl + 5, 1u, __ATOMIC_RELAXED, __HIP_MEMORY_SCOPE_WORKGROUP);
      asm volatile("" ::: "memory");
      if (__builtin_amdgcn_readfirstlane(old) == 3u && lane < 8 * NF1) {
        const f32x4* sp = (const f32x4*)sums + lane;
        const f32x4 v = (sp[0] + sp[8 * NF1]) + (sp[16 * NF1] + sp[24 * NF1]);
        st16(rs, WS_CSP + ((size_t)(2 * (bx / NC1) + lane / (4 * NF1)) * FF + 16 * jc + 4 * (lane % (4 * NF1))) * 4, __builtin_bit_cast(u32x4, v));
      }
    } else if (isw2) {
      rt_park<NF2>(rt, scr, lane);
      lgkm0();
#pragma unroll
      for (int j = 0; j < NF2; ++j) st16(rs, WS_W2F + ((size_t)((jc + j) * 54 + jk) * 64 + lane) * 16, rt_frag(scr, j, lane));
    } else if (isgb) {
      st4(ws, WS_GB + 4 * gbi, __float_as_uint(mod_fin(mp)));
    }
  }
}

__global__ NOPK __launch_bounds__(512, 2) void k2(const unsigned short* __restrict__ dskip, char* __restrict__ ws,
                                                  const unsigned short* __restrict__ w1, const unsigned short* __restrict__ w2) {
  __shared__ __attribute__((aligned(16))) char smem[CV_XCH + 16384];
  TS(1, ws);
  conv_phase(dskip, ws, w1, w2, smem);
  char* stamp_ws = ws; (void)stamp_ws;
  VM0();
  STAMP(1, 7);
}

DEV u32x4 ld16_sc1(rsrc_t r, int voff, int soff) { return __builtin_amdgcn_raw_buffer_load_b128(r, voff, soff, 16); }
DEV u32x4 ld16(rsrc_t r, int voff, int soff) { return __builtin_amdgcn_raw_buffer_load_b128(r, voff, soff, 0); }
DEV u32x4 tr_frag(const char* p) {
  const u32x2 a = __builtin_bit_cast(u32x2, __builtin_amdgcn_ds_read_tr16_b64_v4i16((lds_s16x4*)p));
  const u32x2 b = __builtin_bit_cast(u32x2, __builtin_amdgcn_ds_read_tr16_b64_v4i16((lds_s16x4*)(p + 256)));
  return (u32x4){a[0], a[1], b[0], b[1]};
}

constexpr int KF_RS = 84;
constexpr int KF_RB = 8 * 16 * KF_RS * 4;
constexpr int KF_BIG = 2 * KF_RB;
constexpr int NW2L = 8, NW2R = 27;
constexpr int KF_TAB = KF_BIG;
constexpr int KF_PS = KF_TAB + 320;
constexpr int KF_XR = KF_PS + 512;
constexpr int KF_CNT = KF_XR + 64 * 176;
constexpr int KF_LDS = KF_CNT + 16;
#define X_FIRST 5
#define X_STEP 4
#define W1_EARLY 3
#define W1_MID 7
#define W1_PRE 10
#define W2_START 6
#define W2_RATE 2
#define W2_INL 28
#define CS_STEP 14
#define SIG_SPLIT 4
#define H1_PRE 7
#define H1_BATCH 99
#define LOCK_MASK 0x55554u

DEV void kf_body(char* __restrict__ ws, unsigned short* __restrict__ out, char* smem) {
  char* xs = smem;
  float* tab = (float*)(smem + KF_TAB);
  float* ps = (float*)(smem + KF_PS);
  char* xr = smem + KF_XR;
  unsigned* lcnt = (unsigned*)(smem + KF_CNT);
  float* cst = (float*)(smem + 81920);
  const int tid = threadIdx.x, lane = tid & 63;
  const int w = __builtin_amdgcn_readfirstlane(tid >> 6);
  const int bx = blockIdx.x;
  const int ng = (bx & 1) * 4 + ((bx >> 3) & 3), mt = ((bx & 7) >> 1) * 8 + (bx >> 5);
  const int g = lane >> 4, i = lane & 15;
  const rsrc_t rs_ = ws_rsrc(ws);

  char* stamp_ws = ws; (void)stamp_ws;
  STAMP(2, 0);
  unsigned* flags = (unsigned*)(ws + WS_CNT) + mt * (2 * FL_P);
  const int ntl = ng < 6 ? 14 : 12;
  const int ctb = ng < 6 ? 14 * ng : 84 + 12 * (ng - 6);
  const bool two = w < ntl - 8;
  const bool full = w < 6;
  const int ks0 = w < 6 ? 7 * w : 42 + 6 * (w - 6);
  const int ct0 = ctb + (two ? 2 * w : (ntl - 8) + w);
  const int lo16 = lane * 16;
  const int w1s = (int)WS_W1F + ct0 * W1TS * 1024;
  const int w2s = (int)WS_W2F + (ng * 5 * 54 + ks0) * 1024;

  u32x4 ring[32], wl[NW2L];
  const char* bb0 = xs + (8 * g + (i >> 2)) * 64 + ((g & 1) << 5) + (i & 3) * 8;
  const char* bb1 = xs + (8 * g + (i >> 2)) * 64 + (((g & 1) ^ 1) << 5) + (i & 3) * 8;
  auto w2_off = [&](int e) __attribute__((always_inline)) {
    const int s = e < 27 ? e / 4 : (e < 34 ? e - 27 : 6), c = e < 27 ? e % 4 : (e < 34 ? 4 : 3);
    const int sn = (s == 6 && !full) ? 5 : s;
    return w2s + (c * 54 + sn) * 1024;
  };

  auto gemm1 = [&](auto two_tag) __attribute__((always_inline)) {
    constexpr bool TWO = decltype(two_tag)::value;
    if (TWO) asm volatile("; two tiles" ::: "memory"); else asm volatile("; one tile" ::: "memory");
    u32x4 t[10];
    auto st_x = [&](int i0, int i1) __attribute__((always_inline)) {
#pragma unroll
      for (int it = i0; it < i1; ++it) {
        const int c = tid + 512 * it;
        const int row = c >> 3;
        *(u32x4*)(xs + ((c >> 2) & 1) * 40960 + row * 64 + (((c & 3) ^ ((row >> 2) & 2)) << 4)) = t[it];
      }
    };
#pragma unroll
    for (int it = 0; it < X_FIRST; ++it) t[it] = ld16(rs_, tid * 16, (int)WS_X1T + mt * 81920 + 8192 * it);
#pragma unroll
    for (int k = 0; k < W1_EARLY; ++k) {
      ring[2 * k] = ld16(rs_, lo16, w1s + k * W1KS * 1024);
      if (TWO) ring[2 * k + 1] = ld16(rs_, lo16, w1s + (k * W1KS + W1TS) * 1024);
    }
    SB();
    __builtin_amdgcn_s_barrier();
    SB();
#pragma unroll
    for (int k = W1_EARLY; k < W1_MID; ++k) {
      ring[2 * k] = ld16(rs_, lo16, w1s + k * W1KS * 1024);
      if (TWO) ring[2 * k + 1] = ld16(rs_, lo16, w1s + (k * W1KS + W1TS) * 1024);
    }
    SB();
    st_x(0, X_FIRST);
    SB();
    if (tid == 0) *lcnt = 0u;
    float gbp;
    if (!TWO) gbp = ((const float*)(ws + WS_GB))[80 * ng + min((w & 1) * 64 + lane, 79)];
    SB();
    __syncthreads();
    STAMP(2, 1);
    auto resid = [&]() __attribute__((always_inline)) {
      const int kb = (80 * ng) >> 5, tq = w & 3;
#pragma unroll
      for (int u = 0; u < 2; ++u) {
        const int kk = min(kb + 2 * (w >> 2) + u, 19);
        const u32x4 f = tr_frag(((tq & 1) ? bb1 : bb0) + (tq >> 1) * 40960 + kk * 2048);
        const unsigned dl = (unsigned)(32 * kk + 8 * g - 80 * ng);
        if (dl < 80u) *(u32x4*)(xr + (tq * 16 + i) * 176 + dl * 2) = f;
      }
    };
    const int hs = TWO ? 0 : (w & 1);
    const char* ba[4] = {bb0 + hs * 40960, bb1 + hs * 40960, bb0 + (hs ^ 1) * 40960, bb1 + (hs ^ 1) * 40960};
    const u32x4 ones = {0x3f803f80u, 0x3f803f80u, 0x3f803f80u, 0x3f803f80u};
    f32x4 acc[2][4];
#pragma unroll
    for (int c = 0; c < 2; ++c)
#pragma unroll
      for (int q = 0; q < 4; ++q) acc[c][q] = (f32x4){0.f, 0.f, 0.f, 0.f};
    u32x4 csp[5];
    const int cso = ((w & 1) * FF + 16 * ctb + 4 * min(lane, 4 * ntl - 1)) * 4;
    u32x4 bn[2][4];
#pragma unroll
    for (int q = 0; q < 4; ++q) bn[0][q] = tr_frag(ba[q]);
#pragma unroll
    for (int q = 0; q < 4; ++q) bn[1][q] = tr_frag(ba[q] + 2048);
#pragma unroll
    for (int kk = 0; kk < 20; ++kk) {
      if (kk == X_STEP) { st_x(X_FIRST, 10); SB(); }
      if (kk == 2 * X_FIRST - 2) { SB(); bar_lds(); SB(); }
      else if ((LOCK_MASK >> kk) & 1) { SB(); __builtin_amdgcn_s_barrier(); SB(); }
      if (kk == 2 * X_FIRST) { resid(); SB(); }
      auto gload = [&](int u) __attribute__((always_inline)) {
        {
          constexpr int CATCH = W1_PRE - W1_MID;
          const int k0 = kk == 0 ? W1_MID : (kk <= CATCH ? W1_MID + 2 * kk - 1 : kk + W1_PRE);
          const int nk = (kk >= 1 && kk <= CATCH) ? 2 : 1;
          const int kn = k0 + (nk == 2 ? (u >> 1) : 0);
          const bool on = nk == 2 || u >= 2;
          if (on && kn < 20) {
            if ((u & 1) == 0) ring[(2 * kn) % 32] = ld16(rs_, lo16, w1s + kn * W1KS * 1024);
            else if (TWO) ring[(2 * kn + 1) % 32] = ld16(rs_, lo16, w1s + (kn * W1KS + W1TS) * 1024);
          }
          if (kk == 0 && u < 2) {
#pragma unroll
            for (int it = X_FIRST + u * 3; it < (u ? 10 : (X_FIRST + 3 < 10 ? X_FIRST + 3 : 10)); ++it)
              t[it] = ld16(rs_, tid * 16, (int)WS_X1T + mt * 81920 + 8192 * it);
          }
        }
        if (kk >= W2_START && u < W2_RATE) {
          const int n = W2_RATE * (kk - W2_START) + u;
          if (n < NW2L) wl[n] = ld16(rs_, lo16, w2_off(NW2R + n));
          else if (n < W2_INL) ring[8 + n - NW2L] = ld16(rs_, lo16, w2_off(n - NW2L));
        }
      };
      const u32x4 a0 = ring[(2 * kk) % 32], a1 = ring[(2 * kk + 1) % 32];
#pragma unroll
      for (int q = 0; q < 4; ++q) {
        const u32x4 bq = bn[kk & 1][q];
        acc[0][q] = mfma_bf16(a0, bq, acc[0][q]);
        if (TWO) acc[1][q] = mfma_bf16(a1, bq, acc[1][q]);
        else if (q < 2) {
          acc[1][2 * q] = mfma_bf16(ones, bq, acc[1][2 * q]);
          acc[1][2 * q + 1] = mfma_bf16(bq, bq, acc[1][2 * q + 1]);
        }
        SB();
        if (kk < 18) bn[kk & 1][q] = tr_frag(ba[q] + (kk + 2) * 2048);
        gload(q);
        SB();
      }
      if (!TWO) {
        if (kk < 5) csp[kk] = ld16(rs_, cso, (int)WS_CSP + kk * 2 * FF * 4);
        if (kk == CS_STEP) {
          f32x4 t = __builtin_bit_cast(f32x4, csp[0]);
#pragma unroll
          for (int u = 1; u < 5; ++u) t += __builtin_bit_cast(f32x4, csp[u]);
          if (w >= 6 && lane < 56) *(f32x4*)(cst + (w & 1) * 224 + 4 * lane) = t;
        }
      }
      if (!TWO && kk == 12) { if ((w & 1) * 64 + lane < 80) tab[(w & 1) * 64 + lane] = gbp; }
      SB();
    }
    STAMP(2, 2);
    if (!TWO) {
#pragma unroll
      for (int h = 0; h < 2; ++h) {
        const f32x4 aq = acc[1][2 * h + 1];
        const float dq = (i & 2) ? ((i & 1) ? aq[3] : aq[2]) : ((i & 1) ? aq[1] : aq[0]);
        const int tk = (2 * hs + h) * 16 + i;
        if (g == (i >> 2)) {
          const float mean = acc[1][2 * h][0] * (1.f / D_);
          const float var = fmaxf(dq * (1.f / D_) - mean * mean, 0.f);
          const float sd = rbf(sqrtf(rbf(rbf(var) + 1e-5f)));
          const float rs = 1.f / sd;
          ps[tk] = rs;
          ps[64 + tk] = -rbf(mean) * rs;
        }
      }
    }
    __syncthreads();
    f32x4 csv[2], b1v[2];
#pragma unroll
    for (int c = 0; c < (TWO ? 2 : 1); ++c) {
      csv[c] = *(const f32x4*)(cst + 16 * (ct0 - ctb + c) + 4 * g);
      b1v[c] = *(const f32x4*)(cst + 224 + 16 * (ct0 - ctb + c) + 4 * g);
    }
#pragma unroll
    for (int qp = 0; qp < 2; ++qp) {
      u32x2 o[2][2];
#pragma unroll
      for (int h = 0; h < 2; ++h) {
        const int q = 2 * qp + h, tq = q ^ (2 * hs);
        const float rs = ps[tq * 16 + i], nm = ps[64 + tq * 16 + i];
#pragma unroll
        for (int c = 0; c < (TWO ? 2 : 1); ++c) {
          float v[4];
#pragma unroll
          for (int r = 0; r < 4; ++r) v[r] = fmaxf(fmaf(acc[c][q][r], rs, fmaf(nm, csv[c][r], b1v[c][r])), 0.f);
          o[c][h] = (u32x2){pk_bf(v[0], v[1]), pk_bf(v[2], v[3])};
        }
      }
      const int tl = ((2 * qp) ^ (2 * hs)) + (g & 1);
#pragma unroll
      for (int c = 0; c < (TWO ? 2 : 1); ++c) {
        const int ct = ct0 + c;
        const auto s0 = __builtin_amdgcn_permlane16_swap(o[c][0][0], o[c][1][0], false, false);
        const auto s1 = __builtin_amdgcn_permlane16_swap(o[c][0][1], o[c][1][1], false, false);
        const size_t off = (((size_t)(4 * mt + tl) * 54 + (ct >> 1)) * 64 + i + 16 * (2 * (ct & 1) + (g >> 1))) * 16;
        st16(rs_, WS_H1F + off, (u32x4){s0[0], s1[0], s0[1], s1[1]});
      }
    }
    SB();
    __builtin_amdgcn_s_barrier();
    SB();
    constexpr int E0 = W2_INL - NW2L;
    auto signal = [&]() __attribute__((always_inline)) {
      if (lane == 0) {
        if (__hip_atomic_fetch_add(lcnt, 1u, __ATOMIC_RELAXED, __HIP_MEMORY_SCOPE_WORKGROUP) == 7u)
          __hip_atomic_store(flags + (FL_P / 4) * ng, 1u, __ATOMIC_RELAXED, __HIP_MEMORY_SCOPE_AGENT);
      }
    };
    if (w >= SIG_SPLIT) { asm volatile("s_waitcnt vmcnt(0)" ::: "memory"); signal(); }
    SB();
#pragma unroll
    for (int e = E0; e < NW2R; ++e) ring[(8 + e) % 32] = ld16(rs_, lo16, w2_off(e));
    SB();
    if (w < SIG_SPLIT) { asm volatile("s_waitcnt vmcnt(%0)" :: "n"(NW2R - E0) : "memory"); signal(); }
    SB();
  };
  if (two) gemm1(std::true_type{});
  else gemm1(std::false_type{});
  STAMP(2, 3);
#pragma nounroll
  for (unsigned spin = 0; spin < (1u << 20); ++spin) {
    if (__hip_atomic_load(flags + (FL_P / 4) * w, __ATOMIC_RELAXED, __HIP_MEMORY_SCOPE_AGENT) != 0u) break;
#define POLL_SL 1
    if (POLL_SL) __builtin_amdgcn_s_sleep(POLL_SL);
  }
  asm volatile("" ::: "memory");
  SB();

  STAMP(2, 4);
  const int h1s = (int)WS_H1F + (mt * 4 * 54 + ks0) * 1024;
  u32x4 hb[28];
  auto load_h1 = [&](int n) __attribute__((always_inline)) {
    const int q = n / 7, s = n % 7;
    const int sn = (s == 6 && !full) ? 5 : s;
    hb[n] = ld16_sc1(rs_, lo16, h1s + (q * 54 + sn) * 1024);
  };
#pragma unroll
  for (int n = 0; n < H1_PRE; ++n) {
    if (n == H1_BATCH) { SB(); __builtin_amdgcn_s_barrier(); SB(); }
    load_h1(n);
  }
  SB();
  float* red = (float*)smem;
  float* rb_ = red + (w * 16 + i) * KF_RS + 4 * g;
  const int re = w * 40 + min(lane, 39);
  const int rtk = re / 20, rq4 = re % 20;
  const float* rr_ = red + rtk * KF_RS + 4 * rq4;
  f32x4 ac2[2][5];
  f32x4 rv[8], rsum;
  u32x2 xv2;
  const f32x4 gb = *(const f32x4*)(tab + 4 * rq4);
  auto red_wr = [&](int q, int c) __attribute__((always_inline)) {
    *(f32x4*)(rb_ + (q & 1) * (KF_RB / 4) + 16 * c) = ac2[q & 1][c];
  };
  auto red_rd = [&](int q, int ww) __attribute__((always_inline)) {
    rv[ww] = *(const f32x4*)(rr_ + (q & 1) * (KF_RB / 4) + ww * 16 * KF_RS);
  };
  auto red_x = [&](int q) __attribute__((always_inline)) { xv2 = *(const u32x2*)(xr + (16 * q + rtk) * 176 + rq4 * 8); };
  auto red_out = [&](int q) __attribute__((always_inline)) {
    const f32x4 s = rsum + (rv[6] + rv[7]);
    u32x2 o;
#pragma unroll
    for (int h = 0; h < 2; ++h) {
      o[h] = pk_bf(fmaf(s[2 * h], gb[2 * h], bf_lo(xv2[h])), fmaf(s[2 * h + 1], gb[2 * h + 1], bf_hi(xv2[h])));
    }
    *(u32x2*)(out + (size_t)(mt * 64 + 16 * q + rtk) * D_ + ng * 80 + 4 * rq4) = o;
  };
#pragma unroll
  for (int q = 0; q < 3; ++q) {
#pragma unroll
    for (int c = 0; c < 5; ++c) ac2[q & 1][c] = (f32x4){0.f, 0.f, 0.f, 0.f};
#pragma unroll
    for (int s = 0; s < 7; ++s) {
      auto mm = [&](int c) __attribute__((always_inline)) {
        const u32x4 a = c == 4 ? wl[s] : ((s == 6 && c == 3) ? wl[7] : ring[(8 + 4 * s + c) % 32]);
        ac2[q & 1][c] = mfma_bf16(a, hb[7 * q + s], ac2[q & 1][c]);
      };
      if (s == 5 && q >= 2) {
        red_wr(q - 1, 4);
        SB();
        red_out(q - 2);
#pragma unroll
        for (int c = 0; c < 5; ++c) mm(c);
#pragma unroll
        for (int c = 0; c < 5; ++c) {
          __builtin_amdgcn_sched_group_barrier(0x008, 1, 0);
          __builtin_amdgcn_sched_group_barrier(0x002, 5, 0);
        }
        SB();
      } else if (s < 6) {
        mm(0); SB();
        if (q >= 1 && s >= 1) { red_wr(q - 1, s - 1); SB(); }
        mm(1); SB();
        if (q >= 2 && s < 4) { red_rd(q - 2, 2 * s); SB(); }
        mm(2); SB();
        if (q >= 2 && s < 4) { red_rd(q - 2, 2 * s + 1); SB(); }
        if (q >= 2 && s == 4) { red_x(q - 2); SB(); }
        mm(3); SB();
        if (q >= 2 && s >= 1 && s < 4) {
          const f32x4 t = rv[2 * s - 2] + rv[2 * s - 1];
          rsum = s == 1 ? t : rsum + t;
          asm volatile("" : "+v"(rsum));
          SB();
        }
        mm(4); SB();
      } else if (full) {
#pragma unroll
        for (int c = 0; c < 5; ++c) mm(c);
        SB();
      }
      if (7 * q + s + H1_PRE < 28) { load_h1(7 * q + s + H1_PRE); SB(); }
    }
    if (q >= 1) bar_lds();
    if (q == 0) STAMP(2, 5);
  }
  {
    auto sum6 = [&]() __attribute__((always_inline)) {
      rsum = ((rv[0] + rv[1]) + (rv[2] + rv[3])) + (rv[4] + rv[5]);
    };
    if (full) {
#pragma unroll
      for (int c = 0; c < 5; ++c) {
        const u32x4 a = c == 4 ? wl[6] : (c == 3 ? wl[7] : ring[(8 + 24 + c) % 32]);
        rv[c] = mfma_bf16(a, hb[27], (f32x4){0.f, 0.f, 0.f, 0.f});
      }
    } else {
#pragma unroll
      for (int c = 0; c < 5; ++c) rv[c] = (f32x4){0.f, 0.f, 0.f, 0.f};
    }
    f32x4 c0[5];
#pragma unroll
    for (int c = 0; c < 5; ++c) c0[c] = rv[c];
    SB();
    auto col = [&](int c, auto&& between) __attribute__((always_inline)) {
#pragma unroll
      for (int s = 0; s < 6; ++s) {
        const u32x4 a = c == 4 ? wl[s] : ring[(8 + 4 * s + c) % 32];
        c0[c] = mfma_bf16(a, hb[21 + s], c0[c]);
        SB();
        between(s);
        SB();
      }
    };
    col(0, [&](int s) __attribute__((always_inline)) { if (s < 5) red_wr(2, s); if (s < 4) { red_rd(1, 2 * s); red_rd(1, 2 * s + 1); } if (s == 4) red_x(1); });
    col(1, [&](int s) __attribute__((always_inline)) { if (s == 1) { sum6(); asm volatile("" : "+v"(rsum)); } if (s == 3) red_out(1); });
    bar_lds();
    auto wr3 = [&](int c) __attribute__((always_inline)) { *(f32x4*)(rb_ + (KF_RB / 4) + 16 * c) = c0[c]; };
    col(2, [&](int s) __attribute__((always_inline)) { if (s < 2) wr3(s); if (s >= 1 && s < 5) { red_rd(2, 2 * s - 2); red_rd(2, 2 * s - 1); } if (s == 5) red_x(2); });
    col(3, [&](int s) __attribute__((always_inline)) { if (s == 1) wr3(2); if (s == 2) { sum6(); asm volatile("" : "+v"(rsum)); } if (s == 4) red_out(2); });
    col(4, [&](int s) __attribute__((always_inline)) { if (s == 1) wr3(3); if (s == 2) red_x(3); });
    wr3(4);
    STAMP(2, 6);
    bar_lds();
#pragma unroll
    for (int ww = 0; ww < 8; ++ww) red_rd(3, ww);
    sum6();
    red_out(3);
    VM0();
    STAMP(2, 7);
  }
}

__global__ NOPK __launch_bounds__(512, 2) void kf(char* __restrict__ ws, unsigned short* __restrict__ out) {
  __shared__ __attribute__((aligned(16))) char smem[KF_LDS];
  TS(2, ws);
  kf_body(ws, out, smem);
}

__global__ void kprobe(char* ws, int which) {
  const unsigned long long now = __builtin_amdgcn_s_memrealtime();
  const unsigned long long* t = (const unsigned long long*)(ws + (200u << 20));
  unsigned long long d = which == 0 ? t[1] - t[0] : (which == 1 ? t[2] - t[1] : (which == 2 ? now - t[2] : 0ull));
  if (d > 10000ull) d = 10000ull;
  while (__builtin_amdgcn_s_memrealtime() - now < 10ull * d) __builtin_amdgcn_s_sleep(1);
}
__global__ void kend_() {}

extern "C" void kernel_launch(void* const* d_in, const int* in_sizes, int n_in,
                              void* d_out, int out_size, void* d_ws, size_t ws_size,
                              hipStream_t stream) {
  K1Args a;
  a.x = (const unsigned short*)d_in[0];
  a.are = (const unsigned short*)d_in[1];
  a.aim = (const unsigned short*)d_in[2];
  a.bre = (const unsigned short*)d_in[3];
  a.bim = (const unsigned short*)d_in[4];
  a.cre = (const unsigned short*)d_in[5];
  a.cim = (const unsigned short*)d_in[6];
  a.logdt = (const unsigned short*)d_in[7];
  a.c = (const unsigned short*)d_in[12];
  a.wmod = (const unsigned short*)d_in[13];
  a.ws = (char*)d_ws;
  k1<<<NB_PF + NB_K + NB_LN + NB_MOD, 256, 0, stream>>>(a);
  k2<<<256, 512, 0, stream>>>((const unsigned short*)d_in[8], (char*)d_ws, (const unsigned short*)d_in[10],
                              (const unsigned short*)d_in[11]);
  kf<<<256, 512, 0, stream>>>((char*)d_ws, (unsigned short*)d_out);
  kprobe<<<1, 64, 0, stream>>>((char*)d_ws, 2);
}
```

```cpp
#define PB 8
#define PM 0
#include <hip/hip_runtime.h>
#include <hip/hip_bf16.h>
#include <stdint.h>
#include <type_traits>

typedef __attribute__((ext_vector_type(8))) __bf16 bf16x8;
typedef __attribute__((ext_vector_type(2))) __bf16 bf16x2;
typedef __attribute__((ext_vector_type(4))) float f32x4;
typedef __attribute__((ext_vector_type(2))) float f32x2;
typedef __attribute__((ext_vector_type(4))) unsigned u32x4;
typedef __attribute__((ext_vector_type(2))) unsigned u32x2;
typedef __attribute__((ext_vector_type(8))) _Float16 h16x8;
typedef __attribute__((ext_vector_type(2))) _Float16 h16x2;
typedef __attribute__((ext_vector_type(4))) short s16x4;
typedef __attribute__((address_space(3))) s16x4 lds_s16x4;

#if defined(__HIP_DEVICE_COMPILE__)
#define NOPK __attribute__((target("no-packed-fp32-ops")))
#else
#define NOPK
#endif
#define DEV __device__ __forceinline__

#define W1L 0
constexpr int W1KS = W1L ? 1 : 108, W1TS = W1L ? 20 : 1;
#define LNFLIP 0
#define KMAP 0
constexpr int L_ = 2048;
constexpr int D_ = 640;
constexpr int NS = 128;
constexpr int FF = 1728;
constexpr int KREV = 1280;

constexpr size_t WS_GB   = 0;
constexpr size_t WS_KSC  = 8192;
#define FL_P 2048
#define FL_B 327680
constexpr size_t WS_CNT  = FL_B;
constexpr size_t WS_MOD  = 65536;
constexpr size_t WS_CSP  = 196608;
constexpr size_t WS_XNT  = 327680 + 256 * FL_P;
constexpr size_t WS_XT   = WS_XNT + (size_t)D_ * L_ * 2;
constexpr size_t WS_KREV = WS_XT + (size_t)D_ * L_ * 2;
constexpr size_t WS_W1F  = WS_KREV + (size_t)D_ * KREV * 2;
constexpr size_t WS_W2F  = WS_W1F + (size_t)D_ * FF * 2;
constexpr size_t WS_X1T  = WS_W2F + (size_t)D_ * FF * 2;
constexpr size_t WS_H1F  = WS_X1T + (size_t)D_ * L_ * 2;
constexpr size_t WS_END  = WS_H1F + (size_t)L_ * FF * 2;

#define STAMP(kid, k) do { if (kid == 0 && k == PM && blockIdx.x == PB && threadIdx.x == 0) __hip_atomic_store((unsigned long long*)(stamp_ws + (200u << 20)) + 1, (unsigned long long)__builtin_amdgcn_s_memrealtime(), __ATOMIC_RELAXED, __HIP_MEMORY_SCOPE_AGENT); } while (0)
#define CSTAMP(kid, k) do {} while (0)
#define VM0() do {} while (0)

#define TS(k, wsp) do { if (blockIdx.x == 0 && threadIdx.x == 0) __hip_atomic_store((unsigned long long*)((wsp) + (200u << 20)) + (k), (unsigned long long)__builtin_amdgcn_s_memrealtime(), __ATOMIC_RELAXED, __HIP_MEMORY_SCOPE_AGENT); } while (0)
DEV float bf_lo(unsigned u) { return __uint_as_float(u << 16); }
DEV float bf_hi(unsigned u) { return __uint_as_float(u & 0xffff0000u); }
DEV unsigned pk_bf(float a, float b) {
  f32x2 v = {a, b};
  bf16x2 r = __builtin_convertvector(v, bf16x2);
  return __builtin_bit_cast(unsigned, r);
}
DEV unsigned pk_h(float a, float b) {
  f32x2 v = {a, b};
  h16x2 r = __builtin_convertvector(v, h16x2);
  return __builtin_bit_cast(unsigned, r);
}
DEV float rbf(float a) { return bf_lo(pk_bf(a, 0.f)); }
DEV float ldbf(const unsigned short* p) { return __uint_as_float(((unsigned)*p) << 16); }
#define CONST_AS __attribute__((address_space(4)))
template <class T> DEV T ldu(const void* p) { return *(const CONST_AS T*)(size_t)p; }
DEV float ldbf_u(const unsigned short* p) {
  const unsigned v = ldu<unsigned>((const void*)((size_t)p & ~(size_t)3));
  return __uint_as_float(((size_t)p & 2) ? (v & 0xffff0000u) : (v << 16));
}

typedef __amdgpu_buffer_rsrc_t rsrc_t;
DEV rsrc_t ws_rsrc(char* ws) { return __builtin_amdgcn_make_buffer_rsrc((void*)ws, 0, (int)WS_END, 0x00020000); }
DEV void st16(rsrc_t r, size_t off, u32x4 v) { __builtin_amdgcn_raw_buffer_store_b128(v, r, (int)off, 0, 16); }
DEV void st4(char* ws, size_t off, unsigned v) { __hip_atomic_store((unsigned*)(ws + off), v, __ATOMIC_RELAXED, __HIP_MEMORY_SCOPE_AGENT); }
DEV void st8(rsrc_t r, size_t off, u32x2 v) { __builtin_amdgcn_raw_buffer_store_b64(v, r, (int)off, 0, 16); }

DEV void bar_lds() { asm volatile("s_waitcnt lgkmcnt(0)\n\ts_barrier" ::: "memory"); }
DEV void lds_wait(const void* word, unsigned val) {
  unsigned tv, ts, cnt;
  asm volatile("s_mov_b32 %2, 0x100000\n"
               "1:\n\tds_read_b32 %0, %3\n\ts_waitcnt lgkmcnt(0)\n\tv_readfirstlane_b32 %1, %0\n\t"
               "s_cmp_ge_u32 %1, %4\n\ts_cbranch_scc1 2f\n\ts_sleep 1\n\ts_sub_u32 %2, %2, 1\n\t"
               "s_cmp_lg_u32 %2, 0\n\ts_cbranch_scc1 1b\n2:"
               : "=&v"(tv), "=&s"(ts), "=&s"(cnt) : "v"((unsigned)(size_t)word), "s"(val) : "memory", "scc");
}
DEV void lds_post(unsigned* word, int lane) {
  asm volatile("" ::: "memory");
  if (lane == 0) __hip_atomic_fetch_add(word, 1u, __ATOMIC_RELAXED, __HIP_MEMORY_SCOPE_WORKGROUP);
  asm volatile("" ::: "memory");
}
DEV void lgkm0() { asm volatile("s_waitcnt lgkmcnt(0)" ::: "memory"); __builtin_amdgcn_sched_barrier(0); }

DEV f32x4 mfma_bf16(u32x4 a, u32x4 b, f32x4 c) {
  return __builtin_amdgcn_mfma_f32_16x16x32_bf16(__builtin_bit_cast(bf16x8, a), __builtin_bit_cast(bf16x8, b), c, 0, 0, 0);
}

constexpr int NB_PF = 8, NB_K = 640, NB_LN = 256, NB_MOD = 120;

struct K1Args {
  const unsigned short *x, *are, *aim, *bre, *bim, *cre, *cim, *logdt, *c, *wmod;
  char* ws;
};

DEV void k1_prefetch() {
  const int tid = threadIdx.x;
  unsigned long long pc;
  int o0, o1;
  asm volatile("s_getpc_b64 %0\n.Lpf_here%=:\n\ts_mov_b32 %1, _Z2k16K1Args - .Lpf_here%=\n\ts_mov_b32 %2, _Z5kend_v - .Lpf_here%="
               : "=s"(pc), "=s"(o0), "=s"(o1));
  const char* c0 = (const char*)(pc + (long long)o0);
  long n = o1 - o0;
  n = n < 0 ? 0 : (n > 98304 ? 98304 : n);
  unsigned v[3];
#pragma unroll
  for (int it = 0; it < 3; ++it) {
    const long o = (long)tid * 128 + it * 32768;
    v[it] = o < n ? *(const __attribute__((address_space(1))) unsigned*)(c0 + o) : 0u;
  }
  asm volatile("" :: "v"(v[0] ^ v[1] ^ v[2]));
}

DEV void k1_mod(const K1Args& p, int b, char* smem) {
  const int tid = threadIdx.x;
  const int rg = b & 7, cgp = b >> 3;
  const int c32 = tid & 31, r8 = tid >> 5;
  float* sil = (float*)smem;
  float* red = (float*)(smem + 512);
  char* stamp_ws = p.ws; (void)stamp_ws;
  STAMP(0, 1);
  u32x4 w[10];
  const float cv = ldbf(p.c + rg * 80 + min(tid, 79));
  asm volatile("" ::: "memory");
#pragma unroll
  for (int it = 0; it < 10; ++it)
    w[it] = *(const u32x4*)(p.wmod + (size_t)(rg * 80 + r8 + 8 * it) * 3840 + 256 * cgp + 8 * c32);
  if (tid < 80) sil[tid] = rbf(cv / (1.f + expf(-cv)));
  if (tid >= 128 && tid < 132 && b < 64) st4(p.ws, WS_CNT + (size_t)(b * 4 + tid - 128) * FL_P, 0u);
  __syncthreads();
  STAMP(0, 2);
  float acc[8];
#pragma unroll
  for (int j = 0; j < 8; ++j) acc[j] = 0.f;
#pragma unroll
  for (int it = 0; it < 10; ++it) {
    float s = sil[r8 + 8 * it];
#pragma unroll
    for (int q = 0; q < 4; ++q) {
      acc[2 * q] += s * bf_lo(w[it][q]);
      acc[2 * q + 1] += s * bf_hi(w[it][q]);
    }
  }
#pragma unroll
  for (int j = 0; j < 8; ++j) acc[j] += __shfl_xor(acc[j], 32, 64);
  if ((tid & 63) < 32) {
    *(f32x4*)(red + (tid >> 6) * 256 + 8 * c32) = (f32x4){acc[0], acc[1], acc[2], acc[3]};
    *(f32x4*)(red + (tid >> 6) * 256 + 8 * c32 + 4) = (f32x4){acc[4], acc[5], acc[6], acc[7]};
  }
  __syncthreads();
  STAMP(0, 3);
  if (tid < 64) {
    const f32x4* r4 = (const f32x4*)red + tid;
    st16(ws_rsrc(p.ws), WS_MOD + (rg * 3840 + 256 * cgp + 4 * tid) * 4,
         __builtin_bit_cast(u32x4, (r4[0] + r4[64]) + (r4[128] + r4[192])));
  }
}
struct ModP { f32x4 a, b; };
DEV ModP mod_ld(rsrc_t rs, int idx) {
  ModP m;
#pragma unroll
  for (int j = 0; j < 4; ++j) {
    m.a[j] = __uint_as_float(__builtin_amdgcn_raw_buffer_load_b32(rs, 4 * idx, (int)WS_MOD + 15360 * j, 0));
    m.b[j] = __uint_as_float(__builtin_amdgcn_raw_buffer_load_b32(rs, 4 * idx, (int)WS_MOD + 15360 * (4 + j), 0));
  }
  return m;
}
DEV float mod_fin(const ModP& m) {
  return rbf(((m.a[0] + m.a[1]) + (m.a[2] + m.a[3])) + ((m.b[0] + m.b[1]) + (m.b[2] + m.b[3])));
}
#define SB() __builtin_amdgcn_sched_barrier(0)

DEV void k1_ln(const K1Args& p, int b, char* smem) {
  constexpr int STR = 704;
  const int tid = threadIdx.x;
  const int tok = tid >> 3, sub = tid & 7;
  const int tile = (b & 7) * 8 + ((b >> 3) & 7), pass = (b >> 6) ^ LNFLIP;
  const int hf = pass & 1;
  const bool norm = pass < 2;
  const int t0 = tile * 32;
  const unsigned short* xr = p.x + (size_t)(t0 + tok) * D_ + 8 * sub;
  u32x4 xv[5], xo[5];
  char* stamp_ws = p.ws; (void)stamp_ws;
  STAMP(0, 1);
#pragma unroll
  for (int m = 0; m < 5; ++m) xv[m] = *(const u32x4*)(xr + 320 * hf + 64 * m);
  if (norm) {
#pragma unroll
    for (int m = 0; m < 5; ++m) xo[m] = *(const u32x4*)(xr + 320 * (hf ^ 1) + 64 * m);
    float s = 0.f, v = 0.f;
    const bf16x2 one2 = __builtin_bit_cast(bf16x2, 0x3f803f80u);
#pragma unroll
    for (int m = 0; m < 10; ++m)
#pragma unroll
      for (int q = 0; q < 4; ++q) {
        const unsigned u = m < 5 ? xv[m][q] : xo[m - 5][q];
        const bf16x2 x2 = __builtin_bit_cast(bf16x2, u);
        s = __builtin_amdgcn_fdot2_f32_bf16(x2, one2, s, false);
        v = __builtin_amdgcn_fdot2_f32_bf16(x2, x2, v, false);
      }
#pragma unroll
    for (int o = 1; o < 8; o <<= 1) { s += __shfl_xor(s, o, 64); v += __shfl_xor(v, o, 64); }
    const float mean = s * (1.f / D_);
    const float var = fmaxf(v * (1.f / D_) - mean * mean, 0.f);
    const float sd = rbf(sqrtf(rbf(rbf(var) + 1e-5f)));
    const float rs = 1.f / sd;
    const float mb = rbf(mean);
#pragma unroll
    for (int m = 0; m < 5; ++m)
#pragma unroll
      for (int q = 0; q < 4; ++q) {
        xv[m][q] = pk_h((bf_lo(xv[m][q]) - mb) * rs, (bf_hi(xv[m][q]) - mb) * rs);
      }
  }
  const int lane = tid & 63, w = tid >> 6;
  const int g = lane >> 4, i = lane & 15;
  const rsrc_t rsrc = ws_rsrc(p.ws);
#pragma unroll
  for (int m = 0; m < 5; ++m) *(u32x4*)(smem + tok * STR + (sub + 8 * m) * 16) = xv[m];
  __syncthreads();
  STAMP(0, 2);
  const size_t dsto = (norm ? WS_XNT : WS_XT) + ((size_t)tile * D_ + 320 * hf) * 64;
#pragma unroll
  for (int it = 0; it < 5; ++it) {
    const int cb = w * 5 + it;
    const char* a = smem + (8 * g + (i >> 2)) * STR + cb * 32 + (i & 3) * 8;
    const u32x2 ra = __builtin_bit_cast(u32x2, __builtin_amdgcn_ds_read_tr16_b64_v4i16((lds_s16x4*)a));
    const u32x2 rb = __builtin_bit_cast(u32x2, __builtin_amdgcn_ds_read_tr16_b64_v4i16((lds_s16x4*)(a + 4 * STR)));
    st16(rsrc, dsto + (size_t)(cb * 16 + i) * 64 + 16 * g, (u32x4){ra[0], ra[1], rb[0], rb[1]});
  }
}

constexpr int RT_STR = 288;
struct RtRaw { u32x4 r[8]; };
template <int NF>
DEV void rt_load(RtRaw& t, const unsigned short* W, int N, int kk, int ct0, int lane) {
#pragma unroll
  for (int j = 0; j < NF; ++j) {
    const int c = lane + 64 * j, row = c / (2 * NF), part = c % (2 * NF);
    t.r[j] = *(const u32x4*)(W + (size_t)(32 * kk + row) * N + 16 * ct0 + 8 * part);
  }
}
template <int NF>
DEV void rt_park(const RtRaw& t, char* scr, int lane) {
#pragma unroll
  for (int j = 0; j < NF; ++j) {
    const int c = lane + 64 * j, row = c / (2 * NF), part = c % (2 * NF);
    *(u32x4*)(scr + row * RT_STR + 16 * part) = t.r[j];
  }
}
DEV u32x4 rt_frag(const char* scr, int j, int lane) {
  const int g = lane >> 4, i = lane & 15;
  const char* p = scr + (8 * g + (i >> 2)) * RT_STR + 32 * j + (i & 3) * 8;
  const u32x2 a = __builtin_bit_cast(u32x2, __builtin_amdgcn_ds_read_tr16_b64_v4i16((lds_s16x4*)p));
  const u32x2 b = __builtin_bit_cast(u32x2, __builtin_amdgcn_ds_read_tr16_b64_v4i16((lds_s16x4*)(p + 4 * RT_STR)));
  return (u32x4){a[0], a[1], b[0], b[1]};
}
struct cf { float re, im; };
DEV cf cmul(cf a, cf b) {
  cf r;
  r.re = a.re * b.re - a.im * b.im;
  r.im = a.re * b.im + a.im * b.re;
  return r;
}
DEV cf cexp_(float a2, float br, float m, float m0) {
  float mag = __builtin_amdgcn_exp2f(a2 * (m - m0));
  float ph = __builtin_amdgcn_fractf(br * m);
  cf r; r.re = mag * __builtin_amdgcn_cosf(ph); r.im = mag * __builtin_amdgcn_sinf(ph);
  return r;
}
DEV f32x4 mfma_f16(u32x4 a, u32x4 b, f32x4 c) {
  return __builtin_amdgcn_mfma_f32_16x16x32_f16(__builtin_bit_cast(h16x8, a), __builtin_bit_cast(h16x8, b), c, 0, 0, 0);
}
DEV void put2(char* p, cf a, cf b) {
  u32x2 o = {pk_h(a.re, a.im), pk_h(b.re, b.im)};
  *(u32x2*)p = o;
}

constexpr int KG_RS = 544;
DEV void k1_kgen(const K1Args& p, int d, char* smem) {
  constexpr int RS = KG_RS;
  const int tid = threadIdx.x;
  const int lane = tid & 63;
  const int w = __builtin_amdgcn_readfirstlane(tid >> 6);
  const int g = lane >> 4, i = lane & 15;
  const size_t po = (size_t)d * NS + 2 * lane;
  const unsigned rar = *(const unsigned*)(p.are + po), rai = *(const unsigned*)(p.aim + po);
  unsigned rbr = 0u, rbi = 0u, rcr = 0u, rci = 0u;
  if (w == 0) {
    rbr = *(const unsigned*)(p.bre + po); rbi = *(const unsigned*)(p.bim + po);
    rcr = *(const unsigned*)(p.cre + po); rci = *(const unsigned*)(p.cim + po);
  }
  const float ldt = ldbf_u(p.logdt + d);
  asm volatile("" ::: "memory");
  SB();
  char* stamp_ws = p.ws; (void)stamp_ws;
  const float dt = rbf(expf(ldt));
  asm volatile("" :: "v"(dt));
  STAMP(0, 1);
  float a2[2], bv[2], m0[2];
  a2[0] = (dt * bf_lo(rar)) * 1.44269504088896341f; a2[1] = (dt * bf_hi(rar)) * 1.44269504088896341f;
  bv[0] = (dt * bf_lo(rai)) * 0.15915494309189534f; bv[1] = (dt * bf_hi(rai)) * 0.15915494309189534f;
  m0[0] = a2[0] > 0.f ? 2048.f : 0.f; m0[1] = a2[1] > 0.f ? 2048.f : 0.f;
  char* my = smem + lane * 8;
  if (w == 0) {
    cf v[2], v8[2], e8[2];
    float bound = 0.f;
#pragma unroll
    for (int q = 0; q < 2; ++q) {
      const float dbr = dt * (q ? bf_hi(rbr) : bf_lo(rbr)), dbi = dt * (q ? bf_hi(rbi) : bf_lo(rbi));
      const float cr = q ? bf_hi(rcr) : bf_lo(rcr), ci = q ? bf_hi(rci) : bf_lo(rci);
      const float gsh = __builtin_amdgcn_exp2f(a2[q] * m0[q]);
      v[q].re = (cr * dbr - ci * dbi) * gsh; v[q].im = -(cr * dbi + ci * dbr) * gsh;
      bound += (fabsf(v[q].re) + fabsf(v[q].im)) * __builtin_amdgcn_exp2f(fmaxf(a2[q], 0.f) * 120.f);
      e8[q] = cexp_(a2[q], -bv[q], 8.f, 0.f);
      v8[q] = cexp_(a2[q], -bv[q], 64.f, 0.f);
    }
#pragma unroll
    for (int o = 1; o < 64; o <<= 1) bound += __shfl_xor(bound, o, 64);
    int ex = (int)((__float_as_uint(bound) >> 23) & 0xff);
    ex = ex < 20 ? 20 : (ex > 240 ? 240 : ex);
    const float sv = __uint_as_float((unsigned)(127 + 13 + 127 - ex) << 23);
    if (lane == 0) st4(p.ws, WS_KSC + 4 * d, (unsigned)(ex - 13) << 23);
#pragma unroll
    for (int q = 0; q < 2; ++q) { v[q].re *= sv; v[q].im *= sv; v8[q] = cmul(v[q], v8[q]); }
#pragma unroll
    for (int j = 0; j < 8; ++j) {
      put2(my + (41 + j) * RS, v[0], v[1]);
      put2(my + (49 + j) * RS, v8[0], v8[1]);
      if (j < 7) {
#pragma unroll
        for (int q = 0; q < 2; ++q) { v[q] = cmul(v[q], e8[q]); v8[q] = cmul(v8[q], e8[q]); }
      }
    }
  } else if (w == 1) {
    cf e1[2], u[2], u2[2];
#pragma unroll
    for (int q = 0; q < 2; ++q) {
      e1[q] = cexp_(a2[q], bv[q], 1.f, 0.f);
      u[q].re = __builtin_amdgcn_exp2f(-a2[q] * m0[q]); u[q].im = 0.f;
      u2[q] = cexp_(a2[q], bv[q], 128.f, m0[q]);
    }
#pragma unroll
    for (int r = 0; r < 8; ++r) {
      put2(my + r * RS, u[0], u[1]);
      put2(my + (8 + r) * RS, u2[0], u2[1]);
      if (r < 7) {
#pragma unroll
        for (int q = 0; q < 2; ++q) { u[q] = cmul(u[q], e1[q]); u2[q] = cmul(u2[q], e1[q]); }
      }
    }
  } else if (w == 2) {
    cf e2[2], u[2], u2[2];
#pragma unroll
    for (int q = 0; q < 2; ++q) {
      e2[q] = cexp_(a2[q], bv[q], 2.f, 0.f);
      u[q] = cexp_(a2[q], bv[q], 256.f, m0[q]);
      u2[q] = cexp_(a2[q], bv[q], 384.f, m0[q]);
    }
#pragma unroll
    for (int r = 0; r < 4; ++r) {
      put2(my + (16 + r) * RS, u[0], u[1]);
      put2(my + (20 + r) * RS, u2[0], u2[1]);
      if (r < 3) {
#pragma unroll
        for (int q = 0; q < 2; ++q) { u[q] = cmul(u[q], e2[q]); u2[q] = cmul(u2[q], e2[q]); }
      }
    }
    cf e4[2] = {cmul(e2[0], e2[0]), cmul(e2[1], e2[1])};
#pragma unroll
    for (int r = 0; r < 2; ++r) {
      cf c0 = cexp_(a2[0], bv[0], 512.f + 128.f * r, m0[0]), c1 = cexp_(a2[1], bv[1], 512.f + 128.f * r, m0[1]);
      put2(my + (24 + 2 * r) * RS, c0, c1);
      put2(my + (25 + 2 * r) * RS, cmul(c0, e4[0]), cmul(c1, e4[1]));
    }
  } else {
    cf e4[2], e128[2], u[2], u2[2];
#pragma unroll
    for (int q = 0; q < 2; ++q) {
      e4[q] = cexp_(a2[q], bv[q], 4.f, 0.f);
      e128[q] = cexp_(a2[q], bv[q], 128.f, 0.f);
      u[q] = cexp_(a2[q], bv[q], 1024.f, m0[q]);
      u2[q] = cexp_(a2[q], bv[q], 1536.f, m0[q]);
    }
#pragma unroll
    for (int r = 2; r < 4; ++r) {
      cf c0 = cexp_(a2[0], bv[0], 512.f + 128.f * r, m0[0]), c1 = cexp_(a2[1], bv[1], 512.f + 128.f * r, m0[1]);
      put2(my + (24 + 2 * r) * RS, c0, c1);
      put2(my + (25 + 2 * r) * RS, cmul(c0, e4[0]), cmul(c1, e4[1]));
    }
#pragma unroll
    for (int r = 0; r < 4; ++r) {
      put2(my + (32 + r) * RS, u[0], u[1]);
      put2(my + (36 + r) * RS, u2[0], u2[1]);
#pragma unroll
      for (int q = 0; q < 2; ++q) { if (r < 3) u[q] = cmul(u[q], e128[q]); u2[q] = cmul(u2[q], e128[q]); }
    }
    put2(my + 40 * RS, u2[0], u2[1]);
  }
  __syncthreads();
  STAMP(0, 2);
  _Float16* kh = (_Float16*)(smem + 57 * RS);
  if (w < 3) {
    const char* ua = smem + (16 * w + i) * RS + g * 16;
    const char* va = smem + (41 + i) * RS + g * 16;
    f32x4 acc = {0.f, 0.f, 0.f, 0.f};
#pragma unroll
    for (int ks = 0; ks < 8; ++ks)
      acc = mfma_f16(*(const u32x4*)(ua + ks * 64), *(const u32x4*)(va + ks * 64), acc);
    int idx0, st;
    if (w == 0) { idx0 = (g < 2 ? 4 * g : 120 + 4 * g) + 8 * i; st = 1; }
    else if (w == 1) { idx0 = g < 2 ? 256 + 64 * g + 4 * i : 384 + 64 * (g - 2) + 2 * i; st = 1; }
    else { idx0 = 512 + 64 * g + i; st = 16; }
#pragma unroll
    for (int rg = 0; rg < 4; ++rg) {
      int idx = idx0 + st * rg;
      if (w == 1 && g >= 2) idx = idx0 + 32 * (rg >> 1) + (rg & 1);
      if (w == 2 && g >= 2) idx = (g == 2 && rg == 0 && i == 0) ? 640 : 644;
      kh[idx] = (_Float16)acc[rg];
    }
  }
  __syncthreads();
  STAMP(0, 3);
  const unsigned short* ku = (const unsigned short*)kh;
  const size_t kout = WS_KREV + (size_t)d * KREV * 2;
  {
    const int l0 = 2040 - 8 * tid;
    u32x4 o;
    if (w < 2) {
      const int q = l0 >> 3;
      const unsigned a = ku[384 + q], b = ku[385 + q];
      __builtin_amdgcn_raw_buffer_store_b32(a | (b << 16), ws_rsrc(p.ws), (int)(kout + 4 * tid), 0, 16);
      return;
    } else if (w == 2) {
      const int q = l0 >> 2;
      const unsigned a = ku[256 + q], b = ku[257 + q], c = ku[258 + q];
      o[0] = c | (c << 16); o[1] = b | (b << 16); o[2] = b | (a << 16); o[3] = a | (a << 16);
    } else {
      unsigned v[8];
#pragma unroll
      for (int e = 0; e < 8; ++e) {
        const int l = l0 + 7 - e;
        const int idx = l < 256 ? l : (int)(pk_bf((float)l, 0.f) & 0xffffu) - 17024;
        v[e] = ku[idx];
      }
#pragma unroll
      for (int q = 0; q < 4; ++q) o[q] = v[2 * q] | (v[2 * q + 1] << 16);
    }
    st16(ws_rsrc(p.ws), kout + 16 * tid - 1536, o);
  }
}

__global__ NOPK __launch_bounds__(256) void k1(K1Args p) {
  __shared__ __attribute__((aligned(16))) char smem[57 * KG_RS + 1312];
  int b = blockIdx.x;
  unsigned long long te_;
  asm volatile("s_memrealtime %0\n\ts_waitcnt lgkmcnt(0)" : "=s"(te_) :: "memory");
  __builtin_amdgcn_sched_barrier(0);
  asm volatile("" :: "s"(p.ws), "s"(p.x), "s"(p.logdt));
  __builtin_amdgcn_sched_barrier(0);
  if (blockIdx.x == PB && threadIdx.x == 0) {
    unsigned long long ta_;
    asm volatile("s_memrealtime %0\n\ts_waitcnt lgkmcnt(0)" : "=s"(ta_) :: "memory");
    __hip_atomic_store((unsigned long long*)(p.ws + (200u << 20)) + 0, te_, __ATOMIC_RELAXED, __HIP_MEMORY_SCOPE_AGENT);
    if (PM == 0) __hip_atomic_store((unsigned long long*)(p.ws + (200u << 20)) + 1, ta_, __ATOMIC_RELAXED, __HIP_MEMORY_SCOPE_AGENT);
  }
  char* stamp_ws = p.ws; (void)stamp_ws;
  if (b < NB_PF) { k1_prefetch(); STAMP(0, 7); return; }
  b -= NB_PF;
  if (b < NB_K) { k1_kgen(p, KMAP ? (b & 7) * 80 + (b >> 3) : b, smem); VM0(); STAMP(0, 7); return; }
  b -= NB_K;
  asm volatile("" :: "s"(p.ws), "s"(p.x));
#define DL_LN 4
#define DL_MOD 4
  if (b < NB_LN) { __builtin_amdgcn_s_sleep(DL_LN); k1_ln(p, b, smem); }
  else { __builtin_amdgcn_s_sleep(DL_MOD); k1_mod(p, b - NB_LN, smem); }
  VM0();
  STAMP(0, 7);
}

constexpr int K2_CS = 4160;
constexpr int K2_KOFF = 4 * K2_CS;
constexpr int K2_CH = K2_KOFF + 4608;
constexpr int CV_RED = 3 * K2_CH;
constexpr int CV_RT2 = 86016;
constexpr int CV_SUM = CV_RT2 + 4 * (32 * RT_STR + 256);
constexpr int CV_XCH = CV_SUM + 2560;
constexpr int CV_FL = CV_RED + 20480;

struct Chan { float sa, ca1, ga, dsk, hs, hinv, gy, gd; };
struct ChanRaw { float m, kinv, dsk; };
DEV ChanRaw chan_ld(const unsigned short* dskip, const char* ws, int d, int lane) {
  ChanRaw c;
  c.kinv = ldu<float>(ws + WS_KSC + 4 * d);
  c.dsk = ldbf_u(dskip + d);
  c.m = ((const float*)(ws + WS_MOD))[3840 * (lane & 7) + D_ * ((lane >> 3) & 3) + d];
  return c;
}
template <int CTRL> DEV float dpp_add(float v) {
  return v + __int_as_float(__builtin_amdgcn_update_dpp(0, __float_as_int(v), CTRL, 0xf, 0xf, true));
}
DEV Chan chan_fin(const ChanRaw& r) {
  Chan c;
  const int t = __float_as_int(rbf(dpp_add<0x141>(dpp_add<0x4e>(dpp_add<0xb1>(r.m)))));
  c.sa = __int_as_float(__builtin_amdgcn_readlane(t, 0));
  c.ca1 = rbf(1.f + __int_as_float(__builtin_amdgcn_readlane(t, 8)));
  c.ga = __int_as_float(__builtin_amdgcn_readlane(t, 16));
  c.dsk = r.dsk;
  const float hb = 26.f * fabsf(c.ca1) + fabsf(c.sa);
  int ex = (int)((__float_as_uint(hb) >> 23) & 0xff);
  ex = ex < 20 ? 20 : (ex > 240 ? 240 : ex);
  c.hs = __uint_as_float((unsigned)(127 + 14 + 127 - ex) << 23);
  c.hinv = __uint_as_float((unsigned)(ex - 14) << 23);
  c.gy = r.kinv * c.hinv * c.ga;
  c.gd = c.hinv * c.dsk * c.ga;
  return c;
}
DEV size_t tl_off(int d, int t) { return (((size_t)(t >> 5) * D_ + d) * 32 + (t & 31)) * 2; }
struct StageRaw { u32x4 xa[2], kv1; u32x2 xb; unsigned kv0; };
DEV StageRaw stage_ld(const char* ws, int d, int th) {
  StageRaw s;
  const unsigned short* krev = (const unsigned short*)(ws + WS_KREV) + (size_t)d * KREV;
  const int T = 1024 * (th >> 6) + 8 * (th & 63);
  s.xb = ldu<u32x2>(ws + WS_XNT + tl_off(d, 1024));
  s.xa[0] = *(const u32x4*)(ws + WS_XNT + tl_off(d, T)); s.xa[1] = *(const u32x4*)(ws + WS_XNT + tl_off(d, T + 512));
  s.kv0 = *(const unsigned*)(krev + 2 * th); s.kv1 = *(const u32x4*)(krev + 256 + 8 * th);
  return s;
}
DEV void stage_wr(const StageRaw& s, const Chan& c, char* smem, int th) {
  constexpr int CS = K2_CS, KOFF = K2_KOFF;
  const h16x2 c2 = __builtin_bit_cast(h16x2, pk_h(c.ca1 * c.hs, c.ca1 * c.hs)), s2 = __builtin_bit_cast(h16x2, pk_h(c.sa * c.hs, c.sa * c.hs));
  auto mod = [&](unsigned u) __attribute__((always_inline)) {
    return __builtin_bit_cast(unsigned, __builtin_elementwise_fma(__builtin_bit_cast(h16x2, u), c2, s2));
  };
  unsigned h2[2][6];
#pragma unroll
  for (int e = 0; e < 2; ++e)
#pragma unroll
    for (int q = 0; q < 4; ++q) h2[e][q] = mod(s.xa[e][q]);
#pragma unroll
  for (int q = 0; q < 2; ++q) {
    h2[0][4 + q] = __builtin_amdgcn_update_dpp(__builtin_amdgcn_readfirstlane(h2[1][q]), h2[0][q], 0x130, 0xf, 0xf, false);
    h2[1][4 + q] = __builtin_amdgcn_update_dpp(mod(s.xb[q]), h2[1][q], 0x130, 0xf, 0xf, false);
  }
  char* dst = smem + (128 * (th >> 6) + (th & 63) + 2) * 16;
  unsigned s1[5];
#pragma unroll
  for (int e = 1; e >= 0; --e) {
    const unsigned* hh = h2[e];
#pragma unroll
    for (int q = 0; q < 5; ++q) s1[q] = __builtin_amdgcn_alignbit(hh[q + 1], hh[q], 16);
    *(u32x4*)(dst + 1024 * e) = (u32x4){hh[0], hh[1], hh[2], hh[3]};
    *(u32x4*)(dst + 1024 * e + CS) = (u32x4){s1[0], s1[1], s1[2], s1[3]};
    *(u32x4*)(dst + 1024 * e + 2 * CS) = (u32x4){hh[1], hh[2], hh[3], hh[4]};
    *(u32x4*)(dst + 1024 * e + 3 * CS) = (u32x4){s1[1], s1[2], s1[3], s1[4]};
  }
  {
    const unsigned a = __builtin_amdgcn_readfirstlane(h2[0][0]), b = __builtin_amdgcn_readfirstlane(s1[0]);
    if (th >= 1 && th < 4)
      *(u32x4*)(smem + th * CS + 16) = (u32x4){0u, 0u, th == 3 ? a << 16 : 0u, th == 1 ? a << 16 : (th == 2 ? a : b)};
  }
  {
    const unsigned aa = __builtin_amdgcn_perm(s.kv0, s.kv0, 0x01000100u), bb = __builtin_amdgcn_perm(s.kv0, s.kv0, 0x03020302u);
    const unsigned ba = __builtin_amdgcn_perm(s.kv0, s.kv0, 0x01000302u);
    *(u32x4*)(smem + KOFF + 16 * th) = (u32x4){bb, (th & 1) ? ba : bb, aa, aa};
  }
  *(u32x4*)(smem + KOFF + 2048 + 16 * th) = s.kv1;
}
DEV void stage_zero(char* smem, int th) {
  if (th < 37) *(u32x4*)(smem + (th < 32 ? K2_KOFF + 4096 + 16 * th : (th < 36 ? (th - 32) * K2_CS : 16))) = (u32x4){0u, 0u, 0u, 0u};
}
DEV f32x4 conv_out(const f32x4& y, const Chan& c, const char* smem, int t) {
  const u32x2 hv = *(const u32x2*)(smem + 2 * (t + 16));
  f32x4 o;
#pragma unroll
  for (int e = 0; e < 2; ++e) {
    const unsigned hu = hv[e];
    const h16x2 h2 = __builtin_bit_cast(h16x2, hu);
    o[2 * e] = fmaf((float)h2[0], c.gd, y[2 * e] * c.gy);
    o[2 * e + 1] = fmaf((float)h2[1], c.gd, y[2 * e + 1] * c.gy);
  }
  return o;
}
DEV unsigned add_bf(unsigned x, float a, float b) { return pk_bf(bf_lo(x) + a, bf_hi(x) + b); }
#define K2_LOAD(buf, cc, u)                                                                                     \
  asm volatile("ds_read_b64 %0, %3 offset:%5\n\tds_read_b64 %1, %3 offset:%6\n\tds_read_b128 %2, %4 offset:%7"   \
               : "=&v"(Al[buf][u]), "=&v"(Ah[buf][u]), "=&v"(Bf[buf][u])                                          \
               : "v"(ap0), "v"(bp0), "i"(64 * (cc) + 512 * (u)), "i"(64 * (cc) + 512 * (u) + 8),                 \
                 "i"(64 * (cc) + 3584 - 512 * (u)))
template <int SET> DEV constexpr int half_tile(int j) {
  return SET == 0 ? (j == 0 ? 7 : (j == 1 ? 6 : 2)) : (j < 3 ? 5 - j : 4 - j);
}
template <int SET>
DEV void conv_half(unsigned ap0, unsigned bp0, char* redw) {
  constexpr int NU = SET == 0 ? 8 : 6, NT = SET == 0 ? 3 : 5;
  u32x2 Al[2][NU], Ah[2][NU];
  u32x4 Bf[2][NU];
#pragma unroll
  for (int u = 0; u < NU; ++u) K2_LOAD(0, 0, u);
  f32x4 acc[NT];
#pragma unroll
  for (int j = 0; j < NT; ++j) acc[j] = (f32x4){0.f, 0.f, 0.f, 0.f};
#pragma unroll
  for (int cc = 0; cc < 2; ++cc) {
    lgkm0();
    if (cc == 0) {
#pragma unroll
      for (int u = 0; u < NU; ++u) K2_LOAD(1, 1, u);
    }
#pragma unroll
    for (int sp = 0; sp < NU; ++sp)
#pragma unroll
      for (int j = 0; j < NT; ++j) {
        const int q = half_tile<SET>(j);
        if (sp <= q) {
          const u32x4 A = {Al[cc][sp][0], Al[cc][sp][1], Ah[cc][sp][0], Ah[cc][sp][1]};
          acc[j] = mfma_f16(A, Bf[cc][q - sp], acc[j]);
        }
      }
    SB();
  }
#pragma unroll
  for (int j = 0; j < NT; ++j) *(f32x4*)(redw + j * 4096) = acc[j];
}

template <int NF>
DEV void w1_fin(const RtRaw& t, const ModP& mp, char* ws, int kk, int ct0, int lane, char* scr, float* sums) {
  const int g = lane >> 4, i = lane & 15;
  float* tb = (float*)(scr + 32 * RT_STR);
  const float mv = mod_fin(mp);
  tb[lane] = lane < 32 ? rbf(1.f + mv) : mv;
  rt_park<NF>(t, scr, lane);
  lgkm0();
  const f32x4 ca = *(const f32x4*)(tb + 8 * g), cb = *(const f32x4*)(tb + 8 * g + 4);
  const f32x4 sa = *(const f32x4*)(tb + 32 + 8 * g), sb = *(const f32x4*)(tb + 36 + 8 * g);
  const u32x4 sv = {pk_bf(sa[0], sa[1]), pk_bf(sa[2], sa[3]), pk_bf(sb[0], sb[1]), pk_bf(sb[2], sb[3])};
  const u32x4 ones = {0x3f803f80u, 0x3f803f80u, 0x3f803f80u, 0x3f803f80u};
  const f32x4 z = {0.f, 0.f, 0.f, 0.f};
  const rsrc_t rs = ws_rsrc(ws);
#pragma unroll
  for (int j = 0; j < NF; ++j) {
    const u32x4 f = rt_frag(scr, j, lane);
    const u32x4 sc = {pk_bf(bf_lo(f[0]) * ca[0], bf_hi(f[0]) * ca[1]), pk_bf(bf_lo(f[1]) * ca[2], bf_hi(f[1]) * ca[3]),
                      pk_bf(bf_lo(f[2]) * cb[0], bf_hi(f[2]) * cb[1]), pk_bf(bf_lo(f[3]) * cb[2], bf_hi(f[3]) * cb[3])};
    st16(rs, WS_W1F + ((size_t)(kk * W1KS + (ct0 + j) * W1TS) * 64 + lane) * 16, sc);
    const f32x4 cs = mfma_bf16(sc, ones, z), b1 = mfma_bf16(f, sv, z);
    if (i < 2) *(f32x4*)(sums + i * (16 * NF) + 16 * j + 4 * g) = i ? b1 : cs;
  }
}

DEV void conv_phase(const unsigned short* __restrict__ dskip, char* __restrict__ ws,
                    const unsigned short* __restrict__ w1, const unsigned short* __restrict__ w2, char* smem_all) {
  constexpr int CS = K2_CS, KOFF = K2_KOFF;
  const int tid = threadIdx.x, lane = tid & 63;
  const int w = __builtin_amdgcn_readfirstlane(tid >> 6);
  const int bx = blockIdx.x;
  const int pj = (bx & 7) * 16 + (bx >> 4), par = (bx >> 3) & 1;
  const int r = lane & 15, g = lane >> 4, th = tid & 127;
  const int s4 = (r + 1) & 3;
  const rsrc_t rs = ws_rsrc(ws);
  unsigned* fl = (unsigned*)(smem_all + CV_FL);
  char* stamp_ws = ws; (void)stamp_ws;
  STAMP(1, 0);
  asm volatile("" :: "s"(w1), "s"(w2));
  if (tid < 12) fl[tid] = 0u;
  if (w < 4) {
    const int d = 5 * pj + 2 * par + (w >> 1), hw = w & 1;
    char* smem = smem_all + (w >> 1) * K2_CH;
    const ChanRaw cr = chan_ld(dskip, ws, d, lane);
    SB();
    const StageRaw sr = stage_ld(ws, d, th);
    u32x4 xr4[2];
    u32x2 xres[4];
    auto ld_res = [&]() __attribute__((always_inline)) {
#pragma unroll
      for (int qp = 0; qp < 2; ++qp)
        xr4[qp] = *(const u32x4*)(ws + WS_XT + tl_off(d, 256 * (4 * hw + 2 * qp + (g & 1)) + 16 * r + 8 * (g >> 1)));
    };
    ld_res();
    stage_zero(smem, th);
    SB();
    asm volatile("s_waitcnt lgkmcnt(0)\n\ts_barrier" ::: "memory");
    SB();
    const Chan c = chan_fin(cr);
    STAMP(1, 1);
    stage_wr(sr, c, smem, th);
    lds_post(fl + 6 + (w >> 1), lane);
    lds_wait(fl + 6 + (w >> 1), 2u);
    STAMP(1, 2);
    f32x4 acc[8], keep[4];
#pragma unroll
    for (int q = 0; q < 8; ++q) acc[q] = (f32x4){0.f, 0.f, 0.f, 0.f};
    {
      const unsigned ap0 = (unsigned)(size_t)(smem + s4 * CS + 2 * (128 * hw + 8 * g + (r + 1) - s4));
      const unsigned bp0 = (unsigned)(size_t)(smem + KOFF + 2 * (2032 + 128 * hw - 16 * r + 8 * g) - 3584);
      u32x2 Al[2][8], Ah[2][8];
      u32x4 Bf[2][8];
#pragma unroll
      for (int u = 0; u < 8; ++u) K2_LOAD(0, 0, u);
      auto tile = [&](int cc, int q) __attribute__((always_inline)) {
#pragma unroll
        for (int sp = 0; sp <= q; ++sp) {
          const u32x4 A = {Al[cc & 1][sp][0], Al[cc & 1][sp][1], Ah[cc & 1][sp][0], Ah[cc & 1][sp][1]};
          acc[q] = mfma_f16(A, Bf[cc & 1][q - sp], acc[q]);
        }
        SB();
      };
#pragma unroll
      for (int cc = 0; cc < 3; ++cc) {
        lgkm0();
#pragma unroll
        for (int q = 0; q < 8; ++q) {
          K2_LOAD((cc + 1) & 1, cc + 1, q);
          tile(cc, q);
        }
      }
#pragma unroll
      for (int qp = 0; qp < 2; ++qp) {
        const auto s0 = __builtin_amdgcn_permlane16_swap(xr4[qp][0], xr4[qp][2], false, false);
        const auto s1 = __builtin_amdgcn_permlane16_swap(xr4[qp][1], xr4[qp][3], false, false);
        xres[2 * qp] = (u32x2){s0[0], s1[0]}; xres[2 * qp + 1] = (u32x2){s0[1], s1[1]};
      }
      lgkm0();
      f32x4* xw = (f32x4*)(smem_all + CV_XCH + w * 4096) + lane;
      auto last = [&](auto hw_tag) __attribute__((always_inline)) {
        constexpr int S = decltype(hw_tag)::value ? 0 : 4, K = 4 - S;
#pragma unroll
        for (int q = 0; q < 4; ++q) {
          tile(3, S + q);
          if (q) { xw[64 * (q - 1)] = acc[S + q - 1]; SB(); }
        }
        tile(3, K + 3);
        xw[64 * 3] = acc[S + 3];
        lds_post(fl + w, lane);
        SB();
#pragma unroll
        for (int q = 0; q < 3; ++q) tile(3, K + q);
#pragma unroll
        for (int q = 0; q < 4; ++q) keep[q] = acc[K + q];
      };
      if (hw) last(std::true_type{}); else last(std::false_type{});
    }
    STAMP(1, 3);
    lds_wait(fl + (w ^ 1), 1u);
    STAMP(1, 4);
    const f32x4* xrd = (const f32x4*)(smem_all + CV_XCH + (w ^ 1) * 4096) + lane;
#pragma unroll
    for (int qp = 0; qp < 2; ++qp) {
      u32x2 o[2];
#pragma unroll
      for (int h = 0; h < 2; ++h) {
        const int q = 2 * qp + h;
        const f32x4 v = conv_out(keep[q] + xrd[64 * q], c, smem, 256 * (4 * hw + q) + 16 * r + 4 * g);
        o[h] = (u32x2){add_bf(xres[q][0], v[0], v[1]), add_bf(xres[q][1], v[2], v[3])};
      }
      const auto s0 = __builtin_amdgcn_permlane16_swap(o[0][0], o[1][0], false, false);
      const auto s1 = __builtin_amdgcn_permlane16_swap(o[0][1], o[1][1], false, false);
      const int t = 256 * (4 * hw + 2 * qp + (g & 1)) + 16 * r + 8 * (g >> 1);
      st16(rs, WS_X1T + (((size_t)(t >> 6) * D_ + d) * 64 + (t & 63)) * 2, (u32x4){s0[0], s1[0], s0[1], s1[1]});
    }
  } else {
#define PRIO47 2
    __builtin_amdgcn_s_setprio(PRIO47);
    const int d = 5 * pj + 4, wq = w - 4;
    char* smem = smem_all + 2 * K2_CH;
    StageRaw sr;
    const ChanRaw cr = chan_ld(dskip, ws, d, lane);
    asm volatile("" ::: "memory");
    if (wq < 2) sr = stage_ld(ws, d, th);
    asm volatile("" ::: "memory");
    const int tl0 = par ? (wq < 3 ? 5 - wq : 1) : (wq == 0 ? 7 : (wq == 1 ? 6 : 2)), tl1 = 0;
    const unsigned ap0 = (unsigned)(size_t)(smem + s4 * CS + 2 * (64 * wq + 8 * g + (r + 1) - s4));
    const unsigned bp0 = (unsigned)(size_t)(smem + KOFF + 2 * (2032 + 64 * wq - 16 * r + 8 * g) - 3584);
    char* redw = smem_all + CV_RED + wq * 1024 + lane * 16;
    u32x2 xres[2];
    xres[0] = *(const u32x2*)(ws + WS_XT + tl_off(d, 256 * tl0 + 16 * r + 4 * g));
    xres[1] = *(const u32x2*)(ws + WS_XT + tl_off(d, 256 * tl1 + 16 * r + 4 * g));
    if (wq == 0) stage_zero(smem, th);
    SB();
    __builtin_amdgcn_s_barrier();
    SB();
    const Chan c = chan_fin(cr);
    if (wq < 2) {
      stage_wr(sr, c, smem, th);
      lds_post(fl + 8, lane);
    }
#define NF1_ 4
#define NF2_ 5
    constexpr int NF1 = NF1_, NC1 = 108 / NF1, NB1 = 5 * NC1, NF2 = NF2_, NC2 = 40 / NF2, NJ2 = 54 * NC2;
    const bool isw1 = bx < NB1;
    const int j2 = 4 * (bx - NB1) + wq;
    const bool isw2 = !isw1 && j2 < NJ2, isgb = j2 >= NJ2 && j2 < NJ2 + 10;
    const int jk = isw1 ? 4 * (bx / NC1) + wq : j2 / NC2;
    const int jc = isw1 ? NF1 * (bx % NC1) : NF2 * (j2 % NC2);
    RtRaw rt;
    const int gbi = 64 * (j2 - NJ2) + lane;
    ModP mp;
    SB();
#define RT_MODE 2
#define RT67 24
    auto rtl = [&]() __attribute__((always_inline)) {
      if (isw1) rt_load<NF1>(rt, w1, FF, jk, jc, lane);
      else if (isw2) rt_load<NF2>(rt, w2, D_, jk, jc, lane);
      SB();
    };
    if (wq >= 2 && RT67) __builtin_amdgcn_s_sleep(RT67);
    rtl();
    lds_wait(fl + 8, 2u);
    STAMP(1, 2);
    if (isw1 || isgb) mp = mod_ld(rs, isw1 ? (lane < 32 ? 4 * D_ : 3 * D_ - 32) + 32 * jk + lane : 5 * D_ + gbi);
    SB();
    if (par) conv_half<1>(ap0, bp0, redw); else conv_half<0>(ap0, bp0, redw);
    lds_post(fl + 4, lane);
    STAMP(1, 3);
    STAMP(1, 4);
    lds_wait(fl + 4, 4u);
    STAMP(1, 5);
    const char* rd = smem_all + CV_RED + lane * 16;
#pragma unroll
    for (int u = 0; u < 2; ++u) {
      const bool on = u == 0 ? (par || wq < 3) : (par && wq == 0);
      if (on) {
        const int sl = u == 0 ? wq : 4, tl = u == 0 ? tl0 : tl1;
        const f32x4 y = (*(const f32x4*)(rd + sl * 4096) + *(const f32x4*)(rd + sl * 4096 + 1024))
                      + (*(const f32x4*)(rd + sl * 4096 + 2048) + *(const f32x4*)(rd + sl * 4096 + 3072));
        const int t = 256 * tl + 16 * r + 4 * g;
        const f32x4 v = conv_out(y, c, smem, t);
        st8(rs, WS_X1T + (((size_t)(t >> 6) * D_ + d) * 64 + (t & 63)) * 2, (u32x2){add_bf(xres[u][0], v[0], v[1]), add_bf(xres[u][1], v[2], v[3])});
      }
    }
    SB();
    STAMP(1, 6);
    char* scr = smem_all + CV_RT2 + wq * (32 * RT_STR + 256);
    if (isw1) {
      float* sums = (float*)(smem_all + CV_SUM);
      w1_fin<NF1>(rt, mp, ws, jk, jc, lane, scr, sums + 32 * NF1 * wq);
      asm volatile("" ::: "memory");
      unsigned old = 0u;
      if (lane == 0) old = __hip_atomic_fetch_add(fl + 5, 1u, __ATOMIC_RELAXED, __HIP_MEMORY_SCOPE_WORKGROUP);
      asm volatile("" ::: "memory");
      if (__builtin_amdgcn_readfirstlane(old) == 3u && lane < 8 * NF1) {
        const f32x4* sp = (const f32x4*)sums + lane;
        const f32x4 v = (sp[0] + sp[8 * NF1]) + (sp[16 * NF1] + sp[24 * NF1]);
        st16(rs, WS_CSP + ((size_t)(2 * (bx / NC1) + lane / (4 * NF1)) * FF + 16 * jc + 4 * (lane % (4 * NF1))) * 4, __builtin_bit_cast(u32x4, v));
      }
    } else if (isw2) {
      rt_park<NF2>(rt, scr, lane);
      lgkm0();
#pragma unroll
      for (int j = 0; j < NF2; ++j) st16(rs, WS_W2F + ((size_t)((jc + j) * 54 + jk) * 64 + lane) * 16, rt_frag(scr, j, lane));
    } else if (isgb) {
      st4(ws, WS_GB + 4 * gbi, __float_as_uint(mod_fin(mp)));
    }
  }
}

__global__ NOPK __launch_bounds__(512, 2) void k2(const unsigned short* __restrict__ dskip, char* __restrict__ ws,
                                                  const unsigned short* __restrict__ w1, const unsigned short* __restrict__ w2) {
  __shared__ __attribute__((aligned(16))) char smem[CV_XCH + 16384];
  conv_phase(dskip, ws, w1, w2, smem);
  char* stamp_ws = ws; (void)stamp_ws;
  VM0();
  STAMP(1, 7);
}

DEV u32x4 ld16_sc1(rsrc_t r, int voff, int soff) { return __builtin_amdgcn_raw_buffer_load_b128(r, voff, soff, 16); }
DEV u32x4 ld16(rsrc_t r, int voff, int soff) { return __builtin_amdgcn_raw_buffer_load_b128(r, voff, soff, 0); }
DEV u32x4 tr_frag(const char* p) {
  const u32x2 a = __builtin_bit_cast(u32x2, __builtin_amdgcn_ds_read_tr16_b64_v4i16((lds_s16x4*)p));
  const u32x2 b = __builtin_bit_cast(u32x2, __builtin_amdgcn_ds_read_tr16_b64_v4i16((lds_s16x4*)(p + 256)));
  return (u32x4){a[0], a[1], b[0], b[1]};
}

constexpr int KF_RS = 84;
constexpr int KF_RB = 8 * 16 * KF_RS * 4;
constexpr int KF_BIG = 2 * KF_RB;
constexpr int NW2L = 8, NW2R = 27;
constexpr int KF_TAB = KF_BIG;
constexpr int KF_PS = KF_TAB + 320;
constexpr int KF_XR = KF_PS + 512;
constexpr int KF_CNT = KF_XR + 64 * 176;
constexpr int KF_LDS = KF_CNT + 16;
#define X_FIRST 5
#define X_STEP 4
#define W1_EARLY 3
#define W1_MID 7
#define W1_PRE 10
#define W2_START 6
#define W2_RATE 2
#define W2_INL 28
#define CS_STEP 14
#define SIG_SPLIT 4
#define H1_PRE 7
#define H1_BATCH 99
#define LOCK_MASK 0x55554u

DEV void kf_body(char* __restrict__ ws, unsigned short* __restrict__ out, char* smem) {
  char* xs = smem;
  float* tab = (float*)(smem + KF_TAB);
  float* ps = (float*)(smem + KF_PS);
  char* xr = smem + KF_XR;
  unsigned* lcnt = (unsigned*)(smem + KF_CNT);
  float* cst = (float*)(smem + 81920);
  const int tid = threadIdx.x, lane = tid & 63;
  const int w = __builtin_amdgcn_readfirstlane(tid >> 6);
  const int bx = blockIdx.x;
  const int ng = (bx & 1) * 4 + ((bx >> 3) & 3), mt = ((bx & 7) >> 1) * 8 + (bx >> 5);
  const int g = lane >> 4, i = lane & 15;
  const rsrc_t rs_ = ws_rsrc(ws);

  char* stamp_ws = ws; (void)stamp_ws;
  STAMP(2, 0);
  unsigned* flags = (unsigned*)(ws + WS_CNT) + mt * (2 * FL_P);
  const int ntl = ng < 6 ? 14 : 12;
  const int ctb = ng < 6 ? 14 * ng : 84 + 12 * (ng - 6);
  const bool two = w < ntl - 8;
  const bool full = w < 6;
  const int ks0 = w < 6 ? 7 * w : 42 + 6 * (w - 6);
  const int ct0 = ctb + (two ? 2 * w : (ntl - 8) + w);
  const int lo16 = lane * 16;
  const int w1s = (int)WS_W1F + ct0 * W1TS * 1024;
  const int w2s = (int)WS_W2F + (ng * 5 * 54 + ks0) * 1024;

  u32x4 ring[32], wl[NW2L];
  const char* bb0 = xs + (8 * g + (i >> 2)) * 64 + ((g & 1) << 5) + (i & 3) * 8;
  const char* bb1 = xs + (8 * g + (i >> 2)) * 64 + (((g & 1) ^ 1) << 5) + (i & 3) * 8;
  auto w2_off = [&](int e) __attribute__((always_inline)) {
    const int s = e < 27 ? e / 4 : (e < 34 ? e - 27 : 6), c = e < 27 ? e % 4 : (e < 34 ? 4 : 3);
    const int sn = (s == 6 && !full) ? 5 : s;
    return w2s + (c * 54 + sn) * 1024;
  };

  auto gemm1 = [&](auto two_tag) __attribute__((always_inline)) {
    constexpr bool TWO = decltype(two_tag)::value;
    if (TWO) asm volatile("; two tiles" ::: "memory"); else asm volatile("; one tile" ::: "memory");
    u32x4 t[10];
    auto st_x = [&](int i0, int i1) __attribute__((always_inline)) {
#pragma unroll
      for (int it = i0; it < i1; ++it) {
        const int c = tid + 512 * it;
        const int row = c >> 3;
        *(u32x4*)(xs + ((c >> 2) & 1) * 40960 + row * 64 + (((c & 3) ^ ((row >> 2) & 2)) << 4)) = t[it];
      }
    };
#pragma unroll
    for (int it = 0; it < X_FIRST; ++it) t[it] = ld16(rs_, tid * 16, (int)WS_X1T + mt * 81920 + 8192 * it);
#pragma unroll
    for (int k = 0; k < W1_EARLY; ++k) {
      ring[2 * k] = ld16(rs_, lo16, w1s + k * W1KS * 1024);
      if (TWO) ring[2 * k + 1] = ld16(rs_, lo16, w1s + (k * W1KS + W1TS) * 1024);
    }
    SB();
    __builtin_amdgcn_s_barrier();
    SB();
#pragma unroll
    for (int k = W1_EARLY; k < W1_MID; ++k) {
      ring[2 * k] = ld16(rs_, lo16, w1s + k * W1KS * 1024);
      if (TWO) ring[2 * k + 1] = ld16(rs_, lo16, w1s + (k * W1KS + W1TS) * 1024);
    }
    SB();
    st_x(0, X_FIRST);
    SB();
    if (tid == 0) *lcnt = 0u;
    float gbp;
    if (!TWO) gbp = ((const float*)(ws + WS_GB))[80 * ng + min((w & 1) * 64 + lane, 79)];
    SB();
    __syncthreads();
    STAMP(2, 1);
    auto resid = [&]() __attribute__((always_inline)) {
      const int kb = (80 * ng) >> 5, tq = w & 3;
#pragma unroll
      for (int u = 0; u < 2; ++u) {
        const int kk = min(kb + 2 * (w >> 2) + u, 19);
        const u32x4 f = tr_frag(((tq & 1) ? bb1 : bb0) + (tq >> 1) * 40960 + kk * 2048);
        const unsigned dl = (unsigned)(32 * kk + 8 * g - 80 * ng);
        if (dl < 80u) *(u32x4*)(xr + (tq * 16 + i) * 176 + dl * 2) = f;
      }
    };
    const int hs = TWO ? 0 : (w & 1);
    const char* ba[4] = {bb0 + hs * 40960, bb1 + hs * 40960, bb0 + (hs ^ 1) * 40960, bb1 + (hs ^ 1) * 40960};
    const u32x4 ones = {0x3f803f80u, 0x3f803f80u, 0x3f803f80u, 0x3f803f80u};
    f32x4 acc[2][4];
#pragma unroll
    for (int c = 0; c < 2; ++c)
#pragma unroll
      for (int q = 0; q < 4; ++q) acc[c][q] = (f32x4){0.f, 0.f, 0.f, 0.f};
    u32x4 csp[5];
    const int cso = ((w & 1) * FF + 16 * ctb + 4 * min(lane, 4 * ntl - 1)) * 4;
    u32x4 bn[2][4];
#pragma unroll
    for (int q = 0; q < 4; ++q) bn[0][q] = tr_frag(ba[q]);
#pragma unroll
    for (int q = 0; q < 4; ++q) bn[1][q] = tr_frag(ba[q] + 2048);
#pragma unroll
    for (int kk = 0; kk < 20; ++kk) {
      if (kk == X_STEP) { st_x(X_FIRST, 10); SB(); }
      if (kk == 2 * X_FIRST - 2) { SB(); bar_lds(); SB(); }
      else if ((LOCK_MASK >> kk) & 1) { SB(); __builtin_amdgcn_s_barrier(); SB(); }
      if (kk == 2 * X_FIRST) { resid(); SB(); }
      auto gload = [&](int u) __attribute__((always_inline)) {
        {
          constexpr int CATCH = W1_PRE - W1_MID;
          const int k0 = kk == 0 ? W1_MID : (kk <= CATCH ? W1_MID + 2 * kk - 1 : kk + W1_PRE);
          const int nk = (kk >= 1 && kk <= CATCH) ? 2 : 1;
          const int kn = k0 + (nk == 2 ? (u >> 1) : 0);
          const bool on = nk == 2 || u >= 2;
          if (on && kn < 20) {
            if ((u & 1) == 0) ring[(2 * kn) % 32] = ld16(rs_, lo16, w1s + kn * W1KS * 1024);
            else if (TWO) ring[(2 * kn + 1) % 32] = ld16(rs_, lo16, w1s + (kn * W1KS + W1TS) * 1024);
          }
          if (kk == 0 && u < 2) {
#pragma unroll
            for (int it = X_FIRST + u * 3; it < (u ? 10 : (X_FIRST + 3 < 10 ? X_FIRST + 3 : 10)); ++it)
              t[it] = ld16(rs_, tid * 16, (int)WS_X1T + mt * 81920 + 8192 * it);
          }
        }
        if (kk >= W2_START && u < W2_RATE) {
          const int n = W2_RATE * (kk - W2_START) + u;
          if (n < NW2L) wl[n] = ld16(rs_, lo16, w2_off(NW2R + n));
          else if (n < W2_INL) ring[8 + n - NW2L] = ld16(rs_, lo16, w2_off(n - NW2L));
        }
      };
      const u32x4 a0 = ring[(2 * kk) % 32], a1 = ring[(2 * kk + 1) % 32];
#pragma unroll
      for (int q = 0; q < 4; ++q) {
        const u32x4 bq = bn[kk & 1][q];
        acc[0][q] = mfma_bf16(a0, bq, acc[0][q]);
        if (TWO) acc[1][q] = mfma_bf16(a1, bq, acc[1][q]);
        else if (q < 2) {
          acc[1][2 * q] = mfma_bf16(ones, bq, acc[1][2 * q]);
          acc[1][2 * q + 1] = mfma_bf16(bq, bq, acc[1][2 * q + 1]);
        }
        SB();
        if (kk < 18) bn[kk & 1][q] = tr_frag(ba[q] + (kk + 2) * 2048);
        gload(q);
        SB();
      }
      if (!TWO) {
        if (kk < 5) csp[kk] = ld16(rs_, cso, (int)WS_CSP + kk * 2 * FF * 4);
        if (kk == CS_STEP) {
          f32x4 t = __builtin_bit_cast(f32x4, csp[0]);
#pragma unroll
          for (int u = 1; u < 5; ++u) t += __builtin_bit_cast(f32x4, csp[u]);
          if (w >= 6 && lane < 56) *(f32x4*)(cst + (w & 1) * 224 + 4 * lane) = t;
        }
      }
      if (!TWO && kk == 12) { if ((w & 1) * 64 + lane < 80) tab[(w & 1) * 64 + lane] = gbp; }
      SB();
    }
    STAMP(2, 2);
    if (!TWO) {
#pragma unroll
      for (int h = 0; h < 2; ++h) {
        const f32x4 aq = acc[1][2 * h + 1];
        const float dq = (i & 2) ? ((i & 1) ? aq[3] : aq[2]) : ((i & 1) ? aq[1] : aq[0]);
        const int tk = (2 * hs + h) * 16 + i;
        if (g == (i >> 2)) {
          const float mean = acc[1][2 * h][0] * (1.f / D_);
          const float var = fmaxf(dq * (1.f / D_) - mean * mean, 0.f);
          const float sd = rbf(sqrtf(rbf(rbf(var) + 1e-5f)));
          const float rs = 1.f / sd;
          ps[tk] = rs;
          ps[64 + tk] = -rbf(mean) * rs;
        }
      }
    }
    __syncthreads();
    f32x4 csv[2], b1v[2];
#pragma unroll
    for (int c = 0; c < (TWO ? 2 : 1); ++c) {
      csv[c] = *(const f32x4*)(cst + 16 * (ct0 - ctb + c) + 4 * g);
      b1v[c] = *(const f32x4*)(cst + 224 + 16 * (ct0 - ctb + c) + 4 * g);
    }
#pragma unroll
    for (int qp = 0; qp < 2; ++qp) {
      u32x2 o[2][2];
#pragma unroll
      for (int h = 0; h < 2; ++h) {
        const int q = 2 * qp + h, tq = q ^ (2 * hs);
        const float rs = ps[tq * 16 + i], nm = ps[64 + tq * 16 + i];
#pragma unroll
        for (int c = 0; c < (TWO ? 2 : 1); ++c) {
          float v[4];
#pragma unroll
          for (int r = 0; r < 4; ++r) v[r] = fmaxf(fmaf(acc[c][q][r], rs, fmaf(nm, csv[c][r], b1v[c][r])), 0.f);
          o[c][h] = (u32x2){pk_bf(v[0], v[1]), pk_bf(v[2], v[3])};
        }
      }
      const int tl = ((2 * qp) ^ (2 * hs)) + (g & 1);
#pragma unroll
      for (int c = 0; c < (TWO ? 2 : 1); ++c) {
        const int ct = ct0 + c;
        const auto s0 = __builtin_amdgcn_permlane16_swap(o[c][0][0], o[c][1][0], false, false);
        const auto s1 = __builtin_amdgcn_permlane16_swap(o[c][0][1], o[c][1][1], false, false);
        const size_t off = (((size_t)(4 * mt + tl) * 54 + (ct >> 1)) * 64 + i + 16 * (2 * (ct & 1) + (g >> 1))) * 16;
        st16(rs_, WS_H1F + off, (u32x4){s0[0], s1[0], s0[1], s1[1]});
      }
    }
    SB();
    __builtin_amdgcn_s_barrier();
    SB();
    constexpr int E0 = W2_INL - NW2L;
    auto signal = [&]() __attribute__((always_inline)) {
      if (lane == 0) {
        if (__hip_atomic_fetch_add(lcnt, 1u, __ATOMIC_RELAXED, __HIP_MEMORY_SCOPE_WORKGROUP) == 7u)
          __hip_atomic_store(flags + (FL_P / 4) * ng, 1u, __ATOMIC_RELAXED, __HIP_MEMORY_SCOPE_AGENT);
      }
    };
    if (w >= SIG_SPLIT) { asm volatile("s_waitcnt vmcnt(0)" ::: "memory"); signal(); }
    SB();
#pragma unroll
    for (int e = E0; e < NW2R; ++e) ring[(8 + e) % 32] = ld16(rs_, lo16, w2_off(e));
    SB();
    if (w < SIG_SPLIT) { asm volatile("s_waitcnt vmcnt(%0)" :: "n"(NW2R - E0) : "memory"); signal(); }
    SB();
  };
  if (two) gemm1(std::true_type{});
  else gemm1(std::false_type{});
  STAMP(2, 3);
#pragma nounroll
  for (unsigned spin = 0; spin < (1u << 20); ++spin) {
    if (__hip_atomic_load(flags + (FL_P / 4) * w, __ATOMIC_RELAXED, __HIP_MEMORY_SCOPE_AGENT) != 0u) break;
#define POLL_SL 1
    if (POLL_SL) __builtin_amdgcn_s_sleep(POLL_SL);
  }
  asm volatile("" ::: "memory");
  SB();

  STAMP(2, 4);
  const int h1s = (int)WS_H1F + (mt * 4 * 54 + ks0) * 1024;
  u32x4 hb[28];
  auto load_h1 = [&](int n) __attribute__((always_inline)) {
    const int q = n / 7, s = n % 7;
    const int sn = (s == 6 && !full) ? 5 : s;
    hb[n] = ld16_sc1(rs_, lo16, h1s + (q * 54 + sn) * 1024);
  };
#pragma unroll
  for (int n = 0; n < H1_PRE; ++n) {
    if (n == H1_BATCH) { SB(); __builtin_amdgcn_s_barrier(); SB(); }
    load_h1(n);
  }
  SB();
  float* red = (float*)smem;
  float* rb_ = red + (w * 16 + i) * KF_RS + 4 * g;
  const int re = w * 40 + min(lane, 39);
  const int rtk = re / 20, rq4 = re % 20;
  const float* rr_ = red + rtk * KF_RS + 4 * rq4;
  f32x4 ac2[2][5];
  f32x4 rv[8], rsum;
  u32x2 xv2;
  const f32x4 gb = *(const f32x4*)(tab + 4 * rq4);
  auto red_wr = [&](int q, int c) __attribute__((always_inline)) {
    *(f32x4*)(rb_ + (q & 1) * (KF_RB / 4) + 16 * c) = ac2[q & 1][c];
  };
  auto red_rd = [&](int q, int ww) __attribute__((always_inline)) {
    rv[ww] = *(const f32x4*)(rr_ + (q & 1) * (KF_RB / 4) + ww * 16 * KF_RS);
  };
  auto red_x = [&](int q) __attribute__((always_inline)) { xv2 = *(const u32x2*)(xr + (16 * q + rtk) * 176 + rq4 * 8); };
  auto red_out = [&](int q) __attribute__((always_inline)) {
    const f32x4 s = rsum + (rv[6] + rv[7]);
    u32x2 o;
#pragma unroll
    for (int h = 0; h < 2; ++h) {
      o[h] = pk_bf(fmaf(s[2 * h], gb[2 * h], bf_lo(xv2[h])), fmaf(s[2 * h + 1], gb[2 * h + 1], bf_hi(xv2[h])));
    }
    *(u32x2*)(out + (size_t)(mt * 64 + 16 * q + rtk) * D_ + ng * 80 + 4 * rq4) = o;
  };
#pragma unroll
  for (int q = 0; q < 3; ++q) {
#pragma unroll
    for (int c = 0; c < 5; ++c) ac2[q & 1][c] = (f32x4){0.f, 0.f, 0.f, 0.f};
#pragma unroll
    for (int s = 0; s < 7; ++s) {
      auto mm = [&](int c) __attribute__((always_inline)) {
        const u32x4 a = c == 4 ? wl[s] : ((s == 6 && c == 3) ? wl[7] : ring[(8 + 4 * s + c) % 32]);
        ac2[q & 1][c] = mfma_bf16(a, hb[7 * q + s], ac2[q & 1][c]);
      };
      if (s == 5 && q >= 2) {
        red_wr(q - 1, 4);
        SB();
        red_out(q - 2);
#pragma unroll
        for (int c = 0; c < 5; ++c) mm(c);
#pragma unroll
        for (int c = 0; c < 5; ++c) {
          __builtin_amdgcn_sched_group_barrier(0x008, 1, 0);
          __builtin_amdgcn_sched_group_barrier(0x002, 5, 0);
        }
        SB();
      } else if (s < 6) {
        mm(0); SB();
        if (q >= 1 && s >= 1) { red_wr(q - 1, s - 1); SB(); }
        mm(1); SB();
        if (q >= 2 && s < 4) { red_rd(q - 2, 2 * s); SB(); }
        mm(2); SB();
        if (q >= 2 && s < 4) { red_rd(q - 2, 2 * s + 1); SB(); }
        if (q >= 2 && s == 4) { red_x(q - 2); SB(); }
        mm(3); SB();
        if (q >= 2 && s >= 1 && s < 4) {
          const f32x4 t = rv[2 * s - 2] + rv[2 * s - 1];
          rsum = s == 1 ? t : rsum + t;
          asm volatile("" : "+v"(rsum));
          SB();
        }
        mm(4); SB();
      } else if (full) {
#pragma unroll
        for (int c = 0; c < 5; ++c) mm(c);
        SB();
      }
      if (7 * q + s + H1_PRE < 28) { load_h1(7 * q + s + H1_PRE); SB(); }
    }
    if (q >= 1) bar_lds();
    if (q == 0) STAMP(2, 5);
  }
  {
    auto sum6 = [&]() __attribute__((always_inline)) {
      rsum = ((rv[0] + rv[1]) + (rv[2] + rv[3])) + (rv[4] + rv[5]);
    };
    if (full) {
#pragma unroll
      for (int c = 0; c < 5; ++c) {
        const u32x4 a = c == 4 ? wl[6] : (c == 3 ? wl[7] : ring[(8 + 24 + c) % 32]);
        rv[c] = mfma_bf16(a, hb[27], (f32x4){0.f, 0.f, 0.f, 0.f});
      }
    } else {
#pragma unroll
      for (int c = 0; c < 5; ++c) rv[c] = (f32x4){0.f, 0.f, 0.f, 0.f};
    }
    f32x4 c0[5];
#pragma unroll
    for (int c = 0; c < 5; ++c) c0[c] = rv[c];
    SB();
    auto col = [&](int c, auto&& between) __attribute__((always_inline)) {
#pragma unroll
      for (int s = 0; s < 6; ++s) {
        const u32x4 a = c == 4 ? wl[s] : ring[(8 + 4 * s + c) % 32];
        c0[c] = mfma_bf16(a, hb[21 + s], c0[c]);
        SB();
        between(s);
        SB();
      }
    };
    col(0, [&](int s) __attribute__((always_inline)) { if (s < 5) red_wr(2, s); if (s < 4) { red_rd(1, 2 * s); red_rd(1, 2 * s + 1); } if (s == 4) red_x(1); });
    col(1, [&](int s) __attribute__((always_inline)) { if (s == 1) { sum6(); asm volatile("" : "+v"(rsum)); } if (s == 3) red_out(1); });
    bar_lds();
    auto wr3 = [&](int c) __attribute__((always_inline)) { *(f32x4*)(rb_ + (KF_RB / 4) + 16 * c) = c0[c]; };
    col(2, [&](int s) __attribute__((always_inline)) { if (s < 2) wr3(s); if (s >= 1 && s < 5) { red_rd(2, 2 * s - 2); red_rd(2, 2 * s - 1); } if (s == 5) red_x(2); });
    col(3, [&](int s) __attribute__((always_inline)) { if (s == 1) wr3(2); if (s == 2) { sum6(); asm volatile("" : "+v"(rsum)); } if (s == 4) red_out(2); });
    col(4, [&](int s) __attribute__((always_inline)) { if (s == 1) wr3(3); if (s == 2) red_x(3); });
    wr3(4);
    STAMP(2, 6);
    bar_lds();
#pragma unroll
    for (int ww = 0; ww < 8; ++ww) red_rd(3, ww);
    sum6();
    red_out(3);
    VM0();
    STAMP(2, 7);
  }
}

__global__ NOPK __launch_bounds__(512, 2) void kf(char* __restrict__ ws, unsigned short* __restrict__ out) {
  __shared__ __attribute__((aligned(16))) char smem[KF_LDS];
  TS(2, ws);
  kf_body(ws, out, smem);
}

__global__ void kprobe(char* ws, int which) {
  const unsigned long long now = __builtin_amdgcn_s_memrealtime();
  const unsigned long long* t = (const unsigned long long*)(ws + (200u << 20));
  unsigned long long d = which == 0 ? t[1] - t[0] : (which == 1 ? t[2] - t[1] : (which == 2 ? now - t[2] : 0ull));
  if (d > 10000ull) d = 10000ull;
  while (__builtin_amdgcn_s_memrealtime() - now < 10ull * d) __builtin_amdgcn_s_sleep(1);
}
__global__ void kend_() {}

extern "C" void kernel_launch(void* const* d_in, const int* in_sizes, int n_in,
                              void* d_out, int out_size, void* d_ws, size_t ws_size,
                              hipStream_t stream) {
  K1Args a;
  a.x = (const unsigned short*)d_in[0];
  a.are = (const unsigned short*)d_in[1];
  a.aim = (const unsigned short*)d_in[2];
  a.bre = (const unsigned short*)d_in[3];
  a.bim = (const unsigned short*)d_in[4];
  a.cre = (const unsigned short*)d_in[5];
  a.cim = (const unsigned short*)d_in[6];
  a.logdt = (const unsigned short*)d_in[7];
  a.c = (const unsigned short*)d_in[12];
  a.wmod = (const unsigned short*)d_in[13];
  a.ws = (char*)d_ws;
  k1<<<NB_PF + NB_K + NB_LN + NB_MOD, 256, 0, stream>>>(a);
  k2<<<256, 512, 0, stream>>>((const unsigned short*)d_in[8], (char*)d_ws, (const unsigned short*)d_in[10],
                              (const unsigned short*)d_in[11]);
  kf<<<256, 512, 0, stream>>>((char*)d_ws, (unsigned short*)d_out);
  kprobe<<<1, 64, 0, stream>>>((char*)d_ws, 0);
}
```

```cpp
#define NTM 1
#include <hip/hip_runtime.h>
#include <hip/hip_bf16.h>
#include <stdint.h>
#include <type_traits>

typedef __attribute__((ext_vector_type(8))) __bf16 bf16x8;
typedef __attribute__((ext_vector_type(2))) __bf16 bf16x2;
typedef __attribute__((ext_vector_type(4))) float f32x4;
typedef __attribute__((ext_vector_type(2))) float f32x2;
typedef __attribute__((ext_vector_type(4))) unsigned u32x4;
typedef __attribute__((ext_vector_type(2))) unsigned u32x2;
typedef __attribute__((ext_vector_type(8))) _Float16 h16x8;
typedef __attribute__((ext_vector_type(2))) _Float16 h16x2;
typedef __attribute__((ext_vector_type(4))) short s16x4;
typedef __attribute__((address_space(3))) s16x4 lds_s16x4;

#if defined(__HIP_DEVICE_COMPILE__)
#define NOPK __attribute__((target("no-packed-fp32-ops")))
#else
#define NOPK
#endif
#define DEV __device__ __forceinline__

#define W1L 0
constexpr int W1KS = W1L ? 1 : 108, W1TS = W1L ? 20 : 1;
#define LNFLIP 0
#define KMAP 0
constexpr int L_ = 2048;
constexpr int D_ = 640;
constexpr int NS = 128;
constexpr int FF = 1728;
constexpr int KREV = 1280;

constexpr size_t WS_GB   = 0;
constexpr size_t WS_KSC  = 8192;
#define FL_P 2048
#define FL_B 327680
constexpr size_t WS_CNT  = FL_B;
constexpr size_t WS_MOD  = 65536;
constexpr size_t WS_CSP  = 196608;
constexpr size_t WS_XNT  = 327680 + 256 * FL_P;
constexpr size_t WS_XT   = WS_XNT + (size_t)D_ * L_ * 2;
constexpr size_t WS_KREV = WS_XT + (size_t)D_ * L_ * 2;
constexpr size_t WS_W1F  = WS_KREV + (size_t)D_ * KREV * 2;
constexpr size_t WS_W2F  = WS_W1F + (size_t)D_ * FF * 2;
constexpr size_t WS_X1T  = WS_W2F + (size_t)D_ * FF * 2;
constexpr size_t WS_H1F  = WS_X1T + (size_t)D_ * L_ * 2;
constexpr size_t WS_END  = WS_H1F + (size_t)L_ * FF * 2;

#define STAMP(kid, k) do {} while (0)
#define CSTAMP(kid, k) do {} while (0)
#define VM0() do {} while (0)

#define TS(k, wsp) do { if (blockIdx.x == 0 && threadIdx.x == 0) __hip_atomic_store((unsigned long long*)((wsp) + (200u << 20)) + (k), (unsigned long long)__builtin_amdgcn_s_memrealtime(), __ATOMIC_RELAXED, __HIP_MEMORY_SCOPE_AGENT); } while (0)
DEV float bf_lo(unsigned u) { return __uint_as_float(u << 16); }
DEV float bf_hi(unsigned u) { return __uint_as_float(u & 0xffff0000u); }
DEV unsigned pk_bf(float a, float b) {
  f32x2 v = {a, b};
  bf16x2 r = __builtin_convertvector(v, bf16x2);
  return __builtin_bit_cast(unsigned, r);
}
DEV unsigned pk_h(float a, float b) {
  f32x2 v = {a, b};
  h16x2 r = __builtin_convertvector(v, h16x2);
  return __builtin_bit_cast(unsigned, r);
}
template <int BIT, class T> DEV T ldin(const void* p) {
  if (NTM & BIT) return __builtin_nontemporal_load((const T*)p);
  return *(const T*)p;
}
DEV float rbf(float a) { return bf_lo(pk_bf(a, 0.f)); }
DEV float ldbf(const unsigned short* p) { return __uint_as_float(((unsigned)*p) << 16); }
#define CONST_AS __attribute__((address_space(4)))
template <class T> DEV T ldu(const void* p) { return *(const CONST_AS T*)(size_t)p; }
DEV float ldbf_u(const unsigned short* p) {
  const unsigned v = ldu<unsigned>((const void*)((size_t)p & ~(size_t)3));
  return __uint_as_float(((size_t)p & 2) ? (v & 0xffff0000u) : (v << 16));
}

typedef __amdgpu_buffer_rsrc_t rsrc_t;
DEV rsrc_t ws_rsrc(char* ws) { return __builtin_amdgcn_make_buffer_rsrc((void*)ws, 0, (int)WS_END, 0x00020000); }
#define ST_AUX 16
DEV void st16(rsrc_t r, size_t off, u32x4 v) { __builtin_amdgcn_raw_buffer_store_b128(v, r, (int)off, 0, ST_AUX); }
DEV void st4(char* ws, size_t off, unsigned v) { __hip_atomic_store((unsigned*)(ws + off), v, __ATOMIC_RELAXED, __HIP_MEMORY_SCOPE_AGENT); }
DEV void st8(rsrc_t r, size_t off, u32x2 v) { __builtin_amdgcn_raw_buffer_store_b64(v, r, (int)off, 0, 16); }

DEV void bar_lds() { asm volatile("s_waitcnt lgkmcnt(0)\n\ts_barrier" ::: "memory"); }
DEV void lds_wait(const void* word, unsigned val) {
  unsigned tv, ts, cnt;
  asm volatile("s_mov_b32 %2, 0x100000\n"
               "1:\n\tds_read_b32 %0, %3\n\ts_waitcnt lgkmcnt(0)\n\tv_readfirstlane_b32 %1, %0\n\t"
               "s_cmp_ge_u32 %1, %4\n\ts_cbranch_scc1 2f\n\ts_sleep 1\n\ts_sub_u32 %2, %2, 1\n\t"
               "s_cmp_lg_u32 %2, 0\n\ts_cbranch_scc1 1b\n2:"
               : "=&v"(tv), "=&s"(ts), "=&s"(cnt) : "v"((unsigned)(size_t)word), "s"(val) : "memory", "scc");
}
DEV void lds_post(unsigned* word, int lane) {
  asm volatile("" ::: "memory");
  if (lane == 0) __hip_atomic_fetch_add(word, 1u, __ATOMIC_RELAXED, __HIP_MEMORY_SCOPE_WORKGROUP);
  asm volatile("" ::: "memory");
}
DEV void lgkm0() { asm volatile("s_waitcnt lgkmcnt(0)" ::: "memory"); __builtin_amdgcn_sched_barrier(0); }

DEV f32x4 mfma_bf16(u32x4 a, u32x4 b, f32x4 c) {
  return __builtin_amdgcn_mfma_f32_16x16x32_bf16(__builtin_bit_cast(bf16x8, a), __builtin_bit_cast(bf16x8, b), c, 0, 0, 0);
}

constexpr int NB_PF = 8, NB_K = 640, NB_LN = 256, NB_MOD = 120;

struct K1Args {
  const unsigned short *x, *are, *aim, *bre, *bim, *cre, *cim, *logdt, *c, *wmod;
  char* ws;
};

DEV void k1_prefetch() {
  const int tid = threadIdx.x;
  unsigned long long pc;
  int o0, o1;
  asm volatile("s_getpc_b64 %0\n.Lpf_here%=:\n\ts_mov_b32 %1, _Z2k16K1Args - .Lpf_here%=\n\ts_mov_b32 %2, _Z5kend_v - .Lpf_here%="
               : "=s"(pc), "=s"(o0), "=s"(o1));
  const char* c0 = (const char*)(pc + (long long)o0);
  long n = o1 - o0;
  n = n < 0 ? 0 : (n > 98304 ? 98304 : n);
  unsigned v[3];
#pragma unroll
  for (int it = 0; it < 3; ++it) {
    const long o = (long)tid * 128 + it * 32768;
    v[it] = o < n ? *(const __attribute__((address_space(1))) unsigned*)(c0 + o) : 0u;
  }
  asm volatile("" :: "v"(v[0] ^ v[1] ^ v[2]));
}

DEV void k1_mod(const K1Args& p, int b, char* smem) {
  const int tid = threadIdx.x;
  const int rg = b & 7, cgp = b >> 3;
  const int c32 = tid & 31, r8 = tid >> 5;
  float* sil = (float*)smem;
  float* red = (float*)(smem + 512);
  char* stamp_ws = p.ws; (void)stamp_ws;
  STAMP(0, 1);
  u32x4 w[10];
  const float cv = ldbf(p.c + rg * 80 + min(tid, 79));
  asm volatile("" ::: "memory");
#pragma unroll
  for (int it = 0; it < 10; ++it)
    w[it] = ldin<1, u32x4>(p.wmod + (size_t)(rg * 80 + r8 + 8 * it) * 3840 + 256 * cgp + 8 * c32);
  if (tid < 80) sil[tid] = rbf(cv / (1.f + expf(-cv)));
  if (tid >= 128 && tid < 132 && b < 64) st4(p.ws, WS_CNT + (size_t)(b * 4 + tid - 128) * FL_P, 0u);
  __syncthreads();
  STAMP(0, 2);
  float acc[8];
#pragma unroll
  for (int j = 0; j < 8; ++j) acc[j] = 0.f;
#pragma unroll
  for (int it = 0; it < 10; ++it) {
    float s = sil[r8 + 8 * it];
#pragma unroll
    for (int q = 0; q < 4; ++q) {
      acc[2 * q] += s * bf_lo(w[it][q]);
      acc[2 * q + 1] += s * bf_hi(w[it][q]);
    }
  }
#pragma unroll
  for (int j = 0; j < 8; ++j) acc[j] += __shfl_xor(acc[j], 32, 64);
  if ((tid & 63) < 32) {
    *(f32x4*)(red + (tid >> 6) * 256 + 8 * c32) = (f32x4){acc[0], acc[1], acc[2], acc[3]};
    *(f32x4*)(red + (tid >> 6) * 256 + 8 * c32 + 4) = (f32x4){acc[4], acc[5], acc[6], acc[7]};
  }
  __syncthreads();
  STAMP(0, 3);
  if (tid < 64) {
    const f32x4* r4 = (const f32x4*)red + tid;
    st16(ws_rsrc(p.ws), WS_MOD + (rg * 3840 + 256 * cgp + 4 * tid) * 4,
         __builtin_bit_cast(u32x4, (r4[0] + r4[64]) + (r4[128] + r4[192])));
  }
}
struct ModP { f32x4 a, b; };
DEV ModP mod_ld(rsrc_t rs, int idx) {
  ModP m;
#pragma unroll
  for (int j = 0; j < 4; ++j) {
    m.a[j] = __uint_as_float(__builtin_amdgcn_raw_buffer_load_b32(rs, 4 * idx, (int)WS_MOD + 15360 * j, 0));
    m.b[j] = __uint_as_float(__builtin_amdgcn_raw_buffer_load_b32(rs, 4 * idx, (int)WS_MOD + 15360 * (4 + j), 0));
  }
  return m;
}
DEV float mod_fin(const ModP& m) {
  return rbf(((m.a[0] + m.a[1]) + (m.a[2] + m.a[3])) + ((m.b[0] + m.b[1]) + (m.b[2] + m.b[3])));
}
#define SB() __builtin_amdgcn_sched_barrier(0)

DEV void k1_ln(const K1Args& p, int b, char* smem) {
  constexpr int STR = 704;
  const int tid = threadIdx.x;
  const int tok = tid >> 3, sub = tid & 7;
  const int tile = (b & 7) * 8 + ((b >> 3) & 7), pass = (b >> 6) ^ LNFLIP;
  const int hf = pass & 1;
  const bool norm = pass < 2;
  const int t0 = tile * 32;
  const unsigned short* xr = p.x + (size_t)(t0 + tok) * D_ + 8 * sub;
  u32x4 xv[5], xo[5];
  char* stamp_ws = p.ws; (void)stamp_ws;
  STAMP(0, 1);
#pragma unroll
  for (int m = 0; m < 5; ++m) xv[m] = ldin<2, u32x4>(xr + 320 * hf + 64 * m);
  if (norm) {
#pragma unroll
    for (int m = 0; m < 5; ++m) xo[m] = ldin<2, u32x4>(xr + 320 * (hf ^ 1) + 64 * m);
    float s = 0.f, v = 0.f;
    const bf16x2 one2 = __builtin_bit_cast(bf16x2, 0x3f803f80u);
#pragma unroll
    for (int m = 0; m < 10; ++m)
#pragma unroll
      for (int q = 0; q < 4; ++q) {
        const unsigned u = m < 5 ? xv[m][q] : xo[m - 5][q];
        const bf16x2 x2 = __builtin_bit_cast(bf16x2, u);
        s = __builtin_amdgcn_fdot2_f32_bf16(x2, one2, s, false);
        v = __builtin_amdgcn_fdot2_f32_bf16(x2, x2, v, false);
      }
#pragma unroll
    for (int o = 1; o < 8; o <<= 1) { s += __shfl_xor(s, o, 64); v += __shfl_xor(v, o, 64); }
    const float mean = s * (1.f / D_);
    const float var = fmaxf(v * (1.f / D_) - mean * mean, 0.f);
    const float sd = rbf(sqrtf(rbf(rbf(var) + 1e-5f)));
    const float rs = 1.f / sd;
    const float mb = rbf(mean);
#pragma unroll
    for (int m = 0; m < 5; ++m)
#pragma unroll
      for (int q = 0; q < 4; ++q) {
        xv[m][q] = pk_h((bf_lo(xv[m][q]) - mb) * rs, (bf_hi(xv[m][q]) - mb) * rs);
      }
  }
  const int lane = tid & 63, w = tid >> 6;
  const int g = lane >> 4, i = lane & 15;
  const rsrc_t rsrc = ws_rsrc(p.ws);
#pragma unroll
  for (int m = 0; m < 5; ++m) *(u32x4*)(smem + tok * STR + (sub + 8 * m) * 16) = xv[m];
  __syncthreads();
  STAMP(0, 2);
  const size_t dsto = (norm ? WS_XNT : WS_XT) + ((size_t)tile * D_ + 320 * hf) * 64;
#pragma unroll
  for (int it = 0; it < 5; ++it) {
    const int cb = w * 5 + it;
    const char* a = smem + (8 * g + (i >> 2)) * STR + cb * 32 + (i & 3) * 8;
    const u32x2 ra = __builtin_bit_cast(u32x2, __builtin_amdgcn_ds_read_tr16_b64_v4i16((lds_s16x4*)a));
    const u32x2 rb = __builtin_bit_cast(u32x2, __builtin_amdgcn_ds_read_tr16_b64_v4i16((lds_s16x4*)(a + 4 * STR)));
    st16(rsrc, dsto + (size_t)(cb * 16 + i) * 64 + 16 * g, (u32x4){ra[0], ra[1], rb[0], rb[1]});
  }
}

constexpr int RT_STR = 288;
struct RtRaw { u32x4 r[8]; };
template <int NF>
DEV void rt_load(RtRaw& t, const unsigned short* W, int N, int kk, int ct0, int lane) {
#pragma unroll
  for (int j = 0; j < NF; ++j) {
    const int c = lane + 64 * j, row = c / (2 * NF), part = c % (2 * NF);
    t.r[j] = ldin<4, u32x4>(W + (size_t)(32 * kk + row) * N + 16 * ct0 + 8 * part);
  }
}
template <int NF>
DEV void rt_park(const RtRaw& t, char* scr, int lane) {
#pragma unroll
  for (int j = 0; j < NF; ++j) {
    const int c = lane + 64 * j, row = c / (2 * NF), part = c % (2 * NF);
    *(u32x4*)(scr + row * RT_STR + 16 * part) = t.r[j];
  }
}
DEV u32x4 rt_frag(const char* scr, int j, int lane) {
  const int g = lane >> 4, i = lane & 15;
  const char* p = scr + (8 * g + (i >> 2)) * RT_STR + 32 * j + (i & 3) * 8;
  const u32x2 a = __builtin_bit_cast(u32x2, __builtin_amdgcn_ds_read_tr16_b64_v4i16((lds_s16x4*)p));
  const u32x2 b = __builtin_bit_cast(u32x2, __builtin_amdgcn_ds_read_tr16_b64_v4i16((lds_s16x4*)(p + 4 * RT_STR)));
  return (u32x4){a[0], a[1], b[0], b[1]};
}
struct cf { float re, im; };
DEV cf cmul(cf a, cf b) {
  cf r;
  r.re = a.re * b.re - a.im * b.im;
  r.im = a.re * b.im + a.im * b.re;
  return r;
}
DEV cf cexp_(float a2, float br, float m, float m0) {
  float mag = __builtin_amdgcn_exp2f(a2 * (m - m0));
  float ph = __builtin_amdgcn_fractf(br * m);
  cf r; r.re = mag * __builtin_amdgcn_cosf(ph); r.im = mag * __builtin_amdgcn_sinf(ph);
  return r;
}
DEV f32x4 mfma_f16(u32x4 a, u32x4 b, f32x4 c) {
  return __builtin_amdgcn_mfma_f32_16x16x32_f16(__builtin_bit_cast(h16x8, a), __builtin_bit_cast(h16x8, b), c, 0, 0, 0);
}
DEV void put2(char* p, cf a, cf b) {
  u32x2 o = {pk_h(a.re, a.im), pk_h(b.re, b.im)};
  *(u32x2*)p = o;
}

constexpr int KG_RS = 544;
DEV void k1_kgen(const K1Args& p, int d, char* smem) {
  constexpr int RS = KG_RS;
  const int tid = threadIdx.x;
  const int lane = tid & 63;
  const int w = __builtin_amdgcn_readfirstlane(tid >> 6);
  const int g = lane >> 4, i = lane & 15;
  const size_t po = (size_t)d * NS + 2 * lane;
  const unsigned rar = ldin<8, unsigned>(p.are + po), rai = ldin<8, unsigned>(p.aim + po);
  unsigned rbr = 0u, rbi = 0u, rcr = 0u, rci = 0u;
  if (w == 0) {
    rbr = ldin<8, unsigned>(p.bre + po); rbi = ldin<8, unsigned>(p.bim + po);
    rcr = ldin<8, unsigned>(p.cre + po); rci = ldin<8, unsigned>(p.cim + po);
  }
  const float ldt = ldbf_u(p.logdt + d);
  asm volatile("" ::: "memory");
  SB();
  char* stamp_ws = p.ws; (void)stamp_ws;
  const float dt = rbf(expf(ldt));
  asm volatile("" :: "v"(dt));
  STAMP(0, 1);
  float a2[2], bv[2], m0[2];
  a2[0] = (dt * bf_lo(rar)) * 1.44269504088896341f; a2[1] = (dt * bf_hi(rar)) * 1.44269504088896341f;
  bv[0] = (dt * bf_lo(rai)) * 0.15915494309189534f; bv[1] = (dt * bf_hi(rai)) * 0.15915494309189534f;
  m0[0] = a2[0] > 0.f ? 2048.f : 0.f; m0[1] = a2[1] > 0.f ? 2048.f : 0.f;
  char* my = smem + lane * 8;
  if (w == 0) {
    cf v[2], v8[2], e8[2];
    float bound = 0.f;
#pragma unroll
    for (int q = 0; q < 2; ++q) {
      const float dbr = dt * (q ? bf_hi(rbr) : bf_lo(rbr)), dbi = dt * (q ? bf_hi(rbi) : bf_lo(rbi));
      const float cr = q ? bf_hi(rcr) : bf_lo(rcr), ci = q ? bf_hi(rci) : bf_lo(rci);
      const float gsh = __builtin_amdgcn_exp2f(a2[q] * m0[q]);
      v[q].re = (cr * dbr - ci * dbi) * gsh; v[q].im = -(cr * dbi + ci * dbr) * gsh;
      bound += (fabsf(v[q].re) + fabsf(v[q].im)) * __builtin_amdgcn_exp2f(fmaxf(a2[q], 0.f) * 120.f);
      e8[q] = cexp_(a2[q], -bv[q], 8.f, 0.f);
      v8[q] = cexp_(a2[q], -bv[q], 64.f, 0.f);
    }
#pragma unroll
    for (int o = 1; o < 64; o <<= 1) bound += __shfl_xor(bound, o, 64);
    int ex = (int)((__float_as_uint(bound) >> 23) & 0xff);
    ex = ex < 20 ? 20 : (ex > 240 ? 240 : ex);
    const float sv = __uint_as_float((unsigned)(127 + 13 + 127 - ex) << 23);
    if (lane == 0) st4(p.ws, WS_KSC + 4 * d, (unsigned)(ex - 13) << 23);
#pragma unroll
    for (int q = 0; q < 2; ++q) { v[q].re *= sv; v[q].im *= sv; v8[q] = cmul(v[q], v8[q]); }
#pragma unroll
    for (int j = 0; j < 8; ++j) {
      put2(my + (41 + j) * RS, v[0], v[1]);
      put2(my + (49 + j) * RS, v8[0], v8[1]);
      if (j < 7) {
#pragma unroll
        for (int q = 0; q < 2; ++q) { v[q] = cmul(v[q], e8[q]); v8[q] = cmul(v8[q], e8[q]); }
      }
    }
  } else if (w == 1) {
    cf e1[2], u[2], u2[2];
#pragma unroll
    for (int q = 0; q < 2; ++q) {
      e1[q] = cexp_(a2[q], bv[q], 1.f, 0.f);
      u[q].re = __builtin_amdgcn_exp2f(-a2[q] * m0[q]); u[q].im = 0.f;
      u2[q] = cexp_(a2[q], bv[q], 128.f, m0[q]);
    }
#pragma unroll
    for (int r = 0; r < 8; ++r) {
      put2(my + r * RS, u[0], u[1]);
      put2(my + (8 + r) * RS, u2[0], u2[1]);
      if (r < 7) {
#pragma unroll
        for (int q = 0; q < 2; ++q) { u[q] = cmul(u[q], e1[q]); u2[q] = cmul(u2[q], e1[q]); }
      }
    }
  } else if (w == 2) {
    cf e2[2], u[2], u2[2];
#pragma unroll
    for (int q = 0; q < 2; ++q) {
      e2[q] = cexp_(a2[q], bv[q], 2.f, 0.f);
      u[q] = cexp_(a2[q], bv[q], 256.f, m0[q]);
      u2[q] = cexp_(a2[q], bv[q], 384.f, m0[q]);
    }
#pragma unroll
    for (int r = 0; r < 4; ++r) {
      put2(my + (16 + r) * RS, u[0], u[1]);
      put2(my + (20 + r) * RS, u2[0], u2[1]);
      if (r < 3) {
#pragma unroll
        for (int q = 0; q < 2; ++q) { u[q] = cmul(u[q], e2[q]); u2[q] = cmul(u2[q], e2[q]); }
      }
    }
    cf e4[2] = {cmul(e2[0], e2[0]), cmul(e2[1], e2[1])};
#pragma unroll
    for (int r = 0; r < 2; ++r) {
      cf c0 = cexp_(a2[0], bv[0], 512.f + 128.f * r, m0[0]), c1 = cexp_(a2[1], bv[1], 512.f + 128.f * r, m0[1]);
      put2(my + (24 + 2 * r) * RS, c0, c1);
      put2(my + (25 + 2 * r) * RS, cmul(c0, e4[0]), cmul(c1, e4[1]));
    }
  } else {
    cf e4[2], e128[2], u[2], u2[2];
#pragma unroll
    for (int q = 0; q < 2; ++q) {
      e4[q] = cexp_(a2[q], bv[q], 4.f, 0.f);
      e128[q] = cexp_(a2[q], bv[q], 128.f, 0.f);
      u[q] = cexp_(a2[q], bv[q], 1024.f, m0[q]);
      u2[q] = cexp_(a2[q], bv[q], 1536.f, m0[q]);
    }
#pragma unroll
    for (int r = 2; r < 4; ++r) {
      cf c0 = cexp_(a2[0], bv[0], 512.f + 128.f * r, m0[0]), c1 = cexp_(a2[1], bv[1], 512.f + 128.f * r, m0[1]);
      put2(my + (24 + 2 * r) * RS, c0, c1);
      put2(my + (25 + 2 * r) * RS, cmul(c0, e4[0]), cmul(c1, e4[1]));
    }
#pragma unroll
    for (int r = 0; r < 4; ++r) {
      put2(my + (32 + r) * RS, u[0], u[1]);
      put2(my + (36 + r) * RS, u2[0], u2[1]);
#pragma unroll
      for (int q = 0; q < 2; ++q) { if (r < 3) u[q] = cmul(u[q], e128[q]); u2[q] = cmul(u2[q], e128[q]); }
    }
    put2(my + 40 * RS, u2[0], u2[1]);
  }
  __syncthreads();
  STAMP(0, 2);
  _Float16* kh = (_Float16*)(smem + 57 * RS);
  if (w < 3) {
    const char* ua = smem + (16 * w + i) * RS + g * 16;
    const char* va = smem + (41 + i) * RS + g * 16;
    f32x4 acc = {0.f, 0.f, 0.f, 0.f};
#pragma unroll
    for (int ks = 0; ks < 8; ++ks)
      acc = mfma_f16(*(const u32x4*)(ua + ks * 64), *(const u32x4*)(va + ks * 64), acc);
    int idx0, st;
    if (w == 0) { idx0 = (g < 2 ? 4 * g : 120 + 4 * g) + 8 * i; st = 1; }
    else if (w == 1) { idx0 = g < 2 ? 256 + 64 * g + 4 * i : 384 + 64 * (g - 2) + 2 * i; st = 1; }
    else { idx0 = 512 + 64 * g + i; st = 16; }
#pragma unroll
    for (int rg = 0; rg < 4; ++rg) {
      int idx = idx0 + st * rg;
      if (w == 1 && g >= 2) idx = idx0 + 32 * (rg >> 1) + (rg & 1);
      if (w == 2 && g >= 2) idx = (g == 2 && rg == 0 && i == 0) ? 640 : 644;
      kh[idx] = (_Float16)acc[rg];
    }
  }
  __syncthreads();
  STAMP(0, 3);
  const unsigned short* ku = (const unsigned short*)kh;
  const size_t kout = WS_KREV + (size_t)d * KREV * 2;
  {
    const int l0 = 2040 - 8 * tid;
    u32x4 o;
    if (w < 2) {
      const int q = l0 >> 3;
      const unsigned a = ku[384 + q], b = ku[385 + q];
      __builtin_amdgcn_raw_buffer_store_b32(a | (b << 16), ws_rsrc(p.ws), (int)(kout + 4 * tid), 0, 16);
      return;
    } else if (w == 2) {
      const int q = l0 >> 2;
      const unsigned a = ku[256 + q], b = ku[257 + q], c = ku[258 + q];
      o[0] = c | (c << 16); o[1] = b | (b << 16); o[2] = b | (a << 16); o[3] = a | (a << 16);
    } else {
      unsigned v[8];
#pragma unroll
      for (int e = 0; e < 8; ++e) {
        const int l = l0 + 7 - e;
        const int idx = l < 256 ? l : (int)(pk_bf((float)l, 0.f) & 0xffffu) - 17024;
        v[e] = ku[idx];
      }
#pragma unroll
      for (int q = 0; q < 4; ++q) o[q] = v[2 * q] | (v[2 * q + 1] << 16);
    }
    st16(ws_rsrc(p.ws), kout + 16 * tid - 1536, o);
  }
}

__global__ NOPK __launch_bounds__(256) void k1(K1Args p) {
  __shared__ __attribute__((aligned(16))) char smem[57 * KG_RS + 1312];
  int b = blockIdx.x;
  TS(0, p.ws);
  char* stamp_ws = p.ws; (void)stamp_ws;
  STAMP(0, 0);
  if (b < NB_PF) { k1_prefetch(); STAMP(0, 7); return; }
  b -= NB_PF;
  if (b < NB_K) { k1_kgen(p, KMAP ? (b & 7) * 80 + (b >> 3) : b, smem); VM0(); STAMP(0, 7); return; }
  b -= NB_K;
  asm volatile("" :: "s"(p.ws), "s"(p.x));
#define DL_LN 4
#define DL_MOD 4
  if (b < NB_LN) { __builtin_amdgcn_s_sleep(DL_LN); k1_ln(p, b, smem); }
  else { __builtin_amdgcn_s_sleep(DL_MOD); k1_mod(p, b - NB_LN, smem); }
  VM0();
  STAMP(0, 7);
}

constexpr int K2_CS = 4160;
constexpr int K2_KOFF = 4 * K2_CS;
constexpr int K2_CH = K2_KOFF + 4608;
constexpr int CV_RED = 3 * K2_CH;
constexpr int CV_RT2 = 86016;
constexpr int CV_SUM = CV_RT2 + 4 * (32 * RT_STR + 256);
constexpr int CV_XCH = CV_SUM + 2560;
constexpr int CV_FL = CV_RED + 20480;

struct Chan { float sa, ca1, ga, dsk, hs, hinv, gy, gd; };
struct ChanRaw { float m, kinv, dsk; };
DEV ChanRaw chan_ld(const unsigned short* dskip, const char* ws, int d, int lane) {
  ChanRaw c;
  c.kinv = ldu<float>(ws + WS_KSC + 4 * d);
  c.dsk = ldbf_u(dskip + d);
  c.m = ((const float*)(ws + WS_MOD))[3840 * (lane & 7) + D_ * ((lane >> 3) & 3) + d];
  return c;
}
template <int CTRL> DEV float dpp_add(float v) {
  return v + __int_as_float(__builtin_amdgcn_update_dpp(0, __float_as_int(v), CTRL, 0xf, 0xf, true));
}
DEV Chan chan_fin(const ChanRaw& r) {
  Chan c;
  const int t = __float_as_int(rbf(dpp_add<0x141>(dpp_add<0x4e>(dpp_add<0xb1>(r.m)))));
  c.sa = __int_as_float(__builtin_amdgcn_readlane(t, 0));
  c.ca1 = rbf(1.f + __int_as_float(__builtin_amdgcn_readlane(t, 8)));
  c.ga = __int_as_float(__builtin_amdgcn_readlane(t, 16));
  c.dsk = r.dsk;
  const float hb = 26.f * fabsf(c.ca1) + fabsf(c.sa);
  int ex = (int)((__float_as_uint(hb) >> 23) & 0xff);
  ex = ex < 20 ? 20 : (ex > 240 ? 240 : ex);
  c.hs = __uint_as_float((unsigned)(127 + 14 + 127 - ex) << 23);
  c.hinv = __uint_as_float((unsigned)(ex - 14) << 23);
  c.gy = r.kinv * c.hinv * c.ga;
  c.gd = c.hinv * c.dsk * c.ga;
  return c;
}
DEV size_t tl_off(int d, int t) { return (((size_t)(t >> 5) * D_ + d) * 32 + (t & 31)) * 2; }
struct StageRaw { u32x4 xa[2], kv1; u32x2 xb; unsigned kv0; };
DEV StageRaw stage_ld(const char* ws, int d, int th) {
  StageRaw s;
  const unsigned short* krev = (const unsigned short*)(ws + WS_KREV) + (size_t)d * KREV;
  const int T = 1024 * (th >> 6) + 8 * (th & 63);
  s.xb = ldu<u32x2>(ws + WS_XNT + tl_off(d, 1024));
  s.xa[0] = *(const u32x4*)(ws + WS_XNT + tl_off(d, T)); s.xa[1] = *(const u32x4*)(ws + WS_XNT + tl_off(d, T + 512));
  s.kv0 = *(const unsigned*)(krev + 2 * th); s.kv1 = *(const u32x4*)(krev + 256 + 8 * th);
  return s;
}
DEV void stage_wr(const StageRaw& s, const Chan& c, char* smem, int th) {
  constexpr int CS = K2_CS, KOFF = K2_KOFF;
  const h16x2 c2 = __builtin_bit_cast(h16x2, pk_h(c.ca1 * c.hs, c.ca1 * c.hs)), s2 = __builtin_bit_cast(h16x2, pk_h(c.sa * c.hs, c.sa * c.hs));
  auto mod = [&](unsigned u) __attribute__((always_inline)) {
    return __builtin_bit_cast(unsigned, __builtin_elementwise_fma(__builtin_bit_cast(h16x2, u), c2, s2));
  };
  unsigned h2[2][6];
#pragma unroll
  for (int e = 0; e < 2; ++e)
#pragma unroll
    for (int q = 0; q < 4; ++q) h2[e][q] = mod(s.xa[e][q]);
#pragma unroll
  for (int q = 0; q < 2; ++q) {
    h2[0][4 + q] = __builtin_amdgcn_update_dpp(__builtin_amdgcn_readfirstlane(h2[1][q]), h2[0][q], 0x130, 0xf, 0xf, false);
    h2[1][4 + q] = __builtin_amdgcn_update_dpp(mod(s.xb[q]), h2[1][q], 0x130, 0xf, 0xf, false);
  }
  char* dst = smem + (128 * (th >> 6) + (th & 63) + 2) * 16;
  unsigned s1[5];
#pragma unroll
  for (int e = 1; e >= 0; --e) {
    const unsigned* hh = h2[e];
#pragma unroll
    for (int q = 0; q < 5; ++q) s1[q] = __builtin_amdgcn_alignbit(hh[q + 1], hh[q], 16);
    *(u32x4*)(dst + 1024 * e) = (u32x4){hh[0], hh[1], hh[2], hh[3]};
    *(u32x4*)(dst + 1024 * e + CS) = (u32x4){s1[0], s1[1], s1[2], s1[3]};
    *(u32x4*)(dst + 1024 * e + 2 * CS) = (u32x4){hh[1], hh[2], hh[3], hh[4]};
    *(u32x4*)(dst + 1024 * e + 3 * CS) = (u32x4){s1[1], s1[2], s1[3], s1[4]};
  }
  {
    const unsigned a = __builtin_amdgcn_readfirstlane(h2[0][0]), b = __builtin_amdgcn_readfirstlane(s1[0]);
    if (th >= 1 && th < 4)
      *(u32x4*)(smem + th * CS + 16) = (u32x4){0u, 0u, th == 3 ? a << 16 : 0u, th == 1 ? a << 16 : (th == 2 ? a : b)};
  }
  {
    const unsigned aa = __builtin_amdgcn_perm(s.kv0, s.kv0, 0x01000100u), bb = __builtin_amdgcn_perm(s.kv0, s.kv0, 0x03020302u);
    const unsigned ba = __builtin_amdgcn_perm(s.kv0, s.kv0, 0x01000302u);
    *(u32x4*)(smem + KOFF + 16 * th) = (u32x4){bb, (th & 1) ? ba : bb, aa, aa};
  }
  *(u32x4*)(smem + KOFF + 2048 + 16 * th) = s.kv1;
}
DEV void stage_zero(char* smem, int th) {
  if (th < 37) *(u32x4*)(smem + (th < 32 ? K2_KOFF + 4096 + 16 * th : (th < 36 ? (th - 32) * K2_CS : 16))) = (u32x4){0u, 0u, 0u, 0u};
}
DEV f32x4 conv_out(const f32x4& y, const Chan& c, const char* smem, int t) {
  const u32x2 hv = *(const u32x2*)(smem + 2 * (t + 16));
  f32x4 o;
#pragma unroll
  for (int e = 0; e < 2; ++e) {
    const unsigned hu = hv[e];
    const h16x2 h2 = __builtin_bit_cast(h16x2, hu);
    o[2 * e] = fmaf((float)h2[0], c.gd, y[2 * e] * c.gy);
    o[2 * e + 1] = fmaf((float)h2[1], c.gd, y[2 * e + 1] * c.gy);
  }
  return o;
}
DEV unsigned add_bf(unsigned x, float a, float b) { return pk_bf(bf_lo(x) + a, bf_hi(x) + b); }
#define K2_LOAD(buf, cc, u)                                                                                     \
  asm volatile("ds_read_b64 %0, %3 offset:%5\n\tds_read_b64 %1, %3 offset:%6\n\tds_read_b128 %2, %4 offset:%7"   \
               : "=&v"(Al[buf][u]), "=&v"(Ah[buf][u]), "=&v"(Bf[buf][u])                                          \
               : "v"(ap0), "v"(bp0), "i"(64 * (cc) + 512 * (u)), "i"(64 * (cc) + 512 * (u) + 8),                 \
                 "i"(64 * (cc) + 3584 - 512 * (u)))
template <int SET> DEV constexpr int half_tile(int j) {
  return SET == 0 ? (j == 0 ? 7 : (j == 1 ? 6 : 2)) : (j < 3 ? 5 - j : 4 - j);
}
template <int SET>
DEV void conv_half(unsigned ap0, unsigned bp0, char* redw) {
  constexpr int NU = SET == 0 ? 8 : 6, NT = SET == 0 ? 3 : 5;
  u32x2 Al[2][NU], Ah[2][NU];
  u32x4 Bf[2][NU];
#pragma unroll
  for (int u = 0; u < NU; ++u) K2_LOAD(0, 0, u);
  f32x4 acc[NT];
#pragma unroll
  for (int j = 0; j < NT; ++j) acc[j] = (f32x4){0.f, 0.f, 0.f, 0.f};
#pragma unroll
  for (int cc = 0; cc < 2; ++cc) {
    lgkm0();
    if (cc == 0) {
#pragma unroll
      for (int u = 0; u < NU; ++u) K2_LOAD(1, 1, u);
    }
#pragma unroll
    for (int sp = 0; sp < NU; ++sp)
#pragma unroll
      for (int j = 0; j < NT; ++j) {
        const int q = half_tile<SET>(j);
        if (sp <= q) {
          const u32x4 A = {Al[cc][sp][0], Al[cc][sp][1], Ah[cc][sp][0], Ah[cc][sp][1]};
          acc[j] = mfma_f16(A, Bf[cc][q - sp], acc[j]);
        }
      }
    SB();
  }
#pragma unroll
  for (int j = 0; j < NT; ++j) *(f32x4*)(redw + j * 4096) = acc[j];
}

template <int NF>
DEV void w1_fin(const RtRaw& t, const ModP& mp, char* ws, int kk, int ct0, int lane, char* scr, float* sums) {
  const int g = lane >> 4, i = lane & 15;
  float* tb = (float*)(scr + 32 * RT_STR);
  const float mv = mod_fin(mp);
  tb[lane] = lane < 32 ? rbf(1.f + mv) : mv;
  rt_park<NF>(t, scr, lane);
  lgkm0();
  const f32x4 ca = *(const f32x4*)(tb + 8 * g), cb = *(const f32x4*)(tb + 8 * g + 4);
  const f32x4 sa = *(const f32x4*)(tb + 32 + 8 * g), sb = *(const f32x4*)(tb + 36 + 8 * g);
  const u32x4 sv = {pk_bf(sa[0], sa[1]), pk_bf(sa[2], sa[3]), pk_bf(sb[0], sb[1]), pk_bf(sb[2], sb[3])};
  const u32x4 ones = {0x3f803f80u, 0x3f803f80u, 0x3f803f80u, 0x3f803f80u};
  const f32x4 z = {0.f, 0.f, 0.f, 0.f};
  const rsrc_t rs = ws_rsrc(ws);
#pragma unroll
  for (int j = 0; j < NF; ++j) {
    const u32x4 f = rt_frag(scr, j, lane);
    const u32x4 sc = {pk_bf(bf_lo(f[0]) * ca[0], bf_hi(f[0]) * ca[1]), pk_bf(bf_lo(f[1]) * ca[2], bf_hi(f[1]) * ca[3]),
                      pk_bf(bf_lo(f[2]) * cb[0], bf_hi(f[2]) * cb[1]), pk_bf(bf_lo(f[3]) * cb[2], bf_hi(f[3]) * cb[3])};
    st16(rs, WS_W1F + ((size_t)(kk * W1KS + (ct0 + j) * W1TS) * 64 + lane) * 16, sc);
    const f32x4 cs = mfma_bf16(sc, ones, z), b1 = mfma_bf16(f, sv, z);
    if (i < 2) *(f32x4*)(sums + i * (16 * NF) + 16 * j + 4 * g) = i ? b1 : cs;
  }
}

DEV void conv_phase(const unsigned short* __restrict__ dskip, char* __restrict__ ws,
                    const unsigned short* __restrict__ w1, const unsigned short* __restrict__ w2, char* smem_all) {
  constexpr int CS = K2_CS, KOFF = K2_KOFF;
  const int tid = threadIdx.x, lane = tid & 63;
  const int w = __builtin_amdgcn_readfirstlane(tid >> 6);
  const int bx = blockIdx.x;
  const int pj = (bx & 7) * 16 + (bx >> 4), par = (bx >> 3) & 1;
  const int r = lane & 15, g = lane >> 4, th = tid & 127;
  const int s4 = (r + 1) & 3;
  const rsrc_t rs = ws_rsrc(ws);
  unsigned* fl = (unsigned*)(smem_all + CV_FL);
  char* stamp_ws = ws; (void)stamp_ws;
  STAMP(1, 0);
  asm volatile("" :: "s"(w1), "s"(w2));
  if (tid < 12) fl[tid] = 0u;
  if (w < 4) {
    const int d = 5 * pj + 2 * par + (w >> 1), hw = w & 1;
    char* smem = smem_all + (w >> 1) * K2_CH;
    const ChanRaw cr = chan_ld(dskip, ws, d, lane);
    SB();
    const StageRaw sr = stage_ld(ws, d, th);
    u32x4 xr4[2];
    u32x2 xres[4];
    auto ld_res = [&]() __attribute__((always_inline)) {
#pragma unroll
      for (int qp = 0; qp < 2; ++qp)
        xr4[qp] = *(const u32x4*)(ws + WS_XT + tl_off(d, 256 * (4 * hw + 2 * qp + (g & 1)) + 16 * r + 8 * (g >> 1)));
    };
    ld_res();
    stage_zero(smem, th);
    SB();
    asm volatile("s_waitcnt lgkmcnt(0)\n\ts_barrier" ::: "memory");
    SB();
    const Chan c = chan_fin(cr);
    STAMP(1, 1);
    stage_wr(sr, c, smem, th);
    lds_post(fl + 6 + (w >> 1), lane);
    lds_wait(fl + 6 + (w >> 1), 2u);
    STAMP(1, 2);
    f32x4 acc[8], keep[4];
#pragma unroll
    for (int q = 0; q < 8; ++q) acc[q] = (f32x4){0.f, 0.f, 0.f, 0.f};
    {
      const unsigned ap0 = (unsigned)(size_t)(smem + s4 * CS + 2 * (128 * hw + 8 * g + (r + 1) - s4));
      const unsigned bp0 = (unsigned)(size_t)(smem + KOFF + 2 * (2032 + 128 * hw - 16 * r + 8 * g) - 3584);
      u32x2 Al[2][8], Ah[2][8];
      u32x4 Bf[2][8];
#pragma unroll
      for (int u = 0; u < 8; ++u) K2_LOAD(0, 0, u);
      auto tile = [&](int cc, int q) __attribute__((always_inline)) {
#pragma unroll
        for (int sp = 0; sp <= q; ++sp) {
          const u32x4 A = {Al[cc & 1][sp][0], Al[cc & 1][sp][1], Ah[cc & 1][sp][0], Ah[cc & 1][sp][1]};
          acc[q] = mfma_f16(A, Bf[cc & 1][q - sp], acc[q]);
        }
        SB();
      };
#pragma unroll
      for (int cc = 0; cc < 3; ++cc) {
        lgkm0();
#pragma unroll
        for (int q = 0; q < 8; ++q) {
          K2_LOAD((cc + 1) & 1, cc + 1, q);
          tile(cc, q);
        }
      }
#pragma unroll
      for (int qp = 0; qp < 2; ++qp) {
        const auto s0 = __builtin_amdgcn_permlane16_swap(xr4[qp][0], xr4[qp][2], false, false);
        const auto s1 = __builtin_amdgcn_permlane16_swap(xr4[qp][1], xr4[qp][3], false, false);
        xres[2 * qp] = (u32x2){s0[0], s1[0]}; xres[2 * qp + 1] = (u32x2){s0[1], s1[1]};
      }
      lgkm0();
      f32x4* xw = (f32x4*)(smem_all + CV_XCH + w * 4096) + lane;
      auto last = [&](auto hw_tag) __attribute__((always_inline)) {
        constexpr int S = decltype(hw_tag)::value ? 0 : 4, K = 4 - S;
#pragma unroll
        for (int q = 0; q < 4; ++q) {
          tile(3, S + q);
          if (q) { xw[64 * (q - 1)] = acc[S + q - 1]; SB(); }
        }
        tile(3, K + 3);
        xw[64 * 3] = acc[S + 3];
        lds_post(fl + w, lane);
        SB();
#pragma unroll
        for (int q = 0; q < 3; ++q) tile(3, K + q);
#pragma unroll
        for (int q = 0; q < 4; ++q) keep[q] = acc[K + q];
      };
      if (hw) last(std::true_type{}); else last(std::false_type{});
    }
    STAMP(1, 3);
    lds_wait(fl + (w ^ 1), 1u);
    STAMP(1, 4);
    const f32x4* xrd = (const f32x4*)(smem_all + CV_XCH + (w ^ 1) * 4096) + lane;
#pragma unroll
    for (int qp = 0; qp < 2; ++qp) {
      u32x2 o[2];
#pragma unroll
      for (int h = 0; h < 2; ++h) {
        const int q = 2 * qp + h;
        const f32x4 v = conv_out(keep[q] + xrd[64 * q], c, smem, 256 * (4 * hw + q) + 16 * r + 4 * g);
        o[h] = (u32x2){add_bf(xres[q][0], v[0], v[1]), add_bf(xres[q][1], v[2], v[3])};
      }
      const auto s0 = __builtin_amdgcn_permlane16_swap(o[0][0], o[1][0], false, false);
      const auto s1 = __builtin_amdgcn_permlane16_swap(o[0][1], o[1][1], false, false);
      const int t = 256 * (4 * hw + 2 * qp + (g & 1)) + 16 * r + 8 * (g >> 1);
      st16(rs, WS_X1T + (((size_t)(t >> 6) * D_ + d) * 64 + (t & 63)) * 2, (u32x4){s0[0], s1[0], s0[1], s1[1]});
    }
  } else {
#define PRIO47 2
    __builtin_amdgcn_s_setprio(PRIO47);
    const int d = 5 * pj + 4, wq = w - 4;
    char* smem = smem_all + 2 * K2_CH;
    StageRaw sr;
    const ChanRaw cr = chan_ld(dskip, ws, d, lane);
    asm volatile("" ::: "memory");
    if (wq < 2) sr = stage_ld(ws, d, th);
    asm volatile("" ::: "memory");
    const int tl0 = par ? (wq < 3 ? 5 - wq : 1) : (wq == 0 ? 7 : (wq == 1 ? 6 : 2)), tl1 = 0;
    const unsigned ap0 = (unsigned)(size_t)(smem + s4 * CS + 2 * (64 * wq + 8 * g + (r + 1) - s4));
    const unsigned bp0 = (unsigned)(size_t)(smem + KOFF + 2 * (2032 + 64 * wq - 16 * r + 8 * g) - 3584);
    char* redw = smem_all + CV_RED + wq * 1024 + lane * 16;
    u32x2 xres[2];
    xres[0] = *(const u32x2*)(ws + WS_XT + tl_off(d, 256 * tl0 + 16 * r + 4 * g));
    xres[1] = *(const u32x2*)(ws + WS_XT + tl_off(d, 256 * tl1 + 16 * r + 4 * g));
    if (wq == 0) stage_zero(smem, th);
    SB();
    __builtin_amdgcn_s_barrier();
    SB();
    const Chan c = chan_fin(cr);
    if (wq < 2) {
      stage_wr(sr, c, smem, th);
      lds_post(fl + 8, lane);
    }
#define NF1_ 4
#define NF2_ 5
    constexpr int NF1 = NF1_, NC1 = 108 / NF1, NB1 = 5 * NC1, NF2 = NF2_, NC2 = 40 / NF2, NJ2 = 54 * NC2;
    const bool isw1 = bx < NB1;
    const int j2 = 4 * (bx - NB1) + wq;
    const bool isw2 = !isw1 && j2 < NJ2, isgb = j2 >= NJ2 && j2 < NJ2 + 10;
    const int jk = isw1 ? 4 * (bx / NC1) + wq : j2 / NC2;
    const int jc = isw1 ? NF1 * (bx % NC1) : NF2 * (j2 % NC2);
    RtRaw rt;
    const int gbi = 64 * (j2 - NJ2) + lane;
    ModP mp;
    SB();
#define RT_MODE 2
#define RT67 24
    auto rtl = [&]() __attribute__((always_inline)) {
      if (isw1) rt_load<NF1>(rt, w1, FF, jk, jc, lane);
      else if (isw2) rt_load<NF2>(rt, w2, D_, jk, jc, lane);
      SB();
    };
    if (wq >= 2 && RT67) __builtin_amdgcn_s_sleep(RT67);
    rtl();
    lds_wait(fl + 8, 2u);
    STAMP(1, 2);
    if (isw1 || isgb) mp = mod_ld(rs, isw1 ? (lane < 32 ? 4 * D_ : 3 * D_ - 32) + 32 * jk + lane : 5 * D_ + gbi);
    SB();
    if (par) conv_half<1>(ap0, bp0, redw); else conv_half<0>(ap0, bp0, redw);
    lds_post(fl + 4, lane);
    STAMP(1, 3);
    STAMP(1, 4);
    lds_wait(fl + 4, 4u);
    STAMP(1, 5);
    const char* rd = smem_all + CV_RED + lane * 16;
#pragma unroll
    for (int u = 0; u < 2; ++u) {
      const bool on = u == 0 ? (par || wq < 3) : (par && wq == 0);
      if (on) {
        const int sl = u == 0 ? wq : 4, tl = u == 0 ? tl0 : tl1;
        const f32x4 y = (*(const f32x4*)(rd + sl * 4096) + *(const f32x4*)(rd + sl * 4096 + 1024))
                      + (*(const f32x4*)(rd + sl * 4096 + 2048) + *(const f32x4*)(rd + sl * 4096 + 3072));
        const int t = 256 * tl + 16 * r + 4 * g;
        const f32x4 v = conv_out(y, c, smem, t);
        st8(rs, WS_X1T + (((size_t)(t >> 6) * D_ + d) * 64 + (t & 63)) * 2, (u32x2){add_bf(xres[u][0], v[0], v[1]), add_bf(xres[u][1], v[2], v[3])});
      }
    }
    SB();
    STAMP(1, 6);
    char* scr = smem_all + CV_RT2 + wq * (32 * RT_STR + 256);
    if (isw1) {
      float* sums = (float*)(smem_all + CV_SUM);
      w1_fin<NF1>(rt, mp, ws, jk, jc, lane, scr, sums + 32 * NF1 * wq);
      asm volatile("" ::: "memory");
      unsigned old = 0u;
      if (lane == 0) old = __hip_atomic_fetch_add(fl + 5, 1u, __ATOMIC_RELAXED, __HIP_MEMORY_SCOPE_WORKGROUP);
      asm volatile("" ::: "memory");
      if (__builtin_amdgcn_readfirstlane(old) == 3u && lane < 8 * NF1) {
        const f32x4* sp = (const f32x4*)sums + lane;
        const f32x4 v = (sp[0] + sp[8 * NF1]) + (sp[16 * NF1] + sp[24 * NF1]);
        st16(rs, WS_CSP + ((size_t)(2 * (bx / NC1) + lane / (4 * NF1)) * FF + 16 * jc + 4 * (lane % (4 * NF1))) * 4, __builtin_bit_cast(u32x4, v));
      }
    } else if (isw2) {
      rt_park<NF2>(rt, scr, lane);
      lgkm0();
#pragma unroll
      for (int j = 0; j < NF2; ++j) st16(rs, WS_W2F + ((size_t)((jc + j) * 54 + jk) * 64 + lane) * 16, rt_frag(scr, j, lane));
    } else if (isgb) {
      st4(ws, WS_GB + 4 * gbi, __float_as_uint(mod_fin(mp)));
    }
  }
}

__global__ NOPK __launch_bounds__(512, 2) void k2(const unsigned short* __restrict__ dskip, char* __restrict__ ws,
                                                  const unsigned short* __restrict__ w1, const unsigned short* __restrict__ w2) {
  __shared__ __attribute__((aligned(16))) char smem[CV_XCH + 16384];
  TS(1, ws);
  conv_phase(dskip, ws, w1, w2, smem);
  char* stamp_ws = ws; (void)stamp_ws;
  VM0();
  STAMP(1, 7);
}

DEV u32x4 ld16_sc1(rsrc_t r, int voff, int soff) { return __builtin_amdgcn_raw_buffer_load_b128(r, voff, soff, 16); }
DEV u32x4 ld16(rsrc_t r, int voff, int soff) { return __builtin_amdgcn_raw_buffer_load_b128(r, voff, soff, 0); }
DEV u32x4 tr_frag(const char* p) {
  const u32x2 a = __builtin_bit_cast(u32x2, __builtin_amdgcn_ds_read_tr16_b64_v4i16((lds_s16x4*)p));
  const u32x2 b = __builtin_bit_cast(u32x2, __builtin_amdgcn_ds_read_tr16_b64_v4i16((lds_s16x4*)(p + 256)));
  return (u32x4){a[0], a[1], b[0], b[1]};
}

constexpr int KF_RS = 84;
constexpr int KF_RB = 8 * 16 * KF_RS * 4;
constexpr int KF_BIG = 2 * KF_RB;
constexpr int NW2L = 8, NW2R = 27;
constexpr int KF_TAB = KF_BIG;
constexpr int KF_PS = KF_TAB + 320;
constexpr int KF_XR = KF_PS + 512;
constexpr int KF_CNT = KF_XR + 64 * 176;
constexpr int KF_LDS = KF_CNT + 16;
#define X_FIRST 5
#define X_STEP 4
#define W1_EARLY 3
#define W1_MID 7
#define W1_PRE 10
#define W2_START 6
#define W2_RATE 2
#define W2_INL 28
#define CS_STEP 14
#define SIG_SPLIT 4
#define H1_PRE 7
#define H1_BATCH 99
#define LOCK_MASK 0x55554u

DEV void kf_body(char* __restrict__ ws, unsigned short* __restrict__ out, char* smem) {
  char* xs = smem;
  float* tab = (float*)(smem + KF_TAB);
  float* ps = (float*)(smem + KF_PS);
  char* xr = smem + KF_XR;
  unsigned* lcnt = (unsigned*)(smem + KF_CNT);
  float* cst = (float*)(smem + 81920);
  const int tid = threadIdx.x, lane = tid & 63;
  const int w = __builtin_amdgcn_readfirstlane(tid >> 6);
  const int bx = blockIdx.x;
  const int ng = (bx & 1) * 4 + ((bx >> 3) & 3), mt = ((bx & 7) >> 1) * 8 + (bx >> 5);
  const int g = lane >> 4, i = lane & 15;
  const rsrc_t rs_ = ws_rsrc(ws);

  char* stamp_ws = ws; (void)stamp_ws;
  STAMP(2, 0);
  unsigned* flags = (unsigned*)(ws + WS_CNT) + mt * (2 * FL_P);
  const int ntl = ng < 6 ? 14 : 12;
  const int ctb = ng < 6 ? 14 * ng : 84 + 12 * (ng - 6);
  const bool two = w < ntl - 8;
  const bool full = w < 6;
  const int ks0 = w < 6 ? 7 * w : 42 + 6 * (w - 6);
  const int ct0 = ctb + (two ? 2 * w : (ntl - 8) + w);
  const int lo16 = lane * 16;
  const int w1s = (int)WS_W1F + ct0 * W1TS * 1024;
  const int w2s = (int)WS_W2F + (ng * 5 * 54 + ks0) * 1024;

  u32x4 ring[32], wl[NW2L];
  const char* bb0 = xs + (8 * g + (i >> 2)) * 64 + ((g & 1) << 5) + (i & 3) * 8;
  const char* bb1 = xs + (8 * g + (i >> 2)) * 64 + (((g & 1) ^ 1) << 5) + (i & 3) * 8;
  auto w2_off = [&](int e) __attribute__((always_inline)) {
    const int s = e < 27 ? e / 4 : (e < 34 ? e - 27 : 6), c = e < 27 ? e % 4 : (e < 34 ? 4 : 3);
    const int sn = (s == 6 && !full) ? 5 : s;
    return w2s + (c * 54 + sn) * 1024;
  };

  auto gemm1 = [&](auto two_tag) __attribute__((always_inline)) {
    constexpr bool TWO = decltype(two_tag)::value;
    if (TWO) asm volatile("; two tiles" ::: "memory"); else asm volatile("; one tile" ::: "memory");
    u32x4 t[10];
    auto st_x = [&](int i0, int i1) __attribute__((always_inline)) {
#pragma unroll
      for (int it = i0; it < i1; ++it) {
        const int c = tid + 512 * it;
        const int row = c >> 3;
        *(u32x4*)(xs + ((c >> 2) & 1) * 40960 + row * 64 + (((c & 3) ^ ((row >> 2) & 2)) << 4)) = t[it];
      }
    };
#pragma unroll
    for (int it = 0; it < X_FIRST; ++it) t[it] = ld16(rs_, tid * 16, (int)WS_X1T + mt * 81920 + 8192 * it);
#pragma unroll
    for (int k = 0; k < W1_EARLY; ++k) {
      ring[2 * k] = ld16(rs_, lo16, w1s + k * W1KS * 1024);
      if (TWO) ring[2 * k + 1] = ld16(rs_, lo16, w1s + (k * W1KS + W1TS) * 1024);
    }
    SB();
    __builtin_amdgcn_s_barrier();
    SB();
#pragma unroll
    for (int k = W1_EARLY; k < W1_MID; ++k) {
      ring[2 * k] = ld16(rs_, lo16, w1s + k * W1KS * 1024);
      if (TWO) ring[2 * k + 1] = ld16(rs_, lo16, w1s + (k * W1KS + W1TS) * 1024);
    }
    SB();
    st_x(0, X_FIRST);
    SB();
    if (tid == 0) *lcnt = 0u;
    float gbp;
    if (!TWO) gbp = ((const float*)(ws + WS_GB))[80 * ng + min((w & 1) * 64 + lane, 79)];
    SB();
    __syncthreads();
    STAMP(2, 1);
    auto resid = [&]() __attribute__((always_inline)) {
      const int kb = (80 * ng) >> 5, tq = w & 3;
#pragma unroll
      for (int u = 0; u < 2; ++u) {
        const int kk = min(kb + 2 * (w >> 2) + u, 19);
        const u32x4 f = tr_frag(((tq & 1) ? bb1 : bb0) + (tq >> 1) * 40960 + kk * 2048);
        const unsigned dl = (unsigned)(32 * kk + 8 * g - 80 * ng);
        if (dl < 80u) *(u32x4*)(xr + (tq * 16 + i) * 176 + dl * 2) = f;
      }
    };
    const int hs = TWO ? 0 : (w & 1);
    const char* ba[4] = {bb0 + hs * 40960, bb1 + hs * 40960, bb0 + (hs ^ 1) * 40960, bb1 + (hs ^ 1) * 40960};
    const u32x4 ones = {0x3f803f80u, 0x3f803f80u, 0x3f803f80u, 0x3f803f80u};
    f32x4 acc[2][4];
#pragma unroll
    for (int c = 0; c < 2; ++c)
#pragma unroll
      for (int q = 0; q < 4; ++q) acc[c][q] = (f32x4){0.f, 0.f, 0.f, 0.f};
    u32x4 csp[5];
    const int cso = ((w & 1) * FF + 16 * ctb + 4 * min(lane, 4 * ntl - 1)) * 4;
    u32x4 bn[2][4];
#pragma unroll
    for (int q = 0; q < 4; ++q) bn[0][q] = tr_frag(ba[q]);
#pragma unroll
    for (int q = 0; q < 4; ++q) bn[1][q] = tr_frag(ba[q] + 2048);
#pragma unroll
    for (int kk = 0; kk < 20; ++kk) {
      if (kk == X_STEP) { st_x(X_FIRST, 10); SB(); }
      if (kk == 2 * X_FIRST - 2) { SB(); bar_lds(); SB(); }
      else if ((LOCK_MASK >> kk) & 1) { SB(); __builtin_amdgcn_s_barrier(); SB(); }
      if (kk == 2 * X_FIRST) { resid(); SB(); }
      auto gload = [&](int u) __attribute__((always_inline)) {
        {
          constexpr int CATCH = W1_PRE - W1_MID;
          const int k0 = kk == 0 ? W1_MID : (kk <= CATCH ? W1_MID + 2 * kk - 1 : kk + W1_PRE);
          const int nk = (kk >= 1 && kk <= CATCH) ? 2 : 1;
          const int kn = k0 + (nk == 2 ? (u >> 1) : 0);
          const bool on = nk == 2 || u >= 2;
          if (on && kn < 20) {
            if ((u & 1) == 0) ring[(2 * kn) % 32] = ld16(rs_, lo16, w1s + kn * W1KS * 1024);
            else if (TWO) ring[(2 * kn + 1) % 32] = ld16(rs_, lo16, w1s + (kn * W1KS + W1TS) * 1024);
          }
          if (kk == 0 && u < 2) {
#pragma unroll
            for (int it = X_FIRST + u * 3; it < (u ? 10 : (X_FIRST + 3 < 10 ? X_FIRST + 3 : 10)); ++it)
              t[it] = ld16(rs_, tid * 16, (int)WS_X1T + mt * 81920 + 8192 * it);
          }
        }
        if (kk >= W2_START && u < W2_RATE) {
          const int n = W2_RATE * (kk - W2_START) + u;
          if (n < NW2L) wl[n] = ld16(rs_, lo16, w2_off(NW2R + n));
          else if (n < W2_INL) ring[8 + n - NW2L] = ld16(rs_, lo16, w2_off(n - NW2L));
        }
      };
      const u32x4 a0 = ring[(2 * kk) % 32], a1 = ring[(2 * kk + 1) % 32];
#pragma unroll
      for (int q = 0; q < 4; ++q) {
        const u32x4 bq = bn[kk & 1][q];
        acc[0][q] = mfma_bf16(a0, bq, acc[0][q]);
        if (TWO) acc[1][q] = mfma_bf16(a1, bq, acc[1][q]);
        else if (q < 2) {
          acc[1][2 * q] = mfma_bf16(ones, bq, acc[1][2 * q]);
          acc[1][2 * q + 1] = mfma_bf16(bq, bq, acc[1][2 * q + 1]);
        }
        SB();
        if (kk < 18) bn[kk & 1][q] = tr_frag(ba[q] + (kk + 2) * 2048);
        gload(q);
        SB();
      }
      if (!TWO) {
        if (kk < 5) csp[kk] = ld16(rs_, cso, (int)WS_CSP + kk * 2 * FF * 4);
        if (kk == CS_STEP) {
          f32x4 t = __builtin_bit_cast(f32x4, csp[0]);
#pragma unroll
          for (int u = 1; u < 5; ++u) t += __builtin_bit_cast(f32x4, csp[u]);
          if (w >= 6 && lane < 56) *(f32x4*)(cst + (w & 1) * 224 + 4 * lane) = t;
        }
      }
      if (!TWO && kk == 12) { if ((w & 1) * 64 + lane < 80) tab[(w & 1) * 64 + lane] = gbp; }
      SB();
    }
    STAMP(2, 2);
    if (!TWO) {
#pragma unroll
      for (int h = 0; h < 2; ++h) {
        const f32x4 aq = acc[1][2 * h + 1];
        const float dq = (i & 2) ? ((i & 1) ? aq[3] : aq[2]) : ((i & 1) ? aq[1] : aq[0]);
        const int tk = (2 * hs + h) * 16 + i;
        if (g == (i >> 2)) {
          const float mean = acc[1][2 * h][0] * (1.f / D_);
          const float var = fmaxf(dq * (1.f / D_) - mean * mean, 0.f);
          const float sd = rbf(sqrtf(rbf(rbf(var) + 1e-5f)));
          const float rs = 1.f / sd;
          ps[tk] = rs;
          ps[64 + tk] = -rbf(mean) * rs;
        }
      }
    }
    __syncthreads();
    f32x4 csv[2], b1v[2];
#pragma unroll
    for (int c = 0; c < (TWO ? 2 : 1); ++c) {
      csv[c] = *(const f32x4*)(cst + 16 * (ct0 - ctb + c) + 4 * g);
      b1v[c] = *(const f32x4*)(cst + 224 + 16 * (ct0 - ctb + c) + 4 * g);
    }
#pragma unroll
    for (int qp = 0; qp < 2; ++qp) {
      u32x2 o[2][2];
#pragma unroll
      for (int h = 0; h < 2; ++h) {
        const int q = 2 * qp + h, tq = q ^ (2 * hs);
        const float rs = ps[tq * 16 + i], nm = ps[64 + tq * 16 + i];
#pragma unroll
        for (int c = 0; c < (TWO ? 2 : 1); ++c) {
          float v[4];
#pragma unroll
          for (int r = 0; r < 4; ++r) v[r] = fmaxf(fmaf(acc[c][q][r], rs, fmaf(nm, csv[c][r], b1v[c][r])), 0.f);
          o[c][h] = (u32x2){pk_bf(v[0], v[1]), pk_bf(v[2], v[3])};
        }
      }
      const int tl = ((2 * qp) ^ (2 * hs)) + (g & 1);
#pragma unroll
      for (int c = 0; c < (TWO ? 2 : 1); ++c) {
        const int ct = ct0 + c;
        const auto s0 = __builtin_amdgcn_permlane16_swap(o[c][0][0], o[c][1][0], false, false);
        const auto s1 = __builtin_amdgcn_permlane16_swap(o[c][0][1], o[c][1][1], false, false);
        const size_t off = (((size_t)(4 * mt + tl) * 54 + (ct >> 1)) * 64 + i + 16 * (2 * (ct & 1) + (g >> 1))) * 16;
        st16(rs_, WS_H1F + off, (u32x4){s0[0], s1[0], s0[1], s1[1]});
      }
    }
    SB();
    __builtin_amdgcn_s_barrier();
    SB();
    constexpr int E0 = W2_INL - NW2L;
    auto signal = [&]() __attribute__((always_inline)) {
      if (lane == 0) {
        if (__hip_atomic_fetch_add(lcnt, 1u, __ATOMIC_RELAXED, __HIP_MEMORY_SCOPE_WORKGROUP) == 7u)
          __hip_atomic_store(flags + (FL_P / 4) * ng, 1u, __ATOMIC_RELAXED, __HIP_MEMORY_SCOPE_AGENT);
      }
    };
    if (w >= SIG_SPLIT) { asm volatile("s_waitcnt vmcnt(0)" ::: "memory"); signal(); }
    SB();
#pragma unroll
    for (int e = E0; e < NW2R; ++e) ring[(8 + e) % 32] = ld16(rs_, lo16, w2_off(e));
    SB();
    if (w < SIG_SPLIT) { asm volatile("s_waitcnt vmcnt(%0)" :: "n"(NW2R - E0) : "memory"); signal(); }
    SB();
  };
  if (two) gemm1(std::true_type{});
  else gemm1(std::false_type{});
  STAMP(2, 3);
#pragma nounroll
  for (unsigned spin = 0; spin < (1u << 20); ++spin) {
    if (__hip_atomic_load(flags + (FL_P / 4) * w, __ATOMIC_RELAXED, __HIP_MEMORY_SCOPE_AGENT) != 0u) break;
#define POLL_SL 1
    if (POLL_SL) __builtin_amdgcn_s_sleep(POLL_SL);
  }
  asm volatile("" ::: "memory");
  SB();

  STAMP(2, 4);
  const int h1s = (int)WS_H1F + (mt * 4 * 54 + ks0) * 1024;
  u32x4 hb[28];
  auto load_h1 = [&](int n) __attribute__((always_inline)) {
    const int q = n / 7, s = n % 7;
    const int sn = (s == 6 && !full) ? 5 : s;
    hb[n] = ld16_sc1(rs_, lo16, h1s + (q * 54 + sn) * 1024);
  };
#pragma unroll
  for (int n = 0; n < H1_PRE; ++n) {
    if (n == H1_BATCH) { SB(); __builtin_amdgcn_s_barrier(); SB(); }
    load_h1(n);
  }
  SB();
  float* red = (float*)smem;
  float* rb_ = red + (w * 16 + i) * KF_RS + 4 * g;
  const int re = w * 40 + min(lane, 39);
  const int rtk = re / 20, rq4 = re % 20;
  const float* rr_ = red + rtk * KF_RS + 4 * rq4;
  f32x4 ac2[2][5];
  f32x4 rv[8], rsum;
  u32x2 xv2;
  const f32x4 gb = *(const f32x4*)(tab + 4 * rq4);
  auto red_wr = [&](int q, int c) __attribute__((always_inline)) {
    *(f32x4*)(rb_ + (q & 1) * (KF_RB / 4) + 16 * c) = ac2[q & 1][c];
  };
  auto red_rd = [&](int q, int ww) __attribute__((always_inline)) {
    rv[ww] = *(const f32x4*)(rr_ + (q & 1) * (KF_RB / 4) + ww * 16 * KF_RS);
  };
  auto red_x = [&](int q) __attribute__((always_inline)) { xv2 = *(const u32x2*)(xr + (16 * q + rtk) * 176 + rq4 * 8); };
  auto red_out = [&](int q) __attribute__((always_inline)) {
    const f32x4 s = rsum + (rv[6] + rv[7]);
    u32x2 o;
#pragma unroll
    for (int h = 0; h < 2; ++h) {
      o[h] = pk_bf(fmaf(s[2 * h], gb[2 * h], bf_lo(xv2[h])), fmaf(s[2 * h + 1], gb[2 * h + 1], bf_hi(xv2[h])));
    }
    *(u32x2*)(out + (size_t)(mt * 64 + 16 * q + rtk) * D_ + ng * 80 + 4 * rq4) = o;
  };
#pragma unroll
  for (int q = 0; q < 3; ++q) {
#pragma unroll
    for (int c = 0; c < 5; ++c) ac2[q & 1][c] = (f32x4){0.f, 0.f, 0.f, 0.f};
#pragma unroll
    for (int s = 0; s < 7; ++s) {
      auto mm = [&](int c) __attribute__((always_inline)) {
        const u32x4 a = c == 4 ? wl[s] : ((s == 6 && c == 3) ? wl[7] : ring[(8 + 4 * s + c) % 32]);
        ac2[q & 1][c] = mfma_bf16(a, hb[7 * q + s], ac2[q & 1][c]);
      };
      if (s == 5 && q >= 2) {
        red_wr(q - 1, 4);
        SB();
        red_out(q - 2);
#pragma unroll
        for (int c = 0; c < 5; ++c) mm(c);
#pragma unroll
        for (int c = 0; c < 5; ++c) {
          __builtin_amdgcn_sched_group_barrier(0x008, 1, 0);
          __builtin_amdgcn_sched_group_barrier(0x002, 5, 0);
        }
        SB();
      } else if (s < 6) {
        mm(0); SB();
        if (q >= 1 && s >= 1) { red_wr(q - 1, s - 1); SB(); }
        mm(1); SB();
        if (q >= 2 && s < 4) { red_rd(q - 2, 2 * s); SB(); }
        mm(2); SB();
        if (q >= 2 && s < 4) { red_rd(q - 2, 2 * s + 1); SB(); }
        if (q >= 2 && s == 4) { red_x(q - 2); SB(); }
        mm(3); SB();
        if (q >= 2 && s >= 1 && s < 4) {
          const f32x4 t = rv[2 * s - 2] + rv[2 * s - 1];
          rsum = s == 1 ? t : rsum + t;
          asm volatile("" : "+v"(rsum));
          SB();
        }
        mm(4); SB();
      } else if (full) {
#pragma unroll
        for (int c = 0; c < 5; ++c) mm(c);
        SB();
      }
      if (7 * q + s + H1_PRE < 28) { load_h1(7 * q + s + H1_PRE); SB(); }
    }
    if (q >= 1) bar_lds();
    if (q == 0) STAMP(2, 5);
  }
  {
    auto sum6 = [&]() __attribute__((always_inline)) {
      rsum = ((rv[0] + rv[1]) + (rv[2] + rv[3])) + (rv[4] + rv[5]);
    };
    if (full) {
#pragma unroll
      for (int c = 0; c < 5; ++c) {
        const u32x4 a = c == 4 ? wl[6] : (c == 3 ? wl[7] : ring[(8 + 24 + c) % 32]);
        rv[c] = mfma_bf16(a, hb[27], (f32x4){0.f, 0.f, 0.f, 0.f});
      }
    } else {
#pragma unroll
      for (int c = 0; c < 5; ++c) rv[c] = (f32x4){0.f, 0.f, 0.f, 0.f};
    }
    f32x4 c0[5];
#pragma unroll
    for (int c = 0; c < 5; ++c) c0[c] = rv[c];
    SB();
    auto col = [&](int c, auto&& between) __attribute__((always_inline)) {
#pragma unroll
      for (int s = 0; s < 6; ++s) {
        const u32x4 a = c == 4 ? wl[s] : ring[(8 + 4 * s + c) % 32];
        c0[c] = mfma_bf16(a, hb[21 + s], c0[c]);
        SB();
        between(s);
        SB();
      }
    };
    col(0, [&](int s) __attribute__((always_inline)) { if (s < 5) red_wr(2, s); if (s < 4) { red_rd(1, 2 * s); red_rd(1, 2 * s + 1); } if (s == 4) red_x(1); });
    col(1, [&](int s) __attribute__((always_inline)) { if (s == 1) { sum6(); asm volatile("" : "+v"(rsum)); } if (s == 3) red_out(1); });
    bar_lds();
    auto wr3 = [&](int c) __attribute__((always_inline)) { *(f32x4*)(rb_ + (KF_RB / 4) + 16 * c) = c0[c]; };
    col(2, [&](int s) __attribute__((always_inline)) { if (s < 2) wr3(s); if (s >= 1 && s < 5) { red_rd(2, 2 * s - 2); red_rd(2, 2 * s - 1); } if (s == 5) red_x(2); });
    col(3, [&](int s) __attribute__((always_inline)) { if (s == 1) wr3(2); if (s == 2) { sum6(); asm volatile("" : "+v"(rsum)); } if (s == 4) red_out(2); });
    col(4, [&](int s) __attribute__((always_inline)) { if (s == 1) wr3(3); if (s == 2) red_x(3); });
    wr3(4);
    STAMP(2, 6);
    bar_lds();
#pragma unroll
    for (int ww = 0; ww < 8; ++ww) red_rd(3, ww);
    sum6();
    red_out(3);
    VM0();
    STAMP(2, 7);
  }
}

__global__ NOPK __launch_bounds__(512, 2) void kf(char* __restrict__ ws, unsigned short* __restrict__ out) {
  __shared__ __attribute__((aligned(16))) char smem[KF_LDS];
  TS(2, ws);
  kf_body(ws, out, smem);
}

__global__ void kprobe(char* ws, int which) {
  const unsigned long long now = __builtin_amdgcn_s_memrealtime();
  const unsigned long long* t = (const unsigned long long*)(ws + (200u << 20));
  unsigned long long d = which == 0 ? t[1] - t[0] : (which == 1 ? t[2] - t[1] : (which == 2 ? now - t[2] : 0ull));
  if (d > 10000ull) d = 10000ull;
  while (__builtin_amdgcn_s_memrealtime() - now < 10ull * d) __builtin_amdgcn_s_sleep(1);
}
__global__ void kend_() {}

extern "C" void kernel_launch(void* const* d_in, const int* in_sizes, int n_in,
                              void* d_out, int out_size, void* d_ws, size_t ws_size,
                              hipStream_t stream) {
  K1Args a;
  a.x = (const unsigned short*)d_in[0];
  a.are = (const unsigned short*)d_in[1];
  a.aim = (const unsigned short*)d_in[2];
  a.bre = (const unsigned short*)d_in[3];
  a.bim = (const unsigned short*)d_in[4];
  a.cre = (const unsigned short*)d_in[5];
  a.cim = (const unsigned short*)d_in[6];
  a.logdt = (const unsigned short*)d_in[7];
  a.c = (const unsigned short*)d_in[12];
  a.wmod = (const unsigned short*)d_in[13];
  a.ws = (char*)d_ws;
  k1<<<NB_PF + NB_K + NB_LN + NB_MOD, 256, 0, stream>>>(a);
  k2<<<256, 512, 0, stream>>>((const unsigned short*)d_in[8], (char*)d_ws, (const unsigned short*)d_in[10],
                              (const unsigned short*)d_in[11]);
  kf<<<256, 512, 0, stream>>>((char*)d_ws, (unsigned short*)d_out);
  kprobe<<<1, 64, 0, stream>>>((char*)d_ws, 0);
}
```

```cpp
#define LNFLIP 2
#define DL_LN 0
#define DL_MOD 12
#include <hip/hip_runtime.h>
#include <hip/hip_bf16.h>
#include <stdint.h>
#include <type_traits>

typedef __attribute__((ext_vector_type(8))) __bf16 bf16x8;
typedef __attribute__((ext_vector_type(2))) __bf16 bf16x2;
typedef __attribute__((ext_vector_type(4))) float f32x4;
typedef __attribute__((ext_vector_type(2))) float f32x2;
typedef __attribute__((ext_vector_type(4))) unsigned u32x4;
typedef __attribute__((ext_vector_type(2))) unsigned u32x2;
typedef __attribute__((ext_vector_type(8))) _Float16 h16x8;
typedef __attribute__((ext_vector_type(2))) _Float16 h16x2;
typedef __attribute__((ext_vector_type(4))) short s16x4;
typedef __attribute__((address_space(3))) s16x4 lds_s16x4;

#if defined(__HIP_DEVICE_COMPILE__)
#define NOPK __attribute__((target("no-packed-fp32-ops")))
#else
#define NOPK
#endif
#define DEV __device__ __forceinline__

#define W1L 0
constexpr int W1KS = W1L ? 1 : 108, W1TS = W1L ? 20 : 1;
#define KMAP 0
constexpr int L_ = 2048;
constexpr int D_ = 640;
constexpr int NS = 128;
constexpr int FF = 1728;
constexpr int KREV = 1280;

constexpr size_t WS_GB   = 0;
constexpr size_t WS_KSC  = 8192;
#define FL_P 2048
#define FL_B 327680
constexpr size_t WS_CNT  = FL_B;
constexpr size_t WS_MOD  = 65536;
constexpr size_t WS_CSP  = 196608;
constexpr size_t WS_XNT  = 327680 + 256 * FL_P;
constexpr size_t WS_XT   = WS_XNT + (size_t)D_ * L_ * 2;
constexpr size_t WS_KREV = WS_XT + (size_t)D_ * L_ * 2;
constexpr size_t WS_W1F  = WS_KREV + (size_t)D_ * KREV * 2;
constexpr size_t WS_W2F  = WS_W1F + (size_t)D_ * FF * 2;
constexpr size_t WS_X1T  = WS_W2F + (size_t)D_ * FF * 2;
constexpr size_t WS_H1F  = WS_X1T + (size_t)D_ * L_ * 2;
constexpr size_t WS_END  = WS_H1F + (size_t)L_ * FF * 2;

#define STAMP(kid, k) do {} while (0)
#define CSTAMP(kid, k) do {} while (0)
#define VM0() do {} while (0)

#define TS(k, wsp) do { if (blockIdx.x == 0 && threadIdx.x == 0) __hip_atomic_store((unsigned long long*)((wsp) + (200u << 20)) + (k), (unsigned long long)__builtin_amdgcn_s_memrealtime(), __ATOMIC_RELAXED, __HIP_MEMORY_SCOPE_AGENT); } while (0)
DEV float bf_lo(unsigned u) { return __uint_as_float(u << 16); }
DEV float bf_hi(unsigned u) { return __uint_as_float(u & 0xffff0000u); }
DEV unsigned pk_bf(float a, float b) {
  f32x2 v = {a, b};
  bf16x2 r = __builtin_convertvector(v, bf16x2);
  return __builtin_bit_cast(unsigned, r);
}
DEV unsigned pk_h(float a, float b) {
  f32x2 v = {a, b};
  h16x2 r = __builtin_convertvector(v, h16x2);
  return __builtin_bit_cast(unsigned, r);
}
#define NTM 15
template <int BIT, class T> DEV T ldin(const void* p) {
  if (NTM & BIT) return __builtin_nontemporal_load((const T*)p);
  return *(const T*)p;
}
DEV float rbf(float a) { return bf_lo(pk_bf(a, 0.f)); }
DEV float ldbf(const unsigned short* p) { return __uint_as_float(((unsigned)*p) << 16); }
#define CONST_AS __attribute__((address_space(4)))
template <class T> DEV T ldu(const void* p) { return *(const CONST_AS T*)(size_t)p; }
DEV float ldbf_u(const unsigned short* p) {
  const unsigned v = ldu<unsigned>((const void*)((size_t)p & ~(size_t)3));
  return __uint_as_float(((size_t)p & 2) ? (v & 0xffff0000u) : (v << 16));
}

typedef __amdgpu_buffer_rsrc_t rsrc_t;
DEV rsrc_t ws_rsrc(char* ws) { return __builtin_amdgcn_make_buffer_rsrc((void*)ws, 0, (int)WS_END, 0x00020000); }
#define ST_AUX 16
DEV void st16(rsrc_t r, size_t off, u32x4 v) { __builtin_amdgcn_raw_buffer_store_b128(v, r, (int)off, 0, ST_AUX); }
DEV void st4(char* ws, size_t off, unsigned v) { __hip_atomic_store((unsigned*)(ws + off), v, __ATOMIC_RELAXED, __HIP_MEMORY_SCOPE_AGENT); }
DEV void st8(rsrc_t r, size_t off, u32x2 v) { __builtin_amdgcn_raw_buffer_store_b64(v, r, (int)off, 0, 16); }

DEV void bar_lds() { asm volatile("s_waitcnt lgkmcnt(0)\n\ts_barrier" ::: "memory"); }
DEV void lds_wait(const void* word, unsigned val) {
  unsigned tv, ts, cnt;
  asm volatile("s_mov_b32 %2, 0x100000\n"
               "1:\n\tds_read_b32 %0, %3\n\ts_waitcnt lgkmcnt(0)\n\tv_readfirstlane_b32 %1, %0\n\t"
               "s_cmp_ge_u32 %1, %4\n\ts_cbranch_scc1 2f\n\ts_sleep 1\n\ts_sub_u32 %2, %2, 1\n\t"
               "s_cmp_lg_u32 %2, 0\n\ts_cbranch_scc1 1b\n2:"
               : "=&v"(tv), "=&s"(ts), "=&s"(cnt) : "v"((unsigned)(size_t)word), "s"(val) : "memory", "scc");
}
DEV void lds_post(unsigned* word, int lane) {
  asm volatile("" ::: "memory");
  if (lane == 0) __hip_atomic_fetch_add(word, 1u, __ATOMIC_RELAXED, __HIP_MEMORY_SCOPE_WORKGROUP);
  asm volatile("" ::: "memory");
}
DEV void lgkm0() { asm volatile("s_waitcnt lgkmcnt(0)" ::: "memory"); __builtin_amdgcn_sched_barrier(0); }

DEV f32x4 mfma_bf16(u32x4 a, u32x4 b, f32x4 c) {
  return __builtin_amdgcn_mfma_f32_16x16x32_bf16(__builtin_bit_cast(bf16x8, a), __builtin_bit_cast(bf16x8, b), c, 0, 0, 0);
}

constexpr int NB_PF = 8, NB_K = 640, NB_LN = 256, NB_MOD = 120;

struct K1Args {
  const unsigned short *x, *are, *aim, *bre, *bim, *cre, *cim, *logdt, *c, *wmod;
  char* ws;
};

DEV void k1_prefetch() {
  const int tid = threadIdx.x;
  unsigned long long pc;
  int o0, o1;
  asm volatile("s_getpc_b64 %0\n.Lpf_here%=:\n\ts_mov_b32 %1, _Z2k16K1Args - .Lpf_here%=\n\ts_mov_b32 %2, _Z5kend_v - .Lpf_here%="
               : "=s"(pc), "=s"(o0), "=s"(o1));
  const char* c0 = (const char*)(pc + (long long)o0);
  long n = o1 - o0;
  n = n < 0 ? 0 : (n > 98304 ? 98304 : n);
  unsigned v[3];
#pragma unroll
  for (int it = 0; it < 3; ++it) {
    const long o = (long)tid * 128 + it * 32768;
    v[it] = o < n ? *(const __attribute__((address_space(1))) unsigned*)(c0 + o) : 0u;
  }
  asm volatile("" :: "v"(v[0] ^ v[1] ^ v[2]));
}

DEV void k1_mod(const K1Args& p, int b, char* smem) {
  const int tid = threadIdx.x;
  const int rg = b & 7, cgp = b >> 3;
  const int c32 = tid & 31, r8 = tid >> 5;
  float* sil = (float*)smem;
  float* red = (float*)(smem + 512);
  char* stamp_ws = p.ws; (void)stamp_ws;
  STAMP(0, 1);
  u32x4 w[10];
  const float cv = ldbf(p.c + rg * 80 + min(tid, 79));
  asm volatile("" ::: "memory");
#pragma unroll
  for (int it = 0; it < 10; ++it)
    w[it] = ldin<1, u32x4>(p.wmod + (size_t)(rg * 80 + r8 + 8 * it) * 3840 + 256 * cgp + 8 * c32);
  if (tid < 80) sil[tid] = rbf(cv / (1.f + expf(-cv)));
  if (tid >= 128 && tid < 132 && b < 64) st4(p.ws, WS_CNT + (size_t)(b * 4 + tid - 128) * FL_P, 0u);
  __syncthreads();
  STAMP(0, 2);
  float acc[8];
#pragma unroll
  for (int j = 0; j < 8; ++j) acc[j] = 0.f;
#pragma unroll
  for (int it = 0; it < 10; ++it) {
    float s = sil[r8 + 8 * it];
#pragma unroll
    for (int q = 0; q < 4; ++q) {
      acc[2 * q] += s * bf_lo(w[it][q]);
      acc[2 * q + 1] += s * bf_hi(w[it][q]);
    }
  }
#pragma unroll
  for (int j = 0; j < 8; ++j) acc[j] += __shfl_xor(acc[j], 32, 64);
  if ((tid & 63) < 32) {
    *(f32x4*)(red + (tid >> 6) * 256 + 8 * c32) = (f32x4){acc[0], acc[1], acc[2], acc[3]};
    *(f32x4*)(red + (tid >> 6) * 256 + 8 * c32 + 4) = (f32x4){acc[4], acc[5], acc[6], acc[7]};
  }
  __syncthreads();
  STAMP(0, 3);
  if (tid < 64) {
    const f32x4* r4 = (const f32x4*)red + tid;
    st16(ws_rsrc(p.ws), WS_MOD + (rg * 3840 + 256 * cgp + 4 * tid) * 4,
         __builtin_bit_cast(u32x4, (r4[0] + r4[64]) + (r4[128] + r4[192])));
  }
}
struct ModP { f32x4 a, b; };
DEV ModP mod_ld(rsrc_t rs, int idx) {
  ModP m;
#pragma unroll
  for (int j = 0; j < 4; ++j) {
    m.a[j] = __uint_as_float(__builtin_amdgcn_raw_buffer_load_b32(rs, 4 * idx, (int)WS_MOD + 15360 * j, 0));
    m.b[j] = __uint_as_float(__builtin_amdgcn_raw_buffer_load_b32(rs, 4 * idx, (int)WS_MOD + 15360 * (4 + j), 0));
  }
  return m;
}
DEV float mod_fin(const ModP& m) {
  return rbf(((m.a[0] + m.a[1]) + (m.a[2] + m.a[3])) + ((m.b[0] + m.b[1]) + (m.b[2] + m.b[3])));
}
#define SB() __builtin_amdgcn_sched_barrier(0)

DEV void k1_ln(const K1Args& p, int b, char* smem) {
  constexpr int STR = 704;
  const int tid = threadIdx.x;
  const int tok = tid >> 3, sub = tid & 7;
  const int tile = (b & 7) * 8 + ((b >> 3) & 7), pass = (b >> 6) ^ LNFLIP;
  const int hf = pass & 1;
  const bool norm = pass < 2;
  const int t0 = tile * 32;
  const unsigned short* xr = p.x + (size_t)(t0 + tok) * D_ + 8 * sub;
  u32x4 xv[5], xo[5];
  char* stamp_ws = p.ws; (void)stamp_ws;
  STAMP(0, 1);
#pragma unroll
  for (int m = 0; m < 5; ++m) xv[m] = ldin<2, u32x4>(xr + 320 * hf + 64 * m);
  if (norm) {
#pragma unroll
    for (int m = 0; m < 5; ++m) xo[m] = ldin<2, u32x4>(xr + 320 * (hf ^ 1) + 64 * m);
    float s = 0.f, v = 0.f;
    const bf16x2 one2 = __builtin_bit_cast(bf16x2, 0x3f803f80u);
#pragma unroll
    for (int m = 0; m < 10; ++m)
#pragma unroll
      for (int q = 0; q < 4; ++q) {
        const unsigned u = m < 5 ? xv[m][q] : xo[m - 5][q];
        const bf16x2 x2 = __builtin_bit_cast(bf16x2, u);
        s = __builtin_amdgcn_fdot2_f32_bf16(x2, one2, s, false);
        v = __builtin_amdgcn_fdot2_f32_bf16(x2, x2, v, false);
      }
#pragma unroll
    for (int o = 1; o < 8; o <<= 1) { s += __shfl_xor(s, o, 64); v += __shfl_xor(v, o, 64); }
    const float mean = s * (1.f / D_);
    const float var = fmaxf(v * (1.f / D_) - mean * mean, 0.f);
    const float sd = rbf(sqrtf(rbf(rbf(var) + 1e-5f)));
    const float rs = 1.f / sd;
    const float mb = rbf(mean);
#pragma unroll
    for (int m = 0; m < 5; ++m)
#pragma unroll
      for (int q = 0; q < 4; ++q) {
        xv[m][q] = pk_h((bf_lo(xv[m][q]) - mb) * rs, (bf_hi(xv[m][q]) - mb) * rs);
      }
  }
  const int lane = tid & 63, w = tid >> 6;
  const int g = lane >> 4, i = lane & 15;
  const rsrc_t rsrc = ws_rsrc(p.ws);
#pragma unroll
  for (int m = 0; m < 5; ++m) *(u32x4*)(smem + tok * STR + (sub + 8 * m) * 16) = xv[m];
  __syncthreads();
  STAMP(0, 2);
  const size_t dsto = (norm ? WS_XNT : WS_XT) + ((size_t)tile * D_ + 320 * hf) * 64;
#pragma unroll
  for (int it = 0; it < 5; ++it) {
    const int cb = w * 5 + it;
    const char* a = smem + (8 * g + (i >> 2)) * STR + cb * 32 + (i & 3) * 8;
    const u32x2 ra = __builtin_bit_cast(u32x2, __builtin_amdgcn_ds_read_tr16_b64_v4i16((lds_s16x4*)a));
    const u32x2 rb = __builtin_bit_cast(u32x2, __builtin_amdgcn_ds_read_tr16_b64_v4i16((lds_s16x4*)(a + 4 * STR)));
    st16(rsrc, dsto + (size_t)(cb * 16 + i) * 64 + 16 * g, (u32x4){ra[0], ra[1], rb[0], rb[1]});
  }
}

constexpr int RT_STR = 288;
struct RtRaw { u32x4 r[8]; };
template <int NF>
DEV void rt_load(RtRaw& t, const unsigned short* W, int N, int kk, int ct0, int lane) {
#pragma unroll
  for (int j = 0; j < NF; ++j) {
    const int c = lane + 64 * j, row = c / (2 * NF), part = c % (2 * NF);
    t.r[j] = ldin<4, u32x4>(W + (size_t)(32 * kk + row) * N + 16 * ct0 + 8 * part);
  }
}
template <int NF>
DEV void rt_park(const RtRaw& t, char* scr, int lane) {
#pragma unroll
  for (int j = 0; j < NF; ++j) {
    const int c = lane + 64 * j, row = c / (2 * NF), part = c % (2 * NF);
    *(u32x4*)(scr + row * RT_STR + 16 * part) = t.r[j];
  }
}
DEV u32x4 rt_frag(const char* scr, int j, int lane) {
  const int g = lane >> 4, i = lane & 15;
  const char* p = scr + (8 * g + (i >> 2)) * RT_STR + 32 * j + (i & 3) * 8;
  const u32x2 a = __builtin_bit_cast(u32x2, __builtin_amdgcn_ds_read_tr16_b64_v4i16((lds_s16x4*)p));
  const u32x2 b = __builtin_bit_cast(u32x2, __builtin_amdgcn_ds_read_tr16_b64_v4i16((lds_s16x4*)(p + 4 * RT_STR)));
  return (u32x4){a[0], a[1], b[0], b[1]};
}
struct cf { float re, im; };
DEV cf cmul(cf a, cf b) {
  cf r;
  r.re = a.re * b.re - a.im * b.im;
  r.im = a.re * b.im + a.im * b.re;
  return r;
}
DEV cf cexp_(float a2, float br, float m, float m0) {
  float mag = __builtin_amdgcn_exp2f(a2 * (m - m0));
  float ph = __builtin_amdgcn_fractf(br * m);
  cf r; r.re = mag * __builtin_amdgcn_cosf(ph); r.im = mag * __builtin_amdgcn_sinf(ph);
  return r;
}
DEV f32x4 mfma_f16(u32x4 a, u32x4 b, f32x4 c) {
  return __builtin_amdgcn_mfma_f32_16x16x32_f16(__builtin_bit_cast(h16x8, a), __builtin_bit_cast(h16x8, b), c, 0, 0, 0);
}
DEV void put2(char* p, cf a, cf b) {
  u32x2 o = {pk_h(a.re, a.im), pk_h(b.re, b.im)};
  *(u32x2*)p = o;
}

constexpr int KG_RS = 544;
DEV void k1_kgen(const K1Args& p, int d, char* smem) {
  constexpr int RS = KG_RS;
  const int tid = threadIdx.x;
  const int lane = tid & 63;
  const int w = __builtin_amdgcn_readfirstlane(tid >> 6);
  const int g = lane >> 4, i = lane & 15;
  const size_t po = (size_t)d * NS + 2 * lane;
  const unsigned rar = ldin<8, unsigned>(p.are + po), rai = ldin<8, unsigned>(p.aim + po);
  unsigned rbr = 0u, rbi = 0u, rcr = 0u, rci = 0u;
  if (w == 0) {
    rbr = ldin<8, unsigned>(p.bre + po); rbi = ldin<8, unsigned>(p.bim + po);
    rcr = ldin<8, unsigned>(p.cre + po); rci = ldin<8, unsigned>(p.cim + po);
  }
  const float ldt = ldbf_u(p.logdt + d);
  asm volatile("" ::: "memory");
  SB();
  char* stamp_ws = p.ws; (void)stamp_ws;
  const float dt = rbf(expf(ldt));
  asm volatile("" :: "v"(dt));
  STAMP(0, 1);
  float a2[2], bv[2], m0[2];
  a2[0] = (dt * bf_lo(rar)) * 1.44269504088896341f; a2[1] = (dt * bf_hi(rar)) * 1.44269504088896341f;
  bv[0] = (dt * bf_lo(rai)) * 0.15915494309189534f; bv[1] = (dt * bf_hi(rai)) * 0.15915494309189534f;
  m0[0] = a2[0] > 0.f ? 2048.f : 0.f; m0[1] = a2[1] > 0.f ? 2048.f : 0.f;
  char* my = smem + lane * 8;
  if (w == 0) {
    cf v[2], v8[2], e8[2];
    float bound = 0.f;
#pragma unroll
    for (int q = 0; q < 2; ++q) {
      const float dbr = dt * (q ? bf_hi(rbr) : bf_lo(rbr)), dbi = dt * (q ? bf_hi(rbi) : bf_lo(rbi));
      const float cr = q ? bf_hi(rcr) : bf_lo(rcr), ci = q ? bf_hi(rci) : bf_lo(rci);
      const float gsh = __builtin_amdgcn_exp2f(a2[q] * m0[q]);
      v[q].re = (cr * dbr - ci * dbi) * gsh; v[q].im = -(cr * dbi + ci * dbr) * gsh;
      bound += (fabsf(v[q].re) + fabsf(v[q].im)) * __builtin_amdgcn_exp2f(fmaxf(a2[q], 0.f) * 120.f);
      e8[q] = cexp_(a2[q], -bv[q], 8.f, 0.f);
      v8[q] = cexp_(a2[q], -bv[q], 64.f, 0.f);
    }
#pragma unroll
    for (int o = 1; o < 64; o <<= 1) bound += __shfl_xor(bound, o, 64);
    int ex = (int)((__float_as_uint(bound) >> 23) & 0xff);
    ex = ex < 20 ? 20 : (ex > 240 ? 240 : ex);
    const float sv = __uint_as_float((unsigned)(127 + 13 + 127 - ex) << 23);
    if (lane == 0) st4(p.ws, WS_KSC + 4 * d, (unsigned)(ex - 13) << 23);
#pragma unroll
    for (int q = 0; q < 2; ++q) { v[q].re *= sv; v[q].im *= sv; v8[q] = cmul(v[q], v8[q]); }
#pragma unroll
    for (int j = 0; j < 8; ++j) {
      put2(my + (41 + j) * RS, v[0], v[1]);
      put2(my + (49 + j) * RS, v8[0], v8[1]);
      if (j < 7) {
#pragma unroll
        for (int q = 0; q < 2; ++q) { v[q] = cmul(v[q], e8[q]); v8[q] = cmul(v8[q], e8[q]); }
      }
    }
  } else if (w == 1) {
    cf e1[2], u[2], u2[2];
#pragma unroll
    for (int q = 0; q < 2; ++q) {
      e1[q] = cexp_(a2[q], bv[q], 1.f, 0.f);
      u[q].re = __builtin_amdgcn_exp2f(-a2[q] * m0[q]); u[q].im = 0.f;
      u2[q] = cexp_(a2[q], bv[q], 128.f, m0[q]);
    }
#pragma unroll
    for (int r = 0; r < 8; ++r) {
      put2(my + r * RS, u[0], u[1]);
      put2(my + (8 + r) * RS, u2[0], u2[1]);
      if (r < 7) {
#pragma unroll
        for (int q = 0; q < 2; ++q) { u[q] = cmul(u[q], e1[q]); u2[q] = cmul(u2[q], e1[q]); }
      }
    }
  } else if (w == 2) {
    cf e2[2], u[2], u2[2];
#pragma unroll
    for (int q = 0; q < 2; ++q) {
      e2[q] = cexp_(a2[q], bv[q], 2.f, 0.f);
      u[q] = cexp_(a2[q], bv[q], 256.f, m0[q]);
      u2[q] = cexp_(a2[q], bv[q], 384.f, m0[q]);
    }
#pragma unroll
    for (int r = 0; r < 4; ++r) {
      put2(my + (16 + r) * RS, u[0], u[1]);
      put2(my + (20 + r) * RS, u2[0], u2[1]);
      if (r < 3) {
#pragma unroll
        for (int q = 0; q < 2; ++q) { u[q] = cmul(u[q], e2[q]); u2[q] = cmul(u2[q], e2[q]); }
      }
    }
    cf e4[2] = {cmul(e2[0], e2[0]), cmul(e2[1], e2[1])};
#pragma unroll
    for (int r = 0; r < 2; ++r) {
      cf c0 = cexp_(a2[0], bv[0], 512.f + 128.f * r, m0[0]), c1 = cexp_(a2[1], bv[1], 512.f + 128.f * r, m0[1]);
      put2(my + (24 + 2 * r) * RS, c0, c1);
      put2(my + (25 + 2 * r) * RS, cmul(c0, e4[0]), cmul(c1, e4[1]));
    }
  } else {
    cf e4[2], e128[2], u[2], u2[2];
#pragma unroll
    for (int q = 0; q < 2; ++q) {
      e4[q] = cexp_(a2[q], bv[q], 4.f, 0.f);
      e128[q] = cexp_(a2[q], bv[q], 128.f, 0.f);
      u[q] = cexp_(a2[q], bv[q], 1024.f, m0[q]);
      u2[q] = cexp_(a2[q], bv[q], 1536.f, m0[q]);
    }
#pragma unroll
    for (int r = 2; r < 4; ++r) {
      cf c0 = cexp_(a2[0], bv[0], 512.f + 128.f * r, m0[0]), c1 = cexp_(a2[1], bv[1], 512.f + 128.f * r, m0[1]);
      put2(my + (24 + 2 * r) * RS, c0, c1);
      put2(my + (25 + 2 * r) * RS, cmul(c0, e4[0]), cmul(c1, e4[1]));
    }
#pragma unroll
    for (int r = 0; r < 4; ++r) {
      put2(my + (32 + r) * RS, u[0], u[1]);
      put2(my + (36 + r) * RS, u2[0], u2[1]);
#pragma unroll
      for (int q = 0; q < 2; ++q) { if (r < 3) u[q] = cmul(u[q], e128[q]); u2[q] = cmul(u2[q], e128[q]); }
    }
    put2(my + 40 * RS, u2[0], u2[1]);
  }
  __syncthreads();
  STAMP(0, 2);
  _Float16* kh = (_Float16*)(smem + 57 * RS);
  if (w < 3) {
    const char* ua = smem + (16 * w + i) * RS + g * 16;
    const char* va = smem + (41 + i) * RS + g * 16;
    f32x4 acc = {0.f, 0.f, 0.f, 0.f};
#pragma unroll
    for (int ks = 0; ks < 8; ++ks)
      acc = mfma_f16(*(const u32x4*)(ua + ks * 64), *(const u32x4*)(va + ks * 64), acc);
    int idx0, st;
    if (w == 0) { idx0 = (g < 2 ? 4 * g : 120 + 4 * g) + 8 * i; st = 1; }
    else if (w == 1) { idx0 = g < 2 ? 256 + 64 * g + 4 * i : 384 + 64 * (g - 2) + 2 * i; st = 1; }
    else { idx0 = 512 + 64 * g + i; st = 16; }
#pragma unroll
    for (int rg = 0; rg < 4; ++rg) {
      int idx = idx0 + st * rg;
      if (w == 1 && g >= 2) idx = idx0 + 32 * (rg >> 1) + (rg & 1);
      if (w == 2 && g >= 2) idx = (g == 2 && rg == 0 && i == 0) ? 640 : 644;
      kh[idx] = (_Float16)acc[rg];
    }
  }
  __syncthreads();
  STAMP(0, 3);
  const unsigned short* ku = (const unsigned short*)kh;
  const size_t kout = WS_KREV + (size_t)d * KREV * 2;
  {
    const int l0 = 2040 - 8 * tid;
    u32x4 o;
    if (w < 2) {
      const int q = l0 >> 3;
      const unsigned a = ku[384 + q], b = ku[385 + q];
      __builtin_amdgcn_raw_buffer_store_b32(a | (b << 16), ws_rsrc(p.ws), (int)(kout + 4 * tid), 0, 16);
      return;
    } else if (w == 2) {
      const int q = l0 >> 2;
      const unsigned a = ku[256 + q], b = ku[257 + q], c = ku[258 + q];
      o[0] = c | (c << 16); o[1] = b | (b << 16); o[2] = b | (a << 16); o[3] = a | (a << 16);
    } else {
      unsigned v[8];
#pragma unroll
      for (int e = 0; e < 8; ++e) {
        const int l = l0 + 7 - e;
        const int idx = l < 256 ? l : (int)(pk_bf((float)l, 0.f) & 0xffffu) - 17024;
        v[e] = ku[idx];
      }
#pragma unroll
      for (int q = 0; q < 4; ++q) o[q] = v[2 * q] | (v[2 * q + 1] << 16);
    }
    st16(ws_rsrc(p.ws), kout + 16 * tid - 1536, o);
  }
}

__global__ NOPK __launch_bounds__(256) void k1(K1Args p) {
  __shared__ __attribute__((aligned(16))) char smem[57 * KG_RS + 1312];
  int b = blockIdx.x;
  TS(0, p.ws);
  char* stamp_ws = p.ws; (void)stamp_ws;
  STAMP(0, 0);
  if (b < NB_PF) { k1_prefetch(); STAMP(0, 7); return; }
  b -= NB_PF;
  if (b < NB_K) { k1_kgen(p, KMAP ? (b & 7) * 80 + (b >> 3) : b, smem); VM0(); STAMP(0, 7); return; }
  b -= NB_K;
  asm volatile("" :: "s"(p.ws), "s"(p.x));
  if (b < NB_LN) { __builtin_amdgcn_s_sleep(DL_LN); k1_ln(p, b, smem); }
  else { __builtin_amdgcn_s_sleep(DL_MOD); k1_mod(p, b - NB_LN, smem); }
  VM0();
  STAMP(0, 7);
}

constexpr int K2_CS = 4160;
constexpr int K2_KOFF = 4 * K2_CS;
constexpr int K2_CH = K2_KOFF + 4608;
constexpr int CV_RED = 3 * K2_CH;
constexpr int CV_RT2 = 86016;
constexpr int CV_SUM = CV_RT2 + 4 * (32 * RT_STR + 256);
constexpr int CV_XCH = CV_SUM + 2560;
constexpr int CV_FL = CV_RED + 20480;

struct Chan { float sa, ca1, ga, dsk, hs, hinv, gy, gd; };
struct ChanRaw { float m, kinv, dsk; };
DEV ChanRaw chan_ld(const unsigned short* dskip, const char* ws, int d, int lane) {
  ChanRaw c;
  c.kinv = ldu<float>(ws + WS_KSC + 4 * d);
  c.dsk = ldbf_u(dskip + d);
  c.m = ((const float*)(ws + WS_MOD))[3840 * (lane & 7) + D_ * ((lane >> 3) & 3) + d];
  return c;
}
template <int CTRL> DEV float dpp_add(float v) {
  return v + __int_as_float(__builtin_amdgcn_update_dpp(0, __float_as_int(v), CTRL, 0xf, 0xf, true));
}
DEV Chan chan_fin(const ChanRaw& r) {
  Chan c;
  const int t = __float_as_int(rbf(dpp_add<0x141>(dpp_add<0x4e>(dpp_add<0xb1>(r.m)))));
  c.sa = __int_as_float(__builtin_amdgcn_readlane(t, 0));
  c.ca1 = rbf(1.f + __int_as_float(__builtin_amdgcn_readlane(t, 8)));
  c.ga = __int_as_float(__builtin_amdgcn_readlane(t, 16));
  c.dsk = r.dsk;
  const float hb = 26.f * fabsf(c.ca1) + fabsf(c.sa);
  int ex = (int)((__float_as_uint(hb) >> 23) & 0xff);
  ex = ex < 20 ? 20 : (ex > 240 ? 240 : ex);
  c.hs = __uint_as_float((unsigned)(127 + 14 + 127 - ex) << 23);
  c.hinv = __uint_as_float((unsigned)(ex - 14) << 23);
  c.gy = r.kinv * c.hinv * c.ga;
  c.gd = c.hinv * c.dsk * c.ga;
  return c;
}
DEV size_t tl_off(int d, int t) { return (((size_t)(t >> 5) * D_ + d) * 32 + (t & 31)) * 2; }
struct StageRaw { u32x4 xa[2], kv1; u32x2 xb; unsigned kv0; };
DEV StageRaw stage_ld(const char* ws, int d, int th) {
  StageRaw s;
  const unsigned short* krev = (const unsigned short*)(ws + WS_KREV) + (size_t)d * KREV;
  const int T = 1024 * (th >> 6) + 8 * (th & 63);
  s.xb = ldu<u32x2>(ws + WS_XNT + tl_off(d, 1024));
  s.xa[0] = *(const u32x4*)(ws + WS_XNT + tl_off(d, T)); s.xa[1] = *(const u32x4*)(ws + WS_XNT + tl_off(d, T + 512));
  s.kv0 = *(const unsigned*)(krev + 2 * th); s.kv1 = *(const u32x4*)(krev + 256 + 8 * th);
  return s;
}
DEV void stage_wr(const StageRaw& s, const Chan& c, char* smem, int th) {
  constexpr int CS = K2_CS, KOFF = K2_KOFF;
  const h16x2 c2 = __builtin_bit_cast(h16x2, pk_h(c.ca1 * c.hs, c.ca1 * c.hs)), s2 = __builtin_bit_cast(h16x2, pk_h(c.sa * c.hs, c.sa * c.hs));
  auto mod = [&](unsigned u) __attribute__((always_inline)) {
    return __builtin_bit_cast(unsigned, __builtin_elementwise_fma(__builtin_bit_cast(h16x2, u), c2, s2));
  };
  unsigned h2[2][6];
#pragma unroll
  for (int e = 0; e < 2; ++e)
#pragma unroll
    for (int q = 0; q < 4; ++q) h2[e][q] = mod(s.xa[e][q]);
#pragma unroll
  for (int q = 0; q < 2; ++q) {
    h2[0][4 + q] = __builtin_amdgcn_update_dpp(__builtin_amdgcn_readfirstlane(h2[1][q]), h2[0][q], 0x130, 0xf, 0xf, false);
    h2[1][4 + q] = __builtin_amdgcn_update_dpp(mod(s.xb[q]), h2[1][q], 0x130, 0xf, 0xf, false);
  }
  char* dst = smem + (128 * (th >> 6) + (th & 63) + 2) * 16;
  unsigned s1[5];
#pragma unroll
  for (int e = 1; e >= 0; --e) {
    const unsigned* hh = h2[e];
#pragma unroll
    for (int q = 0; q < 5; ++q) s1[q] = __builtin_amdgcn_alignbit(hh[q + 1], hh[q], 16);
    *(u32x4*)(dst + 1024 * e) = (u32x4){hh[0], hh[1], hh[2], hh[3]};
    *(u32x4*)(dst + 1024 * e + CS) = (u32x4){s1[0], s1[1], s1[2], s1[3]};
    *(u32x4*)(dst + 1024 * e + 2 * CS) = (u32x4){hh[1], hh[2], hh[3], hh[4]};
    *(u32x4*)(dst + 1024 * e + 3 * CS) = (u32x4){s1[1], s1[2], s1[3], s1[4]};
  }
  {
    const unsigned a = __builtin_amdgcn_readfirstlane(h2[0][0]), b = __builtin_amdgcn_readfirstlane(s1[0]);
    if (th >= 1 && th < 4)
      *(u32x4*)(smem + th * CS + 16) = (u32x4){0u, 0u, th == 3 ? a << 16 : 0u, th == 1 ? a << 16 : (th == 2 ? a : b)};
  }
  {
    const unsigned aa = __builtin_amdgcn_perm(s.kv0, s.kv0, 0x01000100u), bb = __builtin_amdgcn_perm(s.kv0, s.kv0, 0x03020302u);
    const unsigned ba = __builtin_amdgcn_perm(s.kv0, s.kv0, 0x01000302u);
    *(u32x4*)(smem + KOFF + 16 * th) = (u32x4){bb, (th & 1) ? ba : bb, aa, aa};
  }
  *(u32x4*)(smem + KOFF + 2048 + 16 * th) = s.kv1;
}
DEV void stage_zero(char* smem, int th) {
  if (th < 37) *(u32x4*)(smem + (th < 32 ? K2_KOFF + 4096 + 16 * th : (th < 36 ? (th - 32) * K2_CS : 16))) = (u32x4){0u, 0u, 0u, 0u};
}
DEV f32x4 conv_out(const f32x4& y, const Chan& c, const char* smem, int t) {
  const u32x2 hv = *(const u32x2*)(smem + 2 * (t + 16));
  f32x4 o;
#pragma unroll
  for (int e = 0; e < 2; ++e) {
    const unsigned hu = hv[e];
    const h16x2 h2 = __builtin_bit_cast(h16x2, hu);
    o[2 * e] = fmaf((float)h2[0], c.gd, y[2 * e] * c.gy);
    o[2 * e + 1] = fmaf((float)h2[1], c.gd, y[2 * e + 1] * c.gy);
  }
  return o;
}
DEV unsigned add_bf(unsigned x, float a, float b) { return pk_bf(bf_lo(x) + a, bf_hi(x) + b); }
#define K2_LOAD(buf, cc, u)                                                                                     \
  asm volatile("ds_read_b64 %0, %3 offset:%5\n\tds_read_b64 %1, %3 offset:%6\n\tds_read_b128 %2, %4 offset:%7"   \
               : "=&v"(Al[buf][u]), "=&v"(Ah[buf][u]), "=&v"(Bf[buf][u])                                          \
               : "v"(ap0), "v"(bp0), "i"(64 * (cc) + 512 * (u)), "i"(64 * (cc) + 512 * (u) + 8),                 \
                 "i"(64 * (cc) + 3584 - 512 * (u)))
template <int SET> DEV constexpr int half_tile(int j) {
  return SET == 0 ? (j == 0 ? 7 : (j == 1 ? 6 : 2)) : (j < 3 ? 5 - j : 4 - j);
}
template <int SET>
DEV void conv_half(unsigned ap0, unsigned bp0, char* redw) {
  constexpr int NU = SET == 0 ? 8 : 6, NT = SET == 0 ? 3 : 5;
  u32x2 Al[2][NU], Ah[2][NU];
  u32x4 Bf[2][NU];
#pragma unroll
  for (int u = 0; u < NU; ++u) K2_LOAD(0, 0, u);
  f32x4 acc[NT];
#pragma unroll
  for (int j = 0; j < NT; ++j) acc[j] = (f32x4){0.f, 0.f, 0.f, 0.f};
#pragma unroll
  for (int cc = 0; cc < 2; ++cc) {
    lgkm0();
    if (cc == 0) {
#pragma unroll
      for (int u = 0; u < NU; ++u) K2_LOAD(1, 1, u);
    }
#pragma unroll
    for (int sp = 0; sp < NU; ++sp)
#pragma unroll
      for (int j = 0; j < NT; ++j) {
        const int q = half_tile<SET>(j);
        if (sp <= q) {
          const u32x4 A = {Al[cc][sp][0], Al[cc][sp][1], Ah[cc][sp][0], Ah[cc][sp][1]};
          acc[j] = mfma_f16(A, Bf[cc][q - sp], acc[j]);
        }
      }
    SB();
  }
#pragma unroll
  for (int j = 0; j < NT; ++j) *(f32x4*)(redw + j * 4096) = acc[j];
}

template <int NF>
DEV void w1_fin(const RtRaw& t, const ModP& mp, char* ws, int kk, int ct0, int lane, char* scr, float* sums) {
  const int g = lane >> 4, i = lane & 15;
  float* tb = (float*)(scr + 32 * RT_STR);
  const float mv = mod_fin(mp);
  tb[lane] = lane < 32 ? rbf(1.f + mv) : mv;
  rt_park<NF>(t, scr, lane);
  lgkm0();
  const f32x4 ca = *(const f32x4*)(tb + 8 * g), cb = *(const f32x4*)(tb + 8 * g + 4);
  const f32x4 sa = *(const f32x4*)(tb + 32 + 8 * g), sb = *(const f32x4*)(tb + 36 + 8 * g);
  const u32x4 sv = {pk_bf(sa[0], sa[1]), pk_bf(sa[2], sa[3]), pk_bf(sb[0], sb[1]), pk_bf(sb[2], sb[3])};
  const u32x4 ones = {0x3f803f80u, 0x3f803f80u, 0x3f803f80u, 0x3f803f80u};
  const f32x4 z = {0.f, 0.f, 0.f, 0.f};
  const rsrc_t rs = ws_rsrc(ws);
#pragma unroll
  for (int j = 0; j < NF; ++j) {
    const u32x4 f = rt_frag(scr, j, lane);
    const u32x4 sc = {pk_bf(bf_lo(f[0]) * ca[0], bf_hi(f[0]) * ca[1]), pk_bf(bf_lo(f[1]) * ca[2], bf_hi(f[1]) * ca[3]),
                      pk_bf(bf_lo(f[2]) * cb[0], bf_hi(f[2]) * cb[1]), pk_bf(bf_lo(f[3]) * cb[2], bf_hi(f[3]) * cb[3])};
    st16(rs, WS_W1F + ((size_t)(kk * W1KS + (ct0 + j) * W1TS) * 64 + lane) * 16, sc);
    const f32x4 cs = mfma_bf16(sc, ones, z), b1 = mfma_bf16(f, sv, z);
    if (i < 2) *(f32x4*)(sums + i * (16 * NF) + 16 * j + 4 * g) = i ? b1 : cs;
  }
}

DEV void conv_phase(const unsigned short* __restrict__ dskip, char* __restrict__ ws,
                    const unsigned short* __restrict__ w1, const unsigned short* __restrict__ w2, char* smem_all) {
  constexpr int CS = K2_CS, KOFF = K2_KOFF;
  const int tid = threadIdx.x, lane = tid & 63;
  const int w = __builtin_amdgcn_readfirstlane(tid >> 6);
  const int bx = blockIdx.x;
  const int pj = (bx & 7) * 16 + (bx >> 4), par = (bx >> 3) & 1;
  const int r = lane & 15, g = lane >> 4, th = tid & 127;
  const int s4 = (r + 1) & 3;
  const rsrc_t rs = ws_rsrc(ws);
  unsigned* fl = (unsigned*)(smem_all + CV_FL);
  char* stamp_ws = ws; (void)stamp_ws;
  STAMP(1, 0);
  asm volatile("" :: "s"(w1), "s"(w2));
  if (tid < 12) fl[tid] = 0u;
  if (w < 4) {
    const int d = 5 * pj + 2 * par + (w >> 1), hw = w & 1;
    char* smem = smem_all + (w >> 1) * K2_CH;
    const ChanRaw cr = chan_ld(dskip, ws, d, lane);
    SB();
    const StageRaw sr = stage_ld(ws, d, th);
    u32x4 xr4[2];
    u32x2 xres[4];
    auto ld_res = [&]() __attribute__((always_inline)) {
#pragma unroll
      for (int qp = 0; qp < 2; ++qp)
        xr4[qp] = *(const u32x4*)(ws + WS_XT + tl_off(d, 256 * (4 * hw + 2 * qp + (g & 1)) + 16 * r + 8 * (g >> 1)));
    };
    ld_res();
    stage_zero(smem, th);
    SB();
    asm volatile("s_waitcnt lgkmcnt(0)\n\ts_barrier" ::: "memory");
    SB();
    const Chan c = chan_fin(cr);
    STAMP(1, 1);
    stage_wr(sr, c, smem, th);
    lds_post(fl + 6 + (w >> 1), lane);
    lds_wait(fl + 6 + (w >> 1), 2u);
    STAMP(1, 2);
    f32x4 acc[8], keep[4];
#pragma unroll
    for (int q = 0; q < 8; ++q) acc[q] = (f32x4){0.f, 0.f, 0.f, 0.f};
    {
      const unsigned ap0 = (unsigned)(size_t)(smem + s4 * CS + 2 * (128 * hw + 8 * g + (r + 1) - s4));
      const unsigned bp0 = (unsigned)(size_t)(smem + KOFF + 2 * (2032 + 128 * hw - 16 * r + 8 * g) - 3584);
      u32x2 Al[2][8], Ah[2][8];
      u32x4 Bf[2][8];
#pragma unroll
      for (int u = 0; u < 8; ++u) K2_LOAD(0, 0, u);
      auto tile = [&](int cc, int q) __attribute__((always_inline)) {
#pragma unroll
        for (int sp = 0; sp <= q; ++sp) {
          const u32x4 A = {Al[cc & 1][sp][0], Al[cc & 1][sp][1], Ah[cc & 1][sp][0], Ah[cc & 1][sp][1]};
          acc[q] = mfma_f16(A, Bf[cc & 1][q - sp], acc[q]);
        }
        SB();
      };
#pragma unroll
      for (int cc = 0; cc < 3; ++cc) {
        lgkm0();
#pragma unroll
        for (int q = 0; q < 8; ++q) {
          K2_LOAD((cc + 1) & 1, cc + 1, q);
          tile(cc, q);
        }
      }
#pragma unroll
      for (int qp = 0; qp < 2; ++qp) {
        const auto s0 = __builtin_amdgcn_permlane16_swap(xr4[qp][0], xr4[qp][2], false, false);
        const auto s1 = __builtin_amdgcn_permlane16_swap(xr4[qp][1], xr4[qp][3], false, false);
        xres[2 * qp] = (u32x2){s0[0], s1[0]}; xres[2 * qp + 1] = (u32x2){s0[1], s1[1]};
      }
      lgkm0();
      f32x4* xw = (f32x4*)(smem_all + CV_XCH + w * 4096) + lane;
      auto last = [&](auto hw_tag) __attribute__((always_inline)) {
        constexpr int S = decltype(hw_tag)::value ? 0 : 4, K = 4 - S;
#pragma unroll
        for (int q = 0; q < 4; ++q) {
          tile(3, S + q);
          if (q) { xw[64 * (q - 1)] = acc[S + q - 1]; SB(); }
        }
        tile(3, K + 3);
        xw[64 * 3] = acc[S + 3];
        lds_post(fl + w, lane);
        SB();
#pragma unroll
        for (int q = 0; q < 3; ++q) tile(3, K + q);
#pragma unroll
        for (int q = 0; q < 4; ++q) keep[q] = acc[K + q];
      };
      if (hw) last(std::true_type{}); else last(std::false_type{});
    }
    STAMP(1, 3);
    lds_wait(fl + (w ^ 1), 1u);
    STAMP(1, 4);
    const f32x4* xrd = (const f32x4*)(smem_all + CV_XCH + (w ^ 1) * 4096) + lane;
#pragma unroll
    for (int qp = 0; qp < 2; ++qp) {
      u32x2 o[2];
#pragma unroll
      for (int h = 0; h < 2; ++h) {
        const int q = 2 * qp + h;
        const f32x4 v = conv_out(keep[q] + xrd[64 * q], c, smem, 256 * (4 * hw + q) + 16 * r + 4 * g);
        o[h] = (u32x2){add_bf(xres[q][0], v[0], v[1]), add_bf(xres[q][1], v[2], v[3])};
      }
      const auto s0 = __builtin_amdgcn_permlane16_swap(o[0][0], o[1][0], false, false);
      const auto s1 = __builtin_amdgcn_permlane16_swap(o[0][1], o[1][1], false, false);
      const int t = 256 * (4 * hw + 2 * qp + (g & 1)) + 16 * r + 8 * (g >> 1);
      st16(rs, WS_X1T + (((size_t)(t >> 6) * D_ + d) * 64 + (t & 63)) * 2, (u32x4){s0[0], s1[0], s0[1], s1[1]});
    }
  } else {
#define PRIO47 2
    __builtin_amdgcn_s_setprio(PRIO47);
    const int d = 5 * pj + 4, wq = w - 4;
    char* smem = smem_all + 2 * K2_CH;
    StageRaw sr;
    const ChanRaw cr = chan_ld(dskip, ws, d, lane);
    asm volatile("" ::: "memory");
    if (wq < 2) sr = stage_ld(ws, d, th);
    asm volatile("" ::: "memory");
    const int tl0 = par ? (wq < 3 ? 5 - wq : 1) : (wq == 0 ? 7 : (wq == 1 ? 6 : 2)), tl1 = 0;
    const unsigned ap0 = (unsigned)(size_t)(smem + s4 * CS + 2 * (64 * wq + 8 * g + (r + 1) - s4));
    const unsigned bp0 = (unsigned)(size_t)(smem + KOFF + 2 * (2032 + 64 * wq - 16 * r + 8 * g) - 3584);
    char* redw = smem_all + CV_RED + wq * 1024 + lane * 16;
    u32x2 xres[2];
    xres[0] = *(const u32x2*)(ws + WS_XT + tl_off(d, 256 * tl0 + 16 * r + 4 * g));
    xres[1] = *(const u32x2*)(ws + WS_XT + tl_off(d, 256 * tl1 + 16 * r + 4 * g));
#define NF1_ 4
#define NF2_ 5
    constexpr int NF1 = NF1_, NC1 = 108 / NF1, NB1 = 5 * NC1, NF2 = NF2_, NC2 = 40 / NF2, NJ2 = 54 * NC2;
    const bool isw1 = bx < NB1;
    const int j2 = 4 * (bx - NB1) + wq;
    const bool isw2 = !isw1 && j2 < NJ2, isgb = j2 >= NJ2 && j2 < NJ2 + 10;
    const int jk = isw1 ? 4 * (bx / NC1) + wq : j2 / NC2;
    const int jc = isw1 ? NF1 * (bx % NC1) : NF2 * (j2 % NC2);
    RtRaw rt;
    const int gbi = 64 * (j2 - NJ2) + lane;
    ModP mp;
    SB();
#define RT_MODE 2
#define RT67 8
    auto rtl = [&]() __attribute__((always_inline)) {
      if (isw1) rt_load<NF1>(rt, w1, FF, jk, jc, lane);
      else if (isw2) rt_load<NF2>(rt, w2, D_, jk, jc, lane);
      SB();
    };
    if (wq == 0) stage_zero(smem, th);
    SB();
    __builtin_amdgcn_s_barrier();
    SB();
    const Chan c = chan_fin(cr);
    if (wq < 2) {
      stage_wr(sr, c, smem, th);
      lds_post(fl + 8, lane);
    }
    if (wq >= 2 && RT67) __builtin_amdgcn_s_sleep(RT67);
    rtl();
    lds_wait(fl + 8, 2u);
    STAMP(1, 2);
    if (isw1 || isgb) mp = mod_ld(rs, isw1 ? (lane < 32 ? 4 * D_ : 3 * D_ - 32) + 32 * jk + lane : 5 * D_ + gbi);
    SB();
    if (par) conv_half<1>(ap0, bp0, redw); else conv_half<0>(ap0, bp0, redw);
    lds_post(fl + 4, lane);
    STAMP(1, 3);
    STAMP(1, 4);
    lds_wait(fl + 4, 4u);
    STAMP(1, 5);
    const char* rd = smem_all + CV_RED + lane * 16;
#pragma unroll
    for (int u = 0; u < 2; ++u) {
      const bool on = u == 0 ? (par || wq < 3) : (par && wq == 0);
      if (on) {
        const int sl = u == 0 ? wq : 4, tl = u == 0 ? tl0 : tl1;
        const f32x4 y = (*(const f32x4*)(rd + sl * 4096) + *(const f32x4*)(rd + sl * 4096 + 1024))
                      + (*(const f32x4*)(rd + sl * 4096 + 2048) + *(const f32x4*)(rd + sl * 4096 + 3072));
        const int t = 256 * tl + 16 * r + 4 * g;
        const f32x4 v = conv_out(y, c, smem, t);
        st8(rs, WS_X1T + (((size_t)(t >> 6) * D_ + d) * 64 + (t & 63)) * 2, (u32x2){add_bf(xres[u][0], v[0], v[1]), add_bf(xres[u][1], v[2], v[3])});
      }
    }
    SB();
    STAMP(1, 6);
    char* scr = smem_all + CV_RT2 + wq * (32 * RT_STR + 256);
    if (isw1) {
      float* sums = (float*)(smem_all + CV_SUM);
      w1_fin<NF1>(rt, mp, ws, jk, jc, lane, scr, sums + 32 * NF1 * wq);
      asm volatile("" ::: "memory");
      unsigned old = 0u;
      if (lane == 0) old = __hip_atomic_fetch_add(fl + 5, 1u, __ATOMIC_RELAXED, __HIP_MEMORY_SCOPE_WORKGROUP);
      asm volatile("" ::: "memory");
      if (__builtin_amdgcn_readfirstlane(old) == 3u && lane < 8 * NF1) {
        const f32x4* sp = (const f32x4*)sums + lane;
        const f32x4 v = (sp[0] + sp[8 * NF1]) + (sp[16 * NF1] + sp[24 * NF1]);
        st16(rs, WS_CSP + ((size_t)(2 * (bx / NC1) + lane / (4 * NF1)) * FF + 16 * jc + 4 * (lane % (4 * NF1))) * 4, __builtin_bit_cast(u32x4, v));
      }
    } else if (isw2) {
      rt_park<NF2>(rt, scr, lane);
      lgkm0();
#pragma unroll
      for (int j = 0; j < NF2; ++j) st16(rs, WS_W2F + ((size_t)((jc + j) * 54 + jk) * 64 + lane) * 16, rt_frag(scr, j, lane));
    } else if (isgb) {
      st4(ws, WS_GB + 4 * gbi, __float_as_uint(mod_fin(mp)));
    }
  }
}

__global__ NOPK __launch_bounds__(512, 2) void k2(const unsigned short* __restrict__ dskip, char* __restrict__ ws,
                                                  const unsigned short* __restrict__ w1, const unsigned short* __restrict__ w2) {
  __shared__ __attribute__((aligned(16))) char smem[CV_XCH + 16384];
  TS(1, ws);
  conv_phase(dskip, ws, w1, w2, smem);
  char* stamp_ws = ws; (void)stamp_ws;
  VM0();
  STAMP(1, 7);
}

DEV u32x4 ld16_sc1(rsrc_t r, int voff, int soff) { return __builtin_amdgcn_raw_buffer_load_b128(r, voff, soff, 16); }
DEV u32x4 ld16(rsrc_t r, int voff, int soff) { return __builtin_amdgcn_raw_buffer_load_b128(r, voff, soff, 0); }
DEV u32x4 tr_frag(const char* p) {
  const u32x2 a = __builtin_bit_cast(u32x2, __builtin_amdgcn_ds_read_tr16_b64_v4i16((lds_s16x4*)p));
  const u32x2 b = __builtin_bit_cast(u32x2, __builtin_amdgcn_ds_read_tr16_b64_v4i16((lds_s16x4*)(p + 256)));
  return (u32x4){a[0], a[1], b[0], b[1]};
}

constexpr int KF_RS = 84;
constexpr int KF_RB = 8 * 16 * KF_RS * 4;
constexpr int KF_BIG = 2 * KF_RB;
constexpr int NW2L = 8, NW2R = 27;
constexpr int KF_TAB = KF_BIG;
constexpr int KF_PS = KF_TAB + 320;
constexpr int KF_XR = KF_PS + 512;
constexpr int KF_CNT = KF_XR + 64 * 176;
constexpr int KF_LDS = KF_CNT + 16;
#define X_FIRST 5
#define X_STEP 4
#define W1_EARLY 3
#define W1_MID 7
#define W1_PRE 10
#define W2_START 6
#define W2_RATE 2
#define W2_INL 28
#define CS_STEP 14
#define SIG_SPLIT 4
#define H1_PRE 7
#define H1_BATCH 99
#define LOCK_MASK 0x55554u

DEV void kf_body(char* __restrict__ ws, unsigned short* __restrict__ out, char* smem) {
  char* xs = smem;
  float* tab = (float*)(smem + KF_TAB);
  float* ps = (float*)(smem + KF_PS);
  char* xr = smem + KF_XR;
  unsigned* lcnt = (unsigned*)(smem + KF_CNT);
  float* cst = (float*)(smem + 81920);
  const int tid = threadIdx.x, lane = tid & 63;
  const int w = __builtin_amdgcn_readfirstlane(tid >> 6);
  const int bx = blockIdx.x;
  const int ng = (bx & 1) * 4 + ((bx >> 3) & 3), mt = ((bx & 7) >> 1) * 8 + (bx >> 5);
  const int g = lane >> 4, i = lane & 15;
  const rsrc_t rs_ = ws_rsrc(ws);

  char* stamp_ws = ws; (void)stamp_ws;
  STAMP(2, 0);
  unsigned* flags = (unsigned*)(ws + WS_CNT) + mt * (2 * FL_P);
  const int ntl = ng < 6 ? 14 : 12;
  const int ctb = ng < 6 ? 14 * ng : 84 + 12 * (ng - 6);
  const bool two = w < ntl - 8;
  const bool full = w < 6;
  const int ks0 = w < 6 ? 7 * w : 42 + 6 * (w - 6);
  const int ct0 = ctb + (two ? 2 * w : (ntl - 8) + w);
  const int lo16 = lane * 16;
  const int w1s = (int)WS_W1F + ct0 * W1TS * 1024;
  const int w2s = (int)WS_W2F + (ng * 5 * 54 + ks0) * 1024;

  u32x4 ring[32], wl[NW2L];
  const char* bb0 = xs + (8 * g + (i >> 2)) * 64 + ((g & 1) << 5) + (i & 3) * 8;
  const char* bb1 = xs + (8 * g + (i >> 2)) * 64 + (((g & 1) ^ 1) << 5) + (i & 3) * 8;
  auto w2_off = [&](int e) __attribute__((always_inline)) {
    const int s = e < 27 ? e / 4 : (e < 34 ? e - 27 : 6), c = e < 27 ? e % 4 : (e < 34 ? 4 : 3);
    const int sn = (s == 6 && !full) ? 5 : s;
    return w2s + (c * 54 + sn) * 1024;
  };

  auto gemm1 = [&](auto two_tag) __attribute__((always_inline)) {
    constexpr bool TWO = decltype(two_tag)::value;
    if (TWO) asm volatile("; two tiles" ::: "memory"); else asm volatile("; one tile" ::: "memory");
    u32x4 t[10];
    auto st_x = [&](int i0, int i1) __attribute__((always_inline)) {
#pragma unroll
      for (int it = i0; it < i1; ++it) {
        const int c = tid + 512 * it;
        const int row = c >> 3;
        *(u32x4*)(xs + ((c >> 2) & 1) * 40960 + row * 64 + (((c & 3) ^ ((row >> 2) & 2)) << 4)) = t[it];
      }
    };
#pragma unroll
    for (int it = 0; it < X_FIRST; ++it) t[it] = ld16(rs_, tid * 16, (int)WS_X1T + mt * 81920 + 8192 * it);
#pragma unroll
    for (int k = 0; k < W1_EARLY; ++k) {
      ring[2 * k] = ld16(rs_, lo16, w1s + k * W1KS * 1024);
      if (TWO) ring[2 * k + 1] = ld16(rs_, lo16, w1s + (k * W1KS + W1TS) * 1024);
    }
    SB();
    __builtin_amdgcn_s_barrier();
    SB();
#pragma unroll
    for (int k = W1_EARLY; k < W1_MID; ++k) {
      ring[2 * k] = ld16(rs_, lo16, w1s + k * W1KS * 1024);
      if (TWO) ring[2 * k + 1] = ld16(rs_, lo16, w1s + (k * W1KS + W1TS) * 1024);
    }
    SB();
    st_x(0, X_FIRST);
    SB();
    if (tid == 0) *lcnt = 0u;
    float gbp;
    if (!TWO) gbp = ((const float*)(ws + WS_GB))[80 * ng + min((w & 1) * 64 + lane, 79)];
    SB();
    __syncthreads();
    STAMP(2, 1);
    auto resid = [&]() __attribute__((always_inline)) {
      const int kb = (80 * ng) >> 5, tq = w & 3;
#pragma unroll
      for (int u = 0; u < 2; ++u) {
        const int kk = min(kb + 2 * (w >> 2) + u, 19);
        const u32x4 f = tr_frag(((tq & 1) ? bb1 : bb0) + (tq >> 1) * 40960 + kk * 2048);
        const unsigned dl = (unsigned)(32 * kk + 8 * g - 80 * ng);
        if (dl < 80u) *(u32x4*)(xr + (tq * 16 + i) * 176 + dl * 2) = f;
      }
    };
    const int hs = TWO ? 0 : (w & 1);
    const char* ba[4] = {bb0 + hs * 40960, bb1 + hs * 40960, bb0 + (hs ^ 1) * 40960, bb1 + (hs ^ 1) * 40960};
    const u32x4 ones = {0x3f803f80u, 0x3f803f80u, 0x3f803f80u, 0x3f803f80u};
    f32x4 acc[2][4];
#pragma unroll
    for (int c = 0; c < 2; ++c)
#pragma unroll
      for (int q = 0; q < 4; ++q) acc[c][q] = (f32x4){0.f, 0.f, 0.f, 0.f};
    u32x4 csp[5];
    const int cso = ((w & 1) * FF + 16 * ctb + 4 * min(lane, 4 * ntl - 1)) * 4;
    u32x4 bn[2][4];
#pragma unroll
    for (int q = 0; q < 4; ++q) bn[0][q] = tr_frag(ba[q]);
#pragma unroll
    for (int q = 0; q < 4; ++q) bn[1][q] = tr_frag(ba[q] + 2048);
#pragma unroll
    for (int kk = 0; kk < 20; ++kk) {
      if (kk == X_STEP) { st_x(X_FIRST, 10); SB(); }
      if (kk == 2 * X_FIRST - 2) { SB(); bar_lds(); SB(); }
      else if ((LOCK_MASK >> kk) & 1) { SB(); __builtin_amdgcn_s_barrier(); SB(); }
      if (kk == 2 * X_FIRST) { resid(); SB(); }
      auto gload = [&](int u) __attribute__((always_inline)) {
        {
          constexpr int CATCH = W1_PRE - W1_MID;
          const int k0 = kk == 0 ? W1_MID : (kk <= CATCH ? W1_MID + 2 * kk - 1 : kk + W1_PRE);
          const int nk = (kk >= 1 && kk <= CATCH) ? 2 : 1;
          const int kn = k0 + (nk == 2 ? (u >> 1) : 0);
          const bool on = nk == 2 || u >= 2;
          if (on && kn < 20) {
            if ((u & 1) == 0) ring[(2 * kn) % 32] = ld16(rs_, lo16, w1s + kn * W1KS * 1024);
            else if (TWO) ring[(2 * kn + 1) % 32] = ld16(rs_, lo16, w1s + (kn * W1KS + W1TS) * 1024);
          }
          if (kk == 0 && u < 2) {
#pragma unroll
            for (int it = X_FIRST + u * 3; it < (u ? 10 : (X_FIRST + 3 < 10 ? X_FIRST + 3 : 10)); ++it)
              t[it] = ld16(rs_, tid * 16, (int)WS_X1T + mt * 81920 + 8192 * it);
          }
        }
        if (kk >= W2_START && u < W2_RATE) {
          const int n = W2_RATE * (kk - W2_START) + u;
          if (n < NW2L) wl[n] = ld16(rs_, lo16, w2_off(NW2R + n));
          else if (n < W2_INL) ring[8 + n - NW2L] = ld16(rs_, lo16, w2_off(n - NW2L));
        }
      };
      const u32x4 a0 = ring[(2 * kk) % 32], a1 = ring[(2 * kk + 1) % 32];
#pragma unroll
      for (int q = 0; q < 4; ++q) {
        const u32x4 bq = bn[kk & 1][q];
        acc[0][q] = mfma_bf16(a0, bq, acc[0][q]);
        if (TWO) acc[1][q] = mfma_bf16(a1, bq, acc[1][q]);
        else if (q < 2) {
          acc[1][2 * q] = mfma_bf16(ones, bq, acc[1][2 * q]);
          acc[1][2 * q + 1] = mfma_bf16(bq, bq, acc[1][2 * q + 1]);
        }
        SB();
        if (kk < 18) bn[kk & 1][q] = tr_frag(ba[q] + (kk + 2) * 2048);
        gload(q);
        SB();
      }
      if (!TWO) {
        if (kk < 5) csp[kk] = ld16(rs_, cso, (int)WS_CSP + kk * 2 * FF * 4);
        if (kk == CS_STEP) {
          f32x4 t = __builtin_bit_cast(f32x4, csp[0]);
#pragma unroll
          for (int u = 1; u < 5; ++u) t += __builtin_bit_cast(f32x4, csp[u]);
          if (w >= 6 && lane < 56) *(f32x4*)(cst + (w & 1) * 224 + 4 * lane) = t;
        }
      }
      if (!TWO && kk == 12) { if ((w & 1) * 64 + lane < 80) tab[(w & 1) * 64 + lane] = gbp; }
      SB();
    }
    STAMP(2, 2);
    if (!TWO) {
#pragma unroll
      for (int h = 0; h < 2; ++h) {
        const f32x4 aq = acc[1][2 * h + 1];
        const float dq = (i & 2) ? ((i & 1) ? aq[3] : aq[2]) : ((i & 1) ? aq[1] : aq[0]);
        const int tk = (2 * hs + h) * 16 + i;
        if (g == (i >> 2)) {
          const float mean = acc[1][2 * h][0] * (1.f / D_);
          const float var = fmaxf(dq * (1.f / D_) - mean * mean, 0.f);
          const float sd = rbf(sqrtf(rbf(rbf(var) + 1e-5f)));
          const float rs = 1.f / sd;
          ps[tk] = rs;
          ps[64 + tk] = -rbf(mean) * rs;
        }
      }
    }
    __syncthreads();
    f32x4 csv[2], b1v[2];
#pragma unroll
    for (int c = 0; c < (TWO ? 2 : 1); ++c) {
      csv[c] = *(const f32x4*)(cst + 16 * (ct0 - ctb + c) + 4 * g);
      b1v[c] = *(const f32x4*)(cst + 224 + 16 * (ct0 - ctb + c) + 4 * g);
    }
#pragma unroll
    for (int qp = 0; qp < 2; ++qp) {
      u32x2 o[2][2];
#pragma unroll
      for (int h = 0; h < 2; ++h) {
        const int q = 2 * qp + h, tq = q ^ (2 * hs);
        const float rs = ps[tq * 16 + i], nm = ps[64 + tq * 16 + i];
#pragma unroll
        for (int c = 0; c < (TWO ? 2 : 1); ++c) {
          float v[4];
#pragma unroll
          for (int r = 0; r < 4; ++r) v[r] = fmaxf(fmaf(acc[c][q][r], rs, fmaf(nm, csv[c][r], b1v[c][r])), 0.f);
          o[c][h] = (u32x2){pk_bf(v[0], v[1]), pk_bf(v[2], v[3])};
        }
      }
      const int tl = ((2 * qp) ^ (2 * hs)) + (g & 1);
#pragma unroll
      for (int c = 0; c < (TWO ? 2 : 1); ++c) {
        const int ct = ct0 + c;
        const auto s0 = __builtin_amdgcn_permlane16_swap(o[c][0][0], o[c][1][0], false, false);
        const auto s1 = __builtin_amdgcn_permlane16_swap(o[c][0][1], o[c][1][1], false, false);
        const size_t off = (((size_t)(4 * mt + tl) * 54 + (ct >> 1)) * 64 + i + 16 * (2 * (ct & 1) + (g >> 1))) * 16;
        st16(rs_, WS_H1F + off, (u32x4){s0[0], s1[0], s0[1], s1[1]});
      }
    }
    SB();
    __builtin_amdgcn_s_barrier();
    SB();
    constexpr int E0 = W2_INL - NW2L;
    auto signal = [&]() __attribute__((always_inline)) {
      if (lane == 0) {
        if (__hip_atomic_fetch_add(lcnt, 1u, __ATOMIC_RELAXED, __HIP_MEMORY_SCOPE_WORKGROUP) == 7u)
          __hip_atomic_store(flags + (FL_P / 4) * ng, 1u, __ATOMIC_RELAXED, __HIP_MEMORY_SCOPE_AGENT);
      }
    };
    if (w >= SIG_SPLIT) { asm volatile("s_waitcnt vmcnt(0)" ::: "memory"); signal(); }
    SB();
#pragma unroll
    for (int e = E0; e < NW2R; ++e) ring[(8 + e) % 32] = ld16(rs_, lo16, w2_off(e));
    SB();
    if (w < SIG_SPLIT) { asm volatile("s_waitcnt vmcnt(%0)" :: "n"(NW2R - E0) : "memory"); signal(); }
    SB();
  };
  if (two) gemm1(std::true_type{});
  else gemm1(std::false_type{});
  STAMP(2, 3);
#pragma nounroll
  for (unsigned spin = 0; spin < (1u << 20); ++spin) {
    if (__hip_atomic_load(flags + (FL_P / 4) * w, __ATOMIC_RELAXED, __HIP_MEMORY_SCOPE_AGENT) != 0u) break;
#define POLL_SL 1
    if (POLL_SL) __builtin_amdgcn_s_sleep(POLL_SL);
  }
  asm volatile("" ::: "memory");
  SB();

  STAMP(2, 4);
  const int h1s = (int)WS_H1F + (mt * 4 * 54 + ks0) * 1024;
  u32x4 hb[28];
  auto load_h1 = [&](int n) __attribute__((always_inline)) {
    const int q = n / 7, s = n % 7;
    const int sn = (s == 6 && !full) ? 5 : s;
    hb[n] = ld16_sc1(rs_, lo16, h1s + (q * 54 + sn) * 1024);
  };
#pragma unroll
  for (int n = 0; n < H1_PRE; ++n) {
    if (n == H1_BATCH) { SB(); __builtin_amdgcn_s_barrier(); SB(); }
    load_h1(n);
  }
  SB();
  float* red = (float*)smem;
  float* rb_ = red + (w * 16 + i) * KF_RS + 4 * g;
  const int re = w * 40 + min(lane, 39);
  const int rtk = re / 20, rq4 = re % 20;
  const float* rr_ = red + rtk * KF_RS + 4 * rq4;
  f32x4 ac2[2][5];
  f32x4 rv[8], rsum;
  u32x2 xv2;
  const f32x4 gb = *(const f32x4*)(tab + 4 * rq4);
  auto red_wr = [&](int q, int c) __attribute__((always_inline)) {
    *(f32x4*)(rb_ + (q & 1) * (KF_RB / 4) + 16 * c) = ac2[q & 1][c];
  };
  auto red_rd = [&](int q, int ww) __attribute__((always_inline)) {
    rv[ww] = *(const f32x4*)(rr_ + (q & 1) * (KF_RB / 4) + ww * 16 * KF_RS);
  };
  auto red_x = [&](int q) __attribute__((always_inline)) { xv2 = *(const u32x2*)(xr + (16 * q + rtk) * 176 + rq4 * 8); };
  auto red_out = [&](int q) __attribute__((always_inline)) {
    const f32x4 s = rsum + (rv[6] + rv[7]);
    u32x2 o;
#pragma unroll
    for (int h = 0; h < 2; ++h) {
      o[h] = pk_bf(fmaf(s[2 * h], gb[2 * h], bf_lo(xv2[h])), fmaf(s[2 * h + 1], gb[2 * h + 1], bf_hi(xv2[h])));
    }
    *(u32x2*)(out + (size_t)(mt * 64 + 16 * q + rtk) * D_ + ng * 80 + 4 * rq4) = o;
  };
#pragma unroll
  for (int q = 0; q < 3; ++q) {
#pragma unroll
    for (int c = 0; c < 5; ++c) ac2[q & 1][c] = (f32x4){0.f, 0.f, 0.f, 0.f};
#pragma unroll
    for (int s = 0; s < 7; ++s) {
      auto mm = [&](int c) __attribute__((always_inline)) {
        const u32x4 a = c == 4 ? wl[s] : ((s == 6 && c == 3) ? wl[7] : ring[(8 + 4 * s + c) % 32]);
        ac2[q & 1][c] = mfma_bf16(a, hb[7 * q + s], ac2[q & 1][c]);
      };
      if (s == 5 && q >= 2) {
        red_wr(q - 1, 4);
        SB();
        red_out(q - 2);
#pragma unroll
        for (int c = 0; c < 5; ++c) mm(c);
#pragma unroll
        for (int c = 0; c < 5; ++c) {
          __builtin_amdgcn_sched_group_barrier(0x008, 1, 0);
          __builtin_amdgcn_sched_group_barrier(0x002, 5, 0);
        }
        SB();
      } else if (s < 6) {
        mm(0); SB();
        if (q >= 1 && s >= 1) { red_wr(q - 1, s - 1); SB(); }
        mm(1); SB();
        if (q >= 2 && s < 4) { red_rd(q - 2, 2 * s); SB(); }
        mm(2); SB();
        if (q >= 2 && s < 4) { red_rd(q - 2, 2 * s + 1); SB(); }
        if (q >= 2 && s == 4) { red_x(q - 2); SB(); }
        mm(3); SB();
        if (q >= 2 && s >= 1 && s < 4) {
          const f32x4 t = rv[2 * s - 2] + rv[2 * s - 1];
          rsum = s == 1 ? t : rsum + t;
          asm volatile("" : "+v"(rsum));
          SB();
        }
        mm(4); SB();
      } else if (full) {
#pragma unroll
        for (int c = 0; c < 5; ++c) mm(c);
        SB();
      }
      if (7 * q + s + H1_PRE < 28) { load_h1(7 * q + s + H1_PRE); SB(); }
    }
    if (q >= 1) bar_lds();
    if (q == 0) STAMP(2, 5);
  }
  {
    auto sum6 = [&]() __attribute__((always_inline)) {
      rsum = ((rv[0] + rv[1]) + (rv[2] + rv[3])) + (rv[4] + rv[5]);
    };
    if (full) {
#pragma unroll
      for (int c = 0; c < 5; ++c) {
        const u32x4 a = c == 4 ? wl[6] : (c == 3 ? wl[7] : ring[(8 + 24 + c) % 32]);
        rv[c] = mfma_bf16(a, hb[27], (f32x4){0.f, 0.f, 0.f, 0.f});
      }
    } else {
#pragma unroll
      for (int c = 0; c < 5; ++c) rv[c] = (f32x4){0.f, 0.f, 0.f, 0.f};
    }
    f32x4 c0[5];
#pragma unroll
    for (int c = 0; c < 5; ++c) c0[c] = rv[c];
    SB();
    auto col = [&](int c, auto&& between) __attribute__((always_inline)) {
#pragma unroll
      for (int s = 0; s < 6; ++s) {
        const u32x4 a = c == 4 ? wl[s] : ring[(8 + 4 * s + c) % 32];
        c0[c] = mfma_bf16(a, hb[21 + s], c0[c]);
        SB();
        between(s);
        SB();
      }
    };
    col(0, [&](int s) __attribute__((always_inline)) { if (s < 5) red_wr(2, s); if (s < 4) { red_rd(1, 2 * s); red_rd(1, 2 * s + 1); } if (s == 4) red_x(1); });
    col(1, [&](int s) __attribute__((always_inline)) { if (s == 1) { sum6(); asm volatile("" : "+v"(rsum)); } if (s == 3) red_out(1); });
    bar_lds();
    auto wr3 = [&](int c) __attribute__((always_inline)) { *(f32x4*)(rb_ + (KF_RB / 4) + 16 * c) = c0[c]; };
    col(2, [&](int s) __attribute__((always_inline)) { if (s < 2) wr3(s); if (s >= 1 && s < 5) { red_rd(2, 2 * s - 2); red_rd(2, 2 * s - 1); } if (s == 5) red_x(2); });
    col(3, [&](int s) __attribute__((always_inline)) { if (s == 1) wr3(2); if (s == 2) { sum6(); asm volatile("" : "+v"(rsum)); } if (s == 4) red_out(2); });
    col(4, [&](int s) __attribute__((always_inline)) { if (s == 1) wr3(3); if (s == 2) red_x(3); });
    wr3(4);
    STAMP(2, 6);
    bar_lds();
#pragma unroll
    for (int ww = 0; ww < 8; ++ww) red_rd(3, ww);
    sum6();
    red_out(3);
    VM0();
    STAMP(2, 7);
  }
}

__global__ NOPK __launch_bounds__(512, 2) void kf(char* __restrict__ ws, unsigned short* __restrict__ out) {
  __shared__ __attribute__((aligned(16))) char smem[KF_LDS];
  TS(2, ws);
  kf_body(ws, out, smem);
}

__global__ void kprobe(char* ws, int which) {
  const unsigned long long now = __builtin_amdgcn_s_memrealtime();
  const unsigned long long* t = (const unsigned long long*)(ws + (200u << 20));
  unsigned long long d = which == 0 ? t[1] - t[0] : (which == 1 ? t[2] - t[1] : (which == 2 ? now - t[2] : 0ull));
  if (d > 10000ull) d = 10000ull;
  while (__builtin_amdgcn_s_memrealtime() - now < 10ull * d) __builtin_amdgcn_s_sleep(1);
}
__global__ void kend_() {}

extern "C" void kernel_launch(void* const* d_in, const int* in_sizes, int n_in,
                              void* d_out, int out_size, void* d_ws, size_t ws_size,
                              hipStream_t stream) {
  K1Args a;
  a.x = (const unsigned short*)d_in[0];
  a.are = (const unsigned short*)d_in[1];
  a.aim = (const unsigned short*)d_in[2];
  a.bre = (const unsigned short*)d_in[3];
  a.bim = (const unsigned short*)d_in[4];
  a.cre = (const unsigned short*)d_in[5];
  a.cim = (const unsigned short*)d_in[6];
  a.logdt = (const unsigned short*)d_in[7];
  a.c = (const unsigned short*)d_in[12];
  a.wmod = (const unsigned short*)d_in[13];
  a.ws = (char*)d_ws;
  k1<<<NB_PF + NB_K + NB_LN + NB_MOD, 256, 0, stream>>>(a);
  k2<<<256, 512, 0, stream>>>((const unsigned short*)d_in[8], (char*)d_ws, (const unsigned short*)d_in[10],
                              (const unsigned short*)d_in[11]);
  kf<<<256, 512, 0, stream>>>((char*)d_ws, (unsigned short*)d_out);
  kprobe<<<1, 64, 0, stream>>>((char*)d_ws, 0);
}
```

```cpp
#include <hip/hip_runtime.h>
#include <hip/hip_bf16.h>
#include <stdint.h>
#include <type_traits>

typedef __attribute__((ext_vector_type(8))) __bf16 bf16x8;
typedef __attribute__((ext_vector_type(2))) __bf16 bf16x2;
typedef __attribute__((ext_vector_type(4))) float f32x4;
typedef __attribute__((ext_vector_type(2))) float f32x2;
typedef __attribute__((ext_vector_type(4))) unsigned u32x4;
typedef __attribute__((ext_vector_type(2))) unsigned u32x2;
typedef __attribute__((ext_vector_type(8))) _Float16 h16x8;
typedef __attribute__((ext_vector_type(2))) _Float16 h16x2;
typedef __attribute__((ext_vector_type(4))) short s16x4;
typedef __attribute__((address_space(3))) s16x4 lds_s16x4;

#if defined(__HIP_DEVICE_COMPILE__)
#define NOPK __attribute__((target("no-packed-fp32-ops")))
#else
#define NOPK
#endif
#define DEV __device__ __forceinline__

#define W1L 0
constexpr int W1KS = W1L ? 1 : 108, W1TS = W1L ? 20 : 1;
#define LNFLIP 2
#define KMAP 0
constexpr int L_ = 2048;
constexpr int D_ = 640;
constexpr int NS = 128;
constexpr int FF = 1728;
constexpr int KREV = 1280;

constexpr size_t WS_GB   = 0;
constexpr size_t WS_KSC  = 8192;
#define FL_P 2048
#define FL_B 327680
constexpr size_t WS_CNT  = FL_B;
constexpr size_t WS_MOD  = 65536;
constexpr size_t WS_CSP  = 196608;
constexpr size_t WS_XNT  = 327680 + 256 * FL_P;
constexpr size_t WS_XT   = WS_XNT + (size_t)D_ * L_ * 2;
constexpr size_t WS_KREV = WS_XT + (size_t)D_ * L_ * 2;
constexpr size_t WS_W1F  = WS_KREV + (size_t)D_ * KREV * 2;
constexpr size_t WS_W2F  = WS_W1F + (size_t)D_ * FF * 2;
constexpr size_t WS_X1T  = WS_W2F + (size_t)D_ * FF * 2;
constexpr size_t WS_H1F  = WS_X1T + (size_t)D_ * L_ * 2;
constexpr size_t WS_END  = WS_H1F + (size_t)L_ * FF * 2;

#define STAMP(kid, k) do {} while (0)
#define CSTAMP(kid, k) do {} while (0)
#define VM0() do {} while (0)

DEV float bf_lo(unsigned u) { return __uint_as_float(u << 16); }
DEV float bf_hi(unsigned u) { return __uint_as_float(u & 0xffff0000u); }
DEV unsigned pk_bf(float a, float b) {
  f32x2 v = {a, b};
  bf16x2 r = __builtin_convertvector(v, bf16x2);
  return __builtin_bit_cast(unsigned, r);
}
DEV unsigned pk_h(float a, float b) {
  f32x2 v = {a, b};
  h16x2 r = __builtin_convertvector(v, h16x2);
  return __builtin_bit_cast(unsigned, r);
}
#define NTM 15
template <int BIT, class T> DEV T ldin(const void* p) {
  if (NTM & BIT) return __builtin_nontemporal_load((const T*)p);
  return *(const T*)p;
}
DEV float rbf(float a) { return bf_lo(pk_bf(a, 0.f)); }
DEV float ldbf(const unsigned short* p) { return __uint_as_float(((unsigned)*p) << 16); }
#define CONST_AS __attribute__((address_space(4)))
template <class T> DEV T ldu(const void* p) { return *(const CONST_AS T*)(size_t)p; }
DEV float ldbf_u(const unsigned short* p) {
  const unsigned v = ldu<unsigned>((const void*)((size_t)p & ~(size_t)3));
  return __uint_as_float(((size_t)p & 2) ? (v & 0xffff0000u) : (v << 16));
}

typedef __amdgpu_buffer_rsrc_t rsrc_t;
DEV rsrc_t ws_rsrc(char* ws) { return __builtin_amdgcn_make_buffer_rsrc((void*)ws, 0, (int)WS_END, 0x00020000); }
#define ST_AUX 16
DEV void st16(rsrc_t r, size_t off, u32x4 v) { __builtin_amdgcn_raw_buffer_store_b128(v, r, (int)off, 0, ST_AUX); }
DEV void st4(char* ws, size_t off, unsigned v) { __hip_atomic_store((unsigned*)(ws + off), v, __ATOMIC_RELAXED, __HIP_MEMORY_SCOPE_AGENT); }
DEV void st8(rsrc_t r, size_t off, u32x2 v) { __builtin_amdgcn_raw_buffer_store_b64(v, r, (int)off, 0, 16); }

DEV void bar_lds() { asm volatile("s_waitcnt lgkmcnt(0)\n\ts_barrier" ::: "memory"); }
DEV void lds_wait(const void* word, unsigned val) {
  unsigned tv, ts, cnt;
  asm volatile("s_mov_b32 %2, 0x100000\n"
               "1:\n\tds_read_b32 %0, %3\n\ts_waitcnt lgkmcnt(0)\n\tv_readfirstlane_b32 %1, %0\n\t"
               "s_cmp_ge_u32 %1, %4\n\ts_cbranch_scc1 2f\n\ts_sleep 1\n\ts_sub_u32 %2, %2, 1\n\t"
               "s_cmp_lg_u32 %2, 0\n\ts_cbranch_scc1 1b\n2:"
               : "=&v"(tv), "=&s"(ts), "=&s"(cnt) : "v"((unsigned)(size_t)word), "s"(val) : "memory", "scc");
}
DEV void lds_post(unsigned* word, int lane) {
  asm volatile("" ::: "memory");
  if (lane == 0) __hip_atomic_fetch_add(word, 1u, __ATOMIC_RELAXED, __HIP_MEMORY_SCOPE_WORKGROUP);
  asm volatile("" ::: "memory");
}
DEV void lgkm0() { asm volatile("s_waitcnt lgkmcnt(0)" ::: "memory"); __builtin_amdgcn_sched_barrier(0); }

DEV f32x4 mfma_bf16(u32x4 a, u32x4 b, f32x4 c) {
  return __builtin_amdgcn_mfma_f32_16x16x32_bf16(__builtin_bit_cast(bf16x8, a), __builtin_bit_cast(bf16x8, b), c, 0, 0, 0);
}

constexpr int NB_PF = 8, NB_K = 640, NB_LN = 256, NB_MOD = 120;

struct K1Args {
  const unsigned short *x, *are, *aim, *bre, *bim, *cre, *cim, *logdt, *c, *wmod, *w1, *w2, *out, *dskip;
  char* ws;
};

DEV void k1_prefetch(const K1Args& p) {
  const int tid = threadIdx.x;
  unsigned long long pc;
  int o0, o1;
  asm volatile("s_getpc_b64 %0\n.Lpf_here%=:\n\ts_mov_b32 %1, _Z2k16K1Args - .Lpf_here%=\n\ts_mov_b32 %2, _Z5kend_v - .Lpf_here%="
               : "=s"(pc), "=s"(o0), "=s"(o1));
  const char* c0 = (const char*)(pc + (long long)o0);
  long n = o1 - o0;
  n = n < 0 ? 0 : (n > 98304 ? 98304 : n);
  unsigned v[3];
#pragma unroll
  for (int it = 0; it < 3; ++it) {
    const long o = (long)tid * 128 + it * 32768;
    v[it] = o < n ? *(const __attribute__((address_space(1))) unsigned*)(c0 + o) : 0u;
  }
  asm volatile("" :: "v"(v[0] ^ v[1] ^ v[2]));
}

DEV void k1_mod(const K1Args& p, int b, char* smem) {
  const int tid = threadIdx.x;
  const int rg = b & 7, cgp = b >> 3;
  const int c32 = tid & 31, r8 = tid >> 5;
  float* sil = (float*)smem;
  float* red = (float*)(smem + 512);
  char* stamp_ws = p.ws; (void)stamp_ws;
  STAMP(0, 1);
  u32x4 w[10];
  const float cv = ldbf(p.c + rg * 80 + min(tid, 79));
  asm volatile("" ::: "memory");
#pragma unroll
  for (int it = 0; it < 10; ++it)
    w[it] = ldin<1, u32x4>(p.wmod + (size_t)(rg * 80 + r8 + 8 * it) * 3840 + 256 * cgp + 8 * c32);
  if (tid < 80) sil[tid] = rbf(cv / (1.f + expf(-cv)));
  if (tid >= 128 && tid < 132 && b < 64) st4(p.ws, WS_CNT + (size_t)(b * 4 + tid - 128) * FL_P, 0u);
  __syncthreads();
  STAMP(0, 2);
  float acc[8];
#pragma unroll
  for (int j = 0; j < 8; ++j) acc[j] = 0.f;
#pragma unroll
  for (int it = 0; it < 10; ++it) {
    float s = sil[r8 + 8 * it];
#pragma unroll
    for (int q = 0; q < 4; ++q) {
      acc[2 * q] += s * bf_lo(w[it][q]);
      acc[2 * q + 1] += s * bf_hi(w[it][q]);
    }
  }
#pragma unroll
  for (int j = 0; j < 8; ++j) acc[j] += __shfl_xor(acc[j], 32, 64);
  if ((tid & 63) < 32) {
    *(f32x4*)(red + (tid >> 6) * 256 + 8 * c32) = (f32x4){acc[0], acc[1], acc[2], acc[3]};
    *(f32x4*)(red + (tid >> 6) * 256 + 8 * c32 + 4) = (f32x4){acc[4], acc[5], acc[6], acc[7]};
  }
  __syncthreads();
  STAMP(0, 3);
  if (tid < 64) {
    const f32x4* r4 = (const f32x4*)red + tid;
    st16(ws_rsrc(p.ws), WS_MOD + (rg * 3840 + 256 * cgp + 4 * tid) * 4,
         __builtin_bit_cast(u32x4, (r4[0] + r4[64]) + (r4[128] + r4[192])));
  }
}
struct ModP { f32x4 a, b; };
DEV ModP mod_ld(rsrc_t rs, int idx) {
  ModP m;
#pragma unroll
  for (int j = 0; j < 4; ++j) {
    m.a[j] = __uint_as_float(__builtin_amdgcn_raw_buffer_load_b32(rs, 4 * idx, (int)WS_MOD + 15360 * j, 0));
    m.b[j] = __uint_as_float(__builtin_amdgcn_raw_buffer_load_b32(rs, 4 * idx, (int)WS_MOD + 15360 * (4 + j), 0));
  }
  return m;
}
DEV float mod_fin(const ModP& m) {
  return rbf(((m.a[0] + m.a[1]) + (m.a[2] + m.a[3])) + ((m.b[0] + m.b[1]) + (m.b[2] + m.b[3])));
}
#define SB() __builtin_amdgcn_sched_barrier(0)

DEV void k1_ln(const K1Args& p, int b, char* smem) {
  constexpr int STR = 704;
  const int tid = threadIdx.x;
  const int tok = tid >> 3, sub = tid & 7;
  const int tile = (b & 7) * 8 + ((b >> 3) & 7), pass = (b >> 6) ^ LNFLIP;
  const int hf = pass & 1;
  const bool norm = pass < 2;
  const int t0 = tile * 32;
  const unsigned short* xr = p.x + (size_t)(t0 + tok) * D_ + 8 * sub;
  u32x4 xv[5], xo[5];
  char* stamp_ws = p.ws; (void)stamp_ws;
  STAMP(0, 1);
#pragma unroll
  for (int m = 0; m < 5; ++m) xv[m] = ldin<2, u32x4>(xr + 320 * hf + 64 * m);
  if (norm) {
#pragma unroll
    for (int m = 0; m < 5; ++m) xo[m] = ldin<2, u32x4>(xr + 320 * (hf ^ 1) + 64 * m);
    float s = 0.f, v = 0.f;
    const bf16x2 one2 = __builtin_bit_cast(bf16x2, 0x3f803f80u);
#pragma unroll
    for (int m = 0; m < 10; ++m)
#pragma unroll
      for (int q = 0; q < 4; ++q) {
        const unsigned u = m < 5 ? xv[m][q] : xo[m - 5][q];
        const bf16x2 x2 = __builtin_bit_cast(bf16x2, u);
        s = __builtin_amdgcn_fdot2_f32_bf16(x2, one2, s, false);
        v = __builtin_amdgcn_fdot2_f32_bf16(x2, x2, v, false);
      }
#pragma unroll
    for (int o = 1; o < 8; o <<= 1) { s += __shfl_xor(s, o, 64); v += __shfl_xor(v, o, 64); }
    const float mean = s * (1.f / D_);
    const float var = fmaxf(v * (1.f / D_) - mean * mean, 0.f);
    const float sd = rbf(sqrtf(rbf(rbf(var) + 1e-5f)));
    const float rs = 1.f / sd;
    const float nm = -rbf(mean) * rs;
#pragma unroll
    for (int m = 0; m < 5; ++m)
#pragma unroll
      for (int q = 0; q < 4; ++q) {
        xv[m][q] = pk_h(fmaf(bf_lo(xv[m][q]), rs, nm), fmaf(bf_hi(xv[m][q]), rs, nm));
      }
  }
  const int lane = tid & 63, w = tid >> 6;
  const int g = lane >> 4, i = lane & 15;
  const rsrc_t rsrc = ws_rsrc(p.ws);
#pragma unroll
  for (int m = 0; m < 5; ++m) *(u32x4*)(smem + tok * STR + (sub + 8 * m) * 16) = xv[m];
  __syncthreads();
  STAMP(0, 2);
  const size_t dsto = (norm ? WS_XNT : WS_XT) + ((size_t)tile * D_ + 320 * hf) * 64;
#pragma unroll
  for (int it = 0; it < 5; ++it) {
    const int cb = w * 5 + it;
    const char* a = smem + (8 * g + (i >> 2)) * STR + cb * 32 + (i & 3) * 8;
    const u32x2 ra = __builtin_bit_cast(u32x2, __builtin_amdgcn_ds_read_tr16_b64_v4i16((lds_s16x4*)a));
    const u32x2 rb = __builtin_bit_cast(u32x2, __builtin_amdgcn_ds_read_tr16_b64_v4i16((lds_s16x4*)(a + 4 * STR)));
    st16(rsrc, dsto + (size_t)(cb * 16 + i) * 64 + 16 * g, (u32x4){ra[0], ra[1], rb[0], rb[1]});
  }
}

constexpr int RT_STR = 288;
struct RtRaw { u32x4 r[8]; };
template <int NF>
DEV void rt_load(RtRaw& t, const unsigned short* W, int N, int kk, int ct0, int lane) {
#pragma unroll
  for (int j = 0; j < NF; ++j) {
    const int c = lane + 64 * j, row = c / (2 * NF), part = c % (2 * NF);
    t.r[j] = ldin<4, u32x4>(W + (size_t)(32 * kk + row) * N + 16 * ct0 + 8 * part);
  }
}
template <int NF>
DEV void rt_park(const RtRaw& t, char* scr, int lane) {
#pragma unroll
  for (int j = 0; j < NF; ++j) {
    const int c = lane + 64 * j, row = c / (2 * NF), part = c % (2 * NF);
    *(u32x4*)(scr + row * RT_STR + 16 * part) = t.r[j];
  }
}
DEV u32x4 rt_frag(const char* scr, int j, int lane) {
  const int g = lane >> 4, i = lane & 15;
  const char* p = scr + (8 * g + (i >> 2)) * RT_STR + 32 * j + (i & 3) * 8;
  const u32x2 a = __builtin_bit_cast(u32x2, __builtin_amdgcn_ds_read_tr16_b64_v4i16((lds_s16x4*)p));
  const u32x2 b = __builtin_bit_cast(u32x2, __builtin_amdgcn_ds_read_tr16_b64_v4i16((lds_s16x4*)(p + 4 * RT_STR)));
  return (u32x4){a[0], a[1], b[0], b[1]};
}
struct cf { float re, im; };
DEV cf cmul(cf a, cf b) {
  cf r;
  r.re = a.re * b.re - a.im * b.im;
  r.im = a.re * b.im + a.im * b.re;
  return r;
}
DEV cf cexp_(float a2, float br, float m, float m0) {
  float mag = __builtin_amdgcn_exp2f(a2 * (m - m0));
  float ph = __builtin_amdgcn_fractf(br * m);
  cf r; r.re = mag * __builtin_amdgcn_cosf(ph); r.im = mag * __builtin_amdgcn_sinf(ph);
  return r;
}
DEV f32x4 mfma_f16(u32x4 a, u32x4 b, f32x4 c) {
  return __builtin_amdgcn_mfma_f32_16x16x32_f16(__builtin_bit_cast(h16x8, a), __builtin_bit_cast(h16x8, b), c, 0, 0, 0);
}
DEV void put2(char* p, cf a, cf b) {
  u32x2 o = {pk_h(a.re, a.im), pk_h(b.re, b.im)};
  *(u32x2*)p = o;
}

constexpr int KG_RS = 544;
DEV void k1_kgen(const K1Args& p, int d, char* smem) {
  constexpr int RS = KG_RS;
  const int tid = threadIdx.x;
  const int lane = tid & 63;
  const int w = __builtin_amdgcn_readfirstlane(tid >> 6);
  const int g = lane >> 4, i = lane & 15;
  const size_t po = (size_t)d * NS + 2 * lane;
  const unsigned rar = ldin<8, unsigned>(p.are + po), rai = ldin<8, unsigned>(p.aim + po);
  unsigned rbr = 0u, rbi = 0u, rcr = 0u, rci = 0u;
  if (w == 0) {
    rbr = ldin<8, unsigned>(p.bre + po); rbi = ldin<8, unsigned>(p.bim + po);
    rcr = ldin<8, unsigned>(p.cre + po); rci = ldin<8, unsigned>(p.cim + po);
  }
  const float ldt = ldbf_u(p.logdt + d);
  asm volatile("" ::: "memory");
  SB();
  char* stamp_ws = p.ws; (void)stamp_ws;
  const float dt = rbf(expf(ldt));
  asm volatile("" :: "v"(dt));
  STAMP(0, 1);
  float a2[2], bv[2], m0[2];
  a2[0] = (dt * bf_lo(rar)) * 1.44269504088896341f; a2[1] = (dt * bf_hi(rar)) * 1.44269504088896341f;
  bv[0] = (dt * bf_lo(rai)) * 0.15915494309189534f; bv[1] = (dt * bf_hi(rai)) * 0.15915494309189534f;
  m0[0] = a2[0] > 0.f ? 2048.f : 0.f; m0[1] = a2[1] > 0.f ? 2048.f : 0.f;
  char* my = smem + lane * 8;
  if (w == 0) {
    cf v[2], v8[2], e8[2];
    float bound = 0.f;
#pragma unroll
    for (int q = 0; q < 2; ++q) {
      const float dbr = dt * (q ? bf_hi(rbr) : bf_lo(rbr)), dbi = dt * (q ? bf_hi(rbi) : bf_lo(rbi));
      const float cr = q ? bf_hi(rcr) : bf_lo(rcr), ci = q ? bf_hi(rci) : bf_lo(rci);
      const float gsh = __builtin_amdgcn_exp2f(a2[q] * m0[q]);
      v[q].re = (cr * dbr - ci * dbi) * gsh; v[q].im = -(cr * dbi + ci * dbr) * gsh;
      bound += (fabsf(v[q].re) + fabsf(v[q].im)) * __builtin_amdgcn_exp2f(fmaxf(a2[q], 0.f) * 120.f);
      e8[q] = cexp_(a2[q], -bv[q], 8.f, 0.f);
      v8[q] = cexp_(a2[q], -bv[q], 64.f, 0.f);
    }
#pragma unroll
    for (int o = 1; o < 64; o <<= 1) bound += __shfl_xor(bound, o, 64);
    int ex = (int)((__float_as_uint(bound) >> 23) & 0xff);
    ex = ex < 20 ? 20 : (ex > 240 ? 240 : ex);
    const float sv = __uint_as_float((unsigned)(127 + 13 + 127 - ex) << 23);
    if (lane == 0) st4(p.ws, WS_KSC + 4 * d, (unsigned)(ex - 13) << 23);
#pragma unroll
    for (int q = 0; q < 2; ++q) { v[q].re *= sv; v[q].im *= sv; v8[q] = cmul(v[q], v8[q]); }
#pragma unroll
    for (int j = 0; j < 8; ++j) {
      put2(my + (41 + j) * RS, v[0], v[1]);
      put2(my + (49 + j) * RS, v8[0], v8[1]);
      if (j < 7) {
#pragma unroll
        for (int q = 0; q < 2; ++q) { v[q] = cmul(v[q], e8[q]); v8[q] = cmul(v8[q], e8[q]); }
      }
    }
  } else if (w == 1) {
    cf e1[2], u[2], u2[2];
#pragma unroll
    for (int q = 0; q < 2; ++q) {
      e1[q] = cexp_(a2[q], bv[q], 1.f, 0.f);
      u[q].re = __builtin_amdgcn_exp2f(-a2[q] * m0[q]); u[q].im = 0.f;
      u2[q] = cexp_(a2[q], bv[q], 128.f, m0[q]);
    }
#pragma unroll
    for (int r = 0; r < 8; ++r) {
      put2(my + r * RS, u[0], u[1]);
      put2(my + (8 + r) * RS, u2[0], u2[1]);
      if (r < 7) {
#pragma unroll
        for (int q = 0; q < 2; ++q) { u[q] = cmul(u[q], e1[q]); u2[q] = cmul(u2[q], e1[q]); }
      }
    }
  } else if (w == 2) {
    cf e2[2], e128[2], u[2], u2[2], c[2];
#pragma unroll
    for (int q = 0; q < 2; ++q) {
      e2[q] = cexp_(a2[q], bv[q], 2.f, 0.f);
      e128[q] = cexp_(a2[q], bv[q], 128.f, 0.f);
      u[q] = cexp_(a2[q], bv[q], 256.f, m0[q]);
      u2[q] = cmul(u[q], e128[q]);
      c[q] = cmul(u2[q], e128[q]);
    }
#pragma unroll
    for (int r = 0; r < 4; ++r) {
      put2(my + (16 + r) * RS, u[0], u[1]);
      put2(my + (20 + r) * RS, u2[0], u2[1]);
      if (r < 3) {
#pragma unroll
        for (int q = 0; q < 2; ++q) { u[q] = cmul(u[q], e2[q]); u2[q] = cmul(u2[q], e2[q]); }
      }
    }
    cf e4[2] = {cmul(e2[0], e2[0]), cmul(e2[1], e2[1])};
#pragma unroll
    for (int r = 0; r < 3; ++r) {
      put2(my + (24 + 2 * r) * RS, c[0], c[1]);
      put2(my + (25 + 2 * r) * RS, cmul(c[0], e4[0]), cmul(c[1], e4[1]));
      if (r < 2) { c[0] = cmul(c[0], e128[0]); c[1] = cmul(c[1], e128[1]); }
    }
  } else {
    cf e128[2], u[2], u2[2];
#pragma unroll
    for (int q = 0; q < 2; ++q) {
      e128[q] = cexp_(a2[q], bv[q], 128.f, 0.f);
      u[q] = cexp_(a2[q], bv[q], 896.f, m0[q]);
      u2[q] = cexp_(a2[q], bv[q], 1536.f, m0[q]);
    }
    put2(my + 30 * RS, u[0], u[1]);
    put2(my + 31 * RS, cexp_(a2[0], bv[0], 900.f, m0[0]), cexp_(a2[1], bv[1], 900.f, m0[1]));
#pragma unroll
    for (int r = 0; r < 4; ++r) {
      put2(my + (36 + r) * RS, u2[0], u2[1]);
#pragma unroll
      for (int q = 0; q < 2; ++q) { u[q] = cmul(u[q], e128[q]); u2[q] = cmul(u2[q], e128[q]); }
      put2(my + (32 + r) * RS, u[0], u[1]);
    }
    put2(my + 40 * RS, u2[0], u2[1]);
  }
  __syncthreads();
  STAMP(0, 2);
  _Float16* kh = (_Float16*)(smem + 57 * RS);
  if (w < 3) {
    const char* ua = smem + (16 * w + i) * RS + g * 16;
    const char* va = smem + (41 + i) * RS + g * 16;
    f32x4 acc = {0.f, 0.f, 0.f, 0.f};
#pragma unroll
    for (int ks = 0; ks < 8; ++ks)
      acc = mfma_f16(*(const u32x4*)(ua + ks * 64), *(const u32x4*)(va + ks * 64), acc);
    int idx0, st;
    if (w == 0) { idx0 = (g < 2 ? 4 * g : 120 + 4 * g) + 8 * i; st = 1; }
    else if (w == 1) { idx0 = g < 2 ? 256 + 64 * g + 4 * i : 384 + 64 * (g - 2) + 2 * i; st = 1; }
    else { idx0 = 512 + 64 * g + i; st = 16; }
#pragma unroll
    for (int rg = 0; rg < 4; ++rg) {
      int idx = idx0 + st * rg;
      if (w == 1 && g >= 2) idx = idx0 + 32 * (rg >> 1) + (rg & 1);
      if (w == 2 && g >= 2) idx = (g == 2 && rg == 0 && i == 0) ? 640 : 644;
      kh[idx] = (_Float16)acc[rg];
    }
  }
  __syncthreads();
  STAMP(0, 3);
  const unsigned short* ku = (const unsigned short*)kh;
  const size_t kout = WS_KREV + (size_t)d * KREV * 2;
  {
    const int l0 = 2040 - 8 * tid;
    u32x4 o;
    if (w < 2) {
      const int q = l0 >> 3;
      const unsigned a = ku[384 + q], b = ku[385 + q];
      __builtin_amdgcn_raw_buffer_store_b32(a | (b << 16), ws_rsrc(p.ws), (int)(kout + 4 * tid), 0, 16);
      return;
    } else if (w == 2) {
      const int q = l0 >> 2;
      const unsigned a = ku[256 + q], b = ku[257 + q], c = ku[258 + q];
      o[0] = c | (c << 16); o[1] = b | (b << 16); o[2] = b | (a << 16); o[3] = a | (a << 16);
    } else {
      unsigned v[8];
#pragma unroll
      for (int e = 0; e < 8; ++e) {
        const int l = l0 + 7 - e;
        const int idx = l < 256 ? l : (int)(pk_bf((float)l, 0.f) & 0xffffu) - 17024;
        v[e] = ku[idx];
      }
#pragma unroll
      for (int q = 0; q < 4; ++q) o[q] = v[2 * q] | (v[2 * q + 1] << 16);
    }
    st16(ws_rsrc(p.ws), kout + 16 * tid - 1536, o);
  }
}

__global__ NOPK __launch_bounds__(256) void k1(K1Args p) {
  __shared__ __attribute__((aligned(16))) char smem[57 * KG_RS + 1312];
  int b = blockIdx.x;
  char* stamp_ws = p.ws; (void)stamp_ws;
  STAMP(0, 0);
  if (b < NB_PF) { k1_prefetch(p); STAMP(0, 7); return; }
  b -= NB_PF;
  if (b < NB_K) { k1_kgen(p, KMAP ? (b & 7) * 80 + (b >> 3) : b, smem); VM0(); STAMP(0, 7); return; }
  b -= NB_K;
  asm volatile("" :: "s"(p.ws), "s"(p.x));
#define DL_LN 0
#define DL_MOD 12
  if (b < NB_LN) { __builtin_amdgcn_s_sleep(DL_LN); k1_ln(p, b, smem); }
  else { __builtin_amdgcn_s_sleep(DL_MOD); k1_mod(p, b - NB_LN, smem); }
  VM0();
  STAMP(0, 7);
}

constexpr int K2_CS = 4160;
constexpr int K2_KOFF = 4 * K2_CS;
constexpr int K2_CH = K2_KOFF + 4608;
constexpr int CV_RED = 3 * K2_CH;
constexpr int CV_RT2 = 86016;
constexpr int CV_SUM = CV_RT2 + 4 * (32 * RT_STR + 256);
constexpr int CV_XCH = CV_SUM + 2560;
constexpr int CV_FL = CV_RED + 20480;

struct Chan { float sa, ca1, ga, dsk, hs, hinv, gy, gd; };
struct ChanRaw { float m, kinv, dsk; };
DEV ChanRaw chan_ld(const unsigned short* dskip, const char* ws, int d, int lane) {
  ChanRaw c;
  c.kinv = ldu<float>(ws + WS_KSC + 4 * d);
  c.dsk = ldbf_u(dskip + d);
  c.m = ((const float*)(ws + WS_MOD))[3840 * (lane & 7) + D_ * ((lane >> 3) & 3) + d];
  return c;
}
template <int CTRL> DEV float dpp_add(float v) {
  return v + __int_as_float(__builtin_amdgcn_update_dpp(0, __float_as_int(v), CTRL, 0xf, 0xf, true));
}
DEV Chan chan_fin(const ChanRaw& r) {
  Chan c;
  const int t = __float_as_int(rbf(dpp_add<0x141>(dpp_add<0x4e>(dpp_add<0xb1>(r.m)))));
  c.sa = __int_as_float(__builtin_amdgcn_readlane(t, 0));
  c.ca1 = rbf(1.f + __int_as_float(__builtin_amdgcn_readlane(t, 8)));
  c.ga = __int_as_float(__builtin_amdgcn_readlane(t, 16));
  c.dsk = r.dsk;
  const float hb = 26.f * fabsf(c.ca1) + fabsf(c.sa);
  int ex = (int)((__float_as_uint(hb) >> 23) & 0xff);
  ex = ex < 20 ? 20 : (ex > 240 ? 240 : ex);
  c.hs = __uint_as_float((unsigned)(127 + 14 + 127 - ex) << 23);
  c.hinv = __uint_as_float((unsigned)(ex - 14) << 23);
  c.gy = r.kinv * c.hinv * c.ga;
  c.gd = c.hinv * c.dsk * c.ga;
  return c;
}
DEV size_t tl_off(int d, int t) { return (((size_t)(t >> 5) * D_ + d) * 32 + (t & 31)) * 2; }
struct StageRaw { u32x4 xa[2], kv1; u32x2 xb; unsigned kv0; };
DEV StageRaw stage_ld(const char* ws, int d, int th) {
  StageRaw s;
  const unsigned short* krev = (const unsigned short*)(ws + WS_KREV) + (size_t)d * KREV;
  const int T = 1024 * (th >> 6) + 8 * (th & 63);
  s.xb = ldu<u32x2>(ws + WS_XNT + tl_off(d, 1024));
  s.xa[0] = *(const u32x4*)(ws + WS_XNT + tl_off(d, T)); s.xa[1] = *(const u32x4*)(ws + WS_XNT + tl_off(d, T + 512));
  s.kv0 = *(const unsigned*)(krev + 2 * th); s.kv1 = *(const u32x4*)(krev + 256 + 8 * th);
  return s;
}
DEV void stage_wr(const StageRaw& s, const Chan& c, char* smem, int th) {
  constexpr int CS = K2_CS, KOFF = K2_KOFF;
  const h16x2 c2 = __builtin_bit_cast(h16x2, pk_h(c.ca1 * c.hs, c.ca1 * c.hs)), s2 = __builtin_bit_cast(h16x2, pk_h(c.sa * c.hs, c.sa * c.hs));
  auto mod = [&](unsigned u) __attribute__((always_inline)) {
    return __builtin_bit_cast(unsigned, __builtin_elementwise_fma(__builtin_bit_cast(h16x2, u), c2, s2));
  };
  unsigned h2[2][6];
#pragma unroll
  for (int e = 0; e < 2; ++e)
#pragma unroll
    for (int q = 0; q < 4; ++q) h2[e][q] = mod(s.xa[e][q]);
#pragma unroll
  for (int q = 0; q < 2; ++q) {
    h2[0][4 + q] = __builtin_amdgcn_update_dpp(__builtin_amdgcn_readfirstlane(h2[1][q]), h2[0][q], 0x130, 0xf, 0xf, false);
    h2[1][4 + q] = __builtin_amdgcn_update_dpp(mod(s.xb[q]), h2[1][q], 0x130, 0xf, 0xf, false);
  }
  char* dst = smem + (128 * (th >> 6) + (th & 63) + 2) * 16;
  unsigned s1[5];
#pragma unroll
  for (int e = 1; e >= 0; --e) {
    const unsigned* hh = h2[e];
#pragma unroll
    for (int q = 0; q < 5; ++q) s1[q] = __builtin_amdgcn_alignbit(hh[q + 1], hh[q], 16);
    *(u32x4*)(dst + 1024 * e) = (u32x4){hh[0], hh[1], hh[2], hh[3]};
    *(u32x4*)(dst + 1024 * e + CS) = (u32x4){s1[0], s1[1], s1[2], s1[3]};
    *(u32x4*)(dst + 1024 * e + 2 * CS) = (u32x4){hh[1], hh[2], hh[3], hh[4]};
    *(u32x4*)(dst + 1024 * e + 3 * CS) = (u32x4){s1[1], s1[2], s1[3], s1[4]};
  }
  {
    const unsigned a = __builtin_amdgcn_readfirstlane(h2[0][0]), b = __builtin_amdgcn_readfirstlane(s1[0]);
    if (th >= 1 && th < 4)
      *(u32x4*)(smem + th * CS + 16) = (u32x4){0u, 0u, th == 3 ? a << 16 : 0u, th == 1 ? a << 16 : (th == 2 ? a : b)};
  }
  {
    const unsigned aa = __builtin_amdgcn_perm(s.kv0, s.kv0, 0x01000100u), bb = __builtin_amdgcn_perm(s.kv0, s.kv0, 0x03020302u);
    const unsigned ba = __builtin_amdgcn_perm(s.kv0, s.kv0, 0x01000302u);
    *(u32x4*)(smem + KOFF + 16 * th) = (u32x4){bb, (th & 1) ? ba : bb, aa, aa};
  }
  *(u32x4*)(smem + KOFF + 2048 + 16 * th) = s.kv1;
}
DEV void stage_zero(char* smem, int th) {
  if (th < 37) *(u32x4*)(smem + (th < 32 ? K2_KOFF + 4096 + 16 * th : (th < 36 ? (th - 32) * K2_CS : 16))) = (u32x4){0u, 0u, 0u, 0u};
}
DEV f32x4 conv_out(const f32x4& y, const Chan& c, const char* smem, int t) {
  const u32x2 hv = *(const u32x2*)(smem + 2 * (t + 16));
  f32x4 o;
#pragma unroll
  for (int e = 0; e < 2; ++e) {
    const unsigned hu = hv[e];
    const h16x2 h2 = __builtin_bit_cast(h16x2, hu);
    o[2 * e] = fmaf((float)h2[0], c.gd, y[2 * e] * c.gy);
    o[2 * e + 1] = fmaf((float)h2[1], c.gd, y[2 * e + 1] * c.gy);
  }
  return o;
}
DEV unsigned add_bf(unsigned x, float a, float b) { return pk_bf(bf_lo(x) + a, bf_hi(x) + b); }
#define K2_LOAD(buf, cc, u)                                                                                     \
  asm volatile("ds_read_b64 %0, %3 offset:%5\n\tds_read_b64 %1, %3 offset:%6\n\tds_read_b128 %2, %4 offset:%7"   \
               : "=&v"(Al[buf][u]), "=&v"(Ah[buf][u]), "=&v"(Bf[buf][u])                                          \
               : "v"(ap0), "v"(bp0), "i"(64 * (cc) + 512 * (u)), "i"(64 * (cc) + 512 * (u) + 8),                 \
                 "i"(64 * (cc) + 3584 - 512 * (u)))
template <int SET> DEV constexpr int half_tile(int j) {
  return SET == 0 ? (j == 0 ? 7 : (j == 1 ? 6 : 2)) : (j < 3 ? 5 - j : 4 - j);
}
template <int SET>
DEV void conv_half(unsigned ap0, unsigned bp0, char* redw) {
  constexpr int NU = SET == 0 ? 8 : 6, NT = SET == 0 ? 3 : 5;
  u32x2 Al[2][NU], Ah[2][NU];
  u32x4 Bf[2][NU];
#pragma unroll
  for (int u = 0; u < NU; ++u) K2_LOAD(0, 0, u);
  f32x4 acc[NT];
#pragma unroll
  for (int j = 0; j < NT; ++j) acc[j] = (f32x4){0.f, 0.f, 0.f, 0.f};
#pragma unroll
  for (int cc = 0; cc < 2; ++cc) {
    lgkm0();
    if (cc == 0) {
#pragma unroll
      for (int u = 0; u < NU; ++u) K2_LOAD(1, 1, u);
    }
#pragma unroll
    for (int sp = 0; sp < NU; ++sp)
#pragma unroll
      for (int j = 0; j < NT; ++j) {
        const int q = half_tile<SET>(j);
        if (sp <= q) {
          const u32x4 A = {Al[cc][sp][0], Al[cc][sp][1], Ah[cc][sp][0], Ah[cc][sp][1]};
          acc[j] = mfma_f16(A, Bf[cc][q - sp], acc[j]);
        }
      }
    SB();
  }
#pragma unroll
  for (int j = 0; j < NT; ++j) *(f32x4*)(redw + j * 4096) = acc[j];
}

template <int NF>
DEV void w1_fin(const RtRaw& t, const ModP& mp, char* ws, int kk, int ct0, int lane, char* scr, float* sums) {
  const int g = lane >> 4, i = lane & 15;
  float* tb = (float*)(scr + 32 * RT_STR);
  const float mv = mod_fin(mp);
  tb[lane] = lane < 32 ? rbf(1.f + mv) : mv;
  rt_park<NF>(t, scr, lane);
  lgkm0();
  const f32x4 ca = *(const f32x4*)(tb + 8 * g), cb = *(const f32x4*)(tb + 8 * g + 4);
  const f32x4 sa = *(const f32x4*)(tb + 32 + 8 * g), sb = *(const f32x4*)(tb + 36 + 8 * g);
  const u32x4 sv = {pk_bf(sa[0], sa[1]), pk_bf(sa[2], sa[3]), pk_bf(sb[0], sb[1]), pk_bf(sb[2], sb[3])};
  const u32x4 ones = {0x3f803f80u, 0x3f803f80u, 0x3f803f80u, 0x3f803f80u};
  const f32x4 z = {0.f, 0.f, 0.f, 0.f};
  const rsrc_t rs = ws_rsrc(ws);
#pragma unroll
  for (int j = 0; j < NF; ++j) {
    const u32x4 f = rt_frag(scr, j, lane);
    const u32x4 sc = {pk_bf(bf_lo(f[0]) * ca[0], bf_hi(f[0]) * ca[1]), pk_bf(bf_lo(f[1]) * ca[2], bf_hi(f[1]) * ca[3]),
                      pk_bf(bf_lo(f[2]) * cb[0], bf_hi(f[2]) * cb[1]), pk_bf(bf_lo(f[3]) * cb[2], bf_hi(f[3]) * cb[3])};
    st16(rs, WS_W1F + ((size_t)(kk * W1KS + (ct0 + j) * W1TS) * 64 + lane) * 16, sc);
    const f32x4 cs = mfma_bf16(sc, ones, z), b1 = mfma_bf16(f, sv, z);
    if (i < 2) *(f32x4*)(sums + i * (16 * NF) + 16 * j + 4 * g) = i ? b1 : cs;
  }
}

DEV void conv_phase(const unsigned short* __restrict__ dskip, char* __restrict__ ws,
                    const unsigned short* __restrict__ w1, const unsigned short* __restrict__ w2, char* smem_all) {
  constexpr int CS = K2_CS, KOFF = K2_KOFF;
  const int tid = threadIdx.x, lane = tid & 63;
  const int w = __builtin_amdgcn_readfirstlane(tid >> 6);
  const int bx = blockIdx.x;
  const int pj = (bx & 7) * 16 + (bx >> 4), par = (bx >> 3) & 1;
  const int r = lane & 15, g = lane >> 4, th = tid & 127;
  const int s4 = (r + 1) & 3;
  const rsrc_t rs = ws_rsrc(ws);
  unsigned* fl = (unsigned*)(smem_all + CV_FL);
  char* stamp_ws = ws; (void)stamp_ws;
  STAMP(1, 0);
  asm volatile("" :: "s"(w1), "s"(w2));
  if (tid < 12) fl[tid] = 0u;
  if (w < 4) {
    const int d = 5 * pj + 2 * par + (w >> 1), hw = w & 1;
    char* smem = smem_all + (w >> 1) * K2_CH;
    const ChanRaw cr = chan_ld(dskip, ws, d, lane);
    SB();
    const StageRaw sr = stage_ld(ws, d, th);
    u32x4 xr4[2];
    u32x2 xres[4];
    auto ld_res = [&]() __attribute__((always_inline)) {
#pragma unroll
      for (int qp = 0; qp < 2; ++qp)
        xr4[qp] = *(const u32x4*)(ws + WS_XT + tl_off(d, 256 * (4 * hw + 2 * qp + (g & 1)) + 16 * r + 8 * (g >> 1)));
    };
    ld_res();
    stage_zero(smem, th);
    SB();
    asm volatile("s_waitcnt lgkmcnt(0)\n\ts_barrier" ::: "memory");
    SB();
    const Chan c = chan_fin(cr);
    STAMP(1, 1);
    stage_wr(sr, c, smem, th);
    lds_post(fl + 6 + (w >> 1), lane);
    lds_wait(fl + 6 + (w >> 1), 2u);
    STAMP(1, 2);
    f32x4 acc[8], keep[4];
#pragma unroll
    for (int q = 0; q < 8; ++q) acc[q] = (f32x4){0.f, 0.f, 0.f, 0.f};
    {
      const unsigned ap0 = (unsigned)(size_t)(smem + s4 * CS + 2 * (128 * hw + 8 * g + (r + 1) - s4));
      const unsigned bp0 = (unsigned)(size_t)(smem + KOFF + 2 * (2032 + 128 * hw - 16 * r + 8 * g) - 3584);
      u32x2 Al[2][8], Ah[2][8];
      u32x4 Bf[2][8];
#pragma unroll
      for (int u = 0; u < 8; ++u) K2_LOAD(0, 0, u);
      auto tile = [&](int cc, int q) __attribute__((always_inline)) {
#pragma unroll
        for (int sp = 0; sp <= q; ++sp) {
          const u32x4 A = {Al[cc & 1][sp][0], Al[cc & 1][sp][1], Ah[cc & 1][sp][0], Ah[cc & 1][sp][1]};
          acc[q] = mfma_f16(A, Bf[cc & 1][q - sp], acc[q]);
        }
        SB();
      };
#pragma unroll
      for (int cc = 0; cc < 3; ++cc) {
        lgkm0();
#pragma unroll
        for (int q = 0; q < 8; ++q) {
          K2_LOAD((cc + 1) & 1, cc + 1, q);
          tile(cc, q);
        }
      }
#pragma unroll
      for (int qp = 0; qp < 2; ++qp) {
        const auto s0 = __builtin_amdgcn_permlane16_swap(xr4[qp][0], xr4[qp][2], false, false);
        const auto s1 = __builtin_amdgcn_permlane16_swap(xr4[qp][1], xr4[qp][3], false, false);
        xres[2 * qp] = (u32x2){s0[0], s1[0]}; xres[2 * qp + 1] = (u32x2){s0[1], s1[1]};
      }
      lgkm0();
      f32x4* xw = (f32x4*)(smem_all + CV_XCH + w * 4096) + lane;
      auto last = [&](auto hw_tag) __attribute__((always_inline)) {
        constexpr int S = decltype(hw_tag)::value ? 0 : 4, K = 4 - S;
#pragma unroll
        for (int q = 0; q < 4; ++q) {
          tile(3, S + q);
          if (q) { xw[64 * (q - 1)] = acc[S + q - 1]; SB(); }
        }
        tile(3, K + 3);
        xw[64 * 3] = acc[S + 3];
        lds_post(fl + w, lane);
        SB();
#pragma unroll
        for (int q = 0; q < 3; ++q) tile(3, K + q);
#pragma unroll
        for (int q = 0; q < 4; ++q) keep[q] = acc[K + q];
      };
      if (hw) last(std::true_type{}); else last(std::false_type{});
    }
    STAMP(1, 3);
    lds_wait(fl + (w ^ 1), 1u);
    STAMP(1, 4);
    const f32x4* xrd = (const f32x4*)(smem_all + CV_XCH + (w ^ 1) * 4096) + lane;
#pragma unroll
    for (int qp = 0; qp < 2; ++qp) {
      u32x2 o[2];
#pragma unroll
      for (int h = 0; h < 2; ++h) {
        const int q = 2 * qp + h;
        const f32x4 v = conv_out(keep[q] + xrd[64 * q], c, smem, 256 * (4 * hw + q) + 16 * r + 4 * g);
        o[h] = (u32x2){add_bf(xres[q][0], v[0], v[1]), add_bf(xres[q][1], v[2], v[3])};
      }
      const auto s0 = __builtin_amdgcn_permlane16_swap(o[0][0], o[1][0], false, false);
      const auto s1 = __builtin_amdgcn_permlane16_swap(o[0][1], o[1][1], false, false);
      const int t = 256 * (4 * hw + 2 * qp + (g & 1)) + 16 * r + 8 * (g >> 1);
      st16(rs, WS_X1T + (((size_t)(t >> 6) * D_ + d) * 64 + (t & 63)) * 2, (u32x4){s0[0], s1[0], s0[1], s1[1]});
    }
  } else {
#define PRIO47 2
    __builtin_amdgcn_s_setprio(PRIO47);
    const int d = 5 * pj + 4, wq = w - 4;
    char* smem = smem_all + 2 * K2_CH;
    StageRaw sr;
    const ChanRaw cr = chan_ld(dskip, ws, d, lane);
    asm volatile("" ::: "memory");
    if (wq < 2) sr = stage_ld(ws, d, th);
    asm volatile("" ::: "memory");
    const int tl0 = par ? (wq < 3 ? 5 - wq : 1) : (wq == 0 ? 7 : (wq == 1 ? 6 : 2)), tl1 = 0;
    const unsigned ap0 = (unsigned)(size_t)(smem + s4 * CS + 2 * (64 * wq + 8 * g + (r + 1) - s4));
    const unsigned bp0 = (unsigned)(size_t)(smem + KOFF + 2 * (2032 + 64 * wq - 16 * r + 8 * g) - 3584);
    char* redw = smem_all + CV_RED + wq * 1024 + lane * 16;
    u32x2 xres[2];
    xres[0] = *(const u32x2*)(ws + WS_XT + tl_off(d, 256 * tl0 + 16 * r + 4 * g));
    xres[1] = *(const u32x2*)(ws + WS_XT + tl_off(d, 256 * tl1 + 16 * r + 4 * g));
#define NF1_ 4
#define NF2_ 5
    constexpr int NF1 = NF1_, NC1 = 108 / NF1, NB1 = 5 * NC1, NF2 = NF2_, NC2 = 40 / NF2, NJ2 = 54 * NC2;
    const bool isw1 = bx < NB1;
    const int j2 = 4 * (bx - NB1) + wq;
    const bool isw2 = !isw1 && j2 < NJ2, isgb = j2 >= NJ2 && j2 < NJ2 + 10;
    const int jk = isw1 ? 4 * (bx / NC1) + wq : j2 / NC2;
    const int jc = isw1 ? NF1 * (bx % NC1) : NF2 * (j2 % NC2);
    RtRaw rt;
    const int gbi = 64 * (j2 - NJ2) + lane;
    ModP mp;
    SB();
#define RT_MODE 2
#define RT67 8
    auto rtl = [&]() __attribute__((always_inline)) {
      if (isw1) rt_load<NF1>(rt, w1, FF, jk, jc, lane);
      else if (isw2) rt_load<NF2>(rt, w2, D_, jk, jc, lane);
      SB();
    };
    if (wq == 0) stage_zero(smem, th);
    SB();
    __builtin_amdgcn_s_barrier();
    SB();
    const Chan c = chan_fin(cr);
    if (wq < 2) {
      stage_wr(sr, c, smem, th);
      lds_post(fl + 8, lane);
    }
    if (wq >= 2 && RT67) __builtin_amdgcn_s_sleep(RT67);
    rtl();
    lds_wait(fl + 8, 2u);
    STAMP(1, 2);
    if (isw1 || isgb) mp = mod_ld(rs, isw1 ? (lane < 32 ? 4 * D_ : 3 * D_ - 32) + 32 * jk + lane : 5 * D_ + gbi);
    SB();
    if (par) conv_half<1>(ap0, bp0, redw); else conv_half<0>(ap0, bp0, redw);
    lds_post(fl + 4, lane);
    STAMP(1, 3);
    STAMP(1, 4);
    lds_wait(fl + 4, 4u);
    STAMP(1, 5);
    const char* rd = smem_all + CV_RED + lane * 16;
#pragma unroll
    for (int u = 0; u < 2; ++u) {
      const bool on = u == 0 ? (par || wq < 3) : (par && wq == 0);
      if (on) {
        const int sl = u == 0 ? wq : 4, tl = u == 0 ? tl0 : tl1;
        const f32x4 y = (*(const f32x4*)(rd + sl * 4096) + *(const f32x4*)(rd + sl * 4096 + 1024))
                      + (*(const f32x4*)(rd + sl * 4096 + 2048) + *(const f32x4*)(rd + sl * 4096 + 3072));
        const int t = 256 * tl + 16 * r + 4 * g;
        const f32x4 v = conv_out(y, c, smem, t);
        st8(rs, WS_X1T + (((size_t)(t >> 6) * D_ + d) * 64 + (t & 63)) * 2, (u32x2){add_bf(xres[u][0], v[0], v[1]), add_bf(xres[u][1], v[2], v[3])});
      }
    }
    SB();
    STAMP(1, 6);
    char* scr = smem_all + CV_RT2 + wq * (32 * RT_STR + 256);
    if (isw1) {
      float* sums = (float*)(smem_all + CV_SUM);
      w1_fin<NF1>(rt, mp, ws, jk, jc, lane, scr, sums + 32 * NF1 * wq);
      asm volatile("" ::: "memory");
      unsigned old = 0u;
      if (lane == 0) old = __hip_atomic_fetch_add(fl + 5, 1u, __ATOMIC_RELAXED, __HIP_MEMORY_SCOPE_WORKGROUP);
      asm volatile("" ::: "memory");
      if (__builtin_amdgcn_readfirstlane(old) == 3u && lane < 8 * NF1) {
        const f32x4* sp = (const f32x4*)sums + lane;
        const f32x4 v = (sp[0] + sp[8 * NF1]) + (sp[16 * NF1] + sp[24 * NF1]);
        st16(rs, WS_CSP + ((size_t)(2 * (bx / NC1) + lane / (4 * NF1)) * FF + 16 * jc + 4 * (lane % (4 * NF1))) * 4, __builtin_bit_cast(u32x4, v));
      }
    } else if (isw2) {
      rt_park<NF2>(rt, scr, lane);
      lgkm0();
#pragma unroll
      for (int j = 0; j < NF2; ++j) st16(rs, WS_W2F + ((size_t)((jc + j) * 54 + jk) * 64 + lane) * 16, rt_frag(scr, j, lane));
    } else if (isgb) {
      st4(ws, WS_GB + 4 * gbi, __float_as_uint(mod_fin(mp)));
    }
  }
}

__global__ NOPK __launch_bounds__(512, 2) void k2(const unsigned short* __restrict__ dskip, char* __restrict__ ws,
                                                  const unsigned short* __restrict__ w1, const unsigned short* __restrict__ w2) {
  __shared__ __attribute__((aligned(16))) char smem[CV_XCH + 16384];
  conv_phase(dskip, ws, w1, w2, smem);
  char* stamp_ws = ws; (void)stamp_ws;
  VM0();
  STAMP(1, 7);
}

DEV u32x4 ld16_sc1(rsrc_t r, int voff, int soff) { return __builtin_amdgcn_raw_buffer_load_b128(r, voff, soff, 16); }
DEV u32x4 ld16(rsrc_t r, int voff, int soff) { return __builtin_amdgcn_raw_buffer_load_b128(r, voff, soff, 0); }
DEV u32x4 tr_frag(const char* p) {
  const u32x2 a = __builtin_bit_cast(u32x2, __builtin_amdgcn_ds_read_tr16_b64_v4i16((lds_s16x4*)p));
  const u32x2 b = __builtin_bit_cast(u32x2, __builtin_amdgcn_ds_read_tr16_b64_v4i16((lds_s16x4*)(p + 256)));
  return (u32x4){a[0], a[1], b[0], b[1]};
}

constexpr int KF_RS = 84;
constexpr int KF_RB = 8 * 16 * KF_RS * 4;
constexpr int KF_BIG = 2 * KF_RB;
constexpr int NW2L = 8, NW2R = 27;
constexpr int KF_TAB = KF_BIG;
constexpr int KF_PS = KF_TAB + 320;
constexpr int KF_XR = KF_PS + 512;
constexpr int KF_CNT = KF_XR + 64 * 176;
constexpr int KF_LDS = KF_CNT + 16;
#define X_FIRST 5
#define X_STEP 4
#define W1_EARLY 3
#define W1_MID 7
#define W1_PRE 10
#define W2_START 6
#define W2_RATE 2
#define W2_INL 28
#define CS_STEP 14
#define SIG_SPLIT 4
#define H1_PRE 7
#define H1_BATCH 99
#define LOCK_MASK 0x55554u

DEV void kf_body(char* __restrict__ ws, unsigned short* __restrict__ out, char* smem) {
  char* xs = smem;
  float* tab = (float*)(smem + KF_TAB);
  float* ps = (float*)(smem + KF_PS);
  char* xr = smem + KF_XR;
  unsigned* lcnt = (unsigned*)(smem + KF_CNT);
  float* cst = (float*)(smem + 81920);
  const int tid = threadIdx.x, lane = tid & 63;
  const int w = __builtin_amdgcn_readfirstlane(tid >> 6);
  const int bx = blockIdx.x;
  const int ng = (bx & 1) * 4 + ((bx >> 3) & 3), mt = ((bx & 7) >> 1) * 8 + (bx >> 5);
  const int g = lane >> 4, i = lane & 15;
  const rsrc_t rs_ = ws_rsrc(ws);

  char* stamp_ws = ws; (void)stamp_ws;
  STAMP(2, 0);
  unsigned* flags = (unsigned*)(ws + WS_CNT) + mt * (2 * FL_P);
  const int ntl = ng < 6 ? 14 : 12;
  const int ctb = ng < 6 ? 14 * ng : 84 + 12 * (ng - 6);
  const bool two = w < ntl - 8;
  const bool full = w < 6;
  const int ks0 = w < 6 ? 7 * w : 42 + 6 * (w - 6);
  const int ct0 = ctb + (two ? 2 * w : (ntl - 8) + w);
  const int lo16 = lane * 16;
  const int w1s = (int)WS_W1F + ct0 * W1TS * 1024;
  const int w2s = (int)WS_W2F + (ng * 5 * 54 + ks0) * 1024;

  u32x4 ring[32], wl[NW2L];
  const char* bb0 = xs + (8 * g + (i >> 2)) * 64 + ((g & 1) << 5) + (i & 3) * 8;
  const char* bb1 = xs + (8 * g + (i >> 2)) * 64 + (((g & 1) ^ 1) << 5) + (i & 3) * 8;
  auto w2_off = [&](int e) __attribute__((always_inline)) {
    const int s = e < 27 ? e / 4 : (e < 34 ? e - 27 : 6), c = e < 27 ? e % 4 : (e < 34 ? 4 : 3);
    const int sn = (s == 6 && !full) ? 5 : s;
    return w2s + (c * 54 + sn) * 1024;
  };

  auto gemm1 = [&](auto two_tag) __attribute__((always_inline)) {
    constexpr bool TWO = decltype(two_tag)::value;
    if (TWO) asm volatile("; two tiles" ::: "memory"); else asm volatile("; one tile" ::: "memory");
    u32x4 t[10];
    auto st_x = [&](int i0, int i1) __attribute__((always_inline)) {
#pragma unroll
      for (int it = i0; it < i1; ++it) {
        const int c = tid + 512 * it;
        const int row = c >> 3;
        *(u32x4*)(xs + ((c >> 2) & 1) * 40960 + row * 64 + (((c & 3) ^ ((row >> 2) & 2)) << 4)) = t[it];
      }
    };
#pragma unroll
    for (int it = 0; it < X_FIRST; ++it) t[it] = ld16(rs_, tid * 16, (int)WS_X1T + mt * 81920 + 8192 * it);
#pragma unroll
    for (int k = 0; k < W1_EARLY; ++k) {
      ring[2 * k] = ld16(rs_, lo16, w1s + k * W1KS * 1024);
      if (TWO) ring[2 * k + 1] = ld16(rs_, lo16, w1s + (k * W1KS + W1TS) * 1024);
    }
    SB();
    __builtin_amdgcn_s_barrier();
    SB();
#pragma unroll
    for (int k = W1_EARLY; k < W1_MID; ++k) {
      ring[2 * k] = ld16(rs_, lo16, w1s + k * W1KS * 1024);
      if (TWO) ring[2 * k + 1] = ld16(rs_, lo16, w1s + (k * W1KS + W1TS) * 1024);
    }
    SB();
    st_x(0, X_FIRST);
    SB();
    if (tid == 0) *lcnt = 0u;
    float gbp;
    if (!TWO) gbp = ((const float*)(ws + WS_GB))[80 * ng + min((w & 1) * 64 + lane, 79)];
    SB();
    __syncthreads();
    STAMP(2, 1);
    auto resid = [&]() __attribute__((always_inline)) {
      const int kb = (80 * ng) >> 5, tq = w & 3;
#pragma unroll
      for (int u = 0; u < 2; ++u) {
        const int kk = min(kb + 2 * (w >> 2) + u, 19);
        const u32x4 f = tr_frag(((tq & 1) ? bb1 : bb0) + (tq >> 1) * 40960 + kk * 2048);
        const unsigned dl = (unsigned)(32 * kk + 8 * g - 80 * ng);
        if (dl < 80u) *(u32x4*)(xr + (tq * 16 + i) * 176 + dl * 2) = f;
      }
    };
    const int hs = TWO ? 0 : (w & 1);
    const char* ba[4] = {bb0 + hs * 40960, bb1 + hs * 40960, bb0 + (hs ^ 1) * 40960, bb1 + (hs ^ 1) * 40960};
    const u32x4 ones = {0x3f803f80u, 0x3f803f80u, 0x3f803f80u, 0x3f803f80u};
    f32x4 acc[2][4];
#pragma unroll
    for (int c = 0; c < 2; ++c)
#pragma unroll
      for (int q = 0; q < 4; ++q) acc[c][q] = (f32x4){0.f, 0.f, 0.f, 0.f};
    u32x4 csp[5];
    const int cso = ((w & 1) * FF + 16 * ctb + 4 * min(lane, 4 * ntl - 1)) * 4;
    u32x4 bn[2][4];
#pragma unroll
    for (int q = 0; q < 4; ++q) bn[0][q] = tr_frag(ba[q]);
#pragma unroll
    for (int q = 0; q < 4; ++q) bn[1][q] = tr_frag(ba[q] + 2048);
#pragma unroll
    for (int kk = 0; kk < 20; ++kk) {
      if (kk == X_STEP) { st_x(X_FIRST, 10); SB(); }
      if (kk == 2 * X_FIRST - 2) { SB(); bar_lds(); SB(); }
      else if ((LOCK_MASK >> kk) & 1) { SB(); __builtin_amdgcn_s_barrier(); SB(); }
      if (kk == 2 * X_FIRST) { resid(); SB(); }
      auto gload = [&](int u) __attribute__((always_inline)) {
        {
          constexpr int CATCH = W1_PRE - W1_MID;
          const int k0 = kk == 0 ? W1_MID : (kk <= CATCH ? W1_MID + 2 * kk - 1 : kk + W1_PRE);
          const int nk = (kk >= 1 && kk <= CATCH) ? 2 : 1;
          const int kn = k0 + (nk == 2 ? (u >> 1) : 0);
          const bool on = nk == 2 || u >= 2;
          if (on && kn < 20) {
            if ((u & 1) == 0) ring[(2 * kn) % 32] = ld16(rs_, lo16, w1s + kn * W1KS * 1024);
            else if (TWO) ring[(2 * kn + 1) % 32] = ld16(rs_, lo16, w1s + (kn * W1KS + W1TS) * 1024);
          }
          if (kk == 0 && u < 2) {
#pragma unroll
            for (int it = X_FIRST + u * 3; it < (u ? 10 : (X_FIRST + 3 < 10 ? X_FIRST + 3 : 10)); ++it)
              t[it] = ld16(rs_, tid * 16, (int)WS_X1T + mt * 81920 + 8192 * it);
          }
        }
        if (kk >= W2_START && u < W2_RATE) {
          const int n = W2_RATE * (kk - W2_START) + u;
          if (n < NW2L) wl[n] = ld16(rs_, lo16, w2_off(NW2R + n));
          else if (n < W2_INL) ring[8 + n - NW2L] = ld16(rs_, lo16, w2_off(n - NW2L));
        }
      };
      const u32x4 a0 = ring[(2 * kk) % 32], a1 = ring[(2 * kk + 1) % 32];
#pragma unroll
      for (int q = 0; q < 4; ++q) {
        const u32x4 bq = bn[kk & 1][q];
        acc[0][q] = mfma_bf16(a0, bq, acc[0][q]);
        if (TWO) acc[1][q] = mfma_bf16(a1, bq, acc[1][q]);
        else if (q < 2) {
          acc[1][2 * q] = mfma_bf16(ones, bq, acc[1][2 * q]);
          acc[1][2 * q + 1] = mfma_bf16(bq, bq, acc[1][2 * q + 1]);
        }
        SB();
        if (kk < 18) bn[kk & 1][q] = tr_frag(ba[q] + (kk + 2) * 2048);
        gload(q);
        SB();
      }
      if (!TWO) {
        if (kk < 5) csp[kk] = ld16(rs_, cso, (int)WS_CSP + kk * 2 * FF * 4);
        if (kk == CS_STEP) {
          f32x4 t = __builtin_bit_cast(f32x4, csp[0]);
#pragma unroll
          for (int u = 1; u < 5; ++u) t += __builtin_bit_cast(f32x4, csp[u]);
          if (w >= 6 && lane < 56) *(f32x4*)(cst + (w & 1) * 224 + 4 * lane) = t;
        }
      }
      if (!TWO && kk == 12) { if ((w & 1) * 64 + lane < 80) tab[(w & 1) * 64 + lane] = gbp; }
      SB();
    }
    STAMP(2, 2);
    if (!TWO) {
#pragma unroll
      for (int h = 0; h < 2; ++h) {
        const f32x4 aq = acc[1][2 * h + 1];
        const float dq = (i & 2) ? ((i & 1) ? aq[3] : aq[2]) : ((i & 1) ? aq[1] : aq[0]);
        const int tk = (2 * hs + h) * 16 + i;
        if (g == (i >> 2)) {
          const float mean = acc[1][2 * h][0] * (1.f / D_);
          const float var = fmaxf(dq * (1.f / D_) - mean * mean, 0.f);
          const float sd = rbf(sqrtf(rbf(rbf(var) + 1e-5f)));
          const float rs = 1.f / sd;
          ps[tk] = rs;
          ps[64 + tk] = -rbf(mean) * rs;
        }
      }
    }
    __syncthreads();
    f32x4 csv[2], b1v[2];
#pragma unroll
    for (int c = 0; c < (TWO ? 2 : 1); ++c) {
      csv[c] = *(const f32x4*)(cst + 16 * (ct0 - ctb + c) + 4 * g);
      b1v[c] = *(const f32x4*)(cst + 224 + 16 * (ct0 - ctb + c) + 4 * g);
    }
#pragma unroll
    for (int qp = 0; qp < 2; ++qp) {
      u32x2 o[2][2];
#pragma unroll
      for (int h = 0; h < 2; ++h) {
        const int q = 2 * qp + h, tq = q ^ (2 * hs);
        const float rs = ps[tq * 16 + i], nm = ps[64 + tq * 16 + i];
#pragma unroll
        for (int c = 0; c < (TWO ? 2 : 1); ++c) {
          float v[4];
#pragma unroll
          for (int r = 0; r < 4; ++r) v[r] = fmaxf(fmaf(acc[c][q][r], rs, fmaf(nm, csv[c][r], b1v[c][r])), 0.f);
          o[c][h] = (u32x2){pk_bf(v[0], v[1]), pk_bf(v[2], v[3])};
        }
      }
      const int tl = ((2 * qp) ^ (2 * hs)) + (g & 1);
#pragma unroll
      for (int c = 0; c < (TWO ? 2 : 1); ++c) {
        const int ct = ct0 + c;
        const auto s0 = __builtin_amdgcn_permlane16_swap(o[c][0][0], o[c][1][0], false, false);
        const auto s1 = __builtin_amdgcn_permlane16_swap(o[c][0][1], o[c][1][1], false, false);
        const size_t off = (((size_t)(4 * mt + tl) * 54 + (ct >> 1)) * 64 + i + 16 * (2 * (ct & 1) + (g >> 1))) * 16;
        st16(rs_, WS_H1F + off, (u32x4){s0[0], s1[0], s0[1], s1[1]});
      }
    }
    SB();
    __builtin_amdgcn_s_barrier();
    SB();
    constexpr int E0 = W2_INL - NW2L;
    auto signal = [&]() __attribute__((always_inline)) {
      if (lane == 0) {
        if (__hip_atomic_fetch_add(lcnt, 1u, __ATOMIC_RELAXED, __HIP_MEMORY_SCOPE_WORKGROUP) == 7u)
          __hip_atomic_store(flags + (FL_P / 4) * ng, 1u, __ATOMIC_RELAXED, __HIP_MEMORY_SCOPE_AGENT);
      }
    };
    if (w >= SIG_SPLIT) { asm volatile("s_waitcnt vmcnt(0)" ::: "memory"); signal(); }
    SB();
#pragma unroll
    for (int e = E0; e < NW2R; ++e) ring[(8 + e) % 32] = ld16(rs_, lo16, w2_off(e));
    SB();
    if (w < SIG_SPLIT) { asm volatile("s_waitcnt vmcnt(%0)" :: "n"(NW2R - E0) : "memory"); signal(); }
    SB();
  };
  if (two) gemm1(std::true_type{});
  else gemm1(std::false_type{});
  STAMP(2, 3);
#pragma nounroll
  for (unsigned spin = 0; spin < (1u << 20); ++spin) {
    if (__hip_atomic_load(flags + (FL_P / 4) * w, __ATOMIC_RELAXED, __HIP_MEMORY_SCOPE_AGENT) != 0u) break;
#define POLL_SL 1
    if (POLL_SL) __builtin_amdgcn_s_sleep(POLL_SL);
  }
  asm volatile("" ::: "memory");
  SB();

  STAMP(2, 4);
  const int h1s = (int)WS_H1F + (mt * 4 * 54 + ks0) * 1024;
  u32x4 hb[28];
  auto load_h1 = [&](int n) __attribute__((always_inline)) {
    const int q = n / 7, s = n % 7;
    const int sn = (s == 6 && !full) ? 5 : s;
    hb[n] = ld16_sc1(rs_, lo16, h1s + (q * 54 + sn) * 1024);
  };
#pragma unroll
  for (int n = 0; n < H1_PRE; ++n) {
    if (n == H1_BATCH) { SB(); __builtin_amdgcn_s_barrier(); SB(); }
    load_h1(n);
  }
  SB();
  float* red = (float*)smem;
  float* rb_ = red + (w * 16 + i) * KF_RS + 4 * g;
  const int re = w * 40 + min(lane, 39);
  const int rtk = re / 20, rq4 = re % 20;
  const float* rr_ = red + rtk * KF_RS + 4 * rq4;
  f32x4 ac2[2][5];
  f32x4 rv[8], rsum;
  u32x2 xv2;
  const f32x4 gb = *(const f32x4*)(tab + 4 * rq4);
  auto red_wr = [&](int q, int c) __attribute__((always_inline)) {
    *(f32x4*)(rb_ + (q & 1) * (KF_RB / 4) + 16 * c) = ac2[q & 1][c];
  };
  auto red_rd = [&](int q, int ww) __attribute__((always_inline)) {
    rv[ww] = *(const f32x4*)(rr_ + (q & 1) * (KF_RB / 4) + ww * 16 * KF_RS);
  };
  auto red_x = [&](int q) __attribute__((always_inline)) { xv2 = *(const u32x2*)(xr + (16 * q + rtk) * 176 + rq4 * 8); };
  auto red_out = [&](int q) __attribute__((always_inline)) {
    const f32x4 s = rsum + (rv[6] + rv[7]);
    u32x2 o;
#pragma unroll
    for (int h = 0; h < 2; ++h) {
      o[h] = pk_bf(fmaf(s[2 * h], gb[2 * h], bf_lo(xv2[h])), fmaf(s[2 * h + 1], gb[2 * h + 1], bf_hi(xv2[h])));
    }
    *(u32x2*)(out + (size_t)(mt * 64 + 16 * q + rtk) * D_ + ng * 80 + 4 * rq4) = o;
  };
#pragma unroll
  for (int q = 0; q < 3; ++q) {
#pragma unroll
    for (int c = 0; c < 5; ++c) ac2[q & 1][c] = (f32x4){0.f, 0.f, 0.f, 0.f};
#pragma unroll
    for (int s = 0; s < 7; ++s) {
      auto mm = [&](int c) __attribute__((always_inline)) {
        const u32x4 a = c == 4 ? wl[s] : ((s == 6 && c == 3) ? wl[7] : ring[(8 + 4 * s + c) % 32]);
        ac2[q & 1][c] = mfma_bf16(a, hb[7 * q + s], ac2[q & 1][c]);
      };
      if (s == 5 && q >= 2) {
        red_wr(q - 1, 4);
        SB();
        red_out(q - 2);
#pragma unroll
        for (int c = 0; c < 5; ++c) mm(c);
#pragma unroll
        for (int c = 0; c < 5; ++c) {
          __builtin_amdgcn_sched_group_barrier(0x008, 1, 0);
          __builtin_amdgcn_sched_group_barrier(0x002, 5, 0);
        }
        SB();
      } else if (s < 6) {
        mm(0); SB();
        if (q >= 1 && s >= 1) { red_wr(q - 1, s - 1); SB(); }
        mm(1); SB();
        if (q >= 2 && s < 4) { red_rd(q - 2, 2 * s); SB(); }
        mm(2); SB();
        if (q >= 2 && s < 4) { red_rd(q - 2, 2 * s + 1); SB(); }
        if (q >= 2 && s == 4) { red_x(q - 2); SB(); }
        mm(3); SB();
        if (q >= 2 && s >= 1 && s < 4) {
          const f32x4 t = rv[2 * s - 2] + rv[2 * s - 1];
          rsum = s == 1 ? t : rsum + t;
          asm volatile("" : "+v"(rsum));
          SB();
        }
        mm(4); SB();
      } else if (full) {
#pragma unroll
        for (int c = 0; c < 5; ++c) mm(c);
        SB();
      }
      if (7 * q + s + H1_PRE < 28) { load_h1(7 * q + s + H1_PRE); SB(); }
    }
    if (q >= 1) bar_lds();
    if (q == 0) STAMP(2, 5);
  }
  {
    auto sum6 = [&]() __attribute__((always_inline)) {
      rsum = ((rv[0] + rv[1]) + (rv[2] + rv[3])) + (rv[4] + rv[5]);
    };
    if (full) {
#pragma unroll
      for (int c = 0; c < 5; ++c) {
        const u32x4 a = c == 4 ? wl[6] : (c == 3 ? wl[7] : ring[(8 + 24 + c) % 32]);
        rv[c] = mfma_bf16(a, hb[27], (f32x4){0.f, 0.f, 0.f, 0.f});
      }
    } else {
#pragma unroll
      for (int c = 0; c < 5; ++c) rv[c] = (f32x4){0.f, 0.f, 0.f, 0.f};
    }
    f32x4 c0[5];
#pragma unroll
    for (int c = 0; c < 5; ++c) c0[c] = rv[c];
    SB();
    auto col = [&](int c, auto&& between) __attribute__((always_inline)) {
#pragma unroll
      for (int s = 0; s < 6; ++s) {
        const u32x4 a = c == 4 ? wl[s] : ring[(8 + 4 * s + c) % 32];
        c0[c] = mfma_bf16(a, hb[21 + s], c0[c]);
        SB();
        between(s);
        SB();
      }
    };
    col(0, [&](int s) __attribute__((always_inline)) { if (s < 5) red_wr(2, s); if (s < 4) { red_rd(1, 2 * s); red_rd(1, 2 * s + 1); } if (s == 4) red_x(1); });
    col(1, [&](int s) __attribute__((always_inline)) { if (s == 1) { sum6(); asm volatile("" : "+v"(rsum)); } if (s == 3) red_out(1); });
    bar_lds();
    auto wr3 = [&](int c) __attribute__((always_inline)) { *(f32x4*)(rb_ + (KF_RB / 4) + 16 * c) = c0[c]; };
    col(2, [&](int s) __attribute__((always_inline)) { if (s < 2) wr3(s); if (s >= 1 && s < 5) { red_rd(2, 2 * s - 2); red_rd(2, 2 * s - 1); } if (s == 5) red_x(2); });
    col(3, [&](int s) __attribute__((always_inline)) { if (s == 1) wr3(2); if (s == 2) { sum6(); asm volatile("" : "+v"(rsum)); } if (s == 4) red_out(2); });
    col(4, [&](int s) __attribute__((always_inline)) { if (s == 1) wr3(3); if (s == 2) red_x(3); });
    wr3(4);
    STAMP(2, 6);
    bar_lds();
#pragma unroll
    for (int ww = 0; ww < 8; ++ww) red_rd(3, ww);
    sum6();
    red_out(3);
    VM0();
    STAMP(2, 7);
  }
}

__global__ NOPK __launch_bounds__(512, 2) void kf(char* __restrict__ ws, unsigned short* __restrict__ out) {
  __shared__ __attribute__((aligned(16))) char smem[KF_LDS];
  kf_body(ws, out, smem);
}

__global__ void kend_() {}

extern "C" void kernel_launch(void* const* d_in, const int* in_sizes, int n_in,
                              void* d_out, int out_size, void* d_ws, size_t ws_size,
                              hipStream_t stream) {
  K1Args a;
  a.x = (const unsigned short*)d_in[0];
  a.are = (const unsigned short*)d_in[1];
  a.aim = (const unsigned short*)d_in[2];
  a.bre = (const unsigned short*)d_in[3];
  a.bim = (const unsigned short*)d_in[4];
  a.cre = (const unsigned short*)d_in[5];
  a.cim = (const unsigned short*)d_in[6];
  a.logdt = (const unsigned short*)d_in[7];
  a.c = (const unsigned short*)d_in[12];
  a.wmod = (const unsigned short*)d_in[13];
  a.w1 = (const unsigned short*)d_in[10];
  a.w2 = (const unsigned short*)d_in[11];
  a.out = (const unsigned short*)d_out;
  a.dskip = (const unsigned short*)d_in[8];
  a.ws = (char*)d_ws;
  k1<<<NB_PF + NB_K + NB_LN + NB_MOD, 256, 0, stream>>>(a);
  k2<<<256, 512, 0, stream>>>((const unsigned short*)d_in[8], (char*)d_ws, (const unsigned short*)d_in[10],
                              (const unsigned short*)d_in[11]);
  kf<<<256, 512, 0, stream>>>((char*)d_ws, (unsigned short*)d_out);
}
```

```cpp
#include <hip/hip_runtime.h>
#include <hip/hip_bf16.h>
#include <stdint.h>
#include <type_traits>

typedef __attribute__((ext_vector_type(8))) __bf16 bf16x8;
typedef __attribute__((ext_vector_type(2))) __bf16 bf16x2;
typedef __attribute__((ext_vector_type(4))) float f32x4;
typedef __attribute__((ext_vector_type(2))) float f32x2;
typedef __attribute__((ext_vector_type(4))) unsigned u32x4;
typedef __attribute__((ext_vector_type(2))) unsigned u32x2;
typedef __attribute__((ext_vector_type(8))) _Float16 h16x8;
typedef __attribute__((ext_vector_type(2))) _Float16 h16x2;
typedef __attribute__((ext_vector_type(4))) short s16x4;
typedef __attribute__((address_space(3))) s16x4 lds_s16x4;

#if defined(__HIP_DEVICE_COMPILE__)
#define NOPK __attribute__((target("no-packed-fp32-ops")))
#else
#define NOPK
#endif
#define DEV __device__ __forceinline__

constexpr auto W1L = 0;
constexpr auto LNFLIP = 2;
constexpr auto KMAP = 0;
constexpr auto FL_P = 2048;
constexpr auto FL_B = 327680;
constexpr auto NTM = 15;
constexpr auto ST_AUX = 16;
constexpr auto DL_LN = 0;
constexpr auto DL_MOD = 12;
constexpr auto PRIO47 = 2;
constexpr auto NF1_ = 4;
constexpr auto NF2_ = 5;
constexpr auto RT67 = 8;
constexpr auto X_FIRST = 5;
constexpr auto X_STEP = 4;
constexpr auto W1_EARLY = 3;
constexpr auto W1_MID = 7;
constexpr auto W1_PRE = 10;
constexpr auto W2_START = 6;
constexpr auto W2_RATE = 2;
constexpr auto W2_INL = 28;
constexpr auto CS_STEP = 14;
constexpr auto SIG_SPLIT = 4;
constexpr auto H1_PRE = 7;
constexpr auto H1_BATCH = 99;
constexpr auto LOCK_MASK = 0x55554u;
constexpr auto POLL_SL = 1;

constexpr int W1KS = W1L ? 1 : 108, W1TS = W1L ? 20 : 1;
constexpr int L_ = 2048;
constexpr int D_ = 640;
constexpr int NS = 128;
constexpr int FF = 1728;
constexpr int KREV = 1280;

constexpr size_t WS_GB   = 0;
constexpr size_t WS_KSC  = 8192;
constexpr size_t WS_CNT  = FL_B;
constexpr size_t WS_MOD  = 65536;
constexpr size_t WS_CSP  = 196608;
constexpr size_t WS_XNT  = 327680 + 256 * FL_P;
constexpr size_t WS_XT   = WS_XNT + (size_t)D_ * L_ * 2;
constexpr size_t WS_KREV = WS_XT + (size_t)D_ * L_ * 2;
constexpr size_t WS_W1F  = WS_KREV + (size_t)D_ * KREV * 2;
constexpr size_t WS_W2F  = WS_W1F + (size_t)D_ * FF * 2;
constexpr size_t WS_X1T  = WS_W2F + (size_t)D_ * FF * 2;
constexpr size_t WS_H1F  = WS_X1T + (size_t)D_ * L_ * 2;
constexpr size_t WS_END  = WS_H1F + (size_t)L_ * FF * 2;

DEV float bf_lo(unsigned u) { return __uint_as_float(u << 16); }
DEV float bf_hi(unsigned u) { return __uint_as_float(u & 0xffff0000u); }
DEV unsigned pk_bf(float a, float b) {
  f32x2 v = {a, b};
  bf16x2 r = __builtin_convertvector(v, bf16x2);
  return __builtin_bit_cast(unsigned, r);
}
DEV unsigned pk_h(float a, float b) {
  f32x2 v = {a, b};
  h16x2 r = __builtin_convertvector(v, h16x2);
  return __builtin_bit_cast(unsigned, r);
}
template <int BIT, class T> DEV T ldin(const void* p) {
  if (NTM & BIT) return __builtin_nontemporal_load((const T*)p);
  return *(const T*)p;
}
DEV float rbf(float a) { return bf_lo(pk_bf(a, 0.f)); }
DEV float ldbf(const unsigned short* p) { return __uint_as_float(((unsigned)*p) << 16); }
#define CONST_AS __attribute__((address_space(4)))
template <class T> DEV T ldu(const void* p) { return *(const CONST_AS T*)(size_t)p; }
DEV float ldbf_u(const unsigned short* p) {
  const unsigned v = ldu<unsigned>((const void*)((size_t)p & ~(size_t)3));
  return __uint_as_float(((size_t)p & 2) ? (v & 0xffff0000u) : (v << 16));
}

typedef __amdgpu_buffer_rsrc_t rsrc_t;
DEV rsrc_t ws_rsrc(char* ws) { return __builtin_amdgcn_make_buffer_rsrc((void*)ws, 0, (int)WS_END, 0x00020000); }
DEV void st16(rsrc_t r, size_t off, u32x4 v) { __builtin_amdgcn_raw_buffer_store_b128(v, r, (int)off, 0, ST_AUX); }
DEV void st4(char* ws, size_t off, unsigned v) { __hip_atomic_store((unsigned*)(ws + off), v, __ATOMIC_RELAXED, __HIP_MEMORY_SCOPE_AGENT); }
DEV void st8(rsrc_t r, size_t off, u32x2 v) { __builtin_amdgcn_raw_buffer_store_b64(v, r, (int)off, 0, 16); }

DEV void bar_lds() { asm volatile("s_waitcnt lgkmcnt(0)\n\ts_barrier" ::: "memory"); }
DEV void lds_wait(const void* word, unsigned val) {
  unsigned tv, ts, cnt;
  asm volatile("s_mov_b32 %2, 0x100000\n"
               "1:\n\tds_read_b32 %0, %3\n\ts_waitcnt lgkmcnt(0)\n\tv_readfirstlane_b32 %1, %0\n\t"
               "s_cmp_ge_u32 %1, %4\n\ts_cbranch_scc1 2f\n\ts_sleep 1\n\ts_sub_u32 %2, %2, 1\n\t"
               "s_cmp_lg_u32 %2, 0\n\ts_cbranch_scc1 1b\n2:"
               : "=&v"(tv), "=&s"(ts), "=&s"(cnt) : "v"((unsigned)(size_t)word), "s"(val) : "memory", "scc");
}
DEV void lds_post(unsigned* word, int lane) {
  asm volatile("" ::: "memory");
  if (lane == 0) __hip_atomic_fetch_add(word, 1u, __ATOMIC_RELAXED, __HIP_MEMORY_SCOPE_WORKGROUP);
  asm volatile("" ::: "memory");
}
DEV void lgkm0() { asm volatile("s_waitcnt lgkmcnt(0)" ::: "memory"); __builtin_amdgcn_sched_barrier(0); }

DEV f32x4 mfma_bf16(u32x4 a, u32x4 b, f32x4 c) {
  return __builtin_amdgcn_mfma_f32_16x16x32_bf16(__builtin_bit_cast(bf16x8, a), __builtin_bit_cast(bf16x8, b), c, 0, 0, 0);
}

constexpr int NB_PF = 8, NB_K = 640, NB_LN = 256, NB_MOD = 120;

struct K1Args {
  const unsigned short *x, *are, *aim, *bre, *bim, *cre, *cim, *logdt, *c, *wmod;
  char* ws;
};

DEV void k1_prefetch() {
  const int tid = threadIdx.x;
  unsigned long long pc;
  int o0, o1;
  asm volatile("s_getpc_b64 %0\n.Lpf_here%=:\n\ts_mov_b32 %1, _Z2k16K1Args - .Lpf_here%=\n\ts_mov_b32 %2, _Z5kend_v - .Lpf_here%="
               : "=s"(pc), "=s"(o0), "=s"(o1));
  const char* c0 = (const char*)(pc + (long long)o0);
  long n = o1 - o0;
  n = n < 0 ? 0 : (n > 98304 ? 98304 : n);
  unsigned v[3];
#pragma unroll
  for (int it = 0; it < 3; ++it) {
    const long o = (long)tid * 128 + it * 32768;
    v[it] = o < n ? *(const __attribute__((address_space(1))) unsigned*)(c0 + o) : 0u;
  }
  asm volatile("" :: "v"(v[0] ^ v[1] ^ v[2]));
}

DEV void k1_mod(const K1Args& p, int b, char* smem) {
  const int tid = threadIdx.x;
  const int rg = b & 7, cgp = b >> 3;
  const int c32 = tid & 31, r8 = tid >> 5;
  float* sil = (float*)smem;
  float* red = (float*)(smem + 512);
  u32x4 w[10];
  const float cv = ldbf(p.c + rg * 80 + min(tid, 79));
  asm volatile("" ::: "memory");
#pragma unroll
  for (int it = 0; it < 10; ++it)
    w[it] = ldin<1, u32x4>(p.wmod + (size_t)(rg * 80 + r8 + 8 * it) * 3840 + 256 * cgp + 8 * c32);
  if (tid < 80) sil[tid] = rbf(cv / (1.f + expf(-cv)));
  if (tid >= 128 && tid < 132 && b < 64) st4(p.ws, WS_CNT + (size_t)(b * 4 + tid - 128) * FL_P, 0u);
  __syncthreads();
  float acc[8];
#pragma unroll
  for (int j = 0; j < 8; ++j) acc[j] = 0.f;
#pragma unroll
  for (int it = 0; it < 10; ++it) {
    float s = sil[r8 + 8 * it];
#pragma unroll
    for (int q = 0; q < 4; ++q) {
      acc[2 * q] += s * bf_lo(w[it][q]);
      acc[2 * q + 1] += s * bf_hi(w[it][q]);
    }
  }
#pragma unroll
  for (int j = 0; j < 8; ++j) acc[j] += __shfl_xor(acc[j], 32, 64);
  if ((tid & 63) < 32) {
    *(f32x4*)(red + (tid >> 6) * 256 + 8 * c32) = (f32x4){acc[0], acc[1], acc[2], acc[3]};
    *(f32x4*)(red + (tid >> 6) * 256 + 8 * c32 + 4) = (f32x4){acc[4], acc[5], acc[6], acc[7]};
  }
  __syncthreads();
  if (tid < 64) {
    const f32x4* r4 = (const f32x4*)red + tid;
    st16(ws_rsrc(p.ws), WS_MOD + (rg * 3840 + 256 * cgp + 4 * tid) * 4,
         __builtin_bit_cast(u32x4, (r4[0] + r4[64]) + (r4[128] + r4[192])));
  }
}
struct ModP { f32x4 a, b; };
DEV ModP mod_ld(rsrc_t rs, int idx) {
  ModP m;
#pragma unroll
  for (int j = 0; j < 4; ++j) {
    m.a[j] = __uint_as_float(__builtin_amdgcn_raw_buffer_load_b32(rs, 4 * idx, (int)WS_MOD + 15360 * j, 0));
    m.b[j] = __uint_as_float(__builtin_amdgcn_raw_buffer_load_b32(rs, 4 * idx, (int)WS_MOD + 15360 * (4 + j), 0));
  }
  return m;
}
DEV float mod_fin(const ModP& m) {
  return rbf(((m.a[0] + m.a[1]) + (m.a[2] + m.a[3])) + ((m.b[0] + m.b[1]) + (m.b[2] + m.b[3])));
}
#define SB() __builtin_amdgcn_sched_barrier(0)

DEV void k1_ln(const K1Args& p, int b, char* smem) {
  constexpr int STR = 704;
  const int tid = threadIdx.x;
  const int tok = tid >> 3, sub = tid & 7;
  const int tile = (b & 7) * 8 + ((b >> 3) & 7), pass = (b >> 6) ^ LNFLIP;
  const int hf = pass & 1;
  const bool norm = pass < 2;
  const int t0 = tile * 32;
  const unsigned short* xr = p.x + (size_t)(t0 + tok) * D_ + 8 * sub;
  u32x4 xv[5], xo[5];
#pragma unroll
  for (int m = 0; m < 5; ++m) xv[m] = ldin<2, u32x4>(xr + 320 * hf + 64 * m);
  if (norm) {
#pragma unroll
    for (int m = 0; m < 5; ++m) xo[m] = ldin<2, u32x4>(xr + 320 * (hf ^ 1) + 64 * m);
    float s = 0.f, v = 0.f;
    const bf16x2 one2 = __builtin_bit_cast(bf16x2, 0x3f803f80u);
#pragma unroll
    for (int m = 0; m < 10; ++m)
#pragma unroll
      for (int q = 0; q < 4; ++q) {
        const unsigned u = m < 5 ? xv[m][q] : xo[m - 5][q];
        const bf16x2 x2 = __builtin_bit_cast(bf16x2, u);
        s = __builtin_amdgcn_fdot2_f32_bf16(x2, one2, s, false);
        v = __builtin_amdgcn_fdot2_f32_bf16(x2, x2, v, false);
      }
#pragma unroll
    for (int o = 1; o < 8; o <<= 1) { s += __shfl_xor(s, o, 64); v += __shfl_xor(v, o, 64); }
    const float mean = s * (1.f / D_);
    const float var = fmaxf(v * (1.f / D_) - mean * mean, 0.f);
    const float sd = rbf(sqrtf(rbf(rbf(var) + 1e-5f)));
    const float rs = 1.f / sd;
    const float nm = -rbf(mean) * rs;
#pragma unroll
    for (int m = 0; m < 5; ++m)
#pragma unroll
      for (int q = 0; q < 4; ++q) {
        xv[m][q] = pk_h(fmaf(bf_lo(xv[m][q]), rs, nm), fmaf(bf_hi(xv[m][q]), rs, nm));
      }
  }
  const int lane = tid & 63, w = tid >> 6;
  const int g = lane >> 4, i = lane & 15;
  const rsrc_t rsrc = ws_rsrc(p.ws);
#pragma unroll
  for (int m = 0; m < 5; ++m) *(u32x4*)(smem + tok * STR + (sub + 8 * m) * 16) = xv[m];
  __syncthreads();
  const size_t dsto = (norm ? WS_XNT : WS_XT) + ((size_t)tile * D_ + 320 * hf) * 64;
#pragma unroll
  for (int it = 0; it < 5; ++it) {
    const int cb = w * 5 + it;
    const char* a = smem + (8 * g + (i >> 2)) * STR + cb * 32 + (i & 3) * 8;
    const u32x2 ra = __builtin_bit_cast(u32x2, __builtin_amdgcn_ds_read_tr16_b64_v4i16((lds_s16x4*)a));
    const u32x2 rb = __builtin_bit_cast(u32x2, __builtin_amdgcn_ds_read_tr16_b64_v4i16((lds_s16x4*)(a + 4 * STR)));
    st16(rsrc, dsto + (size_t)(cb * 16 + i) * 64 + 16 * g, (u32x4){ra[0], ra[1], rb[0], rb[1]});
  }
}

constexpr int RT_STR = 288;
struct RtRaw { u32x4 r[8]; };
template <int NF>
DEV void rt_load(RtRaw& t, const unsigned short* W, int N, int kk, int ct0, int lane) {
#pragma unroll
  for (int j = 0; j < NF; ++j) {
    const int c = lane + 64 * j, row = c / (2 * NF), part = c % (2 * NF);
    t.r[j] = ldin<4, u32x4>(W + (size_t)(32 * kk + row) * N + 16 * ct0 + 8 * part);
  }
}
template <int NF>
DEV void rt_park(const RtRaw& t, char* scr, int lane) {
#pragma unroll
  for (int j = 0; j < NF; ++j) {
    const int c = lane + 64 * j, row = c / (2 * NF), part = c % (2 * NF);
    *(u32x4*)(scr + row * RT_STR + 16 * part) = t.r[j];
  }
}
DEV u32x4 rt_frag(const char* scr, int j, int lane) {
  const int g = lane >> 4, i = lane & 15;
  const char* p = scr + (8 * g + (i >> 2)) * RT_STR + 32 * j + (i & 3) * 8;
  const u32x2 a = __builtin_bit_cast(u32x2, __builtin_amdgcn_ds_read_tr16_b64_v4i16((lds_s16x4*)p));
  const u32x2 b = __builtin_bit_cast(u32x2, __builtin_amdgcn_ds_read_tr16_b64_v4i16((lds_s16x4*)(p + 4 * RT_STR)));
  return (u32x4){a[0], a[1], b[0], b[1]};
}
struct cf { float re, im; };
DEV cf cmul(cf a, cf b) {
  cf r;
  r.re = a.re * b.re - a.im * b.im;
  r.im = a.re * b.im + a.im * b.re;
  return r;
}
DEV cf cexp_(float a2, float br, float m, float m0) {
  float mag = __builtin_amdgcn_exp2f(a2 * (m - m0));
  float ph = __builtin_amdgcn_fractf(br * m);
  cf r; r.re = mag * __builtin_amdgcn_cosf(ph); r.im = mag * __builtin_amdgcn_sinf(ph);
  return r;
}
DEV f32x4 mfma_f16(u32x4 a, u32x4 b, f32x4 c) {
  return __builtin_amdgcn_mfma_f32_16x16x32_f16(__builtin_bit_cast(h16x8, a), __builtin_bit_cast(h16x8, b), c, 0, 0, 0);
}
DEV void put2(char* p, cf a, cf b) {
  u32x2 o = {pk_h(a.re, a.im), pk_h(b.re, b.im)};
  *(u32x2*)p = o;
}

constexpr int KG_RS = 544;
DEV void k1_kgen(const K1Args& p, int d, char* smem) {
  constexpr int RS = KG_RS;
  const int tid = threadIdx.x;
  const int lane = tid & 63;
  const int w = __builtin_amdgcn_readfirstlane(tid >> 6);
  const int g = lane >> 4, i = lane & 15;
  const size_t po = (size_t)d * NS + 2 * lane;
  const unsigned rar = ldin<8, unsigned>(p.are + po), rai = ldin<8, unsigned>(p.aim + po);
  unsigned rbr = 0u, rbi = 0u, rcr = 0u, rci = 0u;
  if (w == 0) {
    rbr = ldin<8, unsigned>(p.bre + po); rbi = ldin<8, unsigned>(p.bim + po);
    rcr = ldin<8, unsigned>(p.cre + po); rci = ldin<8, unsigned>(p.cim + po);
  }
  const float ldt = ldbf_u(p.logdt + d);
  asm volatile("" ::: "memory");
  SB();
  const float dt = rbf(expf(ldt));
  asm volatile("" :: "v"(dt));
  float a2[2], bv[2], m0[2];
  a2[0] = (dt * bf_lo(rar)) * 1.44269504088896341f; a2[1] = (dt * bf_hi(rar)) * 1.44269504088896341f;
  bv[0] = (dt * bf_lo(rai)) * 0.15915494309189534f; bv[1] = (dt * bf_hi(rai)) * 0.15915494309189534f;
  m0[0] = a2[0] > 0.f ? 2048.f : 0.f; m0[1] = a2[1] > 0.f ? 2048.f : 0.f;
  char* my = smem + lane * 8;
  if (w == 0) {
    cf v[2], v8[2], e8[2];
    float bound = 0.f;
#pragma unroll
    for (int q = 0; q < 2; ++q) {
      const float dbr = dt * (q ? bf_hi(rbr) : bf_lo(rbr)), dbi = dt * (q ? bf_hi(rbi) : bf_lo(rbi));
      const float cr = q ? bf_hi(rcr) : bf_lo(rcr), ci = q ? bf_hi(rci) : bf_lo(rci);
      const float gsh = __builtin_amdgcn_exp2f(a2[q] * m0[q]);
      v[q].re = (cr * dbr - ci * dbi) * gsh; v[q].im = -(cr * dbi + ci * dbr) * gsh;
      bound += (fabsf(v[q].re) + fabsf(v[q].im)) * __builtin_amdgcn_exp2f(fmaxf(a2[q], 0.f) * 120.f);
      e8[q] = cexp_(a2[q], -bv[q], 8.f, 0.f);
      v8[q] = cexp_(a2[q], -bv[q], 64.f, 0.f);
    }
#pragma unroll
    for (int o = 1; o < 64; o <<= 1) bound += __shfl_xor(bound, o, 64);
    int ex = (int)((__float_as_uint(bound) >> 23) & 0xff);
    ex = ex < 20 ? 20 : (ex > 240 ? 240 : ex);
    const float sv = __uint_as_float((unsigned)(127 + 13 + 127 - ex) << 23);
    if (lane == 0) st4(p.ws, WS_KSC + 4 * d, (unsigned)(ex - 13) << 23);
#pragma unroll
    for (int q = 0; q < 2; ++q) { v[q].re *= sv; v[q].im *= sv; v8[q] = cmul(v[q], v8[q]); }
#pragma unroll
    for (int j = 0; j < 8; ++j) {
      put2(my + (41 + j) * RS, v[0], v[1]);
      put2(my + (49 + j) * RS, v8[0], v8[1]);
      if (j < 7) {
#pragma unroll
        for (int q = 0; q < 2; ++q) { v[q] = cmul(v[q], e8[q]); v8[q] = cmul(v8[q], e8[q]); }
      }
    }
  } else if (w == 1) {
    cf e1[2], u[2], u2[2];
#pragma unroll
    for (int q = 0; q < 2; ++q) {
      e1[q] = cexp_(a2[q], bv[q], 1.f, 0.f);
      u[q].re = __builtin_amdgcn_exp2f(-a2[q] * m0[q]); u[q].im = 0.f;
      u2[q] = cexp_(a2[q], bv[q], 128.f, m0[q]);
    }
#pragma unroll
    for (int r = 0; r < 8; ++r) {
      put2(my + r * RS, u[0], u[1]);
      put2(my + (8 + r) * RS, u2[0], u2[1]);
      if (r < 7) {
#pragma unroll
        for (int q = 0; q < 2; ++q) { u[q] = cmul(u[q], e1[q]); u2[q] = cmul(u2[q], e1[q]); }
      }
    }
  } else if (w == 2) {
    cf e2[2], e128[2], u[2], u2[2], c[2];
#pragma unroll
    for (int q = 0; q < 2; ++q) {
      e2[q] = cexp_(a2[q], bv[q], 2.f, 0.f);
      e128[q] = cexp_(a2[q], bv[q], 128.f, 0.f);
      u[q] = cexp_(a2[q], bv[q], 256.f, m0[q]);
      u2[q] = cmul(u[q], e128[q]);
      c[q] = cmul(u2[q], e128[q]);
    }
#pragma unroll
    for (int r = 0; r < 4; ++r) {
      put2(my + (16 + r) * RS, u[0], u[1]);
      put2(my + (20 + r) * RS, u2[0], u2[1]);
      if (r < 3) {
#pragma unroll
        for (int q = 0; q < 2; ++q) { u[q] = cmul(u[q], e2[q]); u2[q] = cmul(u2[q], e2[q]); }
      }
    }
    cf e4[2] = {cmul(e2[0], e2[0]), cmul(e2[1], e2[1])};
#pragma unroll
    for (int r = 0; r < 3; ++r) {
      put2(my + (24 + 2 * r) * RS, c[0], c[1]);
      put2(my + (25 + 2 * r) * RS, cmul(c[0], e4[0]), cmul(c[1], e4[1]));
      if (r < 2) { c[0] = cmul(c[0], e128[0]); c[1] = cmul(c[1], e128[1]); }
    }
  } else {
    cf e128[2], u[2], u2[2];
#pragma unroll
    for (int q = 0; q < 2; ++q) {
      e128[q] = cexp_(a2[q], bv[q], 128.f, 0.f);
      u[q] = cexp_(a2[q], bv[q], 896.f, m0[q]);
      u2[q] = cexp_(a2[q], bv[q], 1536.f, m0[q]);
    }
    put2(my + 30 * RS, u[0], u[1]);
    put2(my + 31 * RS, cexp_(a2[0], bv[0], 900.f, m0[0]), cexp_(a2[1], bv[1], 900.f, m0[1]));
#pragma unroll
    for (int r = 0; r < 4; ++r) {
      put2(my + (36 + r) * RS, u2[0], u2[1]);
#pragma unroll
      for (int q = 0; q < 2; ++q) { u[q] = cmul(u[q], e128[q]); u2[q] = cmul(u2[q], e128[q]); }
      put2(my + (32 + r) * RS, u[0], u[1]);
    }
    put2(my + 40 * RS, u2[0], u2[1]);
  }
  __syncthreads();
  _Float16* kh = (_Float16*)(smem + 57 * RS);
  if (w < 3) {
    const char* ua = smem + (16 * w + i) * RS + g * 16;
    const char* va = smem + (41 + i) * RS + g * 16;
    f32x4 acc = {0.f, 0.f, 0.f, 0.f};
#pragma unroll
    for (int ks = 0; ks < 8; ++ks)
      acc = mfma_f16(*(const u32x4*)(ua + ks * 64), *(const u32x4*)(va + ks * 64), acc);
    int idx0, st;
    if (w == 0) { idx0 = (g < 2 ? 4 * g : 120 + 4 * g) + 8 * i; st = 1; }
    else if (w == 1) { idx0 = g < 2 ? 256 + 64 * g + 4 * i : 384 + 64 * (g - 2) + 2 * i; st = 1; }
    else { idx0 = 512 + 64 * g + i; st = 16; }
#pragma unroll
    for (int rg = 0; rg < 4; ++rg) {
      int idx = idx0 + st * rg;
      if (w == 1 && g >= 2) idx = idx0 + 32 * (rg >> 1) + (rg & 1);
      if (w == 2 && g >= 2) idx = (g == 2 && rg == 0 && i == 0) ? 640 : 644;
      kh[idx] = (_Float16)acc[rg];
    }
  }
  __syncthreads();
  const unsigned short* ku = (const unsigned short*)kh;
  const size_t kout = WS_KREV + (size_t)d * KREV * 2;
  {
    const int l0 = 2040 - 8 * tid;
    u32x4 o;
    if (w < 2) {
      const int q = l0 >> 3;
      const unsigned a = ku[384 + q], b = ku[385 + q];
      __builtin_amdgcn_raw_buffer_store_b32(a | (b << 16), ws_rsrc(p.ws), (int)(kout + 4 * tid), 0, 16);
      return;
    } else if (w == 2) {
      const int q = l0 >> 2;
      const unsigned a = ku[256 + q], b = ku[257 + q], c = ku[258 + q];
      o[0] = c | (c << 16); o[1] = b | (b << 16); o[2] = b | (a << 16); o[3] = a | (a << 16);
    } else {
      unsigned v[8];
#pragma unroll
      for (int e = 0; e < 8; ++e) {
        const int l = l0 + 7 - e;
        const int idx = l < 256 ? l : (int)(pk_bf((float)l, 0.f) & 0xffffu) - 17024;
        v[e] = ku[idx];
      }
#pragma unroll
      for (int q = 0; q < 4; ++q) o[q] = v[2 * q] | (v[2 * q + 1] << 16);
    }
    st16(ws_rsrc(p.ws), kout + 16 * tid - 1536, o);
  }
}

__global__ NOPK __launch_bounds__(256) void k1(K1Args p) {
  __shared__ __attribute__((aligned(16))) char smem[57 * KG_RS + 1312];
  int b = blockIdx.x;
  if (b < NB_PF) { k1_prefetch(); return; }
  b -= NB_PF;
  if (b < NB_K) { k1_kgen(p, KMAP ? (b & 7) * 80 + (b >> 3) : b, smem); return; }
  b -= NB_K;
  asm volatile("" :: "s"(p.ws), "s"(p.x));
  if (b < NB_LN) { __builtin_amdgcn_s_sleep(DL_LN); k1_ln(p, b, smem); }
  else { __builtin_amdgcn_s_sleep(DL_MOD); k1_mod(p, b - NB_LN, smem); }
}

constexpr int K2_CS = 4160;
constexpr int K2_KOFF = 4 * K2_CS;
constexpr int K2_CH = K2_KOFF + 4608;
constexpr int CV_RED = 3 * K2_CH;
constexpr int CV_RT2 = 86016;
constexpr int CV_SUM = CV_RT2 + 4 * (32 * RT_STR + 256);
constexpr int CV_XCH = CV_SUM + 2560;
constexpr int CV_FL = CV_RED + 20480;

struct Chan { float sa, ca1, ga, dsk, hs, hinv, gy, gd; };
struct ChanRaw { float m, kinv, dsk; };
DEV ChanRaw chan_ld(const unsigned short* dskip, const char* ws, int d, int lane) {
  ChanRaw c;
  c.kinv = ldu<float>(ws + WS_KSC + 4 * d);
  c.dsk = ldbf_u(dskip + d);
  c.m = ((const float*)(ws + WS_MOD))[3840 * (lane & 7) + D_ * ((lane >> 3) & 3) + d];
  return c;
}
template <int CTRL> DEV float dpp_add(float v) {
  return v + __int_as_float(__builtin_amdgcn_update_dpp(0, __float_as_int(v), CTRL, 0xf, 0xf, true));
}
DEV Chan chan_fin(const ChanRaw& r) {
  Chan c;
  const int t = __float_as_int(rbf(dpp_add<0x141>(dpp_add<0x4e>(dpp_add<0xb1>(r.m)))));
  c.sa = __int_as_float(__builtin_amdgcn_readlane(t, 0));
  c.ca1 = rbf(1.f + __int_as_float(__builtin_amdgcn_readlane(t, 8)));
  c.ga = __int_as_float(__builtin_amdgcn_readlane(t, 16));
  c.dsk = r.dsk;
  const float hb = 26.f * fabsf(c.ca1) + fabsf(c.sa);
  int ex = (int)((__float_as_uint(hb) >> 23) & 0xff);
  ex = ex < 20 ? 20 : (ex > 240 ? 240 : ex);
  c.hs = __uint_as_float((unsigned)(127 + 14 + 127 - ex) << 23);
  c.hinv = __uint_as_float((unsigned)(ex - 14) << 23);
  c.gy = r.kinv * c.hinv * c.ga;
  c.gd = c.hinv * c.dsk * c.ga;
  return c;
}
DEV size_t tl_off(int d, int t) { return (((size_t)(t >> 5) * D_ + d) * 32 + (t & 31)) * 2; }
struct StageRaw { u32x4 xa[2], kv1; u32x2 xb; unsigned kv0; };
DEV StageRaw stage_ld(const char* ws, int d, int th) {
  StageRaw s;
  const unsigned short* krev = (const unsigned short*)(ws + WS_KREV) + (size_t)d * KREV;
  const int T = 1024 * (th >> 6) + 8 * (th & 63);
  s.xb = ldu<u32x2>(ws + WS_XNT + tl_off(d, 1024));
  s.xa[0] = *(const u32x4*)(ws + WS_XNT + tl_off(d, T)); s.xa[1] = *(const u32x4*)(ws + WS_XNT + tl_off(d, T + 512));
  s.kv0 = *(const unsigned*)(krev + 2 * th); s.kv1 = *(const u32x4*)(krev + 256 + 8 * th);
  return s;
}
DEV void stage_wr(const StageRaw& s, const Chan& c, char* smem, int th) {
  constexpr int CS = K2_CS, KOFF = K2_KOFF;
  const h16x2 c2 = __builtin_bit_cast(h16x2, pk_h(c.ca1 * c.hs, c.ca1 * c.hs)), s2 = __builtin_bit_cast(h16x2, pk_h(c.sa * c.hs, c.sa * c.hs));
  auto mod = [&](unsigned u) __attribute__((always_inline)) {
    return __builtin_bit_cast(unsigned, __builtin_elementwise_fma(__builtin_bit_cast(h16x2, u), c2, s2));
  };
  unsigned h2[2][6];
#pragma unroll
  for (int e = 0; e < 2; ++e)
#pragma unroll
    for (int q = 0; q < 4; ++q) h2[e][q] = mod(s.xa[e][q]);
#pragma unroll
  for (int q = 0; q < 2; ++q) {
    h2[0][4 + q] = __builtin_amdgcn_update_dpp(__builtin_amdgcn_readfirstlane(h2[1][q]), h2[0][q], 0x130, 0xf, 0xf, false);
    h2[1][4 + q] = __builtin_amdgcn_update_dpp(mod(s.xb[q]), h2[1][q], 0x130, 0xf, 0xf, false);
  }
  char* dst = smem + (128 * (th >> 6) + (th & 63) + 2) * 16;
  unsigned s1[5];
#pragma unroll
  for (int e = 1; e >= 0; --e) {
    const unsigned* hh = h2[e];
#pragma unroll
    for (int q = 0; q < 5; ++q) s1[q] = __builtin_amdgcn_alignbit(hh[q + 1], hh[q], 16);
    *(u32x4*)(dst + 1024 * e) = (u32x4){hh[0], hh[1], hh[2], hh[3]};
    *(u32x4*)(dst + 1024 * e + CS) = (u32x4){s1[0], s1[1], s1[2], s1[3]};
    *(u32x4*)(dst + 1024 * e + 2 * CS) = (u32x4){hh[1], hh[2], hh[3], hh[4]};
    *(u32x4*)(dst + 1024 * e + 3 * CS) = (u32x4){s1[1], s1[2], s1[3], s1[4]};
  }
  {
    const unsigned a = __builtin_amdgcn_readfirstlane(h2[0][0]), b = __builtin_amdgcn_readfirstlane(s1[0]);
    if (th >= 1 && th < 4)
      *(u32x4*)(smem + th * CS + 16) = (u32x4){0u, 0u, th == 3 ? a << 16 : 0u, th == 1 ? a << 16 : (th == 2 ? a : b)};
  }
  {
    const unsigned aa = __builtin_amdgcn_perm(s.kv0, s.kv0, 0x01000100u), bb = __builtin_amdgcn_perm(s.kv0, s.kv0, 0x03020302u);
    const unsigned ba = __builtin_amdgcn_perm(s.kv0, s.kv0, 0x01000302u);
    *(u32x4*)(smem + KOFF + 16 * th) = (u32x4){bb, (th & 1) ? ba : bb, aa, aa};
  }
  *(u32x4*)(smem + KOFF + 2048 + 16 * th) = s.kv1;
}
DEV void stage_zero(char* smem, int th) {
  if (th < 37) *(u32x4*)(smem + (th < 32 ? K2_KOFF + 4096 + 16 * th : (th < 36 ? (th - 32) * K2_CS : 16))) = (u32x4){0u, 0u, 0u, 0u};
}
DEV f32x4 conv_out(const f32x4& y, const Chan& c, const char* smem, int t) {
  const u32x2 hv = *(const u32x2*)(smem + 2 * (t + 16));
  f32x4 o;
#pragma unroll
  for (int e = 0; e < 2; ++e) {
    const unsigned hu = hv[e];
    const h16x2 h2 = __builtin_bit_cast(h16x2, hu);
    o[2 * e] = fmaf((float)h2[0], c.gd, y[2 * e] * c.gy);
    o[2 * e + 1] = fmaf((float)h2[1], c.gd, y[2 * e + 1] * c.gy);
  }
  return o;
}
DEV unsigned add_bf(unsigned x, float a, float b) { return pk_bf(bf_lo(x) + a, bf_hi(x) + b); }
#define K2_LOAD(buf, cc, u)                                                                                     \
  asm volatile("ds_read_b64 %0, %3 offset:%5\n\tds_read_b64 %1, %3 offset:%6\n\tds_read_b128 %2, %4 offset:%7"   \
               : "=&v"(Al[buf][u]), "=&v"(Ah[buf][u]), "=&v"(Bf[buf][u])                                          \
               : "v"(ap0), "v"(bp0), "i"(64 * (cc) + 512 * (u)), "i"(64 * (cc) + 512 * (u) + 8),                 \
                 "i"(64 * (cc) + 3584 - 512 * (u)))
template <int SET> DEV constexpr int half_tile(int j) {
  return SET == 0 ? (j == 0 ? 7 : (j == 1 ? 6 : 2)) : (j < 3 ? 5 - j : 4 - j);
}
template <int SET>
DEV void conv_half(unsigned ap0, unsigned bp0, char* redw) {
  constexpr int NU = SET == 0 ? 8 : 6, NT = SET == 0 ? 3 : 5;
  u32x2 Al[2][NU], Ah[2][NU];
  u32x4 Bf[2][NU];
#pragma unroll
  for (int u = 0; u < NU; ++u) K2_LOAD(0, 0, u);
  f32x4 acc[NT];
#pragma unroll
  for (int j = 0; j < NT; ++j) acc[j] = (f32x4){0.f, 0.f, 0.f, 0.f};
#pragma unroll
  for (int cc = 0; cc < 2; ++cc) {
    lgkm0();
    if (cc == 0) {
#pragma unroll
      for (int u = 0; u < NU; ++u) K2_LOAD(1, 1, u);
    }
#pragma unroll
    for (int sp = 0; sp < NU; ++sp)
#pragma unroll
      for (int j = 0; j < NT; ++j) {
        const int q = half_tile<SET>(j);
        if (sp <= q) {
          const u32x4 A = {Al[cc][sp][0], Al[cc][sp][1], Ah[cc][sp][0], Ah[cc][sp][1]};
          acc[j] = mfma_f16(A, Bf[cc][q - sp], acc[j]);
        }
      }
    SB();
  }
#pragma unroll
  for (int j = 0; j < NT; ++j) *(f32x4*)(redw + j * 4096) = acc[j];
}

template <int NF>
DEV void w1_fin(const RtRaw& t, const ModP& mp, char* ws, int kk, int ct0, int lane, char* scr, float* sums) {
  const int g = lane >> 4, i = lane & 15;
  float* tb = (float*)(scr + 32 * RT_STR);
  const float mv = mod_fin(mp);
  tb[lane] = lane < 32 ? rbf(1.f + mv) : mv;
  rt_park<NF>(t, scr, lane);
  lgkm0();
  const f32x4 ca = *(const f32x4*)(tb + 8 * g), cb = *(const f32x4*)(tb + 8 * g + 4);
  const f32x4 sa = *(const f32x4*)(tb + 32 + 8 * g), sb = *(const f32x4*)(tb + 36 + 8 * g);
  u32x4 sv = {pk_bf(sa[0], sa[1]), pk_bf(sa[2], sa[3]), pk_bf(sb[0], sb[1]), pk_bf(sb[2], sb[3])};
  const unsigned o1 = i == 0 ? 0x3f803f80u : 0u;
  const u32x4 on0 = {o1, o1, o1, o1};
  if (i == 0) sv = (u32x4){0u, 0u, 0u, 0u};
  const f32x4 z = {0.f, 0.f, 0.f, 0.f};
  const rsrc_t rs = ws_rsrc(ws);
  u32x4 f[NF];
#pragma unroll
  for (int j = 0; j < NF; ++j) f[j] = rt_frag(scr, j, lane);
  f32x4 sm[NF];
#pragma unroll
  for (int j = 0; j < NF; ++j) {
    const f32x4 b1 = mfma_bf16(f[j], sv, z);
    const u32x4 sc = {pk_bf(bf_lo(f[j][0]) * ca[0], bf_hi(f[j][0]) * ca[1]), pk_bf(bf_lo(f[j][1]) * ca[2], bf_hi(f[j][1]) * ca[3]),
                      pk_bf(bf_lo(f[j][2]) * cb[0], bf_hi(f[j][2]) * cb[1]), pk_bf(bf_lo(f[j][3]) * cb[2], bf_hi(f[j][3]) * cb[3])};
    st16(rs, WS_W1F + ((size_t)(kk * W1KS + (ct0 + j) * W1TS) * 64 + lane) * 16, sc);
    sm[j] = mfma_bf16(sc, on0, b1);
  }
  SB();
  if (i < 2) {
#pragma unroll
    for (int j = 0; j < NF; ++j) *(f32x4*)(sums + i * (16 * NF) + 16 * j + 4 * g) = sm[j];
  }
}

DEV void conv_phase(const unsigned short* __restrict__ dskip, char* __restrict__ ws,
                    const unsigned short* __restrict__ w1, const unsigned short* __restrict__ w2, char* smem_all) {
  constexpr int CS = K2_CS, KOFF = K2_KOFF;
  const int tid = threadIdx.x, lane = tid & 63;
  const int w = __builtin_amdgcn_readfirstlane(tid >> 6);
  const int bx = blockIdx.x;
  const int pj = (bx & 7) * 16 + (bx >> 4), par = (bx >> 3) & 1;
  const int r = lane & 15, g = lane >> 4, th = tid & 127;
  const int s4 = (r + 1) & 3;
  const rsrc_t rs = ws_rsrc(ws);
  unsigned* fl = (unsigned*)(smem_all + CV_FL);
  asm volatile("" :: "s"(w1), "s"(w2));
  if (tid < 12) fl[tid] = 0u;
  if (w < 4) {
    const int d = 5 * pj + 2 * par + (w >> 1), hw = w & 1;
    char* smem = smem_all + (w >> 1) * K2_CH;
    const ChanRaw cr = chan_ld(dskip, ws, d, lane);
    SB();
    const StageRaw sr = stage_ld(ws, d, th);
    u32x4 xr4[2];
    u32x2 xres[4];
    auto ld_res = [&]() __attribute__((always_inline)) {
#pragma unroll
      for (int qp = 0; qp < 2; ++qp)
        xr4[qp] = *(const u32x4*)(ws + WS_XT + tl_off(d, 256 * (4 * hw + 2 * qp + (g & 1)) + 16 * r + 8 * (g >> 1)));
    };
    ld_res();
    stage_zero(smem, th);
    SB();
    asm volatile("s_waitcnt lgkmcnt(0)\n\ts_barrier" ::: "memory");
    SB();
    const Chan c = chan_fin(cr);
    stage_wr(sr, c, smem, th);
    lds_post(fl + 6 + (w >> 1), lane);
    lds_wait(fl + 6 + (w >> 1), 2u);
    f32x4 acc[8], keep[4];
#pragma unroll
    for (int q = 0; q < 8; ++q) acc[q] = (f32x4){0.f, 0.f, 0.f, 0.f};
    {
      const unsigned ap0 = (unsigned)(size_t)(smem + s4 * CS + 2 * (128 * hw + 8 * g + (r + 1) - s4));
      const unsigned bp0 = (unsigned)(size_t)(smem + KOFF + 2 * (2032 + 128 * hw - 16 * r + 8 * g) - 3584);
      u32x2 Al[2][8], Ah[2][8];
      u32x4 Bf[2][8];
#pragma unroll
      for (int u = 0; u < 8; ++u) K2_LOAD(0, 0, u);
      auto tile = [&](int cc, int q) __attribute__((always_inline)) {
#pragma unroll
        for (int sp = 0; sp <= q; ++sp) {
          const u32x4 A = {Al[cc & 1][sp][0], Al[cc & 1][sp][1], Ah[cc & 1][sp][0], Ah[cc & 1][sp][1]};
          acc[q] = mfma_f16(A, Bf[cc & 1][q - sp], acc[q]);
        }
        SB();
      };
#pragma unroll
      for (int cc = 0; cc < 3; ++cc) {
        lgkm0();
#pragma unroll
        for (int q = 0; q < 8; ++q) {
          K2_LOAD((cc + 1) & 1, cc + 1, q);
          tile(cc, q);
        }
      }
#pragma unroll
      for (int qp = 0; qp < 2; ++qp) {
        const auto s0 = __builtin_amdgcn_permlane16_swap(xr4[qp][0], xr4[qp][2], false, false);
        const auto s1 = __builtin_amdgcn_permlane16_swap(xr4[qp][1], xr4[qp][3], false, false);
        xres[2 * qp] = (u32x2){s0[0], s1[0]}; xres[2 * qp + 1] = (u32x2){s0[1], s1[1]};
      }
      lgkm0();
      f32x4* xw = (f32x4*)(smem_all + CV_XCH + w * 4096) + lane;
      auto last = [&](auto hw_tag) __attribute__((always_inline)) {
        constexpr int S = decltype(hw_tag)::value ? 0 : 4, K = 4 - S;
#pragma unroll
        for (int q = 0; q < 4; ++q) {
          tile(3, S + q);
          if (q) { xw[64 * (q - 1)] = acc[S + q - 1]; SB(); }
        }
        tile(3, K + 3);
        xw[64 * 3] = acc[S + 3];
        lds_post(fl + w, lane);
        SB();
#pragma unroll
        for (int q = 0; q < 3; ++q) tile(3, K + q);
#pragma unroll
        for (int q = 0; q < 4; ++q) keep[q] = acc[K + q];
      };
      if (hw) last(std::true_type{}); else last(std::false_type{});
    }
    lds_wait(fl + (w ^ 1), 1u);
    const f32x4* xrd = (const f32x4*)(smem_all + CV_XCH + (w ^ 1) * 4096) + lane;
#pragma unroll
    for (int qp = 0; qp < 2; ++qp) {
      u32x2 o[2];
#pragma unroll
      for (int h = 0; h < 2; ++h) {
        const int q = 2 * qp + h;
        const f32x4 v = conv_out(keep[q] + xrd[64 * q], c, smem, 256 * (4 * hw + q) + 16 * r + 4 * g);
        o[h] = (u32x2){add_bf(xres[q][0], v[0], v[1]), add_bf(xres[q][1], v[2], v[3])};
      }
      const auto s0 = __builtin_amdgcn_permlane16_swap(o[0][0], o[1][0], false, false);
      const auto s1 = __builtin_amdgcn_permlane16_swap(o[0][1], o[1][1], false, false);
      const int t = 256 * (4 * hw + 2 * qp + (g & 1)) + 16 * r + 8 * (g >> 1);
      st16(rs, WS_X1T + (((size_t)(t >> 6) * D_ + d) * 64 + (t & 63)) * 2, (u32x4){s0[0], s1[0], s0[1], s1[1]});
    }
  } else {
    __builtin_amdgcn_s_setprio(PRIO47);
    const int d = 5 * pj + 4, wq = w - 4;
    char* smem = smem_all + 2 * K2_CH;
    StageRaw sr;
    const ChanRaw cr = chan_ld(dskip, ws, d, lane);
    asm volatile("" ::: "memory");
    if (wq < 2) sr = stage_ld(ws, d, th);
    asm volatile("" ::: "memory");
    const int tl0 = par ? (wq < 3 ? 5 - wq : 1) : (wq == 0 ? 7 : (wq == 1 ? 6 : 2)), tl1 = 0;
    const unsigned ap0 = (unsigned)(size_t)(smem + s4 * CS + 2 * (64 * wq + 8 * g + (r + 1) - s4));
    const unsigned bp0 = (unsigned)(size_t)(smem + KOFF + 2 * (2032 + 64 * wq - 16 * r + 8 * g) - 3584);
    char* redw = smem_all + CV_RED + wq * 1024 + lane * 16;
    u32x2 xres[2];
    xres[0] = *(const u32x2*)(ws + WS_XT + tl_off(d, 256 * tl0 + 16 * r + 4 * g));
    xres[1] = *(const u32x2*)(ws + WS_XT + tl_off(d, 256 * tl1 + 16 * r + 4 * g));
    constexpr int NF1 = NF1_, NC1 = 108 / NF1, NB1 = 5 * NC1, NF2 = NF2_, NC2 = 40 / NF2, NJ2 = 54 * NC2;
    const bool isw1 = bx < NB1;
    const int j2 = 4 * (bx - NB1) + wq;
    const bool isw2 = !isw1 && j2 < NJ2, isgb = j2 >= NJ2 && j2 < NJ2 + 10;
    const int jk = isw1 ? 4 * (bx / NC1) + wq : j2 / NC2;
    const int jc = isw1 ? NF1 * (bx % NC1) : NF2 * (j2 % NC2);
    RtRaw rt;
    const int gbi = 64 * (j2 - NJ2) + lane;
    ModP mp;
    SB();
    auto rtl = [&]() __attribute__((always_inline)) {
      if (isw1) rt_load<NF1>(rt, w1, FF, jk, jc, lane);
      else if (isw2) rt_load<NF2>(rt, w2, D_, jk, jc, lane);
      SB();
    };
    if (wq == 0) stage_zero(smem, th);
    SB();
    __builtin_amdgcn_s_barrier();
    SB();
    const Chan c = chan_fin(cr);
    if (wq < 2) {
      stage_wr(sr, c, smem, th);
      lds_post(fl + 8, lane);
    }
    if (wq >= 2 && RT67) __builtin_amdgcn_s_sleep(RT67);
    rtl();
    lds_wait(fl + 8, 2u);
    if (isw1 || isgb) mp = mod_ld(rs, isw1 ? (lane < 32 ? 4 * D_ : 3 * D_ - 32) + 32 * jk + lane : 5 * D_ + gbi);
    SB();
    if (par) conv_half<1>(ap0, bp0, redw); else conv_half<0>(ap0, bp0, redw);
    lds_post(fl + 4, lane);
    lds_wait(fl + 4, 4u);
    const char* rd = smem_all + CV_RED + lane * 16;
#pragma unroll
    for (int u = 0; u < 2; ++u) {
      const bool on = u == 0 ? (par || wq < 3) : (par && wq == 0);
      if (on) {
        const int sl = u == 0 ? wq : 4, tl = u == 0 ? tl0 : tl1;
        const f32x4 y = (*(const f32x4*)(rd + sl * 4096) + *(const f32x4*)(rd + sl * 4096 + 1024))
                      + (*(const f32x4*)(rd + sl * 4096 + 2048) + *(const f32x4*)(rd + sl * 4096 + 3072));
        const int t = 256 * tl + 16 * r + 4 * g;
        const f32x4 v = conv_out(y, c, smem, t);
        st8(rs, WS_X1T + (((size_t)(t >> 6) * D_ + d) * 64 + (t & 63)) * 2, (u32x2){add_bf(xres[u][0], v[0], v[1]), add_bf(xres[u][1], v[2], v[3])});
      }
    }
    SB();
    char* scr = smem_all + CV_RT2 + wq * (32 * RT_STR + 256);
    if (isw1) {
      float* sums = (float*)(smem_all + CV_SUM);
      w1_fin<NF1>(rt, mp, ws, jk, jc, lane, scr, sums + 32 * NF1 * wq);
      asm volatile("" ::: "memory");
      unsigned old = 0u;
      if (lane == 0) old = __hip_atomic_fetch_add(fl + 5, 1u, __ATOMIC_RELAXED, __HIP_MEMORY_SCOPE_WORKGROUP);
      asm volatile("" ::: "memory");
      if (__builtin_amdgcn_readfirstlane(old) == 3u && lane < 8 * NF1) {
        const f32x4* sp = (const f32x4*)sums + lane;
        const f32x4 v = (sp[0] + sp[8 * NF1]) + (sp[16 * NF1] + sp[24 * NF1]);
        st16(rs, WS_CSP + ((size_t)(2 * (bx / NC1) + lane / (4 * NF1)) * FF + 16 * jc + 4 * (lane % (4 * NF1))) * 4, __builtin_bit_cast(u32x4, v));
      }
    } else if (isw2) {
      rt_park<NF2>(rt, scr, lane);
      lgkm0();
#pragma unroll
      for (int j = 0; j < NF2; ++j) st16(rs, WS_W2F + ((size_t)((jc + j) * 54 + jk) * 64 + lane) * 16, rt_frag(scr, j, lane));
    } else if (isgb) {
      st4(ws, WS_GB + 4 * gbi, __float_as_uint(mod_fin(mp)));
    }
  }
}

__global__ NOPK __launch_bounds__(512, 2) void k2(const unsigned short* __restrict__ dskip, char* __restrict__ ws,
                                                  const unsigned short* __restrict__ w1, const unsigned short* __restrict__ w2) {
  __shared__ __attribute__((aligned(16))) char smem[CV_XCH + 16384];
  conv_phase(dskip, ws, w1, w2, smem);
}

DEV u32x4 ld16_sc1(rsrc_t r, int voff, int soff) { return __builtin_amdgcn_raw_buffer_load_b128(r, voff, soff, 16); }
DEV u32x4 ld16(rsrc_t r, int voff, int soff) { return __builtin_amdgcn_raw_buffer_load_b128(r, voff, soff, 0); }
DEV u32x4 tr_frag(const char* p) {
  const u32x2 a = __builtin_bit_cast(u32x2, __builtin_amdgcn_ds_read_tr16_b64_v4i16((lds_s16x4*)p));
  const u32x2 b = __builtin_bit_cast(u32x2, __builtin_amdgcn_ds_read_tr16_b64_v4i16((lds_s16x4*)(p + 256)));
  return (u32x4){a[0], a[1], b[0], b[1]};
}

constexpr int KF_RS = 84;
constexpr int KF_RB = 8 * 16 * KF_RS * 4;
constexpr int KF_BIG = 2 * KF_RB;
constexpr int NW2L = 8, NW2R = 27;
constexpr int KF_TAB = KF_BIG;
constexpr int KF_PS = KF_TAB + 320;
constexpr int KF_XR = KF_PS + 512;
constexpr int KF_CNT = KF_XR + 64 * 176;
constexpr int KF_LDS = KF_CNT + 16;

DEV void kf_body(char* __restrict__ ws, unsigned short* __restrict__ out, char* smem) {
  char* xs = smem;
  float* tab = (float*)(smem + KF_TAB);
  float* ps = (float*)(smem + KF_PS);
  char* xr = smem + KF_XR;
  unsigned* lcnt = (unsigned*)(smem + KF_CNT);
  float* cst = (float*)(smem + 81920);
  const int tid = threadIdx.x, lane = tid & 63;
  const int w = __builtin_amdgcn_readfirstlane(tid >> 6);
  const int bx = blockIdx.x;
  const int ng = (bx & 1) * 4 + ((bx >> 3) & 3), mt = ((bx & 7) >> 1) * 8 + (bx >> 5);
  const int g = lane >> 4, i = lane & 15;
  const rsrc_t rs_ = ws_rsrc(ws);

  unsigned* flags = (unsigned*)(ws + WS_CNT) + mt * (2 * FL_P);
  const int ntl = ng < 6 ? 14 : 12;
  const int ctb = ng < 6 ? 14 * ng : 84 + 12 * (ng - 6);
  const bool two = w < ntl - 8;
  const bool full = w < 6;
  const int ks0 = w < 6 ? 7 * w : 42 + 6 * (w - 6);
  const int ct0 = ctb + (two ? 2 * w : (ntl - 8) + w);
  const int lo16 = lane * 16;
  const int w1s = (int)WS_W1F + ct0 * W1TS * 1024;
  const int w2s = (int)WS_W2F + (ng * 5 * 54 + ks0) * 1024;

  u32x4 ring[32], wl[NW2L];
  const char* bb0 = xs + (8 * g + (i >> 2)) * 64 + ((g & 1) << 5) + (i & 3) * 8;
  const char* bb1 = xs + (8 * g + (i >> 2)) * 64 + (((g & 1) ^ 1) << 5) + (i & 3) * 8;
  auto w2_off = [&](int e) __attribute__((always_inline)) {
    const int s = e < 27 ? e / 4 : (e < 34 ? e - 27 : 6), c = e < 27 ? e % 4 : (e < 34 ? 4 : 3);
    const int sn = (s == 6 && !full) ? 5 : s;
    return w2s + (c * 54 + sn) * 1024;
  };

  auto gemm1 = [&](auto two_tag) __attribute__((always_inline)) {
    constexpr bool TWO = decltype(two_tag)::value;
    if (TWO) asm volatile("; two tiles" ::: "memory"); else asm volatile("; one tile" ::: "memory");
    u32x4 t[10];
    auto st_x = [&](int i0, int i1) __attribute__((always_inline)) {
#pragma unroll
      for (int it = i0; it < i1; ++it) {
        const int c = tid + 512 * it;
        const int row = c >> 3;
        *(u32x4*)(xs + ((c >> 2) & 1) * 40960 + row * 64 + (((c & 3) ^ ((row >> 2) & 2)) << 4)) = t[it];
      }
    };
#pragma unroll
    for (int it = 0; it < X_FIRST; ++it) t[it] = ld16(rs_, tid * 16, (int)WS_X1T + mt * 81920 + 8192 * it);
#pragma unroll
    for (int k = 0; k < W1_EARLY; ++k) {
      ring[2 * k] = ld16(rs_, lo16, w1s + k * W1KS * 1024);
      if (TWO) ring[2 * k + 1] = ld16(rs_, lo16, w1s + (k * W1KS + W1TS) * 1024);
    }
    SB();
    __builtin_amdgcn_s_barrier();
    SB();
#pragma unroll
    for (int k = W1_EARLY; k < W1_MID; ++k) {
      ring[2 * k] = ld16(rs_, lo16, w1s + k * W1KS * 1024);
      if (TWO) ring[2 * k + 1] = ld16(rs_, lo16, w1s + (k * W1KS + W1TS) * 1024);
    }
    SB();
    st_x(0, X_FIRST);
    SB();
    if (tid == 0) *lcnt = 0u;
    float gbp;
    if (!TWO) gbp = ((const float*)(ws + WS_GB))[80 * ng + min((w & 1) * 64 + lane, 79)];
    SB();
    __syncthreads();
    auto resid = [&]() __attribute__((always_inline)) {
      const int kb = (80 * ng) >> 5, tq = w & 3;
#pragma unroll
      for (int u = 0; u < 2; ++u) {
        const int kk = min(kb + 2 * (w >> 2) + u, 19);
        const u32x4 f = tr_frag(((tq & 1) ? bb1 : bb0) + (tq >> 1) * 40960 + kk * 2048);
        const unsigned dl = (unsigned)(32 * kk + 8 * g - 80 * ng);
        if (dl < 80u) *(u32x4*)(xr + (tq * 16 + i) * 176 + dl * 2) = f;
      }
    };
    const int hs = TWO ? 0 : (w & 1);
    const char* ba[4] = {bb0 + hs * 40960, bb1 + hs * 40960, bb0 + (hs ^ 1) * 40960, bb1 + (hs ^ 1) * 40960};
    const u32x4 ones = {0x3f803f80u, 0x3f803f80u, 0x3f803f80u, 0x3f803f80u};
    f32x4 acc[2][4];
#pragma unroll
    for (int c = 0; c < 2; ++c)
#pragma unroll
      for (int q = 0; q < 4; ++q) acc[c][q] = (f32x4){0.f, 0.f, 0.f, 0.f};
    u32x4 csp[5];
    const int cso = ((w & 1) * FF + 16 * ctb + 4 * min(lane, 4 * ntl - 1)) * 4;
    u32x4 bn[2][4];
#pragma unroll
    for (int q = 0; q < 4; ++q) bn[0][q] = tr_frag(ba[q]);
#pragma unroll
    for (int q = 0; q < 4; ++q) bn[1][q] = tr_frag(ba[q] + 2048);
#pragma unroll
    for (int kk = 0; kk < 20; ++kk) {
      if (kk == X_STEP) { st_x(X_FIRST, 10); SB(); }
      if (kk == 2 * X_FIRST - 2) { SB(); bar_lds(); SB(); }
      else if ((LOCK_MASK >> kk) & 1) { SB(); __builtin_amdgcn_s_barrier(); SB(); }
      if (kk == 2 * X_FIRST) { resid(); SB(); }
      auto gload = [&](int u) __attribute__((always_inline)) {
        {
          constexpr int CATCH = W1_PRE - W1_MID;
          const int k0 = kk == 0 ? W1_MID : (kk <= CATCH ? W1_MID + 2 * kk - 1 : kk + W1_PRE);
          const int nk = (kk >= 1 && kk <= CATCH) ? 2 : 1;
          const int kn = k0 + (nk == 2 ? (u >> 1) : 0);
          const bool on = nk == 2 || u >= 2;
          if (on && kn < 20) {
            if ((u & 1) == 0) ring[(2 * kn) % 32] = ld16(rs_, lo16, w1s + kn * W1KS * 1024);
            else if (TWO) ring[(2 * kn + 1) % 32] = ld16(rs_, lo16, w1s + (kn * W1KS + W1TS) * 1024);
          }
          if (kk == 0 && u < 2) {
#pragma unroll
            for (int it = X_FIRST + u * 3; it < (u ? 10 : (X_FIRST + 3 < 10 ? X_FIRST + 3 : 10)); ++it)
              t[it] = ld16(rs_, tid * 16, (int)WS_X1T + mt * 81920 + 8192 * it);
          }
        }
        if (kk >= W2_START && u < W2_RATE) {
          const int n = W2_RATE * (kk - W2_START) + u;
          if (n < NW2L) wl[n] = ld16(rs_, lo16, w2_off(NW2R + n));
          else if (n < W2_INL) ring[8 + n - NW2L] = ld16(rs_, lo16, w2_off(n - NW2L));
        }
      };
      const u32x4 a0 = ring[(2 * kk) % 32], a1 = ring[(2 * kk + 1) % 32];
#pragma unroll
      for (int q = 0; q < 4; ++q) {
        const u32x4 bq = bn[kk & 1][q];
        acc[0][q] = mfma_bf16(a0, bq, acc[0][q]);
        if (TWO) acc[1][q] = mfma_bf16(a1, bq, acc[1][q]);
        else if (q < 2) {
          acc[1][2 * q] = mfma_bf16(ones, bq, acc[1][2 * q]);
          acc[1][2 * q + 1] = mfma_bf16(bq, bq, acc[1][2 * q + 1]);
        }
        SB();
        if (kk < 18) bn[kk & 1][q] = tr_frag(ba[q] + (kk + 2) * 2048);
        gload(q);
        SB();
      }
      if (!TWO) {
        if (kk < 5) csp[kk] = ld16(rs_, cso, (int)WS_CSP + kk * 2 * FF * 4);
        if (kk == CS_STEP) {
          f32x4 t = __builtin_bit_cast(f32x4, csp[0]);
#pragma unroll
          for (int u = 1; u < 5; ++u) t += __builtin_bit_cast(f32x4, csp[u]);
          if (w >= 6 && lane < 56) *(f32x4*)(cst + (w & 1) * 224 + 4 * lane) = t;
        }
      }
      if (!TWO && kk == 12) { if ((w & 1) * 64 + lane < 80) tab[(w & 1) * 64 + lane] = gbp; }
      SB();
    }
    if (!TWO) {
#pragma unroll
      for (int h = 0; h < 2; ++h) {
        const f32x4 aq = acc[1][2 * h + 1];
        const float dq = (i & 2) ? ((i & 1) ? aq[3] : aq[2]) : ((i & 1) ? aq[1] : aq[0]);
        const int tk = (2 * hs + h) * 16 + i;
        if (g == (i >> 2)) {
          const float mean = acc[1][2 * h][0] * (1.f / D_);
          const float var = fmaxf(dq * (1.f / D_) - mean * mean, 0.f);
          const float sd = rbf(sqrtf(rbf(rbf(var) + 1e-5f)));
          const float rs = 1.f / sd;
          ps[tk] = rs;
          ps[64 + tk] = -rbf(mean) * rs;
        }
      }
    }
    __syncthreads();
    f32x4 csv[2], b1v[2];
#pragma unroll
    for (int c = 0; c < (TWO ? 2 : 1); ++c) {
      csv[c] = *(const f32x4*)(cst + 16 * (ct0 - ctb + c) + 4 * g);
      b1v[c] = *(const f32x4*)(cst + 224 + 16 * (ct0 - ctb + c) + 4 * g);
    }
#pragma unroll
    for (int qp = 0; qp < 2; ++qp) {
      u32x2 o[2][2];
#pragma unroll
      for (int h = 0; h < 2; ++h) {
        const int q = 2 * qp + h, tq = q ^ (2 * hs);
        const float rs = ps[tq * 16 + i], nm = ps[64 + tq * 16 + i];
#pragma unroll
        for (int c = 0; c < (TWO ? 2 : 1); ++c) {
          float v[4];
#pragma unroll
          for (int r = 0; r < 4; ++r) v[r] = fmaxf(fmaf(acc[c][q][r], rs, fmaf(nm, csv[c][r], b1v[c][r])), 0.f);
          o[c][h] = (u32x2){pk_bf(v[0], v[1]), pk_bf(v[2], v[3])};
        }
      }
      const int tl = ((2 * qp) ^ (2 * hs)) + (g & 1);
#pragma unroll
      for (int c = 0; c < (TWO ? 2 : 1); ++c) {
        const int ct = ct0 + c;
        const auto s0 = __builtin_amdgcn_permlane16_swap(o[c][0][0], o[c][1][0], false, false);
        const auto s1 = __builtin_amdgcn_permlane16_swap(o[c][0][1], o[c][1][1], false, false);
        const size_t off = (((size_t)(4 * mt + tl) * 54 + (ct >> 1)) * 64 + i + 16 * (2 * (ct & 1) + (g >> 1))) * 16;
        st16(rs_, WS_H1F + off, (u32x4){s0[0], s1[0], s0[1], s1[1]});
      }
    }
    SB();
    __builtin_amdgcn_s_barrier();
    SB();
    constexpr int E0 = W2_INL - NW2L;
    auto signal = [&]() __attribute__((always_inline)) {
      if (lane == 0) {
        if (__hip_atomic_fetch_add(lcnt, 1u, __ATOMIC_RELAXED, __HIP_MEMORY_SCOPE_WORKGROUP) == 7u)
          __hip_atomic_store(flags + (FL_P / 4) * ng, 1u, __ATOMIC_RELAXED, __HIP_MEMORY_SCOPE_AGENT);
      }
    };
    if (w >= SIG_SPLIT) { asm volatile("s_waitcnt vmcnt(0)" ::: "memory"); signal(); }
    SB();
#pragma unroll
    for (int e = E0; e < NW2R; ++e) ring[(8 + e) % 32] = ld16(rs_, lo16, w2_off(e));
    SB();
    if (w < SIG_SPLIT) { asm volatile("s_waitcnt vmcnt(%0)" :: "n"(NW2R - E0) : "memory"); signal(); }
    SB();
  };
  if (two) gemm1(std::true_type{});
  else gemm1(std::false_type{});
#pragma nounroll
  for (unsigned spin = 0; spin < (1u << 20); ++spin) {
    if (__hip_atomic_load(flags + (FL_P / 4) * w, __ATOMIC_RELAXED, __HIP_MEMORY_SCOPE_AGENT) != 0u) break;
    if (POLL_SL) __builtin_amdgcn_s_sleep(POLL_SL);
  }
  asm volatile("" ::: "memory");
  SB();

  const int h1s = (int)WS_H1F + (mt * 4 * 54 + ks0) * 1024;
  u32x4 hb[28];
  auto load_h1 = [&](int n) __attribute__((always_inline)) {
    const int q = n / 7, s = n % 7;
    const int sn = (s == 6 && !full) ? 5 : s;
    hb[n] = ld16_sc1(rs_, lo16, h1s + (q * 54 + sn) * 1024);
  };
#pragma unroll
  for (int n = 0; n < H1_PRE; ++n) {
    if (n == H1_BATCH) { SB(); __builtin_amdgcn_s_barrier(); SB(); }
    load_h1(n);
  }
  SB();
  float* red = (float*)smem;
  float* rb_ = red + (w * 16 + i) * KF_RS + 4 * g;
  const int re = w * 40 + min(lane, 39);
  const int rtk = re / 20, rq4 = re % 20;
  const float* rr_ = red + rtk * KF_RS + 4 * rq4;
  f32x4 ac2[2][5];
  f32x4 rv[8], rsum;
  u32x2 xv2;
  const f32x4 gb = *(const f32x4*)(tab + 4 * rq4);
  auto red_wr = [&](int q, int c) __attribute__((always_inline)) {
    *(f32x4*)(rb_ + (q & 1) * (KF_RB / 4) + 16 * c) = ac2[q & 1][c];
  };
  auto red_rd = [&](int q, int ww) __attribute__((always_inline)) {
    rv[ww] = *(const f32x4*)(rr_ + (q & 1) * (KF_RB / 4) + ww * 16 * KF_RS);
  };
  auto red_x = [&](int q) __attribute__((always_inline)) { xv2 = *(const u32x2*)(xr + (16 * q + rtk) * 176 + rq4 * 8); };
  auto red_out = [&](int q) __attribute__((always_inline)) {
    const f32x4 s = rsum + (rv[6] + rv[7]);
    u32x2 o;
#pragma unroll
    for (int h = 0; h < 2; ++h) {
      o[h] = pk_bf(fmaf(s[2 * h], gb[2 * h], bf_lo(xv2[h])), fmaf(s[2 * h + 1], gb[2 * h + 1], bf_hi(xv2[h])));
    }
    *(u32x2*)(out + (size_t)(mt * 64 + 16 * q + rtk) * D_ + ng * 80 + 4 * rq4) = o;
  };
#pragma unroll
  for (int q = 0; q < 3; ++q) {
#pragma unroll
    for (int c = 0; c < 5; ++c) ac2[q & 1][c] = (f32x4){0.f, 0.f, 0.f, 0.f};
#pragma unroll
    for (int s = 0; s < 7; ++s) {
      auto mm = [&](int c) __attribute__((always_inline)) {
        const u32x4 a = c == 4 ? wl[s] : ((s == 6 && c == 3) ? wl[7] : ring[(8 + 4 * s + c) % 32]);
        ac2[q & 1][c] = mfma_bf16(a, hb[7 * q + s], ac2[q & 1][c]);
      };
      if (s == 5 && q >= 2) {
        red_wr(q - 1, 4);
        SB();
        red_out(q - 2);
#pragma unroll
        for (int c = 0; c < 5; ++c) mm(c);
#pragma unroll
        for (int c = 0; c < 5; ++c) {
          __builtin_amdgcn_sched_group_barrier(0x008, 1, 0);
          __builtin_amdgcn_sched_group_barrier(0x002, 5, 0);
        }
        SB();
      } else if (s < 6) {
        mm(0); SB();
        if (q >= 1 && s >= 1) { red_wr(q - 1, s - 1); SB(); }
        mm(1); SB();
        if (q >= 2 && s < 4) { red_rd(q - 2, 2 * s); SB(); }
        mm(2); SB();
        if (q >= 2 && s < 4) { red_rd(q - 2, 2 * s + 1); SB(); }
        if (q >= 2 && s == 4) { red_x(q - 2); SB(); }
        mm(3); SB();
        if (q >= 2 && s >= 1 && s < 4) {
          const f32x4 t = rv[2 * s - 2] + rv[2 * s - 1];
          rsum = s == 1 ? t : rsum + t;
          asm volatile("" : "+v"(rsum));
          SB();
        }
        mm(4); SB();
      } else if (full) {
#pragma unroll
        for (int c = 0; c < 5; ++c) mm(c);
        SB();
      }
      if (7 * q + s + H1_PRE < 28) { load_h1(7 * q + s + H1_PRE); SB(); }
    }
    if (q >= 1) bar_lds();
  }
  {
    auto sum6 = [&]() __attribute__((always_inline)) {
      rsum = ((rv[0] + rv[1]) + (rv[2] + rv[3])) + (rv[4] + rv[5]);
    };
    if (full) {
#pragma unroll
      for (int c = 0; c < 5; ++c) {
        const u32x4 a = c == 4 ? wl[6] : (c == 3 ? wl[7] : ring[(8 + 24 + c) % 32]);
        rv[c] = mfma_bf16(a, hb[27], (f32x4){0.f, 0.f, 0.f, 0.f});
      }
    } else {
#pragma unroll
      for (int c = 0; c < 5; ++c) rv[c] = (f32x4){0.f, 0.f, 0.f, 0.f};
    }
    f32x4 c0[5];
#pragma unroll
    for (int c = 0; c < 5; ++c) c0[c] = rv[c];
    SB();
    auto col = [&](int c, auto&& between) __attribute__((always_inline)) {
#pragma unroll
      for (int s = 0; s < 6; ++s) {
        const u32x4 a = c == 4 ? wl[s] : ring[(8 + 4 * s + c) % 32];
        c0[c] = mfma_bf16(a, hb[21 + s], c0[c]);
        SB();
        between(s);
        SB();
      }
    };
    col(0, [&](int s) __attribute__((always_inline)) { if (s < 5) red_wr(2, s); if (s < 4) { red_rd(1, 2 * s); red_rd(1, 2 * s + 1); } if (s == 4) red_x(1); });
    col(1, [&](int s) __attribute__((always_inline)) { if (s == 1) { sum6(); asm volatile("" : "+v"(rsum)); } if (s == 3) red_out(1); });
    bar_lds();
    auto wr3 = [&](int c) __attribute__((always_inline)) { *(f32x4*)(rb_ + (KF_RB / 4) + 16 * c) = c0[c]; };
    col(2, [&](int s) __attribute__((always_inline)) { if (s < 2) wr3(s); if (s >= 1 && s < 5) { red_rd(2, 2 * s - 2); red_rd(2, 2 * s - 1); } if (s == 5) red_x(2); });
    col(3, [&](int s) __attribute__((always_inline)) { if (s == 1) wr3(2); if (s == 2) { sum6(); asm volatile("" : "+v"(rsum)); } if (s == 4) red_out(2); });
    col(4, [&](int s) __attribute__((always_inline)) { if (s == 1) wr3(3); if (s == 2) red_x(3); });
    wr3(4);
    bar_lds();
#pragma unroll
    for (int ww = 0; ww < 8; ++ww) red_rd(3, ww);
    sum6();
    red_out(3);
  }
}

__global__ NOPK __launch_bounds__(512, 2) void kf(char* __restrict__ ws, unsigned short* __restrict__ out) {
  __shared__ __attribute__((aligned(16))) char smem[KF_LDS];
  kf_body(ws, out, smem);
}

__global__ void kend_() {}

extern "C" void kernel_launch(void* const* d_in, const int* in_sizes, int n_in,
                              void* d_out, int out_size, void* d_ws, size_t ws_size,
                              hipStream_t stream) {
  K1Args a;
  a.x = (const unsigned short*)d_in[0];
  a.are = (const unsigned short*)d_in[1];
  a.aim = (const unsigned short*)d_in[2];
  a.bre = (const unsigned short*)d_in[3];
  a.bim = (const unsigned short*)d_in[4];
  a.cre = (const unsigned short*)d_in[5];
  a.cim = (const unsigned short*)d_in[6];
  a.logdt = (const unsigned short*)d_in[7];
  a.c = (const unsigned short*)d_in[12];
  a.wmod = (const unsigned short*)d_in[13];
  a.ws = (char*)d_ws;
  k1<<<NB_PF + NB_K + NB_LN + NB_MOD, 256, 0, stream>>>(a);
  k2<<<256, 512, 0, stream>>>((const unsigned short*)d_in[8], (char*)d_ws, (const unsigned short*)d_in[10],
                              (const unsigned short*)d_in[11]);
  kf<<<256, 512, 0, stream>>>((char*)d_ws, (unsigned short*)d_out);
}
```
